```python
import torch

def _softmax(x, axis=-1):
    return torch.softmax(x, dim=axis)
def _gelu(x):
    return torch.nn.functional.gelu(x, approximate="tanh")
def _silu(x):
    return torch.nn.functional.silu(x)
def _sigmoid(x):
    return torch.sigmoid(x)
def _layernorm(x, axis=-1, eps=1e-5):
    m = x.mean(dim=axis, keepdim=True)
    v = x.var(dim=axis, keepdim=True, unbiased=False)
    return (x - m) / torch.sqrt(v + eps)
def _scatter_add(tgt, idx, src, axis=0):
    return tgt.clone().index_add_(axis, idx, src)
def _segment_sum(src, seg, n):
    out = torch.zeros((n,) + src.shape[1:], dtype=src.dtype)
    return out.index_add_(0, seg, src)
def _pad(x, pw):
    flat = tuple(v for pair in reversed(pw) for v in pair)
    return torch.nn.functional.pad(x, flat)

kernel_name = 'concat_heads__ssm_conv_s4d__ssm_conv_h3__prompt'

def setup_inputs():

    _w = (lambda a: (lambda t: t.to(torch.bfloat16) if t.is_floating_point() else t)(torch.as_tensor(a)))
    def _A_setup():
        Bsz, L, D, N = 1, 2048, 512, 128
        return {
            "u": _w((lambda *s: torch.randn(*s, dtype=torch.bfloat16) * 0.05)(Bsz, L, D)),
            "log_A_real": _w((lambda *s: torch.randn(*s, dtype=torch.bfloat16) * 3.2)(D, N) * 0.5),
            "A_imag": _w((lambda *s: torch.randn(*s, dtype=torch.bfloat16) * 0.1)(D, N) * 3.14),
            "C_real": _w((lambda *s: torch.randn(*s, dtype=torch.bfloat16) * 0.1)(D, N) / N ** 0.5),
            "C_imag": _w((lambda *s: torch.randn(*s, dtype=torch.bfloat16) * 0.1)(D, N) / N ** 0.5),
            "log_dt": _w((lambda *s: torch.randn(*s, dtype=torch.bfloat16) * 0.05)(D) * 0.5 - 4.0),
            "D_skip": _w((lambda *s: torch.randn(*s, dtype=torch.bfloat16) * 0.05)(D)),
        }
    _A_in = _A_setup()

    def _B_setup():
        B, S, D = 1, 2048, 512
        ks = 4
        return {
            "q": _w((lambda *s: torch.randn(*s, dtype=torch.bfloat16) * 0.05)(B, S, D)),
            "k": _w((lambda *s: torch.randn(*s, dtype=torch.bfloat16) * 0.4)(B, S, D)),
            "v": _w((lambda *s: torch.randn(*s, dtype=torch.bfloat16) * 0.2)(B, S, D)),
            "shift_k": _w((lambda *s: torch.randn(*s, dtype=torch.bfloat16) * 0.4)(D, ks) * 0.5),
            "ssm_k": _w((lambda *s: torch.randn(*s, dtype=torch.bfloat16) * 0.4)(D, S) * (1.0 / S ** 0.5)),
            "ssm_D": _w((lambda *s: torch.randn(*s, dtype=torch.bfloat16) * 0.2)(D)),
        }
    _B_in = _B_setup()

    return {
        "A_u": _A_in["u"],
        "A_log_A_real": _A_in["log_A_real"],
        "A_A_imag": _A_in["A_imag"],
        "A_C_real": _A_in["C_real"],
        "A_C_imag": _A_in["C_imag"],
        "A_log_dt": _A_in["log_dt"],
        "A_D_skip": _A_in["D_skip"],
        "B_q": _B_in["q"],
        "B_k": _B_in["k"],
        "B_v": _B_in["v"],
        "B_shift_k": _B_in["shift_k"],
        "B_ssm_k": _B_in["ssm_k"],
        "B_ssm_D": _B_in["ssm_D"],
    }

def reference(A_u, A_log_A_real, A_A_imag, A_C_real, A_C_imag, A_log_dt, A_D_skip, B_q, B_k, B_v, B_shift_k, B_ssm_k, B_ssm_D):
    def _A_ref(u, log_A_real, A_imag, C_real, C_imag, log_dt, D_skip):
        L = u.shape[1]
        dt = torch.exp(log_dt)
        A = -torch.exp(log_A_real) + 1j * A_imag
        C = (C_real + 1j * C_imag) * dt[:, None]
        pos = (lambda x, dt: x.to(dt))(torch.arange(L), log_dt.dtype)
        P = torch.exp((A * dt[:, None])[:, :, None] * pos[None, None, :])
        K = 2.0 * torch.einsum("dn,dnl->dl", C, P).real
        L2 = 2 * L
        u_t = (lambda x, *d: x.permute(*d))(u, 0, 2, 1)
        Kf = (lambda x, *a, **k: torch.fft.rfft(x.float(), *a, **k))(K, L2)
        Uf = (lambda x, *a, **k: torch.fft.rfft(x.float(), *a, **k))(u_t, L2)
        y = (lambda x, *a, **k: torch.fft.irfft(x, *a, **k).to(torch.bfloat16))(Uf * Kf[None, :, :], L2)[:, :, :L]
        y = (lambda x, *d: x.permute(*d))(y, 0, 2, 1)
        return y + u * D_skip[None, None, :]

    def _B_ref(q, k, v, shift_k, ssm_k, ssm_D):
        B, S, D = q.shape
        ks = shift_k.shape[-1]
        k_pad = _pad(k, ((0, 0), (ks - 1, 0), (0, 0)))
        k_shift = torch.zeros_like(k)
        for t in range(ks):
            k_shift = k_shift + k_pad[:, t:t + S, :] * shift_k[:, ks - 1 - t][None, None, :]
        kv = k_shift * v
        L = 2 * S
        kv_pad = _pad(kv, ((0, 0), (0, S), (0, 0)))
        kf_pad = _pad(ssm_k, ((0, 0), (0, S)))
        kv_f = (lambda x, axes=(-2,-1): torch.fft.rfft2(x.float(), dim=axes))(kv_pad[:, None, :, :], axes=(1, 2))
        kf_f = (lambda x, axes=(-2,-1): torch.fft.rfft2(x.float(), dim=axes))(kf_pad[:, None, :], axes=(1, 2))
        kf_f = (lambda x, *d: x.permute(*d))(kf_f, 1, 2, 0)
        y_f = kv_f * kf_f[None, :, :, :]
        y = (lambda x, s, axes=(-2,-1): torch.fft.irfft2(x, s=s, dim=axes).to(torch.bfloat16))(y_f, (1, L), axes=(1, 2))[:, 0, :S, :]
        y = y + kv * ssm_D[None, None, :]
        return q * y

    a = _A_ref(A_u, A_log_A_real, A_A_imag, A_C_real, A_C_imag, A_log_dt, A_D_skip)
    b = _B_ref(B_q, B_k, B_v, B_shift_k, B_ssm_k, B_ssm_D)
    return torch.concatenate([a, b], axis=-1)
```

```cpp
#include <hip/hip_runtime.h>
#include <hip/hip_bf16.h>
#include <stdint.h>

typedef unsigned short u16;
typedef short bf16x8 __attribute__((ext_vector_type(8)));
typedef float f32x4 __attribute__((ext_vector_type(4)));

#define L_ 2048
#define D_ 512
#define NS_ 128

__device__ __forceinline__ float bf2f(u16 x){ return __builtin_bit_cast(float, (uint32_t)x << 16); }
__device__ __forceinline__ u16 f2bf(float f){ __hip_bfloat16 h = __float2bfloat16(f); return __builtin_bit_cast(u16, h); }
__device__ __forceinline__ float rbf(float f){ return bf2f(f2bf(f)); }

__global__ __launch_bounds__(256) void kgen_simple(const u16* __restrict__ logA, const u16* __restrict__ Aim,
    const u16* __restrict__ Cre, const u16* __restrict__ Cim, const u16* __restrict__ logdt,
    u16* __restrict__ Khi, u16* __restrict__ Klo){
  __shared__ float sz[NS_*4];
  __shared__ float sK[648];
  int d = blockIdx.x;
  float dt = rbf(expf(bf2f(logdt[d])));
  if (threadIdx.x < NS_){
    int n = threadIdx.x;
    float are = -rbf(expf(bf2f(logA[d*NS_+n])));
    float aim = bf2f(Aim[d*NS_+n]);
    sz[n*4+0] = are*dt; sz[n*4+1] = aim*dt;
    sz[n*4+2] = bf2f(Cre[d*NS_+n])*dt; sz[n*4+3] = bf2f(Cim[d*NS_+n])*dt;
  }
  __syncthreads();
  for (int p = threadIdx.x; p < 641; p += 256){
    float pos;
    if (p < 256) pos = p; else if (p < 384) pos = 256 + 2*(p-256); else if (p < 512) pos = 512 + 4*(p-384);
    else pos = 1024 + 8*(p-512);
    float acc = 0.f;
    for (int n=0;n<NS_;n++){
      float e = expf(sz[n*4]*pos); float s,c; sincosf(sz[n*4+1]*pos, &s, &c);
      acc += sz[n*4+2]*(e*c) - sz[n*4+3]*(e*s);
    }
    sK[p] = 2.f*acc;
  }
  __syncthreads();
  for (int l = threadIdx.x; l < L_; l += 256){
    int pr = (int)rbf((float)l);
    int p;
    if (pr < 256) p = pr; else if (pr < 512) p = 256 + (pr-256)/2; else if (pr < 1024) p = 384 + (pr-512)/4;
    else p = 512 + (pr-1024)/8;
    float k = sK[p];
    u16 h = f2bf(k);
    Khi[d*L_+l] = h; Klo[d*L_+l] = f2bf(k - bf2f(h));
  }
}

__global__ __launch_bounds__(256) void pre_kernel(const u16* __restrict__ u, const u16* __restrict__ k,
    const u16* __restrict__ v, const u16* __restrict__ shift_k, u16* __restrict__ uT, u16* __restrict__ kvT){
  __shared__ u16 tile[64][64+8];
  int l0 = blockIdx.x*64, d0 = blockIdx.y*64;
  int t = threadIdx.x;
  int c8 = (t & 7)*8;
  int r = t >> 3;
  for (int rr = r; rr < 64; rr += 32){
    int l = l0 + rr;
    u16 o[8];
    if (blockIdx.z == 0){
      uint4 x = *(const uint4*)(u + l*D_ + d0 + c8);
      *(uint4*)o = x;
    } else {
      u16 kk[4][8], vv[8];
      #pragma unroll
      for (int j=0;j<4;j++){
        if (l-j >= 0) *(uint4*)kk[j] = *(const uint4*)(k + (l-j)*D_ + d0 + c8);
        else *(uint4*)kk[j] = make_uint4(0,0,0,0);
      }
      *(uint4*)vv = *(const uint4*)(v + l*D_ + d0 + c8);
      #pragma unroll
      for (int i=0;i<8;i++){
        const u16* w = shift_k + (d0+c8+i)*4;
        float acc = rbf(bf2f(kk[3][i])*bf2f(w[3]));
        acc = rbf(acc + rbf(bf2f(kk[2][i])*bf2f(w[2])));
        acc = rbf(acc + rbf(bf2f(kk[1][i])*bf2f(w[1])));
        acc = rbf(acc + rbf(bf2f(kk[0][i])*bf2f(w[0])));
        o[i] = f2bf(acc*bf2f(vv[i]));
      }
    }
    #pragma unroll
    for (int i=0;i<8;i++) tile[c8+i][rr] = o[i];
  }
  __syncthreads();
  u16* dst = blockIdx.z == 0 ? uT : kvT;
  for (int dd = r; dd < 64; dd += 32){
    uint4 x = *(const uint4*)&tile[dd][c8];
    *(uint4*)(dst + (d0+dd)*L_ + l0 + c8) = x;
  }
}

#define CSTRIDE 4416
template<int NK>
__global__ __launch_bounds__(128) void conv_kernel(const u16* __restrict__ xT, const u16* __restrict__ K0,
    const u16* __restrict__ K1, u16* __restrict__ yT){
  extern __shared__ __attribute__((aligned(16))) char smem[];
  int d = blockIdx.x;
  int t = threadIdx.x;
  #pragma unroll
  for (int q=0;q<NK;q++){
    const u16* Ksrc = (q==0?K0:K1) + d*L_;
    char* base = smem + q*4*CSTRIDE;
    for (int ch = t; ch < 264+2; ch += 128){
      uint32_t w[5];
      if (ch < 256){
        uint4 x = *(const uint4*)(Ksrc + 2040 - 8*ch);
        w[0] = __builtin_amdgcn_alignbit(x.w, x.w, 16);
        w[1] = __builtin_amdgcn_alignbit(x.z, x.z, 16);
        w[2] = __builtin_amdgcn_alignbit(x.y, x.y, 16);
        w[3] = __builtin_amdgcn_alignbit(x.x, x.x, 16);
        if (ch < 255){ uint32_t y = *(const uint32_t*)(Ksrc + 2038 - 8*ch); w[4] = __builtin_amdgcn_alignbit(y,y,16);} else w[4] = 0;
      } else { w[0]=w[1]=w[2]=w[3]=w[4]=0; }
      uint32_t a[4];
      #pragma unroll
      for (int i=0;i<4;i++) a[i] = __builtin_amdgcn_alignbit(w[i+1], w[i], 16);
      uint32_t w5;
      if (ch < 255){ uint32_t y = *(const uint32_t*)(Ksrc + 2036 - 8*ch); w5 = __builtin_amdgcn_alignbit(y,y,16);} else w5 = 0;
      uint32_t a4 = __builtin_amdgcn_alignbit(w5, w[4], 16);
      *(uint4*)(base + 0*CSTRIDE + 16*ch) = make_uint4(w[0],w[1],w[2],w[3]);
      *(uint4*)(base + 1*CSTRIDE + 16*ch) = make_uint4(a[0],a[1],a[2],a[3]);
      *(uint4*)(base + 2*CSTRIDE + 16*ch) = make_uint4(w[1],w[2],w[3],w[4]);
      *(uint4*)(base + 3*CSTRIDE + 16*ch) = make_uint4(a[1],a[2],a[3],a4);
    }
  }
  int lane = t & 63, h = t >> 6;
  int m = lane & 15, kg = lane >> 4;
  bf16x8 A[2][2];
  #pragma unroll
  for (int mt=0;mt<2;mt++)
    #pragma unroll
    for (int ch=0;ch<2;ch++)
      A[mt][ch] = *(const bf16x8*)(xT + d*L_ + 64*(16*mt+m) + 32*ch + 8*kg);
  __syncthreads();
  f32x4 acc[2][2];
  #pragma unroll
  for (int i=0;i<2;i++) for (int j=0;j<2;j++) acc[i][j] = f32x4{0,0,0,0};
  int s0 = 2047 - 16*h - m + 8*kg;
  int r = s0 & 3;
  const char* fb = smem + r*CSTRIDE + 2*(s0 - r);
  auto ldfrag = [&](int q, int o)->bf16x8 {
    const char* p = fb + q*4*CSTRIDE - 2*o;
    uint2 x = *(const uint2*)p; uint2 y = *(const uint2*)(p+8);
    uint4 z = make_uint4(x.x,x.y,y.x,y.y);
    return __builtin_bit_cast(bf16x8, z);
  };
  bf16x8 Flo[NK];
  #pragma unroll
  for (int q=0;q<NK;q++) Flo[q] = ldfrag(q, -32);
  for (int c=0;c<32;c++){
    #pragma unroll
    for (int q=0;q<NK;q++){
      bf16x8 Fm = ldfrag(q, 64*c), Fh = ldfrag(q, 64*c+32);
      if (c < 16){
        acc[0][0] = __builtin_amdgcn_mfma_f32_16x16x32_bf16(A[0][0], Fm, acc[0][0],0,0,0);
        acc[0][1] = __builtin_amdgcn_mfma_f32_16x16x32_bf16(A[0][0], Fh, acc[0][1],0,0,0);
        acc[0][0] = __builtin_amdgcn_mfma_f32_16x16x32_bf16(A[0][1], Flo[q], acc[0][0],0,0,0);
        acc[0][1] = __builtin_amdgcn_mfma_f32_16x16x32_bf16(A[0][1], Fm, acc[0][1],0,0,0);
      }
      acc[1][0] = __builtin_amdgcn_mfma_f32_16x16x32_bf16(A[1][0], Fm, acc[1][0],0,0,0);
      acc[1][1] = __builtin_amdgcn_mfma_f32_16x16x32_bf16(A[1][0], Fh, acc[1][1],0,0,0);
      acc[1][0] = __builtin_amdgcn_mfma_f32_16x16x32_bf16(A[1][1], Flo[q], acc[1][0],0,0,0);
      acc[1][1] = __builtin_amdgcn_mfma_f32_16x16x32_bf16(A[1][1], Fm, acc[1][1],0,0,0);
      Flo[q] = Fh;
    }
    #pragma unroll
    for (int ch=0;ch<2;ch++){
      uint4 t0 = __builtin_bit_cast(uint4, A[0][ch]), t1 = __builtin_bit_cast(uint4, A[1][ch]);
      uint32_t* p0 = (uint32_t*)&t0; uint32_t* p1 = (uint32_t*)&t1;
      #pragma unroll
      for (int i=0;i<4;i++){
        int ror = __builtin_amdgcn_update_dpp(0, (int)p0[i], 0x121, 0xf, 0xf, false);
        p1[i] = __builtin_amdgcn_update_dpp(ror, (int)p1[i], 0x111, 0xf, 0xf, false);
        p0[i] = __builtin_amdgcn_update_dpp(0, (int)p0[i], 0x111, 0xf, 0xf, true);
      }
      A[0][ch] = __builtin_bit_cast(bf16x8, t0); A[1][ch] = __builtin_bit_cast(bf16x8, t1);
    }
  }
  __syncthreads();
  u16* so = (u16*)smem;
  #pragma unroll
  for (int mt=0;mt<2;mt++)
    #pragma unroll
    for (int nt=0;nt<2;nt++)
      #pragma unroll
      for (int i=0;i<4;i++){
        int l = 64*(16*mt + 4*kg + i) + 16*h + 32*nt + m;
        so[l] = f2bf(acc[mt][nt][i]);
      }
  __syncthreads();
  for (int i=t;i<256;i+=128) *(uint4*)(yT + d*L_ + 8*i) = *(const uint4*)(so + 8*i);
}

__global__ __launch_bounds__(256) void post_kernel(const u16* __restrict__ yT, const u16* __restrict__ u,
    const u16* __restrict__ Dskip, const u16* __restrict__ q, const u16* __restrict__ k, const u16* __restrict__ v,
    const u16* __restrict__ shift_k, const u16* __restrict__ ssmD, u16* __restrict__ out){
  __shared__ u16 tile[64][64+8];
  int l0 = blockIdx.x*64, d0 = blockIdx.y*64, part = blockIdx.z;
  int t = threadIdx.x;
  int c8 = (t & 7)*8, r = t >> 3;
  for (int dd = r; dd < 64; dd += 32)
    *(uint4*)&tile[dd][c8] = *(const uint4*)(yT + (part*D_ + d0+dd)*L_ + l0 + c8);
  __syncthreads();
  for (int rr = r; rr < 64; rr += 32){
    int l = l0 + rr;
    u16 o[8];
    if (part == 0){
      u16 uu[8]; *(uint4*)uu = *(const uint4*)(u + l*D_ + d0 + c8);
      #pragma unroll
      for (int i=0;i<8;i++) o[i] = f2bf(bf2f(tile[c8+i][rr]) + rbf(bf2f(uu[i])*bf2f(Dskip[d0+c8+i])));
    } else {
      u16 kk[4][8], vv[8], qq[8];
      #pragma unroll
      for (int j=0;j<4;j++){
        if (l-j >= 0) *(uint4*)kk[j] = *(const uint4*)(k + (l-j)*D_ + d0 + c8);
        else *(uint4*)kk[j] = make_uint4(0,0,0,0);
      }
      *(uint4*)vv = *(const uint4*)(v + l*D_ + d0 + c8);
      *(uint4*)qq = *(const uint4*)(q + l*D_ + d0 + c8);
      #pragma unroll
      for (int i=0;i<8;i++){
        const u16* w = shift_k + (d0+c8+i)*4;
        float acc = rbf(bf2f(kk[3][i])*bf2f(w[3]));
        acc = rbf(acc + rbf(bf2f(kk[2][i])*bf2f(w[2])));
        acc = rbf(acc + rbf(bf2f(kk[1][i])*bf2f(w[1])));
        acc = rbf(acc + rbf(bf2f(kk[0][i])*bf2f(w[0])));
        float kv = rbf(acc*bf2f(vv[i]));
        float y = rbf(bf2f(tile[c8+i][rr]) + rbf(kv*bf2f(ssmD[d0+c8+i])));
        o[i] = f2bf(bf2f(qq[i])*y);
      }
    }
    *(uint4*)(out + l*(2*D_) + part*D_ + d0 + c8) = *(const uint4*)o;
  }
}

extern "C" void kernel_launch(void* const* d_in, const int* in_sizes, int n_in,
                              void* d_out, int out_size, void* d_ws, size_t ws_size,
                              hipStream_t stream) {
  const u16* u = (const u16*)d_in[0];
  const u16* logA = (const u16*)d_in[1];
  const u16* Aim = (const u16*)d_in[2];
  const u16* Cre = (const u16*)d_in[3];
  const u16* Cim = (const u16*)d_in[4];
  const u16* logdt = (const u16*)d_in[5];
  const u16* Dskip = (const u16*)d_in[6];
  const u16* q = (const u16*)d_in[7];
  const u16* k = (const u16*)d_in[8];
  const u16* v = (const u16*)d_in[9];
  const u16* shift_k = (const u16*)d_in[10];
  const u16* ssm_k = (const u16*)d_in[11];
  const u16* ssmD = (const u16*)d_in[12];
  u16* out = (u16*)d_out;
  u16* ws = (u16*)d_ws;
  const size_t S = (size_t)L_*D_;
  u16* uT = ws; u16* kvT = ws + S; u16* Khi = ws + 2*S; u16* Klo = ws + 3*S; u16* yT = ws + 4*S;
  kgen_simple<<<D_, 256, 0, stream>>>(logA, Aim, Cre, Cim, logdt, Khi, Klo);
  pre_kernel<<<dim3(32,8,2), 256, 0, stream>>>(u, k, v, shift_k, uT, kvT);
  conv_kernel<2><<<D_, 128, 8*CSTRIDE, stream>>>(uT, Khi, Klo, yT);
  conv_kernel<1><<<D_, 128, 4*CSTRIDE, stream>>>(kvT, ssm_k, nullptr, yT + S);
  post_kernel<<<dim3(32,8,2), 256, 0, stream>>>(yT, u, Dskip, q, k, v, shift_k, ssmD, out);
}
```

```cpp
#include <hip/hip_runtime.h>
#include <hip/hip_bf16.h>
#include <stdint.h>

typedef unsigned short u16;
typedef short bf16x8 __attribute__((ext_vector_type(8)));
typedef float f32x4 __attribute__((ext_vector_type(4)));
typedef float f32x2 __attribute__((ext_vector_type(2)));
typedef __bf16 bf16x2_t __attribute__((ext_vector_type(2)));

#define L_ 2048
#define D_ 512
#define NS_ 128

__device__ __forceinline__ float bf2f(u16 x){ return __builtin_bit_cast(float, (uint32_t)x << 16); }
__device__ __forceinline__ u16 f2bf(float f){ __hip_bfloat16 h = __float2bfloat16(f); return __builtin_bit_cast(u16, h); }
__device__ __forceinline__ float rbf(float f){ return bf2f(f2bf(f)); }
__device__ __forceinline__ uint32_t pk2(float a, float b){
  f32x2 v = {a,b}; bf16x2_t r = __builtin_convertvector(v, bf16x2_t); return __builtin_bit_cast(uint32_t, r); }
__device__ __forceinline__ float lo16f(uint32_t w){ return __builtin_bit_cast(float, w << 16); }
__device__ __forceinline__ float hi16f(uint32_t w){ return __builtin_bit_cast(float, w & 0xffff0000u); }

struct cpx { float re, im; };
__device__ __forceinline__ cpx cmul(cpx a, cpx b){ cpx r; r.re = a.re*b.re - a.im*b.im; r.im = a.re*b.im + a.im*b.re; return r; }
__device__ __forceinline__ cpx cexp_(float re, float im){ float e = expf(re); float s,c; sincosf(im,&s,&c); cpx r; r.re=e*c; r.im=e*s; return r; }

#define TROW 272
__device__ __forceinline__ void put_split(char* hi, char* lo, float a, float b){
  uint32_t h = pk2(a,b);
  uint32_t l = pk2(a - lo16f(h), b - hi16f(h));
  *(uint32_t*)hi = h; *(uint32_t*)lo = l;
}

__global__ __launch_bounds__(256) void pre_kernel(const u16* __restrict__ u, const u16* __restrict__ k,
    const u16* __restrict__ v, const u16* __restrict__ shift_k, u16* __restrict__ uT, u16* __restrict__ kvT,
    const u16* __restrict__ logA, const u16* __restrict__ Aim,
    const u16* __restrict__ Cre, const u16* __restrict__ Cim, const u16* __restrict__ logdt,
    u16* __restrict__ Khi, u16* __restrict__ Klo){
  __shared__ __attribute__((aligned(16))) char smem[4*4*16*TROW + 2*656*4];
  int t = threadIdx.x;
  if (blockIdx.x < 256){
    int wave = t >> 6, lane = t & 63;
    int d = blockIdx.x*2 + (wave >> 1);
    int n = (wave & 1)*64 + lane;
    char* tb = smem + wave*(4*16*TROW);
    char* GAh = tb, *GAl = tb + 16*TROW, *XBh = tb + 32*TROW, *XBl = tb + 48*TROW;
    float* sK = (float*)(smem + 4*4*16*TROW) + (wave>>1)*656;
    float dt = rbf(expf(bf2f(logdt[d])));
    float zre = -rbf(expf(bf2f(logA[d*NS_+n]))) * dt;
    float zim = bf2f(Aim[d*NS_+n]) * dt;
    cpx C; C.re = bf2f(Cre[d*NS_+n])*dt; C.im = bf2f(Cim[d*NS_+n])*dt;
    cpx X0[16], Y[16], CZ[9];
    {
      cpx e1 = cexp_(zre, zim), e16 = cexp_(zre*16.f, zim*16.f), e256 = cexp_(zre*256.f, zim*256.f);
      X0[0].re = 1.f; X0[0].im = 0.f; X0[1] = e1; Y[0] = X0[0]; Y[1] = e16;
      #pragma unroll
      for (int i=2;i<16;i++){ X0[i] = cmul(X0[i-1], e1); Y[i] = cmul(Y[i-1], e16); }
      CZ[0] = C;
      #pragma unroll
      for (int i=1;i<9;i++) CZ[i] = cmul(CZ[i-1], e256);
    }
    int m = lane & 15, kg = lane >> 4;
    #pragma unroll
    for (int r=9;r<16;r++){ *(uint32_t*)(GAh + r*TROW + 4*lane) = 0; *(uint32_t*)(GAl + r*TROW + 4*lane) = 0; }
    f32x4 acc[4];
    #pragma unroll
    for (int s=0;s<4;s++){
      #pragma unroll
      for (int b=0;b<16;b++){
        int e = b << s; cpx x;
        if ((e >> 4) == 0) x = X0[e & 15]; else if ((e & 15) == 0) x = Y[e >> 4]; else x = cmul(Y[e>>4], X0[e&15]);
        put_split(XBh + b*TROW + 4*lane, XBl + b*TROW + 4*lane, x.re, -x.im);
      }
      if (s == 0){
        #pragma unroll
        for (int a=0;a<16;a++){ cpx g = (a==0)? C : cmul(C, Y[a]); put_split(GAh + a*TROW + 4*lane, GAl + a*TROW + 4*lane, g.re, g.im); }
      } else {
        #pragma unroll
        for (int a=0;a<9;a++){
          int e = (256 << (s-1)) + ((16*a) << s);
          int hh = e >> 8, yy = (e & 255) >> 4;
          cpx g = (yy==0)? CZ[hh] : cmul(CZ[hh], Y[yy]);
          put_split(GAh + a*TROW + 4*lane, GAl + a*TROW + 4*lane, g.re, g.im);
        }
        if (s == 1){
          #pragma unroll
          for (int r=9;r<16;r++){ *(uint32_t*)(GAh + r*TROW + 4*lane) = 0; *(uint32_t*)(GAl + r*TROW + 4*lane) = 0; }
        }
      }
      __builtin_amdgcn_wave_barrier();
      f32x4 a4 = {0,0,0,0};
      #pragma unroll
      for (int kc=0;kc<4;kc++){
        int off = m*TROW + (16*kc + 4*kg)*4;
        bf16x8 ah = *(const bf16x8*)(GAh + off), al = *(const bf16x8*)(GAl + off);
        bf16x8 bh = *(const bf16x8*)(XBh + off), bl = *(const bf16x8*)(XBl + off);
        a4 = __builtin_amdgcn_mfma_f32_16x16x32_bf16(ah, bh, a4, 0,0,0);
        a4 = __builtin_amdgcn_mfma_f32_16x16x32_bf16(ah, bl, a4, 0,0,0);
        a4 = __builtin_amdgcn_mfma_f32_16x16x32_bf16(al, bh, a4, 0,0,0);
      }
      acc[s] = a4;
      __builtin_amdgcn_wave_barrier();
    }
    if (wave & 1){
      #pragma unroll
      for (int s=0;s<4;s++)
        #pragma unroll
        for (int i=0;i<4;i++){
          int row = 4*kg + i;
          int p = (s==0) ? 16*row + m : 256 + 128*(s-1) + 16*row + m;
          if (s == 0 || row < 8 || (s==3 && row == 8 && m == 0)) sK[p] = acc[s][i];
        }
    }
    __syncthreads();
    if (!(wave & 1)){
      #pragma unroll
      for (int s=0;s<4;s++)
        #pragma unroll
        for (int i=0;i<4;i++){
          int row = 4*kg + i;
          int p = (s==0) ? 16*row + m : 256 + 128*(s-1) + 16*row + m;
          if (s == 0 || row < 8 || (s==3 && row == 8 && m == 0)) sK[p] = 2.f*(sK[p] + acc[s][i]);
        }
    }
    __syncthreads();
    {
      int tt = t & 127;
      #pragma unroll
      for (int rep=0;rep<2;rep++){
        int l0 = rep*1024 + tt*8;
        uint32_t wh[4], wl[4];
        #pragma unroll
        for (int j=0;j<4;j++){
          float kv2[2];
          #pragma unroll
          for (int e=0;e<2;e++){
            int l = l0 + 2*j + e;
            int pr = (int)rbf((float)l);
            int p;
            if (pr < 256) p = pr; else if (pr < 512) p = 128 + (pr>>1); else if (pr < 1024) p = 256 + (pr>>2);
            else p = 384 + (pr>>3);
            kv2[e] = sK[p];
          }
          uint32_t h = pk2(kv2[0], kv2[1]);
          wh[j] = h; wl[j] = pk2(kv2[0]-lo16f(h), kv2[1]-hi16f(h));
        }
        *(uint4*)(Khi + d*L_ + l0) = make_uint4(wh[0],wh[1],wh[2],wh[3]);
        *(uint4*)(Klo + d*L_ + l0) = make_uint4(wl[0],wl[1],wl[2],wl[3]);
      }
    }
  } else {
    int b = blockIdx.x - 256;
    int which = b >> 7; b &= 127;
    int l0 = (b >> 3)*128, d0 = (b & 7)*64;
    const int TS = 272;
    int c8 = (t & 7)*8, rg = t >> 3;
    uint32_t o[4][4];
    if (which == 0){
      #pragma unroll
      for (int j=0;j<4;j++){ uint4 x = *(const uint4*)(u + (l0+4*rg+j)*D_ + d0 + c8); o[j][0]=x.x;o[j][1]=x.y;o[j][2]=x.z;o[j][3]=x.w; }
    } else {
      uint4 kk[7], vv[4];
      #pragma unroll
      for (int j=0;j<7;j++){ int l = l0+4*rg+j-3; kk[j] = (l>=0) ? *(const uint4*)(k + l*D_ + d0 + c8) : make_uint4(0,0,0,0); }
      #pragma unroll
      for (int j=0;j<4;j++) vv[j] = *(const uint4*)(v + (l0+4*rg+j)*D_ + d0 + c8);
      uint4 wq[4];
      #pragma unroll
      for (int j=0;j<4;j++) wq[j] = *(const uint4*)(shift_k + (d0+c8)*4 + 8*j);
      #pragma unroll
      for (int p=0;p<4;p++){
        const uint32_t* wp = (const uint32_t*)&wq[p];
        #pragma unroll
        for (int j=0;j<4;j++){
          float r2[2];
          #pragma unroll
          for (int e=0;e<2;e++){
            float w0 = e? lo16f(wp[2]) : lo16f(wp[0]); float w1 = e? hi16f(wp[2]) : hi16f(wp[0]);
            float w2 = e? lo16f(wp[3]) : lo16f(wp[1]); float w3 = e? hi16f(wp[3]) : hi16f(wp[1]);
            auto g = [&](int idx){ uint32_t w = ((const uint32_t*)&kk[idx])[p]; return e? hi16f(w) : lo16f(w); };
            float acc = rbf(g(j)*w3);
            acc = rbf(acc + rbf(g(j+1)*w2));
            acc = rbf(acc + rbf(g(j+2)*w1));
            acc = rbf(acc + rbf(g(j+3)*w0));
            uint32_t vw = ((const uint32_t*)&vv[j])[p];
            r2[e] = acc * (e? hi16f(vw) : lo16f(vw));
          }
          o[j][p] = pk2(r2[0], r2[1]);
        }
      }
    }
    #pragma unroll
    for (int p=0;p<4;p++){
      uint32_t e0 = __builtin_amdgcn_perm(o[1][p], o[0][p], 0x05040100);
      uint32_t e1 = __builtin_amdgcn_perm(o[3][p], o[2][p], 0x05040100);
      uint32_t f0 = __builtin_amdgcn_perm(o[1][p], o[0][p], 0x07060302);
      uint32_t f1 = __builtin_amdgcn_perm(o[3][p], o[2][p], 0x07060302);
      *(uint2*)(smem + (c8+2*p)*TS + rg*8) = make_uint2(e0,e1);
      *(uint2*)(smem + (c8+2*p+1)*TS + rg*8) = make_uint2(f0,f1);
    }
    __syncthreads();
    u16* dst = which == 0 ? uT : kvT;
    int lc = (t & 15)*8, dr = t >> 4;
    #pragma unroll
    for (int i=0;i<4;i++){
      int dd = dr + 16*i;
      uint4 x = *(const uint4*)(smem + dd*TS + lc*2);
      *(uint4*)(dst + (d0+dd)*L_ + l0 + lc) = x;
    }
  }
}

#define CSTRIDE 4416
template<int NK>
__global__ __launch_bounds__(128) void conv_kernel(const u16* __restrict__ xT, const u16* __restrict__ K0,
    const u16* __restrict__ K1, u16* __restrict__ yT){
  extern __shared__ __attribute__((aligned(16))) char smem[];
  int d = blockIdx.x;
  int t = threadIdx.x;
  #pragma unroll
  for (int q=0;q<NK;q++){
    const u16* Ksrc = (q==0?K0:K1) + d*L_;
    char* base = smem + q*4*CSTRIDE;
    for (int ch = t; ch < 264+2; ch += 128){
      uint32_t w[5];
      if (ch < 256){
        uint4 x = *(const uint4*)(Ksrc + 2040 - 8*ch);
        w[0] = __builtin_amdgcn_alignbit(x.w, x.w, 16);
        w[1] = __builtin_amdgcn_alignbit(x.z, x.z, 16);
        w[2] = __builtin_amdgcn_alignbit(x.y, x.y, 16);
        w[3] = __builtin_amdgcn_alignbit(x.x, x.x, 16);
        if (ch < 255){ uint32_t y = *(const uint32_t*)(Ksrc + 2038 - 8*ch); w[4] = __builtin_amdgcn_alignbit(y,y,16);} else w[4] = 0;
      } else { w[0]=w[1]=w[2]=w[3]=w[4]=0; }
      uint32_t a[4];
      #pragma unroll
      for (int i=0;i<4;i++) a[i] = __builtin_amdgcn_alignbit(w[i+1], w[i], 16);
      uint32_t w5;
      if (ch < 255){ uint32_t y = *(const uint32_t*)(Ksrc + 2036 - 8*ch); w5 = __builtin_amdgcn_alignbit(y,y,16);} else w5 = 0;
      uint32_t a4 = __builtin_amdgcn_alignbit(w5, w[4], 16);
      *(uint4*)(base + 0*CSTRIDE + 16*ch) = make_uint4(w[0],w[1],w[2],w[3]);
      *(uint4*)(base + 1*CSTRIDE + 16*ch) = make_uint4(a[0],a[1],a[2],a[3]);
      *(uint4*)(base + 2*CSTRIDE + 16*ch) = make_uint4(w[1],w[2],w[3],w[4]);
      *(uint4*)(base + 3*CSTRIDE + 16*ch) = make_uint4(a[1],a[2],a[3],a4);
    }
  }
  int lane = t & 63, h = t >> 6;
  int m = lane & 15, kg = lane >> 4;
  bf16x8 A[2][2];
  #pragma unroll
  for (int mt=0;mt<2;mt++)
    #pragma unroll
    for (int ch=0;ch<2;ch++)
      A[mt][ch] = *(const bf16x8*)(xT + d*L_ + 64*(16*mt+m) + 32*ch + 8*kg);
  __syncthreads();
  f32x4 acc[2][2];
  #pragma unroll
  for (int i=0;i<2;i++) for (int j=0;j<2;j++) acc[i][j] = f32x4{0,0,0,0};
  int s0 = 2047 - 16*h - m + 8*kg;
  int r = s0 & 3;
  const char* fb = smem + r*CSTRIDE + 2*(s0 - r);
  auto ldfrag = [&](int q, int o)->bf16x8 {
    const char* p = fb + q*4*CSTRIDE - 2*o;
    uint2 x = *(const uint2*)p; uint2 y = *(const uint2*)(p+8);
    uint4 z = make_uint4(x.x,x.y,y.x,y.y);
    return __builtin_bit_cast(bf16x8, z);
  };
  bf16x8 Flo[NK];
  #pragma unroll
  for (int q=0;q<NK;q++) Flo[q] = ldfrag(q, -32);
  for (int c=0;c<32;c++){
    #pragma unroll
    for (int q=0;q<NK;q++){
      bf16x8 Fm = ldfrag(q, 64*c), Fh = ldfrag(q, 64*c+32);
      if (c < 16){
        acc[0][0] = __builtin_amdgcn_mfma_f32_16x16x32_bf16(A[0][0], Fm, acc[0][0],0,0,0);
        acc[0][1] = __builtin_amdgcn_mfma_f32_16x16x32_bf16(A[0][0], Fh, acc[0][1],0,0,0);
        acc[0][0] = __builtin_amdgcn_mfma_f32_16x16x32_bf16(A[0][1], Flo[q], acc[0][0],0,0,0);
        acc[0][1] = __builtin_amdgcn_mfma_f32_16x16x32_bf16(A[0][1], Fm, acc[0][1],0,0,0);
      }
      acc[1][0] = __builtin_amdgcn_mfma_f32_16x16x32_bf16(A[1][0], Fm, acc[1][0],0,0,0);
      acc[1][1] = __builtin_amdgcn_mfma_f32_16x16x32_bf16(A[1][0], Fh, acc[1][1],0,0,0);
      acc[1][0] = __builtin_amdgcn_mfma_f32_16x16x32_bf16(A[1][1], Flo[q], acc[1][0],0,0,0);
      acc[1][1] = __builtin_amdgcn_mfma_f32_16x16x32_bf16(A[1][1], Fm, acc[1][1],0,0,0);
      Flo[q] = Fh;
    }
    #pragma unroll
    for (int ch=0;ch<2;ch++){
      uint4 t0 = __builtin_bit_cast(uint4, A[0][ch]), t1 = __builtin_bit_cast(uint4, A[1][ch]);
      uint32_t* p0 = (uint32_t*)&t0; uint32_t* p1 = (uint32_t*)&t1;
      #pragma unroll
      for (int i=0;i<4;i++){
        int ror = __builtin_amdgcn_update_dpp(0, (int)p0[i], 0x121, 0xf, 0xf, false);
        p1[i] = __builtin_amdgcn_update_dpp(ror, (int)p1[i], 0x111, 0xf, 0xf, false);
        p0[i] = __builtin_amdgcn_update_dpp(0, (int)p0[i], 0x111, 0xf, 0xf, true);
      }
      A[0][ch] = __builtin_bit_cast(bf16x8, t0); A[1][ch] = __builtin_bit_cast(bf16x8, t1);
    }
  }
  __syncthreads();
  u16* so = (u16*)smem;
  #pragma unroll
  for (int mt=0;mt<2;mt++)
    #pragma unroll
    for (int nt=0;nt<2;nt++)
      #pragma unroll
      for (int i=0;i<4;i++){
        int l = 64*(16*mt + 4*kg + i) + 16*h + 32*nt + m;
        so[l] = f2bf(acc[mt][nt][i]);
      }
  __syncthreads();
  for (int i=t;i<256;i+=128) *(uint4*)(yT + d*L_ + 8*i) = *(const uint4*)(so + 8*i);
}

__global__ __launch_bounds__(256) void post_kernel(const u16* __restrict__ yT, const u16* __restrict__ u,
    const u16* __restrict__ Dskip, const u16* __restrict__ q, const u16* __restrict__ k, const u16* __restrict__ v,
    const u16* __restrict__ shift_k, const u16* __restrict__ ssmD, u16* __restrict__ out){
  __shared__ u16 tile[64][64+8];
  int l0 = blockIdx.x*64, d0 = blockIdx.y*64, part = blockIdx.z;
  int t = threadIdx.x;
  int c8 = (t & 7)*8, r = t >> 3;
  for (int dd = r; dd < 64; dd += 32)
    *(uint4*)&tile[dd][c8] = *(const uint4*)(yT + (part*D_ + d0+dd)*L_ + l0 + c8);
  __syncthreads();
  for (int rr = r; rr < 64; rr += 32){
    int l = l0 + rr;
    u16 o[8];
    if (part == 0){
      u16 uu[8]; *(uint4*)uu = *(const uint4*)(u + l*D_ + d0 + c8);
      #pragma unroll
      for (int i=0;i<8;i++) o[i] = f2bf(bf2f(tile[c8+i][rr]) + rbf(bf2f(uu[i])*bf2f(Dskip[d0+c8+i])));
    } else {
      u16 kk[4][8], vv[8], qq[8];
      #pragma unroll
      for (int j=0;j<4;j++){
        if (l-j >= 0) *(uint4*)kk[j] = *(const uint4*)(k + (l-j)*D_ + d0 + c8);
        else *(uint4*)kk[j] = make_uint4(0,0,0,0);
      }
      *(uint4*)vv = *(const uint4*)(v + l*D_ + d0 + c8);
      *(uint4*)qq = *(const uint4*)(q + l*D_ + d0 + c8);
      #pragma unroll
      for (int i=0;i<8;i++){
        const u16* w = shift_k + (d0+c8+i)*4;
        float acc = rbf(bf2f(kk[3][i])*bf2f(w[3]));
        acc = rbf(acc + rbf(bf2f(kk[2][i])*bf2f(w[2])));
        acc = rbf(acc + rbf(bf2f(kk[1][i])*bf2f(w[1])));
        acc = rbf(acc + rbf(bf2f(kk[0][i])*bf2f(w[0])));
        float kv = rbf(acc*bf2f(vv[i]));
        float y = rbf(bf2f(tile[c8+i][rr]) + rbf(kv*bf2f(ssmD[d0+c8+i])));
        o[i] = f2bf(bf2f(qq[i])*y);
      }
    }
    *(uint4*)(out + l*(2*D_) + part*D_ + d0 + c8) = *(const uint4*)o;
  }
}

extern "C" void kernel_launch(void* const* d_in, const int* in_sizes, int n_in,
                              void* d_out, int out_size, void* d_ws, size_t ws_size,
                              hipStream_t stream) {
  const u16* u = (const u16*)d_in[0];
  const u16* logA = (const u16*)d_in[1];
  const u16* Aim = (const u16*)d_in[2];
  const u16* Cre = (const u16*)d_in[3];
  const u16* Cim = (const u16*)d_in[4];
  const u16* logdt = (const u16*)d_in[5];
  const u16* Dskip = (const u16*)d_in[6];
  const u16* q = (const u16*)d_in[7];
  const u16* k = (const u16*)d_in[8];
  const u16* v = (const u16*)d_in[9];
  const u16* shift_k = (const u16*)d_in[10];
  const u16* ssm_k = (const u16*)d_in[11];
  const u16* ssmD = (const u16*)d_in[12];
  u16* out = (u16*)d_out;
  u16* ws = (u16*)d_ws;
  const size_t S = (size_t)L_*D_;
  u16* uT = ws; u16* kvT = ws + S; u16* Khi = ws + 2*S; u16* Klo = ws + 3*S; u16* yT = ws + 4*S;
  pre_kernel<<<512, 256, 0, stream>>>(u, k, v, shift_k, uT, kvT, logA, Aim, Cre, Cim, logdt, Khi, Klo);
  conv_kernel<2><<<D_, 128, 8*CSTRIDE, stream>>>(uT, Khi, Klo, yT);
  conv_kernel<1><<<D_, 128, 4*CSTRIDE, stream>>>(kvT, ssm_k, nullptr, yT + S);
  post_kernel<<<dim3(32,8,2), 256, 0, stream>>>(yT, u, Dskip, q, k, v, shift_k, ssmD, out);
}
```

```cpp
#include <hip/hip_runtime.h>
#include <hip/hip_bf16.h>
#include <stdint.h>

typedef unsigned short u16;
typedef short bf16x8 __attribute__((ext_vector_type(8)));
typedef float f32x4 __attribute__((ext_vector_type(4)));
typedef float f32x2 __attribute__((ext_vector_type(2)));
typedef __bf16 bf16x2_t __attribute__((ext_vector_type(2)));

#define L_ 2048
#define D_ 512
#define NS_ 128

__device__ __forceinline__ float bf2f(u16 x){ return __builtin_bit_cast(float, (uint32_t)x << 16); }
__device__ __forceinline__ u16 f2bf(float f){ __hip_bfloat16 h = __float2bfloat16(f); return __builtin_bit_cast(u16, h); }
__device__ __forceinline__ float rbf(float f){ return bf2f(f2bf(f)); }
__device__ __forceinline__ uint32_t pk2(float a, float b){
  f32x2 v = {a,b}; bf16x2_t r = __builtin_convertvector(v, bf16x2_t); return __builtin_bit_cast(uint32_t, r); }
__device__ __forceinline__ float lo16f(uint32_t w){ return __builtin_bit_cast(float, w << 16); }
__device__ __forceinline__ float hi16f(uint32_t w){ return __builtin_bit_cast(float, w & 0xffff0000u); }

struct cpx { float re, im; };
__device__ __forceinline__ cpx cmul(cpx a, cpx b){ cpx r; r.re = a.re*b.re - a.im*b.im; r.im = a.re*b.im + a.im*b.re; return r; }
__device__ __forceinline__ cpx cexp_(float re, float im){ float e = expf(re); float s,c; sincosf(im,&s,&c); cpx r; r.re=e*c; r.im=e*s; return r; }

#define TROW 272
__device__ __forceinline__ void put_split(char* hi, char* lo, float a, float b){
  uint32_t h = pk2(a,b);
  uint32_t l = pk2(a - lo16f(h), b - hi16f(h));
  *(uint32_t*)hi = h; *(uint32_t*)lo = l;
}

__global__ __launch_bounds__(256) void pre_kernel(const u16* __restrict__ u, const u16* __restrict__ k,
    const u16* __restrict__ v, const u16* __restrict__ shift_k, u16* __restrict__ uT, u16* __restrict__ kvT,
    const u16* __restrict__ logA, const u16* __restrict__ Aim,
    const u16* __restrict__ Cre, const u16* __restrict__ Cim, const u16* __restrict__ logdt,
    u16* __restrict__ Khi, u16* __restrict__ Klo){
  __shared__ __attribute__((aligned(16))) char smem[4*4*16*TROW + 2*656*4];
  int t = threadIdx.x;
  if (blockIdx.x < 256){
    int wave = t >> 6, lane = t & 63;
    int d = blockIdx.x*2 + (wave >> 1);
    int n = (wave & 1)*64 + lane;
    char* tb = smem + wave*(4*16*TROW);
    char* GAh = tb, *GAl = tb + 16*TROW, *XBh = tb + 32*TROW, *XBl = tb + 48*TROW;
    float* sK = (float*)(smem + 4*4*16*TROW) + (wave>>1)*656;
    float dt = rbf(expf(bf2f(logdt[d])));
    float zre = -rbf(expf(bf2f(logA[d*NS_+n]))) * dt;
    float zim = bf2f(Aim[d*NS_+n]) * dt;
    cpx C; C.re = bf2f(Cre[d*NS_+n])*dt; C.im = bf2f(Cim[d*NS_+n])*dt;
    cpx X0[16], Y[16], CZ[9];
    {
      cpx e1 = cexp_(zre, zim), e16 = cexp_(zre*16.f, zim*16.f), e256 = cexp_(zre*256.f, zim*256.f);
      X0[0].re = 1.f; X0[0].im = 0.f; X0[1] = e1; Y[0] = X0[0]; Y[1] = e16;
      #pragma unroll
      for (int i=2;i<16;i++){ X0[i] = cmul(X0[i-1], e1); Y[i] = cmul(Y[i-1], e16); }
      CZ[0] = C;
      #pragma unroll
      for (int i=1;i<9;i++) CZ[i] = cmul(CZ[i-1], e256);
    }
    int m = lane & 15, kg = lane >> 4;
    #pragma unroll
    for (int r=9;r<16;r++){ *(uint32_t*)(GAh + r*TROW + 4*lane) = 0; *(uint32_t*)(GAl + r*TROW + 4*lane) = 0; }
    f32x4 acc[4];
    #pragma unroll
    for (int s=0;s<4;s++){
      #pragma unroll
      for (int b=0;b<16;b++){
        int e = b << s; cpx x;
        if ((e >> 4) == 0) x = X0[e & 15]; else if ((e & 15) == 0) x = Y[e >> 4]; else x = cmul(Y[e>>4], X0[e&15]);
        put_split(XBh + b*TROW + 4*lane, XBl + b*TROW + 4*lane, x.re, -x.im);
      }
      if (s == 0){
        #pragma unroll
        for (int a=0;a<16;a++){ cpx g = (a==0)? C : cmul(C, Y[a]); put_split(GAh + a*TROW + 4*lane, GAl + a*TROW + 4*lane, g.re, g.im); }
      } else {
        #pragma unroll
        for (int a=0;a<9;a++){
          int e = (256 << (s-1)) + ((16*a) << s);
          int hh = e >> 8, yy = (e & 255) >> 4;
          cpx g = (yy==0)? CZ[hh] : cmul(CZ[hh], Y[yy]);
          put_split(GAh + a*TROW + 4*lane, GAl + a*TROW + 4*lane, g.re, g.im);
        }
        if (s == 1){
          #pragma unroll
          for (int r=9;r<16;r++){ *(uint32_t*)(GAh + r*TROW + 4*lane) = 0; *(uint32_t*)(GAl + r*TROW + 4*lane) = 0; }
        }
      }
      __builtin_amdgcn_wave_barrier();
      f32x4 a4 = {0,0,0,0};
      #pragma unroll
      for (int kc=0;kc<4;kc++){
        int off = m*TROW + (16*kc + 4*kg)*4;
        bf16x8 ah = *(const bf16x8*)(GAh + off), al = *(const bf16x8*)(GAl + off);
        bf16x8 bh = *(const bf16x8*)(XBh + off), bl = *(const bf16x8*)(XBl + off);
        a4 = __builtin_amdgcn_mfma_f32_16x16x32_bf16(ah, bh, a4, 0,0,0);
        a4 = __builtin_amdgcn_mfma_f32_16x16x32_bf16(ah, bl, a4, 0,0,0);
        a4 = __builtin_amdgcn_mfma_f32_16x16x32_bf16(al, bh, a4, 0,0,0);
      }
      acc[s] = a4;
      __builtin_amdgcn_wave_barrier();
    }
    if (wave & 1){
      #pragma unroll
      for (int s=0;s<4;s++)
        #pragma unroll
        for (int i=0;i<4;i++){
          int row = 4*kg + i;
          int p = (s==0) ? 16*row + m : 256 + 128*(s-1) + 16*row + m;
          if (s == 0 || row < 8 || (s==3 && row == 8 && m == 0)) sK[p] = acc[s][i];
        }
    }
    __syncthreads();
    if (!(wave & 1)){
      #pragma unroll
      for (int s=0;s<4;s++)
        #pragma unroll
        for (int i=0;i<4;i++){
          int row = 4*kg + i;
          int p = (s==0) ? 16*row + m : 256 + 128*(s-1) + 16*row + m;
          if (s == 0 || row < 8 || (s==3 && row == 8 && m == 0)) sK[p] = 2.f*(sK[p] + acc[s][i]);
        }
    }
    __syncthreads();
    {
      int tt = t & 127;
      #pragma unroll
      for (int rep=0;rep<2;rep++){
        int l0 = rep*1024 + tt*8;
        uint32_t wh[4], wl[4];
        #pragma unroll
        for (int j=0;j<4;j++){
          float kv2[2];
          #pragma unroll
          for (int e=0;e<2;e++){
            int l = l0 + 2*j + e;
            int pr = (int)rbf((float)l);
            int p;
            if (pr < 256) p = pr; else if (pr < 512) p = 128 + (pr>>1); else if (pr < 1024) p = 256 + (pr>>2);
            else p = 384 + (pr>>3);
            kv2[e] = sK[p];
          }
          uint32_t h = pk2(kv2[0], kv2[1]);
          wh[j] = h; wl[j] = pk2(kv2[0]-lo16f(h), kv2[1]-hi16f(h));
        }
        *(uint4*)(Khi + d*L_ + l0) = make_uint4(wh[0],wh[1],wh[2],wh[3]);
        *(uint4*)(Klo + d*L_ + l0) = make_uint4(wl[0],wl[1],wl[2],wl[3]);
      }
    }
  } else {
    int b = blockIdx.x - 256;
    int which = b >> 7; b &= 127;
    int l0 = (b >> 3)*128, d0 = (b & 7)*64;
    const int TS = 272;
    int c8 = (t & 7)*8, rg = t >> 3;
    uint32_t o[4][4];
    if (which == 0){
      #pragma unroll
      for (int j=0;j<4;j++){ uint4 x = *(const uint4*)(u + (l0+4*rg+j)*D_ + d0 + c8); o[j][0]=x.x;o[j][1]=x.y;o[j][2]=x.z;o[j][3]=x.w; }
    } else {
      uint4 kk[7], vv[4];
      #pragma unroll
      for (int j=0;j<7;j++){ int l = l0+4*rg+j-3; kk[j] = (l>=0) ? *(const uint4*)(k + l*D_ + d0 + c8) : make_uint4(0,0,0,0); }
      #pragma unroll
      for (int j=0;j<4;j++) vv[j] = *(const uint4*)(v + (l0+4*rg+j)*D_ + d0 + c8);
      uint4 wq[4];
      #pragma unroll
      for (int j=0;j<4;j++) wq[j] = *(const uint4*)(shift_k + (d0+c8)*4 + 8*j);
      #pragma unroll
      for (int p=0;p<4;p++){
        const uint32_t* wp = (const uint32_t*)&wq[p];
        #pragma unroll
        for (int j=0;j<4;j++){
          float r2[2];
          #pragma unroll
          for (int e=0;e<2;e++){
            float w0 = e? lo16f(wp[2]) : lo16f(wp[0]); float w1 = e? hi16f(wp[2]) : hi16f(wp[0]);
            float w2 = e? lo16f(wp[3]) : lo16f(wp[1]); float w3 = e? hi16f(wp[3]) : hi16f(wp[1]);
            auto g = [&](int idx){ uint32_t w = ((const uint32_t*)&kk[idx])[p]; return e? hi16f(w) : lo16f(w); };
            float acc = rbf(g(j)*w3);
            acc = rbf(acc + rbf(g(j+1)*w2));
            acc = rbf(acc + rbf(g(j+2)*w1));
            acc = rbf(acc + rbf(g(j+3)*w0));
            uint32_t vw = ((const uint32_t*)&vv[j])[p];
            r2[e] = acc * (e? hi16f(vw) : lo16f(vw));
          }
          o[j][p] = pk2(r2[0], r2[1]);
        }
      }
    }
    #pragma unroll
    for (int p=0;p<4;p++){
      uint32_t e0 = __builtin_amdgcn_perm(o[1][p], o[0][p], 0x05040100);
      uint32_t e1 = __builtin_amdgcn_perm(o[3][p], o[2][p], 0x05040100);
      uint32_t f0 = __builtin_amdgcn_perm(o[1][p], o[0][p], 0x07060302);
      uint32_t f1 = __builtin_amdgcn_perm(o[3][p], o[2][p], 0x07060302);
      *(uint2*)(smem + (c8+2*p)*TS + rg*8) = make_uint2(e0,e1);
      *(uint2*)(smem + (c8+2*p+1)*TS + rg*8) = make_uint2(f0,f1);
    }
    __syncthreads();
    u16* dst = which == 0 ? uT : kvT;
    int lc = (t & 15)*8, dr = t >> 4;
    #pragma unroll
    for (int i=0;i<4;i++){
      int dd = dr + 16*i;
      uint4 x = *(const uint4*)(smem + dd*TS + lc*2);
      *(uint4*)(dst + (d0+dd)*L_ + l0 + lc) = x;
    }
  }
}

#define CSTRIDE 4416
#define CONV_LDS 49664

__device__ __forceinline__ bf16x8 shr1(bf16x8 x){
  uint4 t = __builtin_bit_cast(uint4, x);
  t.x = __builtin_amdgcn_update_dpp(0, (int)t.x, 0x111, 0xf, 0xf, true);
  t.y = __builtin_amdgcn_update_dpp(0, (int)t.y, 0x111, 0xf, 0xf, true);
  t.z = __builtin_amdgcn_update_dpp(0, (int)t.z, 0x111, 0xf, 0xf, true);
  t.w = __builtin_amdgcn_update_dpp(0, (int)t.w, 0x111, 0xf, 0xf, true);
  return __builtin_bit_cast(bf16x8, t);
}
#define MFMA16(a,b,c) __builtin_amdgcn_mfma_f32_16x16x32_bf16(a,b,c,0,0,0)

__global__ __launch_bounds__(256) void conv_kernel(const u16* __restrict__ uT, const u16* __restrict__ kvT,
    const u16* __restrict__ Khi, const u16* __restrict__ Klo, const u16* __restrict__ ssmk, u16* __restrict__ yT){
  extern __shared__ __attribute__((aligned(16))) char smem[];
  const int bid = blockIdx.x;
  const bool isA = bid < 512;
  const int t = threadIdx.x, wave = t >> 6, lane = t & 63;
  const int q = wave >> 1, h = wave & 1;
  const int m = lane & 15, kg = lane >> 4;
  const int d = isA ? bid : (bid - 512) * 2;
  const u16* x = isA ? uT + d*L_ : kvT + (d+q)*L_;
  bf16x8 X[2], Y[2], Z[2];
  #pragma unroll
  for (int ch=0;ch<2;ch++){
    X[ch] = *(const bf16x8*)(x + 64*(2*m)   + 32*ch + 8*kg);
    Y[ch] = *(const bf16x8*)(x + 64*(2*m+1) + 32*ch + 8*kg);
    Z[ch] = *(const bf16x8*)(x + 64*m       + 32*ch + 8*kg);
  }
  #pragma unroll
  for (int a=0;a<2;a++){
    const u16* Ksrc = isA ? (a==0 ? Khi : Klo) + d*L_ : ssmk + (d+a)*L_;
    char* base = smem + a*4*CSTRIDE;
    #pragma unroll
    for (int rep=0;rep<2;rep++){
      int ch = t + rep*256;
      if (ch < 266){
        uint32_t w[6];
        if (ch < 256){
          uint4 xx = *(const uint4*)(Ksrc + 2040 - 8*ch);
          w[0] = __builtin_amdgcn_alignbit(xx.w, xx.w, 16);
          w[1] = __builtin_amdgcn_alignbit(xx.z, xx.z, 16);
          w[2] = __builtin_amdgcn_alignbit(xx.y, xx.y, 16);
          w[3] = __builtin_amdgcn_alignbit(xx.x, xx.x, 16);
          if (ch < 255){ uint2 y = *(const uint2*)(Ksrc + 2036 - 8*ch);
            w[4] = __builtin_amdgcn_alignbit(y.y,y.y,16); w[5] = __builtin_amdgcn_alignbit(y.x,y.x,16); }
          else { w[4] = 0; w[5] = 0; }
        } else { w[0]=w[1]=w[2]=w[3]=w[4]=w[5]=0; }
        uint32_t al[5];
        #pragma unroll
        for (int i=0;i<5;i++) al[i] = __builtin_amdgcn_alignbit(w[i+1], w[i], 16);
        *(uint4*)(base + 0*CSTRIDE + 16*ch) = make_uint4(w[0],w[1],w[2],w[3]);
        *(uint4*)(base + 1*CSTRIDE + 16*ch) = make_uint4(al[0],al[1],al[2],al[3]);
        *(uint4*)(base + 2*CSTRIDE + 16*ch) = make_uint4(w[1],w[2],w[3],w[4]);
        *(uint4*)(base + 3*CSTRIDE + 16*ch) = make_uint4(al[1],al[2],al[3],al[4]);
      }
    }
  }
  __syncthreads();
  f32x4 P[2][2], Q[2];
  #pragma unroll
  for (int i=0;i<2;i++){ Q[i] = f32x4{0,0,0,0}; for (int j=0;j<2;j++) P[i][j] = f32x4{0,0,0,0}; }
  const int s0 = 2047 - 16*h - m + 8*kg;
  const int r = s0 & 3;
  const char* fb = smem + q*4*CSTRIDE + r*CSTRIDE + 2*(s0 - r);
  auto ldfrag = [&](int o)->bf16x8 {
    const char* p = fb - 2*o;
    uint2 a = *(const uint2*)p; uint2 b = *(const uint2*)(p+8);
    uint4 z = make_uint4(a.x,a.y,b.x,b.y);
    return __builtin_bit_cast(bf16x8, z);
  };
  bf16x8 Flo = ldfrag(-32), Fm = ldfrag(0), Fh = ldfrag(32);
  #pragma unroll 1
  for (int c=0;c<16;c+=2){
    bf16x8 Nm = ldfrag(64*c+64), Nh = ldfrag(64*c+96);
    P[0][0] = MFMA16(X[0], Fm, P[0][0]);
    P[0][1] = MFMA16(X[0], Fh, P[0][1]);
    P[1][0] = MFMA16(Y[0], Fm, P[1][0]);
    P[1][1] = MFMA16(Y[0], Fh, P[1][1]);
    P[0][0] = MFMA16(X[1], Flo, P[0][0]);
    P[0][1] = MFMA16(X[1], Fm, P[0][1]);
    P[1][0] = MFMA16(Y[1], Flo, P[1][0]);
    P[1][1] = MFMA16(Y[1], Fm, P[1][1]);
    Y[0] = shr1(Y[0]); Y[1] = shr1(Y[1]);
    Flo = Fh; Fm = Nm; Fh = Nh;
    Nm = ldfrag(64*c+128); Nh = ldfrag(64*c+160);
    P[0][0] = MFMA16(Y[0], Fm, P[0][0]);
    P[0][1] = MFMA16(Y[0], Fh, P[0][1]);
    P[1][0] = MFMA16(X[0], Fm, P[1][0]);
    P[1][1] = MFMA16(X[0], Fh, P[1][1]);
    P[0][0] = MFMA16(Y[1], Flo, P[0][0]);
    P[0][1] = MFMA16(Y[1], Fm, P[0][1]);
    P[1][0] = MFMA16(X[1], Flo, P[1][0]);
    P[1][1] = MFMA16(X[1], Fm, P[1][1]);
    X[0] = shr1(X[0]); X[1] = shr1(X[1]);
    Flo = Fh; Fm = Nm; Fh = Nh;
  }
  #pragma unroll 1
  for (int c=16;c<32;c+=2){
    bf16x8 Nm = ldfrag(64*c+64), Nh = ldfrag(64*c+96);
    Q[0] = MFMA16(Z[0], Fm, Q[0]);
    Q[1] = MFMA16(Z[0], Fh, Q[1]);
    Q[0] = MFMA16(Z[1], Flo, Q[0]);
    Q[1] = MFMA16(Z[1], Fm, Q[1]);
    Z[0] = shr1(Z[0]); Z[1] = shr1(Z[1]);
    Flo = Fh; Fm = Nm; Fh = Nh;
    if (c < 30){ Nm = ldfrag(64*c+128); Nh = ldfrag(64*c+160); }
    Q[0] = MFMA16(Z[0], Fm, Q[0]);
    Q[1] = MFMA16(Z[0], Fh, Q[1]);
    Q[0] = MFMA16(Z[1], Flo, Q[0]);
    Q[1] = MFMA16(Z[1], Fm, Q[1]);
    Z[0] = shr1(Z[0]); Z[1] = shr1(Z[1]);
    Flo = Fh; Fm = Nm; Fh = Nh;
  }
  __syncthreads();
  float* G1 = (float*)smem + q*2112;
  float* G2 = (float*)smem + 2*2112 + q*1088;
  #pragma unroll
  for (int mt=0;mt<2;mt++)
    #pragma unroll
    for (int nt=0;nt<2;nt++)
      #pragma unroll
      for (int i=0;i<4;i++){
        int l = 64*(2*(4*kg+i)+mt) + 16*h + 32*nt + m;
        G1[l + 16*(l>>9)] = P[mt][nt][i];
      }
  #pragma unroll
  for (int nt=0;nt<2;nt++)
    #pragma unroll
    for (int i=0;i<4;i++){
      int l = 64*(4*kg+i) + 16*h + 32*nt + m;
      G2[l + 16*(l>>8)] = Q[nt][i];
    }
  __syncthreads();
  {
    const float* A1 = (const float*)smem; const float* A2 = A1 + 2*2112;
    int l0 = 8*t;
    int i1 = l0 + 16*(l0>>9);
    int l2 = l0 - 1024; int i2 = l2 + 16*(l2>>8);
    if (isA){
      float v[8];
      #pragma unroll
      for (int j=0;j<8;j++) v[j] = A1[i1+j] + A1[2112+i1+j];
      if (l0 >= 1024){
        #pragma unroll
        for (int j=0;j<8;j++) v[j] += A2[i2+j] + A2[1088+i2+j];
      }
      *(uint4*)(yT + d*L_ + l0) = make_uint4(pk2(v[0],v[1]),pk2(v[2],v[3]),pk2(v[4],v[5]),pk2(v[6],v[7]));
    } else {
      #pragma unroll
      for (int a=0;a<2;a++){
        float v[8];
        #pragma unroll
        for (int j=0;j<8;j++) v[j] = A1[a*2112+i1+j];
        if (l0 >= 1024){
          #pragma unroll
          for (int j=0;j<8;j++) v[j] += A2[a*1088+i2+j];
        }
        *(uint4*)(yT + (D_ + d + a)*L_ + l0) = make_uint4(pk2(v[0],v[1]),pk2(v[2],v[3]),pk2(v[4],v[5]),pk2(v[6],v[7]));
      }
    }
  }
}

__global__ __launch_bounds__(256) void post_kernel(const u16* __restrict__ yT, const u16* __restrict__ u,
    const u16* __restrict__ Dskip, const u16* __restrict__ q, const u16* __restrict__ k, const u16* __restrict__ v,
    const u16* __restrict__ shift_k, const u16* __restrict__ ssmD, u16* __restrict__ out){
  __shared__ u16 tile[64][64+8];
  int l0 = blockIdx.x*64, d0 = blockIdx.y*64, part = blockIdx.z;
  int t = threadIdx.x;
  int c8 = (t & 7)*8, r = t >> 3;
  for (int dd = r; dd < 64; dd += 32)
    *(uint4*)&tile[dd][c8] = *(const uint4*)(yT + (part*D_ + d0+dd)*L_ + l0 + c8);
  __syncthreads();
  for (int rr = r; rr < 64; rr += 32){
    int l = l0 + rr;
    u16 o[8];
    if (part == 0){
      u16 uu[8]; *(uint4*)uu = *(const uint4*)(u + l*D_ + d0 + c8);
      #pragma unroll
      for (int i=0;i<8;i++) o[i] = f2bf(bf2f(tile[c8+i][rr]) + rbf(bf2f(uu[i])*bf2f(Dskip[d0+c8+i])));
    } else {
      u16 kk[4][8], vv[8], qq[8];
      #pragma unroll
      for (int j=0;j<4;j++){
        if (l-j >= 0) *(uint4*)kk[j] = *(const uint4*)(k + (l-j)*D_ + d0 + c8);
        else *(uint4*)kk[j] = make_uint4(0,0,0,0);
      }
      *(uint4*)vv = *(const uint4*)(v + l*D_ + d0 + c8);
      *(uint4*)qq = *(const uint4*)(q + l*D_ + d0 + c8);
      #pragma unroll
      for (int i=0;i<8;i++){
        const u16* w = shift_k + (d0+c8+i)*4;
        float acc = rbf(bf2f(kk[3][i])*bf2f(w[3]));
        acc = rbf(acc + rbf(bf2f(kk[2][i])*bf2f(w[2])));
        acc = rbf(acc + rbf(bf2f(kk[1][i])*bf2f(w[1])));
        acc = rbf(acc + rbf(bf2f(kk[0][i])*bf2f(w[0])));
        float kv = rbf(acc*bf2f(vv[i]));
        float y = rbf(bf2f(tile[c8+i][rr]) + rbf(kv*bf2f(ssmD[d0+c8+i])));
        o[i] = f2bf(bf2f(qq[i])*y);
      }
    }
    *(uint4*)(out + l*(2*D_) + part*D_ + d0 + c8) = *(const uint4*)o;
  }
}

extern "C" void kernel_launch(void* const* d_in, const int* in_sizes, int n_in,
                              void* d_out, int out_size, void* d_ws, size_t ws_size,
                              hipStream_t stream) {
  const u16* u = (const u16*)d_in[0];
  const u16* logA = (const u16*)d_in[1];
  const u16* Aim = (const u16*)d_in[2];
  const u16* Cre = (const u16*)d_in[3];
  const u16* Cim = (const u16*)d_in[4];
  const u16* logdt = (const u16*)d_in[5];
  const u16* Dskip = (const u16*)d_in[6];
  const u16* q = (const u16*)d_in[7];
  const u16* k = (const u16*)d_in[8];
  const u16* v = (const u16*)d_in[9];
  const u16* shift_k = (const u16*)d_in[10];
  const u16* ssm_k = (const u16*)d_in[11];
  const u16* ssmD = (const u16*)d_in[12];
  u16* out = (u16*)d_out;
  u16* ws = (u16*)d_ws;
  const size_t S = (size_t)L_*D_;
  u16* uT = ws; u16* kvT = ws + S; u16* Khi = ws + 2*S; u16* Klo = ws + 3*S; u16* yT = ws + 4*S;
  pre_kernel<<<512, 256, 0, stream>>>(u, k, v, shift_k, uT, kvT, logA, Aim, Cre, Cim, logdt, Khi, Klo);
  conv_kernel<<<768, 256, CONV_LDS, stream>>>(uT, kvT, Khi, Klo, ssm_k, yT);
  post_kernel<<<dim3(32,8,2), 256, 0, stream>>>(yT, u, Dskip, q, k, v, shift_k, ssmD, out);
}
```

```cpp
#include <hip/hip_runtime.h>
#include <hip/hip_bf16.h>
#include <stdint.h>

typedef unsigned short u16;
typedef short bf16x8 __attribute__((ext_vector_type(8)));
typedef float f32x4 __attribute__((ext_vector_type(4)));
typedef float f32x2 __attribute__((ext_vector_type(2)));
typedef __bf16 bf16x2_t __attribute__((ext_vector_type(2)));

#define L_ 2048
#define D_ 512
#define NS_ 128
#define KC_ 656

__device__ __forceinline__ float bf2f(u16 x){ return __builtin_bit_cast(float, (uint32_t)x << 16); }
__device__ __forceinline__ u16 f2bf(float f){ __hip_bfloat16 h = __float2bfloat16(f); return __builtin_bit_cast(u16, h); }
__device__ __forceinline__ float rbf(float f){ return bf2f(f2bf(f)); }
__device__ __forceinline__ uint32_t pk2(float a, float b){
  f32x2 v = {a,b}; bf16x2_t r = __builtin_convertvector(v, bf16x2_t); return __builtin_bit_cast(uint32_t, r); }
__device__ __forceinline__ float lo16f(uint32_t w){ return __builtin_bit_cast(float, w << 16); }
__device__ __forceinline__ float hi16f(uint32_t w){ return __builtin_bit_cast(float, w & 0xffff0000u); }

struct cpx { float re, im; };
__device__ __forceinline__ cpx cmul(cpx a, cpx b){ cpx r; r.re = a.re*b.re - a.im*b.im; r.im = a.re*b.im + a.im*b.re; return r; }
__device__ __forceinline__ cpx cexp_(float re, float im){ float e = expf(re); float s,c; sincosf(im,&s,&c); cpx r; r.re=e*c; r.im=e*s; return r; }

#define TROW 528
#define TLO  (64*TROW)
__device__ __forceinline__ void put_split(char* p, float a, float b){
  uint32_t h = pk2(a,b);
  uint32_t l = pk2(a - lo16f(h), b - hi16f(h));
  *(uint32_t*)p = h; *(uint32_t*)(p + TLO) = l;
}

__global__ __launch_bounds__(256) void pre_kernel(const u16* __restrict__ u, const u16* __restrict__ k,
    const u16* __restrict__ v, const u16* __restrict__ shift_k, u16* __restrict__ uT, u16* __restrict__ kvT,
    const u16* __restrict__ logA, const u16* __restrict__ Aim,
    const u16* __restrict__ Cre, const u16* __restrict__ Cim, const u16* __restrict__ logdt,
    float* __restrict__ Kc){
  __shared__ __attribute__((aligned(16))) char smem[2*64*TROW];
  const int t = threadIdx.x, wave = t >> 6, lane = t & 63;
  const int b = blockIdx.x;
  const int which = b >> 8;
  const int l0 = ((b & 255) >> 3)*64, d0 = (b & 7)*64;
  const int c8 = (t & 7)*8, rg = t >> 3;
  uint4 kk[5], vv[2], wq[4];
  if (which == 0){
    vv[0] = *(const uint4*)(u + (l0+2*rg)*D_ + d0 + c8);
    vv[1] = *(const uint4*)(u + (l0+2*rg+1)*D_ + d0 + c8);
  } else {
    #pragma unroll
    for (int j=0;j<5;j++){ int l = l0+2*rg+j-3; int lc = l < 0 ? 0 : l; kk[j] = *(const uint4*)(k + lc*D_ + d0 + c8); }
    vv[0] = *(const uint4*)(v + (l0+2*rg)*D_ + d0 + c8);
    vv[1] = *(const uint4*)(v + (l0+2*rg+1)*D_ + d0 + c8);
    #pragma unroll
    for (int j=0;j<4;j++) wq[j] = *(const uint4*)(shift_k + (d0+c8)*4 + 8*j);
  }
  {
    const int d = b;
    const int n = (wave & 1)*64 + lane;
    float dt = rbf(expf(bf2f(logdt[d])));
    float zre = -rbf(expf(bf2f(logA[d*NS_+n]))) * dt;
    float zim = bf2f(Aim[d*NS_+n]) * dt;
    cpx C; C.re = bf2f(Cre[d*NS_+n])*dt; C.im = bf2f(Cim[d*NS_+n])*dt;
    char* col = smem + 4*n;
    cpx e128 = cexp_(zre*128.f, zim*128.f);
    if (wave < 2){
      cpx e8 = cexp_(zre*8.f, zim*8.f);
      cpx cb = cmul(C, cexp_(zre*1024.f, zim*1024.f));
      cpx x; x.re = 1.f; x.im = 0.f;
      #pragma unroll 1
      for (int i=0;i<16;i++){ put_split(col + (48+i)*TROW, x.re, -x.im); x = cmul(x, e8); }
      #pragma unroll 1
      for (int i=0;i<9;i++){ put_split(col + (32+i)*TROW, cb.re, cb.im); cb = cmul(cb, e128); }
    } else {
      cpx S[8];
      S[1] = cexp_(zre, zim);
      #pragma unroll
      for (int i=2;i<8;i++) S[i] = cmul(S[i-1], S[1]);
      cpx cb = C;
      #pragma unroll 1
      for (int a=0;a<2;a++){
        put_split(col + (8*a)*TROW, cb.re, cb.im);
        #pragma unroll
        for (int p=1;p<8;p++){ cpx g = cmul(cb, S[p]); put_split(col + (8*a+p)*TROW, g.re, g.im); }
        cb = cmul(cb, e128);
      }
      #pragma unroll 1
      for (int a=0;a<2;a++){
        put_split(col + (16+4*a)*TROW, cb.re, cb.im);
        #pragma unroll
        for (int p=1;p<4;p++){ cpx g = cmul(cb, S[2*p]); put_split(col + (16+4*a+p)*TROW, g.re, g.im); }
        cb = cmul(cb, e128);
      }
      #pragma unroll 1
      for (int a=0;a<4;a++){
        put_split(col + (24+2*a)*TROW, cb.re, cb.im);
        cpx g = cmul(cb, S[4]); put_split(col + (24+2*a+1)*TROW, g.re, g.im);
        cb = cmul(cb, e128);
      }
    }
    __syncthreads();
    if (wave < 3){
      const int m = lane & 15, kg = lane >> 4;
      const char* ga = smem + (16*wave + m)*TROW + 16*kg;
      const char* xb = smem + (48 + m)*TROW + 16*kg;
      f32x4 a4 = {0,0,0,0};
      #pragma unroll
      for (int kc=0;kc<8;kc++){
        bf16x8 ah = *(const bf16x8*)(ga + 64*kc), al = *(const bf16x8*)(ga + 64*kc + TLO);
        bf16x8 bh = *(const bf16x8*)(xb + 64*kc), bl = *(const bf16x8*)(xb + 64*kc + TLO);
        a4 = __builtin_amdgcn_mfma_f32_16x16x32_bf16(ah, bh, a4, 0,0,0);
        a4 = __builtin_amdgcn_mfma_f32_16x16x32_bf16(ah, bl, a4, 0,0,0);
        a4 = __builtin_amdgcn_mfma_f32_16x16x32_bf16(al, bh, a4, 0,0,0);
      }
      #pragma unroll
      for (int i=0;i<4;i++){
        int r = 16*wave + 4*kg + i;
        int idx;
        if (r < 16) idx = 128*(r>>3) + (r&7) + 8*m;
        else if (r < 24) idx = 256 + 64*((r-16)>>2) + (r&3) + 4*m;
        else if (r < 32) idx = 384 + 32*((r-24)>>1) + (r&1) + 2*m;
        else idx = 512 + 16*(r-32) + m;
        if (r < 40 || (r == 40 && m == 0)) Kc[d*KC_ + idx] = 2.f*a4[i];
      }
    }
    __syncthreads();
  }
  {
    const int TS = 144;
    uint32_t o[2][4];
    if (which == 0){
      o[0][0]=vv[0].x;o[0][1]=vv[0].y;o[0][2]=vv[0].z;o[0][3]=vv[0].w;
      o[1][0]=vv[1].x;o[1][1]=vv[1].y;o[1][2]=vv[1].z;o[1][3]=vv[1].w;
    } else {
      #pragma unroll
      for (int j=0;j<3;j++) if (l0+2*rg+j-3 < 0) kk[j] = make_uint4(0,0,0,0);
      #pragma unroll
      for (int p=0;p<4;p++){
        const uint32_t* wp = (const uint32_t*)&wq[p];
        #pragma unroll
        for (int j=0;j<2;j++){
          float r2[2];
          #pragma unroll
          for (int e=0;e<2;e++){
            float w0 = e? lo16f(wp[2]) : lo16f(wp[0]); float w1 = e? hi16f(wp[2]) : hi16f(wp[0]);
            float w2 = e? lo16f(wp[3]) : lo16f(wp[1]); float w3 = e? hi16f(wp[3]) : hi16f(wp[1]);
            auto g = [&](int idx){ uint32_t w = ((const uint32_t*)&kk[idx])[p]; return e? hi16f(w) : lo16f(w); };
            float acc = rbf(g(j)*w3);
            acc = rbf(acc + rbf(g(j+1)*w2));
            acc = rbf(acc + rbf(g(j+2)*w1));
            acc = rbf(acc + rbf(g(j+3)*w0));
            uint32_t vw = ((const uint32_t*)&vv[j])[p];
            r2[e] = acc * (e? hi16f(vw) : lo16f(vw));
          }
          o[j][p] = pk2(r2[0], r2[1]);
        }
      }
    }
    #pragma unroll
    for (int p=0;p<4;p++){
      *(uint32_t*)(smem + (c8+2*p)*TS + rg*4)   = __builtin_amdgcn_perm(o[1][p], o[0][p], 0x05040100);
      *(uint32_t*)(smem + (c8+2*p+1)*TS + rg*4) = __builtin_amdgcn_perm(o[1][p], o[0][p], 0x07060302);
    }
    __syncthreads();
    u16* dst = which == 0 ? uT : kvT;
    const int lc = (t & 7)*8, dr = t >> 3;
    #pragma unroll
    for (int i=0;i<2;i++){
      int dd = dr + 32*i;
      uint4 x = *(const uint4*)(smem + dd*TS + lc*2);
      *(uint4*)(dst + (d0+dd)*L_ + l0 + lc) = x;
    }
  }
}

#define CSTRIDE 4416
#define CONV_LDS 49664

__device__ __forceinline__ bf16x8 shr1(bf16x8 x){
  uint4 t = __builtin_bit_cast(uint4, x);
  t.x = __builtin_amdgcn_update_dpp(0, (int)t.x, 0x111, 0xf, 0xf, true);
  t.y = __builtin_amdgcn_update_dpp(0, (int)t.y, 0x111, 0xf, 0xf, true);
  t.z = __builtin_amdgcn_update_dpp(0, (int)t.z, 0x111, 0xf, 0xf, true);
  t.w = __builtin_amdgcn_update_dpp(0, (int)t.w, 0x111, 0xf, 0xf, true);
  return __builtin_bit_cast(bf16x8, t);
}
__device__ __forceinline__ uint2 ldsv(const char* p){
  typedef uint32_t u32x2 __attribute__((ext_vector_type(2)));
  u32x2 v = *(const volatile u32x2*)p; return make_uint2(v.x, v.y); }
typedef uint32_t u32x2 __attribute__((ext_vector_type(2)));
struct Frag { u32x2 a, b; };
__device__ __forceinline__ bf16x8 fr(const Frag& f){ uint4 z = make_uint4(f.a.x, f.a.y, f.b.x, f.b.y); return __builtin_bit_cast(bf16x8, z); }
#define MFMA16(a,b,c) __builtin_amdgcn_mfma_f32_16x16x32_bf16(a,b,c,0,0,0)

__global__ __launch_bounds__(256) void conv_kernel(const u16* __restrict__ uT, const u16* __restrict__ kvT,
    const float* __restrict__ Kc, const u16* __restrict__ ssmk, u16* __restrict__ yT){
  extern __shared__ __attribute__((aligned(16))) char smem[];
  const int bid = blockIdx.x;
  const bool isA = bid < 512;
  const int t = threadIdx.x, wave = t >> 6, lane = t & 63;
  const int q = wave >> 1, h = wave & 1;
  const int m = lane & 15, kg = lane >> 4;
  const int d = isA ? bid : (bid - 512) * 2;
  const u16* x = isA ? uT + d*L_ : kvT + (d+q)*L_;
  bf16x8 X[2], Y[2], Z[2];
  #pragma unroll
  for (int ch=0;ch<2;ch++){
    X[ch] = *(const bf16x8*)(x + 64*(2*m)   + 32*ch + 8*kg);
    Y[ch] = *(const bf16x8*)(x + 64*(2*m+1) + 32*ch + 8*kg);
    Z[ch] = *(const bf16x8*)(x + 64*m       + 32*ch + 8*kg);
  }
  {
    uint32_t w[2][6];
    if (isA){
      float kv[12];
      #pragma unroll
      for (int j=0;j<12;j++){
        int l = 2047 - 8*t - j; int lc = l < 0 ? 0 : l;
        int pr = (int)rbf((float)lc);
        int p = pr < 256 ? pr : (pr < 512 ? 128 + (pr>>1) : (pr < 1024 ? 256 + (pr>>2) : 384 + (pr>>3)));
        float x = Kc[d*KC_ + p];
        kv[j] = l < 0 ? 0.f : x;
      }
      #pragma unroll
      for (int j=0;j<6;j++){
        uint32_t hh = pk2(kv[2*j], kv[2*j+1]);
        w[0][j] = hh; w[1][j] = pk2(kv[2*j] - lo16f(hh), kv[2*j+1] - hi16f(hh));
      }
    } else {
      int o2 = t < 255 ? 2036 - 8*t : 0;
      #pragma unroll
      for (int a=0;a<2;a++){
        const u16* K0 = ssmk + (d+a)*L_;
        uint4 xa = *(const uint4*)(K0 + 2040 - 8*t);
        uint2 ya = *(const uint2*)(K0 + o2);
        w[a][0] = __builtin_amdgcn_alignbit(xa.w, xa.w, 16);
        w[a][1] = __builtin_amdgcn_alignbit(xa.z, xa.z, 16);
        w[a][2] = __builtin_amdgcn_alignbit(xa.y, xa.y, 16);
        w[a][3] = __builtin_amdgcn_alignbit(xa.x, xa.x, 16);
        w[a][4] = t < 255 ? __builtin_amdgcn_alignbit(ya.y,ya.y,16) : 0u;
        w[a][5] = t < 255 ? __builtin_amdgcn_alignbit(ya.x,ya.x,16) : 0u;
      }
    }
    if (t < 80) *(uint4*)(smem + (t/10)*CSTRIDE + 4096 + 16*(t%10)) = make_uint4(0,0,0,0);
    #pragma unroll
    for (int a=0;a<2;a++){
      char* base = smem + a*4*CSTRIDE;
      uint32_t al[5];
      #pragma unroll
      for (int i=0;i<5;i++) al[i] = __builtin_amdgcn_alignbit(w[a][i+1], w[a][i], 16);
      *(uint4*)(base + 0*CSTRIDE + 16*t) = make_uint4(w[a][0],w[a][1],w[a][2],w[a][3]);
      *(uint4*)(base + 1*CSTRIDE + 16*t) = make_uint4(al[0],al[1],al[2],al[3]);
      *(uint4*)(base + 2*CSTRIDE + 16*t) = make_uint4(w[a][1],w[a][2],w[a][3],w[a][4]);
      *(uint4*)(base + 3*CSTRIDE + 16*t) = make_uint4(al[1],al[2],al[3],al[4]);
    }
  }
  __syncthreads();
  f32x4 P[2][2], Q[2];
  #pragma unroll
  for (int i=0;i<2;i++){ Q[i] = f32x4{0,0,0,0}; for (int j=0;j<2;j++) P[i][j] = f32x4{0,0,0,0}; }
  const int s0 = 2047 - 16*h - m + 8*kg;
  const int r = s0 & 3;
  const uint32_t fbase = (uint32_t)(uintptr_t)smem + q*4*CSTRIDE + r*CSTRIDE + 2*(s0 - r) - 4032;
#define LDF(F, o) asm volatile("ds_read_b64 %0, %2 offset:%3\n\tds_read_b64 %1, %2 offset:%4" : "=v"(F.a), "=v"(F.b) : "v"(fbase), "i"(4032-2*(o)), "i"(4040-2*(o)))
#define WAITF(n, F, G) asm volatile("s_waitcnt lgkmcnt(" #n ")" : "+v"(F.a), "+v"(F.b), "+v"(G.a), "+v"(G.b))
  Frag F0, F1, F2, F3, F4;
  LDF(F0, -32); LDF(F1, 0); LDF(F2, 32);
  asm volatile("s_waitcnt lgkmcnt(4)" : "+v"(F0.a), "+v"(F0.b));
#define STEP1(c, EV, OD, LO, MM, HH, NM, NH) \
    LDF(NM, 64*(c)+64); LDF(NH, 64*(c)+96); WAITF(4, MM, HH); \
    P[0][0] = MFMA16(EV[0], fr(MM), P[0][0]); P[0][1] = MFMA16(EV[0], fr(HH), P[0][1]); \
    P[1][0] = MFMA16(OD[0], fr(MM), P[1][0]); P[1][1] = MFMA16(OD[0], fr(HH), P[1][1]); \
    P[0][0] = MFMA16(EV[1], fr(LO), P[0][0]); P[0][1] = MFMA16(EV[1], fr(MM), P[0][1]); \
    P[1][0] = MFMA16(OD[1], fr(LO), P[1][0]); P[1][1] = MFMA16(OD[1], fr(MM), P[1][1]); \
    OD[0] = shr1(OD[0]); OD[1] = shr1(OD[1]);
#define STEP2(c, LO, MM, HH, NM, NH) \
    if ((c) < 31){ LDF(NM, 64*(c)+64); LDF(NH, 64*(c)+96); WAITF(4, MM, HH); } else { WAITF(0, MM, HH); } \
    Q[0] = MFMA16(Z[0], fr(MM), Q[0]); Q[1] = MFMA16(Z[0], fr(HH), Q[1]); \
    Q[0] = MFMA16(Z[1], fr(LO), Q[0]); Q[1] = MFMA16(Z[1], fr(MM), Q[1]); \
    Z[0] = shr1(Z[0]); Z[1] = shr1(Z[1]);
  STEP1(0, X, Y, F0, F1, F2, F3, F4)
  STEP1(1, Y, X, F2, F3, F4, F0, F1)
  STEP1(2, X, Y, F4, F0, F1, F2, F3)
  STEP1(3, Y, X, F1, F2, F3, F4, F0)
  STEP1(4, X, Y, F3, F4, F0, F1, F2)
  STEP1(5, Y, X, F0, F1, F2, F3, F4)
  STEP1(6, X, Y, F2, F3, F4, F0, F1)
  STEP1(7, Y, X, F4, F0, F1, F2, F3)
  STEP1(8, X, Y, F1, F2, F3, F4, F0)
  STEP1(9, Y, X, F3, F4, F0, F1, F2)
  STEP1(10, X, Y, F0, F1, F2, F3, F4)
  STEP1(11, Y, X, F2, F3, F4, F0, F1)
  STEP1(12, X, Y, F4, F0, F1, F2, F3)
  STEP1(13, Y, X, F1, F2, F3, F4, F0)
  STEP1(14, X, Y, F3, F4, F0, F1, F2)
  STEP1(15, Y, X, F0, F1, F2, F3, F4)
  STEP2(16, F2, F3, F4, F0, F1)
  STEP2(17, F4, F0, F1, F2, F3)
  STEP2(18, F1, F2, F3, F4, F0)
  STEP2(19, F3, F4, F0, F1, F2)
  STEP2(20, F0, F1, F2, F3, F4)
  STEP2(21, F2, F3, F4, F0, F1)
  STEP2(22, F4, F0, F1, F2, F3)
  STEP2(23, F1, F2, F3, F4, F0)
  STEP2(24, F3, F4, F0, F1, F2)
  STEP2(25, F0, F1, F2, F3, F4)
  STEP2(26, F2, F3, F4, F0, F1)
  STEP2(27, F4, F0, F1, F2, F3)
  STEP2(28, F1, F2, F3, F4, F0)
  STEP2(29, F3, F4, F0, F1, F2)
  STEP2(30, F0, F1, F2, F3, F4)
  STEP2(31, F2, F3, F4, F0, F1)
  __syncthreads();
  float* G1 = (float*)smem + q*2112;
  float* G2 = (float*)smem + 2*2112 + q*1088;
  #pragma unroll
  for (int mt=0;mt<2;mt++)
    #pragma unroll
    for (int nt=0;nt<2;nt++)
      #pragma unroll
      for (int i=0;i<4;i++){
        int l = 64*(2*(4*kg+i)+mt) + 16*h + 32*nt + m;
        G1[l + 16*(l>>9)] = P[mt][nt][i];
      }
  #pragma unroll
  for (int nt=0;nt<2;nt++)
    #pragma unroll
    for (int i=0;i<4;i++){
      int l = 64*(4*kg+i) + 16*h + 32*nt + m;
      G2[l + 16*(l>>8)] = Q[nt][i];
    }
  __syncthreads();
  {
    const float* A1 = (const float*)smem; const float* A2 = A1 + 2*2112;
    int l0 = 8*t;
    int i1 = l0 + 16*(l0>>9);
    int l2 = l0 - 1024; int i2 = l2 + 16*(l2>>8);
    if (isA){
      float v[8];
      #pragma unroll
      for (int j=0;j<8;j++) v[j] = A1[i1+j] + A1[2112+i1+j];
      if (l0 >= 1024){
        #pragma unroll
        for (int j=0;j<8;j++) v[j] += A2[i2+j] + A2[1088+i2+j];
      }
      *(uint4*)(yT + d*L_ + l0) = make_uint4(pk2(v[0],v[1]),pk2(v[2],v[3]),pk2(v[4],v[5]),pk2(v[6],v[7]));
    } else {
      #pragma unroll
      for (int a=0;a<2;a++){
        float v[8];
        #pragma unroll
        for (int j=0;j<8;j++) v[j] = A1[a*2112+i1+j];
        if (l0 >= 1024){
          #pragma unroll
          for (int j=0;j<8;j++) v[j] += A2[a*1088+i2+j];
        }
        *(uint4*)(yT + (D_ + d + a)*L_ + l0) = make_uint4(pk2(v[0],v[1]),pk2(v[2],v[3]),pk2(v[4],v[5]),pk2(v[6],v[7]));
      }
    }
  }
}

__global__ __launch_bounds__(256) void post_kernel(const u16* __restrict__ yT, const u16* __restrict__ u,
    const u16* __restrict__ Dskip, const u16* __restrict__ q, const u16* __restrict__ k, const u16* __restrict__ v,
    const u16* __restrict__ shift_k, const u16* __restrict__ ssmD, u16* __restrict__ out){
  __shared__ u16 tile[64][64+8];
  int l0 = blockIdx.x*64, d0 = blockIdx.y*64, part = blockIdx.z;
  int t = threadIdx.x;
  int c8 = (t & 7)*8, r = t >> 3;
  for (int dd = r; dd < 64; dd += 32)
    *(uint4*)&tile[dd][c8] = *(const uint4*)(yT + (part*D_ + d0+dd)*L_ + l0 + c8);
  __syncthreads();
  for (int rr = r; rr < 64; rr += 32){
    int l = l0 + rr;
    u16 o[8];
    if (part == 0){
      u16 uu[8]; *(uint4*)uu = *(const uint4*)(u + l*D_ + d0 + c8);
      #pragma unroll
      for (int i=0;i<8;i++) o[i] = f2bf(bf2f(tile[c8+i][rr]) + rbf(bf2f(uu[i])*bf2f(Dskip[d0+c8+i])));
    } else {
      u16 kk[4][8], vv[8], qq[8];
      #pragma unroll
      for (int j=0;j<4;j++){
        if (l-j >= 0) *(uint4*)kk[j] = *(const uint4*)(k + (l-j)*D_ + d0 + c8);
        else *(uint4*)kk[j] = make_uint4(0,0,0,0);
      }
      *(uint4*)vv = *(const uint4*)(v + l*D_ + d0 + c8);
      *(uint4*)qq = *(const uint4*)(q + l*D_ + d0 + c8);
      #pragma unroll
      for (int i=0;i<8;i++){
        const u16* w = shift_k + (d0+c8+i)*4;
        float acc = rbf(bf2f(kk[3][i])*bf2f(w[3]));
        acc = rbf(acc + rbf(bf2f(kk[2][i])*bf2f(w[2])));
        acc = rbf(acc + rbf(bf2f(kk[1][i])*bf2f(w[1])));
        acc = rbf(acc + rbf(bf2f(kk[0][i])*bf2f(w[0])));
        float kv = rbf(acc*bf2f(vv[i]));
        float y = rbf(bf2f(tile[c8+i][rr]) + rbf(kv*bf2f(ssmD[d0+c8+i])));
        o[i] = f2bf(bf2f(qq[i])*y);
      }
    }
    *(uint4*)(out + l*(2*D_) + part*D_ + d0 + c8) = *(const uint4*)o;
  }
}

extern "C" void kernel_launch(void* const* d_in, const int* in_sizes, int n_in,
                              void* d_out, int out_size, void* d_ws, size_t ws_size,
                              hipStream_t stream) {
  const u16* u = (const u16*)d_in[0];
  const u16* logA = (const u16*)d_in[1];
  const u16* Aim = (const u16*)d_in[2];
  const u16* Cre = (const u16*)d_in[3];
  const u16* Cim = (const u16*)d_in[4];
  const u16* logdt = (const u16*)d_in[5];
  const u16* Dskip = (const u16*)d_in[6];
  const u16* q = (const u16*)d_in[7];
  const u16* k = (const u16*)d_in[8];
  const u16* v = (const u16*)d_in[9];
  const u16* shift_k = (const u16*)d_in[10];
  const u16* ssm_k = (const u16*)d_in[11];
  const u16* ssmD = (const u16*)d_in[12];
  u16* out = (u16*)d_out;
  u16* ws = (u16*)d_ws;
  const size_t S = (size_t)L_*D_;
  u16* uT = ws; u16* kvT = ws + S; float* Kc = (float*)(ws + 2*S); u16* yT = ws + 4*S;
  pre_kernel<<<512, 256, 0, stream>>>(u, k, v, shift_k, uT, kvT, logA, Aim, Cre, Cim, logdt, Kc);
  conv_kernel<<<768, 256, CONV_LDS, stream>>>(uT, kvT, Kc, ssm_k, yT);
  post_kernel<<<dim3(32,8,2), 256, 0, stream>>>(yT, u, Dskip, q, k, v, shift_k, ssmD, out);
}
```

```cpp
#include <hip/hip_runtime.h>
#include <hip/hip_bf16.h>
#include <stdint.h>

typedef unsigned short u16;
typedef short bf16x8 __attribute__((ext_vector_type(8)));
typedef float f32x4 __attribute__((ext_vector_type(4)));
typedef float f32x2 __attribute__((ext_vector_type(2)));
typedef __bf16 bf16x2_t __attribute__((ext_vector_type(2)));

#define L_ 2048
#define D_ 512
#define NS_ 128

__device__ __forceinline__ float bf2f(u16 x){ return __builtin_bit_cast(float, (uint32_t)x << 16); }
__device__ __forceinline__ u16 f2bf(float f){ __hip_bfloat16 h = __float2bfloat16(f); return __builtin_bit_cast(u16, h); }
__device__ __forceinline__ float rbf(float f){ return bf2f(f2bf(f)); }
__device__ __forceinline__ uint32_t pk2(float a, float b){
  f32x2 v = {a,b}; bf16x2_t r = __builtin_convertvector(v, bf16x2_t); return __builtin_bit_cast(uint32_t, r); }
__device__ __forceinline__ float lo16f(uint32_t w){ return __builtin_bit_cast(float, w << 16); }
__device__ __forceinline__ float hi16f(uint32_t w){ return __builtin_bit_cast(float, w & 0xffff0000u); }

struct cpx { float re, im; };
__device__ __forceinline__ cpx cmul(cpx a, cpx b){ cpx r; r.re = a.re*b.re - a.im*b.im; r.im = a.re*b.im + a.im*b.re; return r; }
__device__ __forceinline__ cpx cexp_(float re, float im){ float e = expf(re); float s,c; sincosf(im,&s,&c); cpx r; r.re=e*c; r.im=e*s; return r; }

#define TROW 272
__device__ __forceinline__ void put_split(char* hi, char* lo, float a, float b){
  uint32_t h = pk2(a,b);
  uint32_t l = pk2(a - lo16f(h), b - hi16f(h));
  *(uint32_t*)hi = h; *(uint32_t*)lo = l;
}

__global__ __launch_bounds__(256) void pre_kernel(const u16* __restrict__ u, const u16* __restrict__ k,
    const u16* __restrict__ v, const u16* __restrict__ shift_k, u16* __restrict__ uT, u16* __restrict__ kvT,
    const u16* __restrict__ logA, const u16* __restrict__ Aim,
    const u16* __restrict__ Cre, const u16* __restrict__ Cim, const u16* __restrict__ logdt,
    u16* __restrict__ Khi, u16* __restrict__ Klo){
  __shared__ __attribute__((aligned(16))) char smem[4*4*16*TROW + 2*656*4];
  int t = threadIdx.x;
  if (blockIdx.x < 256){
    int wave = t >> 6, lane = t & 63;
    int d = blockIdx.x*2 + (wave >> 1);
    int n = (wave & 1)*64 + lane;
    char* tb = smem + wave*(4*16*TROW);
    char* GAh = tb, *GAl = tb + 16*TROW, *XBh = tb + 32*TROW, *XBl = tb + 48*TROW;
    float* sK = (float*)(smem + 4*4*16*TROW) + (wave>>1)*656;
    float dt = rbf(expf(bf2f(logdt[d])));
    float zre = -rbf(expf(bf2f(logA[d*NS_+n]))) * dt;
    float zim = bf2f(Aim[d*NS_+n]) * dt;
    cpx C; C.re = bf2f(Cre[d*NS_+n])*dt; C.im = bf2f(Cim[d*NS_+n])*dt;
    cpx X0[16], Y[16], CZ[9];
    {
      cpx e1 = cexp_(zre, zim), e16 = cexp_(zre*16.f, zim*16.f), e256 = cexp_(zre*256.f, zim*256.f);
      X0[0].re = 1.f; X0[0].im = 0.f; X0[1] = e1; Y[0] = X0[0]; Y[1] = e16;
      #pragma unroll
      for (int i=2;i<16;i++){ X0[i] = cmul(X0[i-1], e1); Y[i] = cmul(Y[i-1], e16); }
      CZ[0] = C;
      #pragma unroll
      for (int i=1;i<9;i++) CZ[i] = cmul(CZ[i-1], e256);
    }
    int m = lane & 15, kg = lane >> 4;
    #pragma unroll
    for (int r=9;r<16;r++){ *(uint32_t*)(GAh + r*TROW + 4*lane) = 0; *(uint32_t*)(GAl + r*TROW + 4*lane) = 0; }
    f32x4 acc[4];
    #pragma unroll
    for (int s=0;s<4;s++){
      #pragma unroll
      for (int b=0;b<16;b++){
        int e = b << s; cpx x;
        if ((e >> 4) == 0) x = X0[e & 15]; else if ((e & 15) == 0) x = Y[e >> 4]; else x = cmul(Y[e>>4], X0[e&15]);
        put_split(XBh + b*TROW + 4*lane, XBl + b*TROW + 4*lane, x.re, -x.im);
      }
      if (s == 0){
        #pragma unroll
        for (int a=0;a<16;a++){ cpx g = (a==0)? C : cmul(C, Y[a]); put_split(GAh + a*TROW + 4*lane, GAl + a*TROW + 4*lane, g.re, g.im); }
      } else {
        #pragma unroll
        for (int a=0;a<9;a++){
          int e = (256 << (s-1)) + ((16*a) << s);
          int hh = e >> 8, yy = (e & 255) >> 4;
          cpx g = (yy==0)? CZ[hh] : cmul(CZ[hh], Y[yy]);
          put_split(GAh + a*TROW + 4*lane, GAl + a*TROW + 4*lane, g.re, g.im);
        }
        if (s == 1){
          #pragma unroll
          for (int r=9;r<16;r++){ *(uint32_t*)(GAh + r*TROW + 4*lane) = 0; *(uint32_t*)(GAl + r*TROW + 4*lane) = 0; }
        }
      }
      __builtin_amdgcn_wave_barrier();
      f32x4 a4 = {0,0,0,0};
      #pragma unroll
      for (int kc=0;kc<4;kc++){
        int off = m*TROW + (16*kc + 4*kg)*4;
        bf16x8 ah = *(const bf16x8*)(GAh + off), al = *(const bf16x8*)(GAl + off);
        bf16x8 bh = *(const bf16x8*)(XBh + off), bl = *(const bf16x8*)(XBl + off);
        a4 = __builtin_amdgcn_mfma_f32_16x16x32_bf16(ah, bh, a4, 0,0,0);
        a4 = __builtin_amdgcn_mfma_f32_16x16x32_bf16(ah, bl, a4, 0,0,0);
        a4 = __builtin_amdgcn_mfma_f32_16x16x32_bf16(al, bh, a4, 0,0,0);
      }
      acc[s] = a4;
      __builtin_amdgcn_wave_barrier();
    }
    if (wave & 1){
      #pragma unroll
      for (int s=0;s<4;s++)
        #pragma unroll
        for (int i=0;i<4;i++){
          int row = 4*kg + i;
          int p = (s==0) ? 16*row + m : 256 + 128*(s-1) + 16*row + m;
          if (s == 0 || row < 8 || (s==3 && row == 8 && m == 0)) sK[p] = acc[s][i];
        }
    }
    __syncthreads();
    if (!(wave & 1)){
      #pragma unroll
      for (int s=0;s<4;s++)
        #pragma unroll
        for (int i=0;i<4;i++){
          int row = 4*kg + i;
          int p = (s==0) ? 16*row + m : 256 + 128*(s-1) + 16*row + m;
          if (s == 0 || row < 8 || (s==3 && row == 8 && m == 0)) sK[p] = 2.f*(sK[p] + acc[s][i]);
        }
    }
    __syncthreads();
    {
      int tt = t & 127;
      #pragma unroll
      for (int rep=0;rep<2;rep++){
        int l0 = rep*1024 + tt*8;
        uint32_t wh[4], wl[4];
        #pragma unroll
        for (int j=0;j<4;j++){
          float kv2[2];
          #pragma unroll
          for (int e=0;e<2;e++){
            int l = l0 + 2*j + e;
            int pr = (int)rbf((float)l);
            int p;
            if (pr < 256) p = pr; else if (pr < 512) p = 128 + (pr>>1); else if (pr < 1024) p = 256 + (pr>>2);
            else p = 384 + (pr>>3);
            kv2[e] = sK[p];
          }
          uint32_t h = pk2(kv2[0], kv2[1]);
          wh[j] = h; wl[j] = pk2(kv2[0]-lo16f(h), kv2[1]-hi16f(h));
        }
        *(uint4*)(Khi + d*L_ + l0) = make_uint4(wh[0],wh[1],wh[2],wh[3]);
        *(uint4*)(Klo + d*L_ + l0) = make_uint4(wl[0],wl[1],wl[2],wl[3]);
      }
    }
  } else {
    int b = blockIdx.x - 256;
    int which = b >> 7; b &= 127;
    int l0 = (b >> 3)*128, d0 = (b & 7)*64;
    const int TS = 272;
    int c8 = (t & 7)*8, rg = t >> 3;
    uint32_t o[4][4];
    if (which == 0){
      #pragma unroll
      for (int j=0;j<4;j++){ uint4 x = *(const uint4*)(u + (l0+4*rg+j)*D_ + d0 + c8); o[j][0]=x.x;o[j][1]=x.y;o[j][2]=x.z;o[j][3]=x.w; }
    } else {
      uint4 kk[7], vv[4];
      #pragma unroll
      for (int j=0;j<7;j++){ int l = l0+4*rg+j-3; kk[j] = (l>=0) ? *(const uint4*)(k + l*D_ + d0 + c8) : make_uint4(0,0,0,0); }
      #pragma unroll
      for (int j=0;j<4;j++) vv[j] = *(const uint4*)(v + (l0+4*rg+j)*D_ + d0 + c8);
      uint4 wq[4];
      #pragma unroll
      for (int j=0;j<4;j++) wq[j] = *(const uint4*)(shift_k + (d0+c8)*4 + 8*j);
      #pragma unroll
      for (int p=0;p<4;p++){
        const uint32_t* wp = (const uint32_t*)&wq[p];
        #pragma unroll
        for (int j=0;j<4;j++){
          float r2[2];
          #pragma unroll
          for (int e=0;e<2;e++){
            float w0 = e? lo16f(wp[2]) : lo16f(wp[0]); float w1 = e? hi16f(wp[2]) : hi16f(wp[0]);
            float w2 = e? lo16f(wp[3]) : lo16f(wp[1]); float w3 = e? hi16f(wp[3]) : hi16f(wp[1]);
            auto g = [&](int idx){ uint32_t w = ((const uint32_t*)&kk[idx])[p]; return e? hi16f(w) : lo16f(w); };
            float acc = rbf(g(j)*w3);
            acc = rbf(acc + rbf(g(j+1)*w2));
            acc = rbf(acc + rbf(g(j+2)*w1));
            acc = rbf(acc + rbf(g(j+3)*w0));
            uint32_t vw = ((const uint32_t*)&vv[j])[p];
            r2[e] = acc * (e? hi16f(vw) : lo16f(vw));
          }
          o[j][p] = pk2(r2[0], r2[1]);
        }
      }
    }
    #pragma unroll
    for (int p=0;p<4;p++){
      uint32_t e0 = __builtin_amdgcn_perm(o[1][p], o[0][p], 0x05040100);
      uint32_t e1 = __builtin_amdgcn_perm(o[3][p], o[2][p], 0x05040100);
      uint32_t f0 = __builtin_amdgcn_perm(o[1][p], o[0][p], 0x07060302);
      uint32_t f1 = __builtin_amdgcn_perm(o[3][p], o[2][p], 0x07060302);
      *(uint2*)(smem + (c8+2*p)*TS + rg*8) = make_uint2(e0,e1);
      *(uint2*)(smem + (c8+2*p+1)*TS + rg*8) = make_uint2(f0,f1);
    }
    __syncthreads();
    u16* dst = which == 0 ? uT : kvT;
    int lc = (t & 15)*8, dr = t >> 4;
    #pragma unroll
    for (int i=0;i<4;i++){
      int dd = dr + 16*i;
      uint4 x = *(const uint4*)(smem + dd*TS + lc*2);
      *(uint4*)(dst + (d0+dd)*L_ + l0 + lc) = x;
    }
  }
}

#define CSTRIDE 4416
#define CONV_LDS 49664

__device__ __forceinline__ bf16x8 shr1(bf16x8 x){
  uint4 t = __builtin_bit_cast(uint4, x);
  t.x = __builtin_amdgcn_update_dpp(0, (int)t.x, 0x111, 0xf, 0xf, true);
  t.y = __builtin_amdgcn_update_dpp(0, (int)t.y, 0x111, 0xf, 0xf, true);
  t.z = __builtin_amdgcn_update_dpp(0, (int)t.z, 0x111, 0xf, 0xf, true);
  t.w = __builtin_amdgcn_update_dpp(0, (int)t.w, 0x111, 0xf, 0xf, true);
  return __builtin_bit_cast(bf16x8, t);
}
__device__ __forceinline__ uint2 ldsv(const char* p){
  typedef uint32_t u32x2 __attribute__((ext_vector_type(2)));
  u32x2 v = *(const volatile u32x2*)p; return make_uint2(v.x, v.y); }
typedef uint32_t u32x2 __attribute__((ext_vector_type(2)));
struct Frag { u32x2 a, b; };
__device__ __forceinline__ bf16x8 fr(const Frag& f){ uint4 z = make_uint4(f.a.x, f.a.y, f.b.x, f.b.y); return __builtin_bit_cast(bf16x8, z); }
#define MFMA16(a,b,c) __builtin_amdgcn_mfma_f32_16x16x32_bf16(a,b,c,0,0,0)

__global__ __launch_bounds__(256) void conv_kernel(const u16* __restrict__ uT, const u16* __restrict__ kvT,
    const u16* __restrict__ Khi, const u16* __restrict__ Klo, const u16* __restrict__ ssmk, u16* __restrict__ yT){
  extern __shared__ __attribute__((aligned(16))) char smem[];
  const int bid = blockIdx.x;
  const bool isA = bid < 512;
  const int t = threadIdx.x, wave = t >> 6, lane = t & 63;
  const int q = wave >> 1, h = wave & 1;
  const int m = lane & 15, kg = lane >> 4;
  const int d = isA ? bid : (bid - 512) * 2;
  const u16* x = isA ? uT + d*L_ : kvT + (d+q)*L_;
  bf16x8 X[2], Y[2], Z[2];
  #pragma unroll
  for (int ch=0;ch<2;ch++){
    X[ch] = *(const bf16x8*)(x + 64*(2*m)   + 32*ch + 8*kg);
    Y[ch] = *(const bf16x8*)(x + 64*(2*m+1) + 32*ch + 8*kg);
    Z[ch] = *(const bf16x8*)(x + 64*m       + 32*ch + 8*kg);
  }
  {
    const u16* K0 = isA ? Khi + d*L_ : ssmk + d*L_;
    const u16* K1 = isA ? Klo + d*L_ : ssmk + (d+1)*L_;
    int o2 = t < 255 ? 2036 - 8*t : 0;
    uint4 xa[2]; uint2 ya[2];
    xa[0] = *(const uint4*)(K0 + 2040 - 8*t); xa[1] = *(const uint4*)(K1 + 2040 - 8*t);
    ya[0] = *(const uint2*)(K0 + o2); ya[1] = *(const uint2*)(K1 + o2);
    if (t < 80) *(uint4*)(smem + (t/10)*CSTRIDE + 4096 + 16*(t%10)) = make_uint4(0,0,0,0);
    #pragma unroll
    for (int a=0;a<2;a++){
      char* base = smem + a*4*CSTRIDE;
      uint32_t w[6];
      w[0] = __builtin_amdgcn_alignbit(xa[a].w, xa[a].w, 16);
      w[1] = __builtin_amdgcn_alignbit(xa[a].z, xa[a].z, 16);
      w[2] = __builtin_amdgcn_alignbit(xa[a].y, xa[a].y, 16);
      w[3] = __builtin_amdgcn_alignbit(xa[a].x, xa[a].x, 16);
      w[4] = t < 255 ? __builtin_amdgcn_alignbit(ya[a].y,ya[a].y,16) : 0u;
      w[5] = t < 255 ? __builtin_amdgcn_alignbit(ya[a].x,ya[a].x,16) : 0u;
      uint32_t al[5];
      #pragma unroll
      for (int i=0;i<5;i++) al[i] = __builtin_amdgcn_alignbit(w[i+1], w[i], 16);
      *(uint4*)(base + 0*CSTRIDE + 16*t) = make_uint4(w[0],w[1],w[2],w[3]);
      *(uint4*)(base + 1*CSTRIDE + 16*t) = make_uint4(al[0],al[1],al[2],al[3]);
      *(uint4*)(base + 2*CSTRIDE + 16*t) = make_uint4(w[1],w[2],w[3],w[4]);
      *(uint4*)(base + 3*CSTRIDE + 16*t) = make_uint4(al[1],al[2],al[3],al[4]);
    }
  }
  __syncthreads();
  f32x4 P[2][2], Q[2];
  #pragma unroll
  for (int i=0;i<2;i++){ Q[i] = f32x4{0,0,0,0}; for (int j=0;j<2;j++) P[i][j] = f32x4{0,0,0,0}; }
  const int s0 = 2047 - 16*h - m + 8*kg;
  const int r = s0 & 3;
  const uint32_t fbase = (uint32_t)(uintptr_t)smem + q*4*CSTRIDE + r*CSTRIDE + 2*(s0 - r) - 4032;
#define LDF(F, o) asm volatile("ds_read_b64 %0, %2 offset:%3\n\tds_read_b64 %1, %2 offset:%4" : "=v"(F.a), "=v"(F.b) : "v"(fbase), "i"(4032-2*(o)), "i"(4040-2*(o)))
#define WAITF(n, F, G) asm volatile("s_waitcnt lgkmcnt(" #n ")" : "+v"(F.a), "+v"(F.b), "+v"(G.a), "+v"(G.b))
  Frag F0, F1, F2, F3, F4;
  LDF(F0, -32); LDF(F1, 0); LDF(F2, 32);
  asm volatile("s_waitcnt lgkmcnt(4)" : "+v"(F0.a), "+v"(F0.b));
#define STEP1(c, EV, OD, LO, MM, HH, NM, NH) \
    LDF(NM, 64*(c)+64); LDF(NH, 64*(c)+96); WAITF(4, MM, HH); \
    P[0][0] = MFMA16(EV[0], fr(MM), P[0][0]); P[0][1] = MFMA16(EV[0], fr(HH), P[0][1]); \
    P[1][0] = MFMA16(OD[0], fr(MM), P[1][0]); P[1][1] = MFMA16(OD[0], fr(HH), P[1][1]); \
    P[0][0] = MFMA16(EV[1], fr(LO), P[0][0]); P[0][1] = MFMA16(EV[1], fr(MM), P[0][1]); \
    P[1][0] = MFMA16(OD[1], fr(LO), P[1][0]); P[1][1] = MFMA16(OD[1], fr(MM), P[1][1]); \
    OD[0] = shr1(OD[0]); OD[1] = shr1(OD[1]);
#define STEP2(c, LO, MM, HH, NM, NH) \
    if ((c) < 31){ LDF(NM, 64*(c)+64); LDF(NH, 64*(c)+96); WAITF(4, MM, HH); } else { WAITF(0, MM, HH); } \
    Q[0] = MFMA16(Z[0], fr(MM), Q[0]); Q[1] = MFMA16(Z[0], fr(HH), Q[1]); \
    Q[0] = MFMA16(Z[1], fr(LO), Q[0]); Q[1] = MFMA16(Z[1], fr(MM), Q[1]); \
    Z[0] = shr1(Z[0]); Z[1] = shr1(Z[1]);
  STEP1(0, X, Y, F0, F1, F2, F3, F4)
  STEP1(1, Y, X, F2, F3, F4, F0, F1)
  STEP1(2, X, Y, F4, F0, F1, F2, F3)
  STEP1(3, Y, X, F1, F2, F3, F4, F0)
  STEP1(4, X, Y, F3, F4, F0, F1, F2)
  STEP1(5, Y, X, F0, F1, F2, F3, F4)
  STEP1(6, X, Y, F2, F3, F4, F0, F1)
  STEP1(7, Y, X, F4, F0, F1, F2, F3)
  STEP1(8, X, Y, F1, F2, F3, F4, F0)
  STEP1(9, Y, X, F3, F4, F0, F1, F2)
  STEP1(10, X, Y, F0, F1, F2, F3, F4)
  STEP1(11, Y, X, F2, F3, F4, F0, F1)
  STEP1(12, X, Y, F4, F0, F1, F2, F3)
  STEP1(13, Y, X, F1, F2, F3, F4, F0)
  STEP1(14, X, Y, F3, F4, F0, F1, F2)
  STEP1(15, Y, X, F0, F1, F2, F3, F4)
  STEP2(16, F2, F3, F4, F0, F1)
  STEP2(17, F4, F0, F1, F2, F3)
  STEP2(18, F1, F2, F3, F4, F0)
  STEP2(19, F3, F4, F0, F1, F2)
  STEP2(20, F0, F1, F2, F3, F4)
  STEP2(21, F2, F3, F4, F0, F1)
  STEP2(22, F4, F0, F1, F2, F3)
  STEP2(23, F1, F2, F3, F4, F0)
  STEP2(24, F3, F4, F0, F1, F2)
  STEP2(25, F0, F1, F2, F3, F4)
  STEP2(26, F2, F3, F4, F0, F1)
  STEP2(27, F4, F0, F1, F2, F3)
  STEP2(28, F1, F2, F3, F4, F0)
  STEP2(29, F3, F4, F0, F1, F2)
  STEP2(30, F0, F1, F2, F3, F4)
  STEP2(31, F2, F3, F4, F0, F1)
  __syncthreads();
  float* G1 = (float*)smem + q*2112;
  float* G2 = (float*)smem + 2*2112 + q*1088;
  #pragma unroll
  for (int mt=0;mt<2;mt++)
    #pragma unroll
    for (int nt=0;nt<2;nt++)
      #pragma unroll
      for (int i=0;i<4;i++){
        int l = 64*(2*(4*kg+i)+mt) + 16*h + 32*nt + m;
        G1[l + 16*(l>>9)] = P[mt][nt][i];
      }
  #pragma unroll
  for (int nt=0;nt<2;nt++)
    #pragma unroll
    for (int i=0;i<4;i++){
      int l = 64*(4*kg+i) + 16*h + 32*nt + m;
      G2[l + 16*(l>>8)] = Q[nt][i];
    }
  __syncthreads();
  {
    const float* A1 = (const float*)smem; const float* A2 = A1 + 2*2112;
    int l0 = 8*t;
    int i1 = l0 + 16*(l0>>9);
    int l2 = l0 - 1024; int i2 = l2 + 16*(l2>>8);
    if (isA){
      float v[8];
      #pragma unroll
      for (int j=0;j<8;j++) v[j] = A1[i1+j] + A1[2112+i1+j];
      if (l0 >= 1024){
        #pragma unroll
        for (int j=0;j<8;j++) v[j] += A2[i2+j] + A2[1088+i2+j];
      }
      *(uint4*)(yT + d*L_ + l0) = make_uint4(pk2(v[0],v[1]),pk2(v[2],v[3]),pk2(v[4],v[5]),pk2(v[6],v[7]));
    } else {
      #pragma unroll
      for (int a=0;a<2;a++){
        float v[8];
        #pragma unroll
        for (int j=0;j<8;j++) v[j] = A1[a*2112+i1+j];
        if (l0 >= 1024){
          #pragma unroll
          for (int j=0;j<8;j++) v[j] += A2[a*1088+i2+j];
        }
        *(uint4*)(yT + (D_ + d + a)*L_ + l0) = make_uint4(pk2(v[0],v[1]),pk2(v[2],v[3]),pk2(v[4],v[5]),pk2(v[6],v[7]));
      }
    }
  }
}

__global__ __launch_bounds__(256) void post_kernel(const u16* __restrict__ yT, const u16* __restrict__ u,
    const u16* __restrict__ Dskip, const u16* __restrict__ q, const u16* __restrict__ k, const u16* __restrict__ v,
    const u16* __restrict__ shift_k, const u16* __restrict__ ssmD, u16* __restrict__ out){
  __shared__ u16 tile[64][64+8];
  int l0 = blockIdx.x*64, d0 = blockIdx.y*64, part = blockIdx.z;
  int t = threadIdx.x;
  int c8 = (t & 7)*8, r = t >> 3;
  for (int dd = r; dd < 64; dd += 32)
    *(uint4*)&tile[dd][c8] = *(const uint4*)(yT + (part*D_ + d0+dd)*L_ + l0 + c8);
  __syncthreads();
  for (int rr = r; rr < 64; rr += 32){
    int l = l0 + rr;
    u16 o[8];
    if (part == 0){
      u16 uu[8]; *(uint4*)uu = *(const uint4*)(u + l*D_ + d0 + c8);
      #pragma unroll
      for (int i=0;i<8;i++) o[i] = f2bf(bf2f(tile[c8+i][rr]) + rbf(bf2f(uu[i])*bf2f(Dskip[d0+c8+i])));
    } else {
      u16 kk[4][8], vv[8], qq[8];
      #pragma unroll
      for (int j=0;j<4;j++){
        if (l-j >= 0) *(uint4*)kk[j] = *(const uint4*)(k + (l-j)*D_ + d0 + c8);
        else *(uint4*)kk[j] = make_uint4(0,0,0,0);
      }
      *(uint4*)vv = *(const uint4*)(v + l*D_ + d0 + c8);
      *(uint4*)qq = *(const uint4*)(q + l*D_ + d0 + c8);
      #pragma unroll
      for (int i=0;i<8;i++){
        const u16* w = shift_k + (d0+c8+i)*4;
        float acc = rbf(bf2f(kk[3][i])*bf2f(w[3]));
        acc = rbf(acc + rbf(bf2f(kk[2][i])*bf2f(w[2])));
        acc = rbf(acc + rbf(bf2f(kk[1][i])*bf2f(w[1])));
        acc = rbf(acc + rbf(bf2f(kk[0][i])*bf2f(w[0])));
        float kv = rbf(acc*bf2f(vv[i]));
        float y = rbf(bf2f(tile[c8+i][rr]) + rbf(kv*bf2f(ssmD[d0+c8+i])));
        o[i] = f2bf(bf2f(qq[i])*y);
      }
    }
    *(uint4*)(out + l*(2*D_) + part*D_ + d0 + c8) = *(const uint4*)o;
  }
}

extern "C" void kernel_launch(void* const* d_in, const int* in_sizes, int n_in,
                              void* d_out, int out_size, void* d_ws, size_t ws_size,
                              hipStream_t stream) {
  const u16* u = (const u16*)d_in[0];
  const u16* logA = (const u16*)d_in[1];
  const u16* Aim = (const u16*)d_in[2];
  const u16* Cre = (const u16*)d_in[3];
  const u16* Cim = (const u16*)d_in[4];
  const u16* logdt = (const u16*)d_in[5];
  const u16* Dskip = (const u16*)d_in[6];
  const u16* q = (const u16*)d_in[7];
  const u16* k = (const u16*)d_in[8];
  const u16* v = (const u16*)d_in[9];
  const u16* shift_k = (const u16*)d_in[10];
  const u16* ssm_k = (const u16*)d_in[11];
  const u16* ssmD = (const u16*)d_in[12];
  u16* out = (u16*)d_out;
  u16* ws = (u16*)d_ws;
  const size_t S = (size_t)L_*D_;
  u16* uT = ws; u16* kvT = ws + S; u16* Khi = ws + 2*S; u16* Klo = ws + 3*S; u16* yT = ws + 4*S;
  pre_kernel<<<512, 256, 0, stream>>>(u, k, v, shift_k, uT, kvT, logA, Aim, Cre, Cim, logdt, Khi, Klo);
  conv_kernel<<<768, 256, CONV_LDS, stream>>>(uT, kvT, Khi, Klo, ssm_k, yT);
  post_kernel<<<dim3(32,8,2), 256, 0, stream>>>(yT, u, Dskip, q, k, v, shift_k, ssmD, out);
}
```

```cpp
#include <hip/hip_runtime.h>
#include <hip/hip_bf16.h>
#include <stdint.h>

typedef unsigned short u16;
typedef short bf16x8 __attribute__((ext_vector_type(8)));
typedef float f32x4 __attribute__((ext_vector_type(4)));
typedef float f32x2 __attribute__((ext_vector_type(2)));
typedef __bf16 bf16x2_t __attribute__((ext_vector_type(2)));

#define L_ 2048
#define D_ 512
#define NS_ 128

__device__ __forceinline__ float bf2f(u16 x){ return __builtin_bit_cast(float, (uint32_t)x << 16); }
__device__ __forceinline__ u16 f2bf(float f){ __hip_bfloat16 h = __float2bfloat16(f); return __builtin_bit_cast(u16, h); }
__device__ __forceinline__ float rbf(float f){ return bf2f(f2bf(f)); }
__device__ __forceinline__ uint32_t pk2(float a, float b){
  f32x2 v = {a,b}; bf16x2_t r = __builtin_convertvector(v, bf16x2_t); return __builtin_bit_cast(uint32_t, r); }
__device__ __forceinline__ float lo16f(uint32_t w){ return __builtin_bit_cast(float, w << 16); }
__device__ __forceinline__ float hi16f(uint32_t w){ return __builtin_bit_cast(float, w & 0xffff0000u); }

struct cpx { float re, im; };
__device__ __forceinline__ float amul(float a, float b){ float r; asm("v_mul_f32 %0, %1, %2" : "=v"(r) : "v"(a), "v"(b)); return r; }
__device__ __forceinline__ float afma(float a, float b, float c){ float r; asm("v_fma_f32 %0, %1, %2, %3" : "=v"(r) : "v"(a), "v"(b), "v"(c)); return r; }
__device__ __forceinline__ float afms(float a, float b, float c){ float r; asm("v_fma_f32 %0, %1, %2, -%3" : "=v"(r) : "v"(a), "v"(b), "v"(c)); return r; }
__device__ __forceinline__ cpx cmul(cpx a, cpx b){ cpx r; r.re = afms(a.re, b.re, amul(a.im,b.im)); r.im = afma(a.re, b.im, amul(a.im,b.re)); return r; }
__device__ __forceinline__ cpx cexp_(float re, float im){ float e = expf(re); float s,c; sincosf(im,&s,&c); cpx r; r.re=e*c; r.im=e*s; return r; }

__global__ __launch_bounds__(256) void pre_kernel(const u16* __restrict__ u, const u16* __restrict__ k,
    const u16* __restrict__ v, const u16* __restrict__ q, const u16* __restrict__ shift_k,
    u16* __restrict__ uT, u16* __restrict__ kvT, u16* __restrict__ qT){
  __shared__ __attribute__((aligned(16))) char smem[64*144];
  const int t = threadIdx.x;
  const int b = blockIdx.x;
  const int which = b >> 8;
  const int l0 = ((b & 255) >> 3)*64, d0 = (b & 7)*64;
  const int c8 = (t & 7)*8, rg = t >> 3;
  uint32_t o[2][4];
  if (which != 1){
    const u16* src = which == 0 ? u : q;
    uint4 a = *(const uint4*)(src + (l0+2*rg)*D_ + d0 + c8);
    uint4 c = *(const uint4*)(src + (l0+2*rg+1)*D_ + d0 + c8);
    o[0][0]=a.x;o[0][1]=a.y;o[0][2]=a.z;o[0][3]=a.w;
    o[1][0]=c.x;o[1][1]=c.y;o[1][2]=c.z;o[1][3]=c.w;
  } else {
    uint4 kk[5], vv[2], wq[4];
    #pragma unroll
    for (int j=0;j<5;j++){ int l = l0+2*rg+j-3; int lc = l < 0 ? 0 : l; kk[j] = *(const uint4*)(k + lc*D_ + d0 + c8); }
    vv[0] = *(const uint4*)(v + (l0+2*rg)*D_ + d0 + c8);
    vv[1] = *(const uint4*)(v + (l0+2*rg+1)*D_ + d0 + c8);
    #pragma unroll
    for (int j=0;j<4;j++) wq[j] = *(const uint4*)(shift_k + (d0+c8)*4 + 8*j);
    #pragma unroll
    for (int j=0;j<3;j++) if (l0+2*rg+j-3 < 0) kk[j] = make_uint4(0,0,0,0);
    #pragma unroll
    for (int p=0;p<4;p++){
      const uint32_t* wp = (const uint32_t*)&wq[p];
      #pragma unroll
      for (int j=0;j<2;j++){
        float r2[2];
        #pragma unroll
        for (int e=0;e<2;e++){
          float w0 = e? lo16f(wp[2]) : lo16f(wp[0]); float w1 = e? hi16f(wp[2]) : hi16f(wp[0]);
          float w2 = e? lo16f(wp[3]) : lo16f(wp[1]); float w3 = e? hi16f(wp[3]) : hi16f(wp[1]);
          auto g = [&](int idx){ uint32_t w = ((const uint32_t*)&kk[idx])[p]; return e? hi16f(w) : lo16f(w); };
          float acc = rbf(g(j)*w3);
          acc = rbf(acc + rbf(g(j+1)*w2));
          acc = rbf(acc + rbf(g(j+2)*w1));
          acc = rbf(acc + rbf(g(j+3)*w0));
          uint32_t vw = ((const uint32_t*)&vv[j])[p];
          r2[e] = acc * (e? hi16f(vw) : lo16f(vw));
        }
        o[j][p] = pk2(r2[0], r2[1]);
      }
    }
  }
  const int TS = 144;
  #pragma unroll
  for (int p=0;p<4;p++){
    *(uint32_t*)(smem + (c8+2*p)*TS + rg*4)   = __builtin_amdgcn_perm(o[1][p], o[0][p], 0x05040100);
    *(uint32_t*)(smem + (c8+2*p+1)*TS + rg*4) = __builtin_amdgcn_perm(o[1][p], o[0][p], 0x07060302);
  }
  __syncthreads();
  u16* dst = which == 0 ? uT : (which == 1 ? kvT : qT);
  const int lc = (t & 7)*8, dr = t >> 3;
  #pragma unroll
  for (int i=0;i<2;i++){
    int dd = dr + 32*i;
    uint4 x = *(const uint4*)(smem + dd*TS + lc*2);
    *(uint4*)(dst + (d0+dd)*L_ + l0 + lc) = x;
  }
}

#define CSTRIDE 4416
#define CONV_LDS 49664

__device__ __forceinline__ bf16x8 shr1(bf16x8 x){
  uint4 t = __builtin_bit_cast(uint4, x);
  t.x = __builtin_amdgcn_update_dpp(0, (int)t.x, 0x111, 0xf, 0xf, true);
  t.y = __builtin_amdgcn_update_dpp(0, (int)t.y, 0x111, 0xf, 0xf, true);
  t.z = __builtin_amdgcn_update_dpp(0, (int)t.z, 0x111, 0xf, 0xf, true);
  t.w = __builtin_amdgcn_update_dpp(0, (int)t.w, 0x111, 0xf, 0xf, true);
  return __builtin_bit_cast(bf16x8, t);
}
__device__ __forceinline__ uint2 ldsv(const char* p){
  typedef uint32_t u32x2 __attribute__((ext_vector_type(2)));
  u32x2 v = *(const volatile u32x2*)p; return make_uint2(v.x, v.y); }
typedef uint32_t u32x2 __attribute__((ext_vector_type(2)));
struct Frag { u32x2 a, b; };
__device__ __forceinline__ bf16x8 fr(const Frag& f){ uint4 z = make_uint4(f.a.x, f.a.y, f.b.x, f.b.y); return __builtin_bit_cast(bf16x8, z); }
#define MFMA16(a,b,c) __builtin_amdgcn_mfma_f32_16x16x32_bf16(a,b,c,0,0,0)

#define TR 272
#define TLO (57*TR)
#define KCOFF 35328
__device__ __forceinline__ void put_split(char* p, float a, float b){
  uint32_t h = pk2(a,b);
  uint32_t l = pk2(a - lo16f(h), b - hi16f(h));
  *(uint32_t*)p = h; *(uint32_t*)(p + TLO) = l;
}

__global__ __launch_bounds__(256) void conv_kernel(const u16* __restrict__ uT, const u16* __restrict__ kvT,
    const u16* __restrict__ qT, const u16* __restrict__ ssmk,
    const u16* __restrict__ logA, const u16* __restrict__ Aim,
    const u16* __restrict__ Cre, const u16* __restrict__ Cim, const u16* __restrict__ logdt,
    const u16* __restrict__ Dskip, const u16* __restrict__ ssmD, u16* __restrict__ oT){
  extern __shared__ __attribute__((aligned(16))) char smem[];
  const int bid = blockIdx.x;
  const bool isA = bid < 512;
  const int t = threadIdx.x, wave = t >> 6, lane = t & 63;
  const int q = wave >> 1, h = wave & 1;
  const int m = lane & 15, kg = lane >> 4;
  const int d = isA ? bid : (bid - 512) * 2;
  const u16* x = isA ? uT + d*L_ : kvT + (d+q)*L_;
  uint32_t w[2][6];
  if (isA){
    u16 pa[2][4];
    #pragma unroll
    for (int hp=0;hp<2;hp++){
      pa[hp][0] = logA[d*NS_+64*hp+lane]; pa[hp][1] = Aim[d*NS_+64*hp+lane];
      pa[hp][2] = Cre[d*NS_+64*hp+lane];  pa[hp][3] = Cim[d*NS_+64*hp+lane];
    }
    const float dt = rbf(expf(bf2f(logdt[d])));
    f32x4 a4 = {0,0,0,0};
    char* col = smem + 4*lane;
    #pragma unroll 1
    for (int hp=0;hp<2;hp++){
      const float zre = -rbf(expf(bf2f(hp ? pa[1][0] : pa[0][0]))) * dt;
      const float zim = bf2f(hp ? pa[1][1] : pa[0][1]) * dt;
      cpx C; C.re = bf2f(hp ? pa[1][2] : pa[0][2])*dt; C.im = bf2f(hp ? pa[1][3] : pa[0][3])*dt;
      const float f1 = wave == 0 ? 8.f : 128.f;
      const float f2 = wave == 1 ? 1024.f : (wave == 2 ? 1.f : 2.f);
      cpx e1 = cexp_(zre*f1, zim*f1);
      cpx e2 = cexp_(zre*f2, zim*f2);
      if (wave == 0){
        cpx xx; xx.re = 1.f; xx.im = 0.f;
        #pragma unroll 1
        for (int i=0;i<16;i++){ put_split(col + (41+i)*TR, xx.re, -xx.im); xx = cmul(xx, e1); }
      } else if (wave == 1){
        cpx cb = cmul(C, e2);
        #pragma unroll 1
        for (int i=0;i<9;i++){ put_split(col + (32+i)*TR, cb.re, cb.im); cb = cmul(cb, e1); }
      } else if (wave == 2){
        cpx S[8];
        S[1] = e2;
        #pragma unroll
        for (int i=2;i<8;i++) S[i] = cmul(S[i-1], S[1]);
        cpx cb = C;
        #pragma unroll 1
        for (int a=0;a<2;a++){
          put_split(col + (8*a)*TR, cb.re, cb.im);
          #pragma unroll
          for (int p=1;p<8;p++){ cpx g = cmul(cb, S[p]); put_split(col + (8*a+p)*TR, g.re, g.im); }
          cb = cmul(cb, e1);
        }
      } else {
        cpx S2 = e2, S4 = cmul(S2, S2), S6 = cmul(S4, S2);
        cpx cb = cmul(C, cmul(e1, e1));
        #pragma unroll 1
        for (int a=0;a<2;a++){
          put_split(col + (16+4*a)*TR, cb.re, cb.im);
          { cpx g = cmul(cb, S2); put_split(col + (16+4*a+1)*TR, g.re, g.im); }
          { cpx g = cmul(cb, S4); put_split(col + (16+4*a+2)*TR, g.re, g.im); }
          { cpx g = cmul(cb, S6); put_split(col + (16+4*a+3)*TR, g.re, g.im); }
          cb = cmul(cb, e1);
        }
        #pragma unroll 1
        for (int a=0;a<4;a++){
          put_split(col + (24+2*a)*TR, cb.re, cb.im);
          cpx g = cmul(cb, S4); put_split(col + (24+2*a+1)*TR, g.re, g.im);
          cb = cmul(cb, e1);
        }
      }
      __syncthreads();
      if (wave < 3){
        const char* ga = smem + (16*wave + m)*TR + 16*kg;
        const char* xb = smem + (41 + m)*TR + 16*kg;
        #pragma unroll
        for (int kc=0;kc<4;kc++){
          bf16x8 ah = *(const bf16x8*)(ga + 64*kc), al = *(const bf16x8*)(ga + 64*kc + TLO);
          bf16x8 bh = *(const bf16x8*)(xb + 64*kc), bl = *(const bf16x8*)(xb + 64*kc + TLO);
          a4 = MFMA16(ah, bh, a4);
          a4 = MFMA16(ah, bl, a4);
          a4 = MFMA16(al, bh, a4);
        }
      }
      if (hp == 0) __syncthreads();
    }
    float* Kc = (float*)(smem + KCOFF);
    if (wave < 3){
      #pragma unroll
      for (int i=0;i<4;i++){
        int r = 16*wave + 4*kg + i;
        int idx;
        if (r < 16) idx = 128*(r>>3) + (r&7) + 8*m;
        else if (r < 24) idx = 256 + 64*((r-16)>>2) + (r&3) + 4*m;
        else if (r < 32) idx = 384 + 32*((r-24)>>1) + (r&1) + 2*m;
        else idx = 512 + 16*(r-32) + m;
        if (r < 40 || (r == 40 && m == 0)) Kc[idx] = 2.f*a4[i];
      }
    }
    __syncthreads();
    float kv[12];
    #pragma unroll
    for (int j=0;j<12;j++){
      int l = 2047 - 8*t - j; int lc = l < 0 ? 0 : l;
      int pr = (int)rbf((float)lc);
      int p = pr < 256 ? pr : (pr < 512 ? 128 + (pr>>1) : (pr < 1024 ? 256 + (pr>>2) : 384 + (pr>>3)));
      float xv = Kc[p];
      kv[j] = l < 0 ? 0.f : xv;
    }
    #pragma unroll
    for (int j=0;j<6;j++){
      uint32_t hh = pk2(kv[2*j], kv[2*j+1]);
      w[0][j] = hh; w[1][j] = pk2(kv[2*j] - lo16f(hh), kv[2*j+1] - hi16f(hh));
    }
  } else {
    int o2 = t < 255 ? 2036 - 8*t : 0;
    #pragma unroll
    for (int a=0;a<2;a++){
      const u16* K0 = ssmk + (d+a)*L_;
      uint4 xa = *(const uint4*)(K0 + 2040 - 8*t);
      uint2 ya = *(const uint2*)(K0 + o2);
      w[a][0] = __builtin_amdgcn_alignbit(xa.w, xa.w, 16);
      w[a][1] = __builtin_amdgcn_alignbit(xa.z, xa.z, 16);
      w[a][2] = __builtin_amdgcn_alignbit(xa.y, xa.y, 16);
      w[a][3] = __builtin_amdgcn_alignbit(xa.x, xa.x, 16);
      w[a][4] = t < 255 ? __builtin_amdgcn_alignbit(ya.y,ya.y,16) : 0u;
      w[a][5] = t < 255 ? __builtin_amdgcn_alignbit(ya.x,ya.x,16) : 0u;
    }
  }
  bf16x8 X[2], Y[2], Z[2];
  #pragma unroll
  for (int ch=0;ch<2;ch++){
    X[ch] = *(const bf16x8*)(x + 64*(2*m)   + 32*ch + 8*kg);
    Y[ch] = *(const bf16x8*)(x + 64*(2*m+1) + 32*ch + 8*kg);
    Z[ch] = *(const bf16x8*)(x + 64*m       + 32*ch + 8*kg);
  }
  {
    if (t < 80) *(uint4*)(smem + (t/10)*CSTRIDE + 4096 + 16*(t%10)) = make_uint4(0,0,0,0);
    #pragma unroll
    for (int a=0;a<2;a++){
      char* base = smem + a*4*CSTRIDE;
      uint32_t al[5];
      #pragma unroll
      for (int i=0;i<5;i++) al[i] = __builtin_amdgcn_alignbit(w[a][i+1], w[a][i], 16);
      *(uint4*)(base + 0*CSTRIDE + 16*t) = make_uint4(w[a][0],w[a][1],w[a][2],w[a][3]);
      *(uint4*)(base + 1*CSTRIDE + 16*t) = make_uint4(al[0],al[1],al[2],al[3]);
      *(uint4*)(base + 2*CSTRIDE + 16*t) = make_uint4(w[a][1],w[a][2],w[a][3],w[a][4]);
      *(uint4*)(base + 3*CSTRIDE + 16*t) = make_uint4(al[1],al[2],al[3],al[4]);
    }
  }
  __syncthreads();
  f32x4 P[2][2], Q[2];
  #pragma unroll
  for (int i=0;i<2;i++){ Q[i] = f32x4{0,0,0,0}; for (int j=0;j<2;j++) P[i][j] = f32x4{0,0,0,0}; }
  const int s0 = 2047 - 16*h - m + 8*kg;
  const int r = s0 & 3;
  const uint32_t fbase = (uint32_t)(uintptr_t)smem + q*4*CSTRIDE + r*CSTRIDE + 2*(s0 - r) - 4032;
#define LDF(F, o) asm volatile("ds_read_b64 %0, %2 offset:%3\n\tds_read_b64 %1, %2 offset:%4" : "=v"(F.a), "=v"(F.b) : "v"(fbase), "i"(4032-2*(o)), "i"(4040-2*(o)))
#define WAITF(n, F, G) asm volatile("s_waitcnt lgkmcnt(" #n ")" : "+v"(F.a), "+v"(F.b), "+v"(G.a), "+v"(G.b))
  Frag F0, F1, F2, F3, F4;
  LDF(F0, -32); LDF(F1, 0); LDF(F2, 32);
  asm volatile("s_waitcnt lgkmcnt(4)" : "+v"(F0.a), "+v"(F0.b));
#define STEP1(c, EV, OD, LO, MM, HH, NM, NH) \
    LDF(NM, 64*(c)+64); LDF(NH, 64*(c)+96); WAITF(4, MM, HH); \
    P[0][0] = MFMA16(EV[0], fr(MM), P[0][0]); P[0][1] = MFMA16(EV[0], fr(HH), P[0][1]); \
    P[1][0] = MFMA16(OD[0], fr(MM), P[1][0]); P[1][1] = MFMA16(OD[0], fr(HH), P[1][1]); \
    P[0][0] = MFMA16(EV[1], fr(LO), P[0][0]); P[0][1] = MFMA16(EV[1], fr(MM), P[0][1]); \
    P[1][0] = MFMA16(OD[1], fr(LO), P[1][0]); P[1][1] = MFMA16(OD[1], fr(MM), P[1][1]); \
    OD[0] = shr1(OD[0]); OD[1] = shr1(OD[1]);
#define STEP2(c, LO, MM, HH, NM, NH) \
    if ((c) < 31){ LDF(NM, 64*(c)+64); LDF(NH, 64*(c)+96); WAITF(4, MM, HH); } else { WAITF(0, MM, HH); } \
    Q[0] = MFMA16(Z[0], fr(MM), Q[0]); Q[1] = MFMA16(Z[0], fr(HH), Q[1]); \
    Q[0] = MFMA16(Z[1], fr(LO), Q[0]); Q[1] = MFMA16(Z[1], fr(MM), Q[1]); \
    Z[0] = shr1(Z[0]); Z[1] = shr1(Z[1]);
  STEP1(0, X, Y, F0, F1, F2, F3, F4)
  STEP1(1, Y, X, F2, F3, F4, F0, F1)
  STEP1(2, X, Y, F4, F0, F1, F2, F3)
  STEP1(3, Y, X, F1, F2, F3, F4, F0)
  STEP1(4, X, Y, F3, F4, F0, F1, F2)
  STEP1(5, Y, X, F0, F1, F2, F3, F4)
  STEP1(6, X, Y, F2, F3, F4, F0, F1)
  STEP1(7, Y, X, F4, F0, F1, F2, F3)
  STEP1(8, X, Y, F1, F2, F3, F4, F0)
  STEP1(9, Y, X, F3, F4, F0, F1, F2)
  STEP1(10, X, Y, F0, F1, F2, F3, F4)
  STEP1(11, Y, X, F2, F3, F4, F0, F1)
  STEP1(12, X, Y, F4, F0, F1, F2, F3)
  STEP1(13, Y, X, F1, F2, F3, F4, F0)
  STEP1(14, X, Y, F3, F4, F0, F1, F2)
  STEP1(15, Y, X, F0, F1, F2, F3, F4)
  STEP2(16, F2, F3, F4, F0, F1)
  STEP2(17, F4, F0, F1, F2, F3)
  STEP2(18, F1, F2, F3, F4, F0)
  STEP2(19, F3, F4, F0, F1, F2)
  STEP2(20, F0, F1, F2, F3, F4)
  STEP2(21, F2, F3, F4, F0, F1)
  STEP2(22, F4, F0, F1, F2, F3)
  STEP2(23, F1, F2, F3, F4, F0)
  STEP2(24, F3, F4, F0, F1, F2)
  STEP2(25, F0, F1, F2, F3, F4)
  STEP2(26, F2, F3, F4, F0, F1)
  STEP2(27, F4, F0, F1, F2, F3)
  STEP2(28, F1, F2, F3, F4, F0)
  STEP2(29, F3, F4, F0, F1, F2)
  STEP2(30, F0, F1, F2, F3, F4)
  STEP2(31, F2, F3, F4, F0, F1)
  uint4 e0, e1, e2, e3; float sc0, sc1;
  {
    const int l0 = 8*t;
    if (isA){
      e0 = *(const uint4*)(uT + d*L_ + l0); e1 = e0; e2 = e0; e3 = e0;
      sc0 = bf2f(Dskip[d]); sc1 = sc0;
    } else {
      e0 = *(const uint4*)(kvT + d*L_ + l0); e1 = *(const uint4*)(kvT + (d+1)*L_ + l0);
      e2 = *(const uint4*)(qT + d*L_ + l0);  e3 = *(const uint4*)(qT + (d+1)*L_ + l0);
      sc0 = bf2f(ssmD[d]); sc1 = bf2f(ssmD[d+1]);
    }
  }
  __syncthreads();
  float* G1 = (float*)smem + q*2112;
  float* G2 = (float*)smem + 2*2112 + q*1088;
  #pragma unroll
  for (int mt=0;mt<2;mt++)
    #pragma unroll
    for (int nt=0;nt<2;nt++)
      #pragma unroll
      for (int i=0;i<4;i++){
        int l = 64*(2*(4*kg+i)+mt) + 16*h + 32*nt + m;
        G1[l + 16*(l>>9)] = P[mt][nt][i];
      }
  #pragma unroll
  for (int nt=0;nt<2;nt++)
    #pragma unroll
    for (int i=0;i<4;i++){
      int l = 64*(4*kg+i) + 16*h + 32*nt + m;
      G2[l + 16*(l>>8)] = Q[nt][i];
    }
  __syncthreads();
  {
    const float* A1 = (const float*)smem; const float* A2 = A1 + 2*2112;
    int l0 = 8*t;
    int i1 = l0 + 16*(l0>>9);
    int l2 = l0 - 1024; int i2 = l2 + 16*(l2>>8);
    if (isA){
      float v[8];
      #pragma unroll
      for (int j=0;j<8;j++) v[j] = A1[i1+j] + A1[2112+i1+j];
      if (l0 >= 1024){
        #pragma unroll
        for (int j=0;j<8;j++) v[j] += A2[i2+j] + A2[1088+i2+j];
      }
      const uint32_t* up = (const uint32_t*)&e0;
      #pragma unroll
      for (int j=0;j<8;j++){
        float uu = (j&1) ? hi16f(up[j>>1]) : lo16f(up[j>>1]);
        v[j] = rbf(v[j]) + rbf(uu*sc0);
      }
      *(uint4*)(oT + d*L_ + l0) = make_uint4(pk2(v[0],v[1]),pk2(v[2],v[3]),pk2(v[4],v[5]),pk2(v[6],v[7]));
    } else {
      #pragma unroll
      for (int a=0;a<2;a++){
        float v[8];
        #pragma unroll
        for (int j=0;j<8;j++) v[j] = A1[a*2112+i1+j];
        if (l0 >= 1024){
          #pragma unroll
          for (int j=0;j<8;j++) v[j] += A2[a*1088+i2+j];
        }
        const uint32_t* kp = a ? (const uint32_t*)&e1 : (const uint32_t*)&e0;
        const uint32_t* qp = a ? (const uint32_t*)&e3 : (const uint32_t*)&e2;
        const float sd = a ? sc1 : sc0;
        #pragma unroll
        for (int j=0;j<8;j++){
          float kk = (j&1) ? hi16f(kp[j>>1]) : lo16f(kp[j>>1]);
          float qq = (j&1) ? hi16f(qp[j>>1]) : lo16f(qp[j>>1]);
          float y = rbf(rbf(v[j]) + rbf(kk*sd));
          v[j] = qq*y;
        }
        *(uint4*)(oT + (D_ + d + a)*L_ + l0) = make_uint4(pk2(v[0],v[1]),pk2(v[2],v[3]),pk2(v[4],v[5]),pk2(v[6],v[7]));
      }
    }
  }
}
__global__ __launch_bounds__(256) void post_kernel(const u16* __restrict__ oT, u16* __restrict__ out){
  __shared__ __attribute__((aligned(16))) char smem[64*144];
  const int t = threadIdx.x;
  const int b = blockIdx.x;
  const int c0 = (b >> 5)*64, l0 = (b & 31)*64;
  const int c8 = (t & 7)*8, rg = t >> 3;
  uint4 a = *(const uint4*)(oT + (c0+2*rg)*L_ + l0 + c8);
  uint4 c = *(const uint4*)(oT + (c0+2*rg+1)*L_ + l0 + c8);
  uint32_t o0[4] = {a.x,a.y,a.z,a.w}, o1[4] = {c.x,c.y,c.z,c.w};
  const int TS = 144;
  #pragma unroll
  for (int p=0;p<4;p++){
    *(uint32_t*)(smem + (c8+2*p)*TS + rg*4)   = __builtin_amdgcn_perm(o1[p], o0[p], 0x05040100);
    *(uint32_t*)(smem + (c8+2*p+1)*TS + rg*4) = __builtin_amdgcn_perm(o1[p], o0[p], 0x07060302);
  }
  __syncthreads();
  const int lc = (t & 7)*8, dr = t >> 3;
  #pragma unroll
  for (int i=0;i<2;i++){
    int dd = dr + 32*i;
    uint4 x = *(const uint4*)(smem + dd*TS + lc*2);
    *(uint4*)(out + (l0+dd)*(2*D_) + c0 + lc) = x;
  }
}

extern "C" void kernel_launch(void* const* d_in, const int* in_sizes, int n_in,
                              void* d_out, int out_size, void* d_ws, size_t ws_size,
                              hipStream_t stream) {
  const u16* u = (const u16*)d_in[0];
  const u16* logA = (const u16*)d_in[1];
  const u16* Aim = (const u16*)d_in[2];
  const u16* Cre = (const u16*)d_in[3];
  const u16* Cim = (const u16*)d_in[4];
  const u16* logdt = (const u16*)d_in[5];
  const u16* Dskip = (const u16*)d_in[6];
  const u16* q = (const u16*)d_in[7];
  const u16* k = (const u16*)d_in[8];
  const u16* v = (const u16*)d_in[9];
  const u16* shift_k = (const u16*)d_in[10];
  const u16* ssm_k = (const u16*)d_in[11];
  const u16* ssmD = (const u16*)d_in[12];
  u16* out = (u16*)d_out;
  u16* ws = (u16*)d_ws;
  const size_t S = (size_t)L_*D_;
  u16* uT = ws; u16* kvT = ws + S; u16* qT = ws + 2*S; u16* oT = ws + 4*S;
  pre_kernel<<<768, 256, 0, stream>>>(u, k, v, q, shift_k, uT, kvT, qT);
  conv_kernel<<<768, 256, CONV_LDS, stream>>>(uT, kvT, qT, ssm_k, logA, Aim, Cre, Cim, logdt, Dskip, ssmD, oT);
  post_kernel<<<512, 256, 0, stream>>>(oT, out);
}
```

```cpp
#include <hip/hip_runtime.h>
#include <hip/hip_bf16.h>
#include <stdint.h>

typedef unsigned short u16;
typedef short bf16x8 __attribute__((ext_vector_type(8)));
typedef float f32x4 __attribute__((ext_vector_type(4)));
typedef float f32x2 __attribute__((ext_vector_type(2)));
typedef __bf16 bf16x2_t __attribute__((ext_vector_type(2)));

#define L_ 2048
#define D_ 512
#define NS_ 128

__device__ __forceinline__ float bf2f(u16 x){ return __builtin_bit_cast(float, (uint32_t)x << 16); }
__device__ __forceinline__ u16 f2bf(float f){ __hip_bfloat16 h = __float2bfloat16(f); return __builtin_bit_cast(u16, h); }
__device__ __forceinline__ float rbf(float f){ return bf2f(f2bf(f)); }
__device__ __forceinline__ uint32_t pk2(float a, float b){
  f32x2 v = {a,b}; bf16x2_t r = __builtin_convertvector(v, bf16x2_t); return __builtin_bit_cast(uint32_t, r); }
__device__ __forceinline__ float lo16f(uint32_t w){ return __builtin_bit_cast(float, w << 16); }
__device__ __forceinline__ float hi16f(uint32_t w){ return __builtin_bit_cast(float, w & 0xffff0000u); }

struct cpx { float re, im; };
__device__ __forceinline__ float amul(float a, float b){ float r; asm("v_mul_f32 %0, %1, %2" : "=v"(r) : "v"(a), "v"(b)); return r; }
__device__ __forceinline__ float afma(float a, float b, float c){ float r; asm("v_fma_f32 %0, %1, %2, %3" : "=v"(r) : "v"(a), "v"(b), "v"(c)); return r; }
__device__ __forceinline__ float afms(float a, float b, float c){ float r; asm("v_fma_f32 %0, %1, %2, -%3" : "=v"(r) : "v"(a), "v"(b), "v"(c)); return r; }
__device__ __forceinline__ cpx cmul(cpx a, cpx b){ cpx r; r.re = afms(a.re, b.re, amul(a.im,b.im)); r.im = afma(a.re, b.im, amul(a.im,b.re)); return r; }
__device__ __forceinline__ cpx cexp_(float re, float im){ float e = expf(re); float s,c; sincosf(im,&s,&c); cpx r; r.re=e*c; r.im=e*s; return r; }

#define KC_ 656
#define MFMA16(a,b,c) __builtin_amdgcn_mfma_f32_16x16x32_bf16(a,b,c,0,0,0)
#define TR 272
#define TLO (57*TR)
__device__ __forceinline__ void put_split(char* p, float a, float b){
  uint32_t h = pk2(a,b);
  uint32_t l = pk2(a - lo16f(h), b - hi16f(h));
  *(uint32_t*)p = h; *(uint32_t*)(p + TLO) = l;
}

__global__ __launch_bounds__(256) void pre_kernel(const u16* __restrict__ u, const u16* __restrict__ k,
    const u16* __restrict__ v, const u16* __restrict__ q, const u16* __restrict__ shift_k,
    u16* __restrict__ uT, u16* __restrict__ kvT, u16* __restrict__ qT,
    const u16* __restrict__ logA, const u16* __restrict__ Aim,
    const u16* __restrict__ Cre, const u16* __restrict__ Cim, const u16* __restrict__ logdt,
    float* __restrict__ Kcg){
  __shared__ __attribute__((aligned(16))) char smem[2*TLO + 2048];
  const int t = threadIdx.x;
  if (blockIdx.x < 512){
    const int d = blockIdx.x, wave = t >> 6, lane = t & 63, m = lane & 15, kg = lane >> 4;
    const float dt = rbf(expf(bf2f(logdt[d])));
    u16 pc[2][2];
    #pragma unroll
    for (int hp=0;hp<2;hp++){ pc[hp][0] = Cre[d*NS_+64*hp+lane];  pc[hp][1] = Cim[d*NS_+64*hp+lane]; }
    {
      const int n = t & 127;
      const float zre = -rbf(expf(bf2f(logA[d*NS_+n]))) * dt;
      const float zim = bf2f(Aim[d*NS_+n]) * dt;
      const float f = (t >> 7) ? 128.f : 1.f;
      cpx e = cexp_(zre*f, zim*f);
      *(float2*)(smem + 2*TLO + 8*t) = make_float2(e.re, e.im);
    }
    __syncthreads();
    f32x4 a4 = {0,0,0,0};
    char* col = smem + 4*lane;
    #pragma unroll 1
    for (int hp=0;hp<2;hp++){
      cpx C; C.re = bf2f(hp ? pc[1][0] : pc[0][0])*dt; C.im = bf2f(hp ? pc[1][1] : pc[0][1])*dt;
      cpx e1, e2;
      { float2 a = *(const float2*)(smem + 2*TLO + 8*(64*hp+lane)); e2.re = a.x; e2.im = a.y;
        float2 c = *(const float2*)(smem + 2*TLO + 1024 + 8*(64*hp+lane)); e1.re = c.x; e1.im = c.y; }
      if (wave == 0){
        cpx e8 = cmul(e2, e2); e8 = cmul(e8, e8); e8 = cmul(e8, e8);
        cpx xx; xx.re = 1.f; xx.im = 0.f;
        #pragma unroll
        for (int i=0;i<16;i++){ put_split(col + (41+i)*TR, xx.re, -xx.im); if (i < 15) xx = (i == 0) ? e8 : cmul(xx, e8); }
      } else if (wave == 1){
        cpx eb = cmul(e1, e1); eb = cmul(eb, eb); eb = cmul(eb, eb);
        cpx cb = cmul(C, eb);
        #pragma unroll
        for (int i=0;i<9;i++){ put_split(col + (32+i)*TR, cb.re, cb.im); if (i < 8) cb = cmul(cb, e1); }
      } else if (wave == 2){
        cpx S[8];
        S[1] = e2;
        #pragma unroll
        for (int i=2;i<8;i++) S[i] = cmul(S[i-1], S[1]);
        cpx cb = C;
        #pragma unroll
        for (int a=0;a<2;a++){
          put_split(col + (8*a)*TR, cb.re, cb.im);
          #pragma unroll
          for (int p=1;p<8;p++){ cpx g = cmul(cb, S[p]); put_split(col + (8*a+p)*TR, g.re, g.im); }
          if (a == 0) cb = cmul(cb, e1);
        }
      } else {
        cpx S2 = cmul(e2, e2), S4 = cmul(S2, S2), S6 = cmul(S4, S2);
        cpx cb = cmul(C, cmul(e1, e1));
        #pragma unroll
        for (int a=0;a<2;a++){
          put_split(col + (16+4*a)*TR, cb.re, cb.im);
          { cpx g = cmul(cb, S2); put_split(col + (16+4*a+1)*TR, g.re, g.im); }
          { cpx g = cmul(cb, S4); put_split(col + (16+4*a+2)*TR, g.re, g.im); }
          { cpx g = cmul(cb, S6); put_split(col + (16+4*a+3)*TR, g.re, g.im); }
          cb = cmul(cb, e1);
        }
        #pragma unroll
        for (int a=0;a<4;a++){
          put_split(col + (24+2*a)*TR, cb.re, cb.im);
          cpx g = cmul(cb, S4); put_split(col + (24+2*a+1)*TR, g.re, g.im);
          if (a < 3) cb = cmul(cb, e1);
        }
      }
      __syncthreads();
      if (wave < 3){
        const char* ga = smem + (16*wave + m)*TR + 16*kg;
        const char* xb = smem + (41 + m)*TR + 16*kg;
        #pragma unroll
        for (int kc=0;kc<4;kc++){
          bf16x8 ah = *(const bf16x8*)(ga + 64*kc), al = *(const bf16x8*)(ga + 64*kc + TLO);
          bf16x8 bh = *(const bf16x8*)(xb + 64*kc), bl = *(const bf16x8*)(xb + 64*kc + TLO);
          a4 = MFMA16(ah, bh, a4);
          a4 = MFMA16(ah, bl, a4);
          a4 = MFMA16(al, bh, a4);
        }
      }
      if (hp == 0) __syncthreads();
    }
    float* Kc = Kcg + d*KC_;
    if (wave < 3){
      #pragma unroll
      for (int i=0;i<4;i++){
        int r = 16*wave + 4*kg + i;
        int idx;
        if (r < 16) idx = 128*(r>>3) + (r&7) + 8*m;
        else if (r < 24) idx = 256 + 64*((r-16)>>2) + (r&3) + 4*m;
        else if (r < 32) idx = 384 + 32*((r-24)>>1) + (r&1) + 2*m;
        else idx = 512 + 16*(r-32) + m;
        if (r < 40 || (r == 40 && m == 0)) Kc[idx] = 2.f*a4[i];
      }
    }
    return;
  }
  const int b = blockIdx.x - 512;
  const int which = b >> 8;
  const int l0 = ((b & 255) >> 3)*64, d0 = (b & 7)*64;
  const int c8 = (t & 7)*8, rg = t >> 3;
  uint32_t o[2][4];
  if (which != 1){
    const u16* src = which == 0 ? u : q;
    uint4 a = *(const uint4*)(src + (l0+2*rg)*D_ + d0 + c8);
    uint4 c = *(const uint4*)(src + (l0+2*rg+1)*D_ + d0 + c8);
    o[0][0]=a.x;o[0][1]=a.y;o[0][2]=a.z;o[0][3]=a.w;
    o[1][0]=c.x;o[1][1]=c.y;o[1][2]=c.z;o[1][3]=c.w;
  } else {
    uint4 kk[5], vv[2], wq[4];
    #pragma unroll
    for (int j=0;j<5;j++){ int l = l0+2*rg+j-3; int lc = l < 0 ? 0 : l; kk[j] = *(const uint4*)(k + lc*D_ + d0 + c8); }
    vv[0] = *(const uint4*)(v + (l0+2*rg)*D_ + d0 + c8);
    vv[1] = *(const uint4*)(v + (l0+2*rg+1)*D_ + d0 + c8);
    #pragma unroll
    for (int j=0;j<4;j++) wq[j] = *(const uint4*)(shift_k + (d0+c8)*4 + 8*j);
    #pragma unroll
    for (int j=0;j<3;j++) if (l0+2*rg+j-3 < 0) kk[j] = make_uint4(0,0,0,0);
    #pragma unroll
    for (int p=0;p<4;p++){
      const uint32_t* wp = (const uint32_t*)&wq[p];
      #pragma unroll
      for (int j=0;j<2;j++){
        float r2[2];
        #pragma unroll
        for (int e=0;e<2;e++){
          float w0 = e? lo16f(wp[2]) : lo16f(wp[0]); float w1 = e? hi16f(wp[2]) : hi16f(wp[0]);
          float w2 = e? lo16f(wp[3]) : lo16f(wp[1]); float w3 = e? hi16f(wp[3]) : hi16f(wp[1]);
          auto g = [&](int idx){ uint32_t w = ((const uint32_t*)&kk[idx])[p]; return e? hi16f(w) : lo16f(w); };
          float acc = rbf(g(j)*w3);
          acc = rbf(acc + rbf(g(j+1)*w2));
          acc = rbf(acc + rbf(g(j+2)*w1));
          acc = rbf(acc + rbf(g(j+3)*w0));
          uint32_t vw = ((const uint32_t*)&vv[j])[p];
          r2[e] = acc * (e? hi16f(vw) : lo16f(vw));
        }
        o[j][p] = pk2(r2[0], r2[1]);
      }
    }
  }
  const int TS = 144;
  #pragma unroll
  for (int p=0;p<4;p++){
    *(uint32_t*)(smem + (c8+2*p)*TS + rg*4)   = __builtin_amdgcn_perm(o[1][p], o[0][p], 0x05040100);
    *(uint32_t*)(smem + (c8+2*p+1)*TS + rg*4) = __builtin_amdgcn_perm(o[1][p], o[0][p], 0x07060302);
  }
  __syncthreads();
  u16* dst = which == 0 ? uT : (which == 1 ? kvT : qT);
  const int lc = (t & 7)*8, dr = t >> 3;
  #pragma unroll
  for (int i=0;i<2;i++){
    int dd = dr + 32*i;
    uint4 x = *(const uint4*)(smem + dd*TS + lc*2);
    *(uint4*)(dst + (d0+dd)*L_ + l0 + lc) = x;
  }
}

#define CSTRIDE 4416
#define CONV_LDS 49664

__device__ __forceinline__ bf16x8 shr1(bf16x8 x){
  uint4 t = __builtin_bit_cast(uint4, x);
  t.x = __builtin_amdgcn_update_dpp(0, (int)t.x, 0x111, 0xf, 0xf, true);
  t.y = __builtin_amdgcn_update_dpp(0, (int)t.y, 0x111, 0xf, 0xf, true);
  t.z = __builtin_amdgcn_update_dpp(0, (int)t.z, 0x111, 0xf, 0xf, true);
  t.w = __builtin_amdgcn_update_dpp(0, (int)t.w, 0x111, 0xf, 0xf, true);
  return __builtin_bit_cast(bf16x8, t);
}
__device__ __forceinline__ uint2 ldsv(const char* p){
  typedef uint32_t u32x2 __attribute__((ext_vector_type(2)));
  u32x2 v = *(const volatile u32x2*)p; return make_uint2(v.x, v.y); }
typedef uint32_t u32x2 __attribute__((ext_vector_type(2)));
struct Frag { u32x2 a, b; };
__device__ __forceinline__ bf16x8 fr(const Frag& f){ uint4 z = make_uint4(f.a.x, f.a.y, f.b.x, f.b.y); return __builtin_bit_cast(bf16x8, z); }

__global__ __launch_bounds__(256) void conv_kernel(const u16* __restrict__ uT, const u16* __restrict__ kvT,
    const u16* __restrict__ qT, const u16* __restrict__ ssmk,
    const float* __restrict__ Kcg,
    const u16* __restrict__ Dskip, const u16* __restrict__ ssmD, u16* __restrict__ oT){
  extern __shared__ __attribute__((aligned(16))) char smem[];
  const int bid = blockIdx.x;
  const bool isA = bid < 512;
  const int t = threadIdx.x, wave = t >> 6, lane = t & 63;
  const int q = wave >> 1, h = wave & 1;
  const int m = lane & 15, kg = lane >> 4;
  const int d = isA ? bid : (bid - 512) * 2;
  const u16* x = isA ? uT + d*L_ : kvT + (d+q)*L_;
  uint32_t w[2][6];
  if (isA){
    const float* Kc = Kcg + d*KC_;
    float kv[12];
    #pragma unroll
    for (int j=0;j<12;j++){
      int l = 2047 - 8*t - j; int lc = l < 0 ? 0 : l;
      int pr = (int)rbf((float)lc);
      int p = pr < 256 ? pr : (pr < 512 ? 128 + (pr>>1) : (pr < 1024 ? 256 + (pr>>2) : 384 + (pr>>3)));
      float xv = Kc[p];
      kv[j] = l < 0 ? 0.f : xv;
    }
    #pragma unroll
    for (int j=0;j<6;j++){
      uint32_t hh = pk2(kv[2*j], kv[2*j+1]);
      w[0][j] = hh; w[1][j] = pk2(kv[2*j] - lo16f(hh), kv[2*j+1] - hi16f(hh));
    }
  } else {
    int o2 = t < 255 ? 2036 - 8*t : 0;
    #pragma unroll
    for (int a=0;a<2;a++){
      const u16* K0 = ssmk + (d+a)*L_;
      uint4 xa = *(const uint4*)(K0 + 2040 - 8*t);
      uint2 ya = *(const uint2*)(K0 + o2);
      w[a][0] = __builtin_amdgcn_alignbit(xa.w, xa.w, 16);
      w[a][1] = __builtin_amdgcn_alignbit(xa.z, xa.z, 16);
      w[a][2] = __builtin_amdgcn_alignbit(xa.y, xa.y, 16);
      w[a][3] = __builtin_amdgcn_alignbit(xa.x, xa.x, 16);
      w[a][4] = t < 255 ? __builtin_amdgcn_alignbit(ya.y,ya.y,16) : 0u;
      w[a][5] = t < 255 ? __builtin_amdgcn_alignbit(ya.x,ya.x,16) : 0u;
    }
  }
  bf16x8 X[2], Y[2], Z[2];
  #pragma unroll
  for (int ch=0;ch<2;ch++){
    X[ch] = *(const bf16x8*)(x + 64*(2*m)   + 32*ch + 8*kg);
    Y[ch] = *(const bf16x8*)(x + 64*(2*m+1) + 32*ch + 8*kg);
    Z[ch] = *(const bf16x8*)(x + 64*m       + 32*ch + 8*kg);
  }
  {
    if (t < 80) *(uint4*)(smem + (t/10)*CSTRIDE + 4096 + 16*(t%10)) = make_uint4(0,0,0,0);
    #pragma unroll
    for (int a=0;a<2;a++){
      char* base = smem + a*4*CSTRIDE;
      uint32_t al[5];
      #pragma unroll
      for (int i=0;i<5;i++) al[i] = __builtin_amdgcn_alignbit(w[a][i+1], w[a][i], 16);
      *(uint4*)(base + 0*CSTRIDE + 16*t) = make_uint4(w[a][0],w[a][1],w[a][2],w[a][3]);
      *(uint4*)(base + 1*CSTRIDE + 16*t) = make_uint4(al[0],al[1],al[2],al[3]);
      *(uint4*)(base + 2*CSTRIDE + 16*t) = make_uint4(w[a][1],w[a][2],w[a][3],w[a][4]);
      *(uint4*)(base + 3*CSTRIDE + 16*t) = make_uint4(al[1],al[2],al[3],al[4]);
    }
  }
  __syncthreads();
  f32x4 P[2][2], Q[2];
  #pragma unroll
  for (int i=0;i<2;i++){ Q[i] = f32x4{0,0,0,0}; for (int j=0;j<2;j++) P[i][j] = f32x4{0,0,0,0}; }
  const int s0 = 2047 - 16*h - m + 8*kg;
  const int r = s0 & 3;
  const uint32_t fbase = (uint32_t)(uintptr_t)smem + q*4*CSTRIDE + r*CSTRIDE + 2*(s0 - r) - 4032;
#define LDF(F, o) asm volatile("ds_read_b64 %0, %2 offset:%3\n\tds_read_b64 %1, %2 offset:%4" : "=v"(F.a), "=v"(F.b) : "v"(fbase), "i"(4032-2*(o)), "i"(4040-2*(o)))
#define WAITF(n, F, G) asm volatile("s_waitcnt lgkmcnt(" #n ")" : "+v"(F.a), "+v"(F.b), "+v"(G.a), "+v"(G.b))
  Frag F0, F1, F2, F3, F4;
  LDF(F0, -32); LDF(F1, 0); LDF(F2, 32);
  asm volatile("s_waitcnt lgkmcnt(4)" : "+v"(F0.a), "+v"(F0.b));
#define STEP1(c, EV, OD, LO, MM, HH, NM, NH) \
    LDF(NM, 64*(c)+64); LDF(NH, 64*(c)+96); WAITF(4, MM, HH); \
    P[0][0] = MFMA16(EV[0], fr(MM), P[0][0]); P[0][1] = MFMA16(EV[0], fr(HH), P[0][1]); \
    P[1][0] = MFMA16(OD[0], fr(MM), P[1][0]); P[1][1] = MFMA16(OD[0], fr(HH), P[1][1]); \
    P[0][0] = MFMA16(EV[1], fr(LO), P[0][0]); P[0][1] = MFMA16(EV[1], fr(MM), P[0][1]); \
    P[1][0] = MFMA16(OD[1], fr(LO), P[1][0]); P[1][1] = MFMA16(OD[1], fr(MM), P[1][1]); \
    OD[0] = shr1(OD[0]); OD[1] = shr1(OD[1]);
#define STEP2(c, LO, MM, HH, NM, NH) \
    if ((c) < 31){ LDF(NM, 64*(c)+64); LDF(NH, 64*(c)+96); WAITF(4, MM, HH); } else { WAITF(0, MM, HH); } \
    Q[0] = MFMA16(Z[0], fr(MM), Q[0]); Q[1] = MFMA16(Z[0], fr(HH), Q[1]); \
    Q[0] = MFMA16(Z[1], fr(LO), Q[0]); Q[1] = MFMA16(Z[1], fr(MM), Q[1]); \
    Z[0] = shr1(Z[0]); Z[1] = shr1(Z[1]);
  STEP1(0, X, Y, F0, F1, F2, F3, F4)
  STEP1(1, Y, X, F2, F3, F4, F0, F1)
  STEP1(2, X, Y, F4, F0, F1, F2, F3)
  STEP1(3, Y, X, F1, F2, F3, F4, F0)
  STEP1(4, X, Y, F3, F4, F0, F1, F2)
  STEP1(5, Y, X, F0, F1, F2, F3, F4)
  STEP1(6, X, Y, F2, F3, F4, F0, F1)
  STEP1(7, Y, X, F4, F0, F1, F2, F3)
  STEP1(8, X, Y, F1, F2, F3, F4, F0)
  STEP1(9, Y, X, F3, F4, F0, F1, F2)
  STEP1(10, X, Y, F0, F1, F2, F3, F4)
  STEP1(11, Y, X, F2, F3, F4, F0, F1)
  STEP1(12, X, Y, F4, F0, F1, F2, F3)
  STEP1(13, Y, X, F1, F2, F3, F4, F0)
  STEP1(14, X, Y, F3, F4, F0, F1, F2)
  STEP1(15, Y, X, F0, F1, F2, F3, F4)
  STEP2(16, F2, F3, F4, F0, F1)
  STEP2(17, F4, F0, F1, F2, F3)
  STEP2(18, F1, F2, F3, F4, F0)
  STEP2(19, F3, F4, F0, F1, F2)
  STEP2(20, F0, F1, F2, F3, F4)
  STEP2(21, F2, F3, F4, F0, F1)
  STEP2(22, F4, F0, F1, F2, F3)
  STEP2(23, F1, F2, F3, F4, F0)
  STEP2(24, F3, F4, F0, F1, F2)
  STEP2(25, F0, F1, F2, F3, F4)
  STEP2(26, F2, F3, F4, F0, F1)
  STEP2(27, F4, F0, F1, F2, F3)
  STEP2(28, F1, F2, F3, F4, F0)
  STEP2(29, F3, F4, F0, F1, F2)
  STEP2(30, F0, F1, F2, F3, F4)
  STEP2(31, F2, F3, F4, F0, F1)
  uint4 e0, e1, e2, e3; float sc0, sc1;
  {
    const int l0 = 8*t;
    if (isA){
      e0 = *(const uint4*)(uT + d*L_ + l0); e1 = e0; e2 = e0; e3 = e0;
      sc0 = bf2f(Dskip[d]); sc1 = sc0;
    } else {
      e0 = *(const uint4*)(kvT + d*L_ + l0); e1 = *(const uint4*)(kvT + (d+1)*L_ + l0);
      e2 = *(const uint4*)(qT + d*L_ + l0);  e3 = *(const uint4*)(qT + (d+1)*L_ + l0);
      sc0 = bf2f(ssmD[d]); sc1 = bf2f(ssmD[d+1]);
    }
  }
  __syncthreads();
  float* G1 = (float*)smem + q*2112;
  float* G2 = (float*)smem + 2*2112 + q*1088;
  #pragma unroll
  for (int mt=0;mt<2;mt++)
    #pragma unroll
    for (int nt=0;nt<2;nt++)
      #pragma unroll
      for (int i=0;i<4;i++){
        int l = 64*(2*(4*kg+i)+mt) + 16*h + 32*nt + m;
        G1[l + 16*(l>>9)] = P[mt][nt][i];
      }
  #pragma unroll
  for (int nt=0;nt<2;nt++)
    #pragma unroll
    for (int i=0;i<4;i++){
      int l = 64*(4*kg+i) + 16*h + 32*nt + m;
      G2[l + 16*(l>>8)] = Q[nt][i];
    }
  __syncthreads();
  {
    const float* A1 = (const float*)smem; const float* A2 = A1 + 2*2112;
    int l0 = 8*t;
    int i1 = l0 + 16*(l0>>9);
    int l2 = l0 - 1024; int i2 = l2 + 16*(l2>>8);
    if (isA){
      float v[8];
      #pragma unroll
      for (int j=0;j<8;j++) v[j] = A1[i1+j] + A1[2112+i1+j];
      if (l0 >= 1024){
        #pragma unroll
        for (int j=0;j<8;j++) v[j] += A2[i2+j] + A2[1088+i2+j];
      }
      const uint32_t* up = (const uint32_t*)&e0;
      #pragma unroll
      for (int j=0;j<8;j++){
        float uu = (j&1) ? hi16f(up[j>>1]) : lo16f(up[j>>1]);
        v[j] = rbf(v[j]) + rbf(uu*sc0);
      }
      *(uint4*)(oT + d*L_ + l0) = make_uint4(pk2(v[0],v[1]),pk2(v[2],v[3]),pk2(v[4],v[5]),pk2(v[6],v[7]));
    } else {
      #pragma unroll
      for (int a=0;a<2;a++){
        float v[8];
        #pragma unroll
        for (int j=0;j<8;j++) v[j] = A1[a*2112+i1+j];
        if (l0 >= 1024){
          #pragma unroll
          for (int j=0;j<8;j++) v[j] += A2[a*1088+i2+j];
        }
        const uint32_t* kp = a ? (const uint32_t*)&e1 : (const uint32_t*)&e0;
        const uint32_t* qp = a ? (const uint32_t*)&e3 : (const uint32_t*)&e2;
        const float sd = a ? sc1 : sc0;
        #pragma unroll
        for (int j=0;j<8;j++){
          float kk = (j&1) ? hi16f(kp[j>>1]) : lo16f(kp[j>>1]);
          float qq = (j&1) ? hi16f(qp[j>>1]) : lo16f(qp[j>>1]);
          float y = rbf(rbf(v[j]) + rbf(kk*sd));
          v[j] = qq*y;
        }
        *(uint4*)(oT + (D_ + d + a)*L_ + l0) = make_uint4(pk2(v[0],v[1]),pk2(v[2],v[3]),pk2(v[4],v[5]),pk2(v[6],v[7]));
      }
    }
  }
}
__global__ __launch_bounds__(256) void post_kernel(const u16* __restrict__ oT, u16* __restrict__ out){
  __shared__ __attribute__((aligned(16))) char smem[64*144];
  const int t = threadIdx.x;
  const int b = blockIdx.x;
  const int c0 = (b >> 5)*64, l0 = (b & 31)*64;
  const int c8 = (t & 7)*8, rg = t >> 3;
  uint4 a = *(const uint4*)(oT + (c0+2*rg)*L_ + l0 + c8);
  uint4 c = *(const uint4*)(oT + (c0+2*rg+1)*L_ + l0 + c8);
  uint32_t o0[4] = {a.x,a.y,a.z,a.w}, o1[4] = {c.x,c.y,c.z,c.w};
  const int TS = 144;
  #pragma unroll
  for (int p=0;p<4;p++){
    *(uint32_t*)(smem + (c8+2*p)*TS + rg*4)   = __builtin_amdgcn_perm(o1[p], o0[p], 0x05040100);
    *(uint32_t*)(smem + (c8+2*p+1)*TS + rg*4) = __builtin_amdgcn_perm(o1[p], o0[p], 0x07060302);
  }
  __syncthreads();
  const int lc = (t & 7)*8, dr = t >> 3;
  #pragma unroll
  for (int i=0;i<2;i++){
    int dd = dr + 32*i;
    uint4 x = *(const uint4*)(smem + dd*TS + lc*2);
    *(uint4*)(out + (l0+dd)*(2*D_) + c0 + lc) = x;
  }
}

extern "C" void kernel_launch(void* const* d_in, const int* in_sizes, int n_in,
                              void* d_out, int out_size, void* d_ws, size_t ws_size,
                              hipStream_t stream) {
  const u16* u = (const u16*)d_in[0];
  const u16* logA = (const u16*)d_in[1];
  const u16* Aim = (const u16*)d_in[2];
  const u16* Cre = (const u16*)d_in[3];
  const u16* Cim = (const u16*)d_in[4];
  const u16* logdt = (const u16*)d_in[5];
  const u16* Dskip = (const u16*)d_in[6];
  const u16* q = (const u16*)d_in[7];
  const u16* k = (const u16*)d_in[8];
  const u16* v = (const u16*)d_in[9];
  const u16* shift_k = (const u16*)d_in[10];
  const u16* ssm_k = (const u16*)d_in[11];
  const u16* ssmD = (const u16*)d_in[12];
  u16* out = (u16*)d_out;
  u16* ws = (u16*)d_ws;
  const size_t S = (size_t)L_*D_;
  u16* uT = ws; u16* kvT = ws + S; u16* qT = ws + 2*S; float* Kc = (float*)(ws + 3*S); u16* oT = ws + 4*S;
  pre_kernel<<<1280, 256, 0, stream>>>(u, k, v, q, shift_k, uT, kvT, qT, logA, Aim, Cre, Cim, logdt, Kc);
  conv_kernel<<<768, 256, CONV_LDS, stream>>>(uT, kvT, qT, ssm_k, Kc, Dskip, ssmD, oT);
  post_kernel<<<512, 256, 0, stream>>>(oT, out);
}
```

```cpp
#include <hip/hip_runtime.h>
#include <hip/hip_bf16.h>
#include <stdint.h>

typedef unsigned short u16;
typedef short bf16x8 __attribute__((ext_vector_type(8)));
typedef float f32x4 __attribute__((ext_vector_type(4)));
typedef float f32x2 __attribute__((ext_vector_type(2)));
typedef __bf16 bf16x2_t __attribute__((ext_vector_type(2)));

#define L_ 2048
#define D_ 512
#define NS_ 128

__device__ __forceinline__ float bf2f(u16 x){ return __builtin_bit_cast(float, (uint32_t)x << 16); }
__device__ __forceinline__ u16 f2bf(float f){ __hip_bfloat16 h = __float2bfloat16(f); return __builtin_bit_cast(u16, h); }
__device__ __forceinline__ float rbf(float f){ return bf2f(f2bf(f)); }
__device__ __forceinline__ uint32_t pk2(float a, float b){
  f32x2 v = {a,b}; bf16x2_t r = __builtin_convertvector(v, bf16x2_t); return __builtin_bit_cast(uint32_t, r); }
__device__ __forceinline__ float lo16f(uint32_t w){ return __builtin_bit_cast(float, w << 16); }
__device__ __forceinline__ float hi16f(uint32_t w){ return __builtin_bit_cast(float, w & 0xffff0000u); }

struct cpx { float re, im; };
__device__ __forceinline__ float amul(float a, float b){ float r; asm("v_mul_f32 %0, %1, %2" : "=v"(r) : "v"(a), "v"(b)); return r; }
__device__ __forceinline__ float afma(float a, float b, float c){ float r; asm("v_fma_f32 %0, %1, %2, %3" : "=v"(r) : "v"(a), "v"(b), "v"(c)); return r; }
__device__ __forceinline__ float afms(float a, float b, float c){ float r; asm("v_fma_f32 %0, %1, %2, -%3" : "=v"(r) : "v"(a), "v"(b), "v"(c)); return r; }
__device__ __forceinline__ cpx cmul(cpx a, cpx b){ cpx r; r.re = afms(a.re, b.re, amul(a.im,b.im)); r.im = afma(a.re, b.im, amul(a.im,b.re)); return r; }
__device__ __forceinline__ cpx cexp_(float re, float im){ float e = expf(re); float s,c; sincosf(im,&s,&c); cpx r; r.re=e*c; r.im=e*s; return r; }

#define MFMA16(a,b,c) __builtin_amdgcn_mfma_f32_16x16x32_bf16(a,b,c,0,0,0)
typedef uint32_t u32x2 __attribute__((ext_vector_type(2)));
typedef uint32_t u32x4 __attribute__((ext_vector_type(4)));
#define KC_ 656
#define TR 272
#define TLO (57*TR)
#define TSZ (2*TLO + 2048)
#define UST 4352
#define KCOFF 17408
#define TOFF 27904
#define KST 4352
#define YOFF (TOFF + 36864)
#define YST 8448
#define QOFF (TOFF + 112128)
#define LDS_TOTAL (TOFF + 4*TSZ)

__device__ __forceinline__ void put_split(char* p, float a, float b){
  uint32_t h = pk2(a,b);
  uint32_t l = pk2(a - lo16f(h), b - hi16f(h));
  *(uint32_t*)p = h; *(uint32_t*)(p + TLO) = l;
}
__device__ __forceinline__ int f2i(float x){ return __builtin_bit_cast(int, x); }
__device__ __forceinline__ float i2f(int x){ return __builtin_bit_cast(float, x); }
__device__ __forceinline__ f32x4 shrN1(f32x4 x){
  f32x4 r;
  #pragma unroll
  for (int i=0;i<4;i++) r[i] = i2f(__builtin_amdgcn_update_dpp(0, f2i(x[i]), 0x111, 0xf, 0xf, true));
  return r;
}
__device__ __forceinline__ f32x4 shrN2(f32x4 x){
  f32x4 r;
  #pragma unroll
  for (int i=0;i<4;i++) r[i] = i2f(__builtin_amdgcn_update_dpp(0, f2i(x[i]), 0x112, 0xf, 0xf, true));
  return r;
}
struct W6 { uint32_t w[6]; };
__device__ __forceinline__ W6 uwin(const char* p){
  u32x2 a = *(const u32x2*)p, b = *(const u32x2*)(p+8), c = *(const u32x2*)(p+16);
  W6 r; r.w[0]=a.x; r.w[1]=a.y; r.w[2]=b.x; r.w[3]=b.y; r.w[4]=c.x; r.w[5]=c.y; return r;
}
__device__ __forceinline__ bf16x8 usel(const W6& W, int x, uint32_t sel){
  u32x4 r;
  r.x = __builtin_amdgcn_perm(W.w[x+1], W.w[x],   sel); r.y = __builtin_amdgcn_perm(W.w[x+2], W.w[x+1], sel);
  r.z = __builtin_amdgcn_perm(W.w[x+3], W.w[x+2], sel); r.w = __builtin_amdgcn_perm(W.w[x+4], W.w[x+3], sel);
  return __builtin_bit_cast(bf16x8, r);
}
__device__ __forceinline__ bf16x8 uodd(const W6& W, int x){
  u32x4 r;
  r.x = __builtin_amdgcn_alignbit(W.w[x+1], W.w[x],   16); r.y = __builtin_amdgcn_alignbit(W.w[x+2], W.w[x+1], 16);
  r.z = __builtin_amdgcn_alignbit(W.w[x+3], W.w[x+2], 16); r.w = __builtin_amdgcn_alignbit(W.w[x+4], W.w[x+3], 16);
  return __builtin_bit_cast(bf16x8, r);
}
__device__ __forceinline__ bf16x8 uevn(const W6& W, int x){
  u32x4 r = {W.w[x], W.w[x+1], W.w[x+2], W.w[x+3]};
  return __builtin_bit_cast(bf16x8, r);
}
__device__ __forceinline__ void put_u(char* smem, int tr, u32x2 r0, u32x2 r1){
  uint32_t d[4];
  d[0] = __builtin_amdgcn_perm(r1.x, r0.x, 0x05040100); d[1] = __builtin_amdgcn_perm(r1.x, r0.x, 0x07060302);
  d[2] = __builtin_amdgcn_perm(r1.y, r0.y, 0x05040100); d[3] = __builtin_amdgcn_perm(r1.y, r0.y, 0x07060302);
  const int a = tr >> 6, pos = tr & 63;
  char* p = smem + 272*a + 16 + 4*pos;
  #pragma unroll
  for (int c=0;c<4;c++) *(uint32_t*)(p + c*UST) = d[c];
  if (pos >= 60 && a < 15){
    #pragma unroll
    for (int c=0;c<4;c++) *(uint32_t*)(p + c*UST + 16) = d[c];
  }
  if (tr < 16) *(uint32_t*)(smem + (tr>>2)*UST + 4*(tr&3)) = 0u;
}

__global__ __launch_bounds__(1024) void mono_kernel(const u16* __restrict__ u, const u16* __restrict__ logA, const u16* __restrict__ Aim,
    const u16* __restrict__ Cre, const u16* __restrict__ Cim, const u16* __restrict__ logdt, const u16* __restrict__ Dskip,
    const u16* __restrict__ q, const u16* __restrict__ k, const u16* __restrict__ v, const u16* __restrict__ shift_k,
    const u16* __restrict__ ssmk, const u16* __restrict__ ssmD, u16* __restrict__ out
){
#define TSP(i)
  TSP(0); TSP(1); TSP(2); TSP(3); TSP(4); TSP(5);
  extern __shared__ __attribute__((aligned(16))) char smem[];
  const int bid = blockIdx.x, t = threadIdx.x;
  const int w5 = bid >> 3;
  const bool isA = w5 < 16;
  const int c0 = (bid & 7)*64 + (w5 & 15)*4;
  const int tr = (t + 64*(w5 & 15)) & 1023;
  const int wave = t >> 6, lane = t & 63, m = lane & 15, kg = lane >> 4;
  const u32x2 dsk = *(const u32x2*)((isA ? Dskip : ssmD) + c0);
  if (isA){
    const u32x2 u0 = *(const u32x2*)(u + (2*tr)*D_ + c0);
    const u32x2 u1 = *(const u32x2*)(u + (2*tr+1)*D_ + c0);
    const int ch = t >> 8, tg = t & 255, wv = wave & 3;
    const int d = c0 + ch;
    char* tb = smem + TOFF + ch*TSZ;
    const float dt = rbf(expf(bf2f(logdt[d])));
    u16 pc[2][2];
    #pragma unroll
    for (int hp=0;hp<2;hp++){ pc[hp][0] = Cre[d*NS_+64*hp+lane];  pc[hp][1] = Cim[d*NS_+64*hp+lane]; }
    {
      const int n = tg & 127;
      const float zre = -rbf(expf(bf2f(logA[d*NS_+n]))) * dt;
      const float zim = bf2f(Aim[d*NS_+n]) * dt;
      const float f = (tg >> 7) ? 128.f : 1.f;
      cpx e = cexp_(zre*f, zim*f);
      *(float2*)(tb + 2*TLO + 8*tg) = make_float2(e.re, e.im);
    }
    __syncthreads();
    TSP(1);
    f32x4 a4 = {0,0,0,0};
    char* col = tb + 4*lane;
    #pragma unroll 1
    for (int hp=0;hp<2;hp++){
      cpx C; C.re = bf2f(hp ? pc[1][0] : pc[0][0])*dt; C.im = bf2f(hp ? pc[1][1] : pc[0][1])*dt;
      cpx e1, e2;
      { float2 a = *(const float2*)(tb + 2*TLO + 8*(64*hp+lane)); e2.re = a.x; e2.im = a.y;
        float2 c = *(const float2*)(tb + 2*TLO + 1024 + 8*(64*hp+lane)); e1.re = c.x; e1.im = c.y; }
      if (wv == 0){
        cpx e8 = cmul(e2, e2); e8 = cmul(e8, e8); e8 = cmul(e8, e8);
        cpx xx; xx.re = 1.f; xx.im = 0.f;
        #pragma unroll
        for (int i=0;i<16;i++){ put_split(col + (41+i)*TR, xx.re, -xx.im); if (i < 15) xx = (i == 0) ? e8 : cmul(xx, e8); }
      } else if (wv == 1){
        cpx eb = cmul(e1, e1); eb = cmul(eb, eb); eb = cmul(eb, eb);
        cpx cb = cmul(C, eb);
        #pragma unroll
        for (int i=0;i<9;i++){ put_split(col + (32+i)*TR, cb.re, cb.im); if (i < 8) cb = cmul(cb, e1); }
      } else if (wv == 2){
        cpx S[8];
        S[1] = e2;
        #pragma unroll
        for (int i=2;i<8;i++) S[i] = cmul(S[i-1], S[1]);
        cpx cb = C;
        #pragma unroll
        for (int a=0;a<2;a++){
          put_split(col + (8*a)*TR, cb.re, cb.im);
          #pragma unroll
          for (int p=1;p<8;p++){ cpx g = cmul(cb, S[p]); put_split(col + (8*a+p)*TR, g.re, g.im); }
          if (a == 0) cb = cmul(cb, e1);
        }
      } else {
        cpx S2 = cmul(e2, e2), S4 = cmul(S2, S2), S6 = cmul(S4, S2);
        cpx cb = cmul(C, cmul(e1, e1));
        #pragma unroll
        for (int a=0;a<2;a++){
          put_split(col + (16+4*a)*TR, cb.re, cb.im);
          { cpx g = cmul(cb, S2); put_split(col + (16+4*a+1)*TR, g.re, g.im); }
          { cpx g = cmul(cb, S4); put_split(col + (16+4*a+2)*TR, g.re, g.im); }
          { cpx g = cmul(cb, S6); put_split(col + (16+4*a+3)*TR, g.re, g.im); }
          cb = cmul(cb, e1);
        }
        #pragma unroll
        for (int a=0;a<4;a++){
          put_split(col + (24+2*a)*TR, cb.re, cb.im);
          cpx g = cmul(cb, S4); put_split(col + (24+2*a+1)*TR, g.re, g.im);
          if (a < 3) cb = cmul(cb, e1);
        }
      }
      __syncthreads();
      if (wv < 3){
        const char* ga = tb + (16*wv + m)*TR + 16*kg;
        const char* xb = tb + (41 + m)*TR + 16*kg;
        #pragma unroll
        for (int kc=0;kc<4;kc++){
          bf16x8 ah = *(const bf16x8*)(ga + 64*kc), al = *(const bf16x8*)(ga + 64*kc + TLO);
          bf16x8 bh = *(const bf16x8*)(xb + 64*kc), bl = *(const bf16x8*)(xb + 64*kc + TLO);
          a4 = MFMA16(ah, bh, a4);
          a4 = MFMA16(ah, bl, a4);
          a4 = MFMA16(al, bh, a4);
        }
      }
      if (hp == 0) __syncthreads();
    }
    float* Kc = (float*)(smem + KCOFF) + ch*KC_;
    if (wv < 3){
      #pragma unroll
      for (int i=0;i<4;i++){
        int r = 16*wv + 4*kg + i;
        int idx;
        if (r < 16) idx = 128*(r>>3) + (r&7) + 8*m;
        else if (r < 24) idx = 256 + 64*((r-16)>>2) + (r&3) + 4*m;
        else if (r < 32) idx = 384 + 32*((r-24)>>1) + (r&1) + 2*m;
        else idx = 512 + 16*(r-32) + m;
        if (r < 40 || (r == 40 && m == 0)) Kc[idx] = 2.f*a4[i];
      }
    }
    put_u(smem, tr, u0, u1);
    __syncthreads();
    TSP(2);
    {
      float kv[8];
      #pragma unroll
      for (int i=0;i<8;i++){
        int l = 8*tg + 7 - i;
        int pr = (int)rbf((float)l);
        int p = pr < 256 ? pr : (pr < 512 ? 128 + (pr>>1) : (pr < 1024 ? 256 + (pr>>2) : 384 + (pr>>3)));
        kv[i] = Kc[p];
      }
      u32x4 h, lo;
      #pragma unroll
      for (int j=0;j<4;j++){
        uint32_t hh = pk2(kv[2*j], kv[2*j+1]);
        h[j] = hh; lo[j] = pk2(kv[2*j] - lo16f(hh), kv[2*j+1] - hi16f(hh));
      }
      char* kb = smem + TOFF + (2*ch)*KST;
      *(u32x4*)(kb + 16*(tg+15)) = h;
      *(u32x4*)(kb + KST + 16*(tg+15)) = lo;
      if (tg < 30) *(u32x4*)(kb + (tg >= 15 ? KST - 240 : 0) + 16*tg) = u32x4{0,0,0,0};
    }
    __syncthreads();
    TSP(3);
  } else {
    const u32x2 k0 = *(const u32x2*)(k + (2*tr)*D_ + c0);
    const u32x2 k1 = *(const u32x2*)(k + (2*tr+1)*D_ + c0);
    const u32x2 v0 = *(const u32x2*)(v + (2*tr)*D_ + c0);
    const u32x2 v1 = *(const u32x2*)(v + (2*tr+1)*D_ + c0);
    const u32x4 wa = *(const u32x4*)(shift_k + c0*4), wb = *(const u32x4*)(shift_k + c0*4 + 8);
    {
      const int ch = t >> 8, j = t & 255;
      u32x4 x = *(const u32x4*)(ssmk + (c0+ch)*L_ + 8*j);
      u32x4 r;
      r.x = __builtin_amdgcn_alignbit(x.w, x.w, 16); r.y = __builtin_amdgcn_alignbit(x.z, x.z, 16);
      r.z = __builtin_amdgcn_alignbit(x.y, x.y, 16); r.w = __builtin_amdgcn_alignbit(x.x, x.x, 16);
      char* kb = smem + TOFF + ch*KST;
      *(u32x4*)(kb + 16*(j+15)) = r;
      if (j < 15) *(u32x4*)(kb + 16*j) = u32x4{0,0,0,0};
    }
    char* ks = smem + YOFF;
    *(u32x4*)(ks + 32 + 16*tr) = u32x4{k0.x, k0.y, k1.x, k1.y};
    if (t < 2) *(u32x4*)(ks + 16*t) = u32x4{0,0,0,0};
    __syncthreads();
    TSP(1);
    u32x2 kk[5];
    kk[0] = *(const u32x2*)(ks + 8 + 16*tr);
    { u32x4 x = *(const u32x4*)(ks + 16 + 16*tr); kk[1] = u32x2{x.x, x.y}; kk[2] = u32x2{x.z, x.w}; }
    kk[3] = k0; kk[4] = k1;
    u32x2 o[2];
    #pragma unroll
    for (int j=0;j<2;j++){
      const u32x2 vv = j ? v1 : v0;
      #pragma unroll
      for (int p=0;p<2;p++){
        float r2[2];
        #pragma unroll
        for (int e=0;e<2;e++){
          const u32x4 ww = p ? wb : wa;
          const uint32_t wl = e ? ww.z : ww.x, wh = e ? ww.w : ww.y;
          const float w0 = lo16f(wl), w1 = hi16f(wl), w2 = lo16f(wh), w3 = hi16f(wh);
          auto g = [&](int idx){ uint32_t x = kk[idx][p]; return e? hi16f(x) : lo16f(x); };
          float acc = rbf(g(j)*w3);
          acc = rbf(acc + rbf(g(j+1)*w2));
          acc = rbf(acc + rbf(g(j+2)*w1));
          acc = rbf(acc + rbf(g(j+3)*w0));
          r2[e] = acc * (e? hi16f(vv[p]) : lo16f(vv[p]));
        }
        o[j][p] = pk2(r2[0], r2[1]);
      }
    }
    put_u(smem, tr, o[0], o[1]);
    __syncthreads();
    TSP(2); TSP(3);
    if (wave >= 8){
      const int r4 = (((t - 512) + 32*(w5 & 15)) & 511)*4;
      u32x2 a[4];
      #pragma unroll
      for (int i=0;i<4;i++) a[i] = *(const u32x2*)(q + (r4+i)*D_ + c0);
      *(u32x4*)(smem + QOFF + 8*r4)      = u32x4{a[0].x, a[0].y, a[1].x, a[1].y};
      *(u32x4*)(smem + QOFF + 8*r4 + 16) = u32x4{a[2].x, a[2].y, a[3].x, a[3].y};
    }
  }
  if (isA || wave < 8){
    const int ch = isA ? (wave >> 2) : (wave >> 1);
    const int g = isA ? ((wave >> 1) & 1) : (wave & 1);
    const int part = isA ? (wave & 1) : 0;
    const int unit = isA ? 2*ch + part : ch;
    const uint32_t kbase = (uint32_t)(uintptr_t)smem + TOFF + unit*KST + 16*(m - kg + 3);
    const char* ub = smem + ch*UST;
#define LDK(F, dl, c) asm volatile("ds_read_b128 %0, %1 offset:%2" : "=v"(F) : "v"(kbase), "i"(256*(dl) + 64*(3-(c))))
#define LDK4(S, dl) LDK(S[0], dl, 0); LDK(S[1], dl, 1); LDK(S[2], dl, 2); LDK(S[3], dl, 3)
#define WAITK(n, S) asm volatile("s_waitcnt lgkmcnt(" #n ")" : "+v"(S[0]), "+v"(S[1]), "+v"(S[2]), "+v"(S[3]))
    bf16x8 KA[4], KB[4];
    LDK4(KA, 15);
    f32x4 acc[4];
    {
      bf16x8 Up[2][4];
      {
        const uint32_t sel = (m & 1) ? 0x07060504u : 0x05040302u;
        const char* pp = ub + 272*(m >> 1) + 16*kg + 8*g;
        #pragma unroll
        for (int c=0;c<4;c++){ W6 W = uwin(pp + 64*c); Up[0][c] = usel(W, 0, sel); Up[1][c] = usel(W, 1, sel); }
      }
      f32x4 P0 = {0,0,0,0}, P1 = {0,0,0,0};
#define PH1(dl, CUR, NXT) \
      LDK4(NXT, (dl)-1); WAITK(4, CUR); \
      if ((dl) < 15){ P0 = shrN2(P0); P1 = shrN2(P1); } \
      P0 = MFMA16(CUR[0], Up[0][0], P0); P1 = MFMA16(CUR[0], Up[1][0], P1); \
      P0 = MFMA16(CUR[1], Up[0][1], P0); P1 = MFMA16(CUR[1], Up[1][1], P1); \
      P0 = MFMA16(CUR[2], Up[0][2], P0); P1 = MFMA16(CUR[2], Up[1][2], P1); \
      P0 = MFMA16(CUR[3], Up[0][3], P0); P1 = MFMA16(CUR[3], Up[1][3], P1);
      PH1(15, KA, KB) PH1(14, KB, KA) PH1(13, KA, KB) PH1(12, KB, KA)
      PH1(11, KA, KB) PH1(10, KB, KA) PH1(9, KA, KB) PH1(8, KB, KA)
      const bool ok = m >= 1 && m <= 8;
      const int src = 4*((lane & 48) + ((2*(m-1)) & 15));
      #pragma unroll
      for (int i=0;i<4;i++){
        float a0 = i2f(__builtin_amdgcn_ds_bpermute(src,   f2i(P0[i])));
        float a1 = i2f(__builtin_amdgcn_ds_bpermute(src+4, f2i(P0[i])));
        float a2 = i2f(__builtin_amdgcn_ds_bpermute(src,   f2i(P1[i])));
        float a3 = i2f(__builtin_amdgcn_ds_bpermute(src+4, f2i(P1[i])));
        acc[0][i] = ok ? a0 : 0.f; acc[1][i] = ok ? a1 : 0.f; acc[2][i] = ok ? a2 : 0.f; acc[3][i] = ok ? a3 : 0.f;
      }
    }
    TSP(4);
    bf16x8 U[4][4];
    {
      const char* pp = ub + 272*m + 16*kg + 8*g;
      #pragma unroll
      for (int c=0;c<4;c++){ W6 W = uwin(pp + 64*c); U[0][c] = uodd(W, 0); U[1][c] = uevn(W, 1); U[2][c] = uodd(W, 1); U[3][c] = uevn(W, 2); }
    }
#define PH2(dl, CUR, NXT) \
    if ((dl) > 0){ LDK4(NXT, (dl)-1); WAITK(4, CUR); } else { WAITK(0, CUR); } \
    if ((dl) < 7){ acc[0] = shrN1(acc[0]); acc[1] = shrN1(acc[1]); acc[2] = shrN1(acc[2]); acc[3] = shrN1(acc[3]); } \
    _Pragma("unroll") for (int c=0;c<4;c++){ \
      acc[0] = MFMA16(CUR[c], U[0][c], acc[0]); acc[1] = MFMA16(CUR[c], U[1][c], acc[1]); \
      acc[2] = MFMA16(CUR[c], U[2][c], acc[2]); acc[3] = MFMA16(CUR[c], U[3][c], acc[3]); }
    PH2(7, KA, KB) PH2(6, KB, KA) PH2(5, KA, KB) PH2(4, KB, KA)
    PH2(3, KA, KB) PH2(2, KB, KA) PH2(1, KA, KB) PH2(0, KB, KA)
    TSP(5);
    char* yb = smem + YOFF + (part*4 + ch)*YST + 4*(4*g + 32*kg + 132*m);
    #pragma unroll
    for (int i=0;i<4;i++) *(f32x4*)(yb + 32*i) = f32x4{acc[0][i], acc[1][i], acc[2][i], acc[3][i]};
  }
  __syncthreads();
  TSP(6);
  {
    const char* yb = smem + YOFF + 8*tr + 16*(tr >> 6);
    const char* up = smem + 272*(tr >> 6) + 16 + 4*(tr & 63);
    float o0[4], o1[4];
    if (isA){
      #pragma unroll
      for (int c=0;c<4;c++){
        f32x2 a = *(const f32x2*)(yb + c*YST), b = *(const f32x2*)(yb + (4+c)*YST);
        uint32_t uu = *(const uint32_t*)(up + c*UST);
        float sd = (c & 1) ? hi16f(dsk[c>>1]) : lo16f(dsk[c>>1]);
        o0[c] = rbf(a.x + b.x) + rbf(lo16f(uu)*sd);
        o1[c] = rbf(a.y + b.y) + rbf(hi16f(uu)*sd);
      }
    } else {
      u32x4 qq = *(const u32x4*)(smem + QOFF + 16*tr);
      #pragma unroll
      for (int c=0;c<4;c++){
        f32x2 a = *(const f32x2*)(yb + c*YST);
        uint32_t uu = *(const uint32_t*)(up + c*UST);
        float sd = (c & 1) ? hi16f(dsk[c>>1]) : lo16f(dsk[c>>1]);
        float q0 = (c & 1) ? hi16f(qq[c>>1]) : lo16f(qq[c>>1]);
        float q1 = (c & 1) ? hi16f(qq[2+(c>>1)]) : lo16f(qq[2+(c>>1)]);
        o0[c] = q0 * rbf(rbf(a.x) + rbf(lo16f(uu)*sd));
        o1[c] = q1 * rbf(rbf(a.y) + rbf(hi16f(uu)*sd));
      }
    }
    u16* op = out + (isA ? 0 : D_) + c0;
    *(u32x2*)(op + (2*tr)*(2*D_))   = u32x2{pk2(o0[0],o0[1]), pk2(o0[2],o0[3])};
    *(u32x2*)(op + (2*tr+1)*(2*D_)) = u32x2{pk2(o1[0],o1[1]), pk2(o1[2],o1[3])};
  }
}

extern "C" void kernel_launch(void* const* d_in, const int* in_sizes, int n_in,
                              void* d_out, int out_size, void* d_ws, size_t ws_size,
                              hipStream_t stream) {
  mono_kernel<<<256, 1024, LDS_TOTAL, stream>>>((const u16*)d_in[0], (const u16*)d_in[1], (const u16*)d_in[2], (const u16*)d_in[3],
     (const u16*)d_in[4], (const u16*)d_in[5], (const u16*)d_in[6], (const u16*)d_in[7], (const u16*)d_in[8], (const u16*)d_in[9],
     (const u16*)d_in[10], (const u16*)d_in[11], (const u16*)d_in[12], (u16*)d_out
);
}
```

```cpp
#include <hip/hip_runtime.h>
#include <hip/hip_bf16.h>
#include <stdint.h>

typedef unsigned short u16;
typedef short bf16x8 __attribute__((ext_vector_type(8)));
typedef float f32x4 __attribute__((ext_vector_type(4)));
typedef float f32x2 __attribute__((ext_vector_type(2)));
typedef __bf16 bf16x2_t __attribute__((ext_vector_type(2)));

#define L_ 2048
#define D_ 512
#define NS_ 128

__device__ __forceinline__ float bf2f(u16 x){ return __builtin_bit_cast(float, (uint32_t)x << 16); }
__device__ __forceinline__ u16 f2bf(float f){ __hip_bfloat16 h = __float2bfloat16(f); return __builtin_bit_cast(u16, h); }
__device__ __forceinline__ float rbf(float f){ return bf2f(f2bf(f)); }
__device__ __forceinline__ uint32_t pk2(float a, float b){
  f32x2 v = {a,b}; bf16x2_t r = __builtin_convertvector(v, bf16x2_t); return __builtin_bit_cast(uint32_t, r); }
__device__ __forceinline__ float lo16f(uint32_t w){ return __builtin_bit_cast(float, w << 16); }
__device__ __forceinline__ float hi16f(uint32_t w){ return __builtin_bit_cast(float, w & 0xffff0000u); }

struct cpx { float re, im; };
__device__ __forceinline__ float amul(float a, float b){ float r; asm("v_mul_f32 %0, %1, %2" : "=v"(r) : "v"(a), "v"(b)); return r; }
__device__ __forceinline__ float afma(float a, float b, float c){ float r; asm("v_fma_f32 %0, %1, %2, %3" : "=v"(r) : "v"(a), "v"(b), "v"(c)); return r; }
__device__ __forceinline__ float afms(float a, float b, float c){ float r; asm("v_fma_f32 %0, %1, %2, -%3" : "=v"(r) : "v"(a), "v"(b), "v"(c)); return r; }
__device__ __forceinline__ cpx cmul(cpx a, cpx b){ cpx r; r.re = afms(a.re, b.re, amul(a.im,b.im)); r.im = afma(a.re, b.im, amul(a.im,b.re)); return r; }
__device__ __forceinline__ cpx cexp_(float re, float im){ float e = expf(re); float s,c; sincosf(im,&s,&c); cpx r; r.re=e*c; r.im=e*s; return r; }

#define MFMA16(a,b,c) __builtin_amdgcn_mfma_f32_16x16x32_bf16(a,b,c,0,0,0)
typedef uint32_t u32x2 __attribute__((ext_vector_type(2)));
typedef uint32_t u32x4 __attribute__((ext_vector_type(4)));
#define KC_ 656
#define TR 272
#define TLO (57*TR)
#define TSZ (2*TLO + 2048)
#define UST 4352
#define KCOFF 17408
#define TOFF 27904
#define KST 4352
#define KS2 1536
#define FTOFF (TOFF + 12288)
#define FCS 320
#define YFOFF (TOFF + 45056 + 16)
#define YFST 6176
#define YAOFF (TOFF + 69888)
#define YOFF (TOFF + 36864)
#define YST 8448
#define QOFF (TOFF + 112128)
#define LDS_TOTAL (TOFF + 4*TSZ)

__device__ __forceinline__ void put_split(char* p, float a, float b){
  uint32_t h = pk2(a,b);
  uint32_t l = pk2(a - lo16f(h), b - hi16f(h));
  *(uint32_t*)p = h; *(uint32_t*)(p + TLO) = l;
}
__device__ __forceinline__ int f2i(float x){ return __builtin_bit_cast(int, x); }
__device__ __forceinline__ float i2f(int x){ return __builtin_bit_cast(float, x); }
__device__ __forceinline__ f32x4 shrN1(f32x4 x){
  f32x4 r;
  #pragma unroll
  for (int i=0;i<4;i++) r[i] = i2f(__builtin_amdgcn_update_dpp(0, f2i(x[i]), 0x111, 0xf, 0xf, true));
  return r;
}
__device__ __forceinline__ f32x4 shrN2(f32x4 x){
  f32x4 r;
  #pragma unroll
  for (int i=0;i<4;i++) r[i] = i2f(__builtin_amdgcn_update_dpp(0, f2i(x[i]), 0x112, 0xf, 0xf, true));
  return r;
}
struct W6 { uint32_t w[6]; };
__device__ __forceinline__ W6 uwin(const char* p){
  u32x2 a = *(const u32x2*)p, b = *(const u32x2*)(p+8), c = *(const u32x2*)(p+16);
  W6 r; r.w[0]=a.x; r.w[1]=a.y; r.w[2]=b.x; r.w[3]=b.y; r.w[4]=c.x; r.w[5]=c.y; return r;
}
__device__ __forceinline__ bf16x8 usel(const W6& W, int x, uint32_t sel){
  u32x4 r;
  r.x = __builtin_amdgcn_perm(W.w[x+1], W.w[x],   sel); r.y = __builtin_amdgcn_perm(W.w[x+2], W.w[x+1], sel);
  r.z = __builtin_amdgcn_perm(W.w[x+3], W.w[x+2], sel); r.w = __builtin_amdgcn_perm(W.w[x+4], W.w[x+3], sel);
  return __builtin_bit_cast(bf16x8, r);
}
__device__ __forceinline__ bf16x8 uodd(const W6& W, int x){
  u32x4 r;
  r.x = __builtin_amdgcn_alignbit(W.w[x+1], W.w[x],   16); r.y = __builtin_amdgcn_alignbit(W.w[x+2], W.w[x+1], 16);
  r.z = __builtin_amdgcn_alignbit(W.w[x+3], W.w[x+2], 16); r.w = __builtin_amdgcn_alignbit(W.w[x+4], W.w[x+3], 16);
  return __builtin_bit_cast(bf16x8, r);
}
__device__ __forceinline__ bf16x8 uevn(const W6& W, int x){
  u32x4 r = {W.w[x], W.w[x+1], W.w[x+2], W.w[x+3]};
  return __builtin_bit_cast(bf16x8, r);
}
__device__ __forceinline__ void put_u(char* smem, int tr, u32x2 r0, u32x2 r1){
  uint32_t d[4];
  d[0] = __builtin_amdgcn_perm(r1.x, r0.x, 0x05040100); d[1] = __builtin_amdgcn_perm(r1.x, r0.x, 0x07060302);
  d[2] = __builtin_amdgcn_perm(r1.y, r0.y, 0x05040100); d[3] = __builtin_amdgcn_perm(r1.y, r0.y, 0x07060302);
  const int a = tr >> 6, pos = tr & 63;
  char* p = smem + 272*a + 16 + 4*pos;
  #pragma unroll
  for (int c=0;c<4;c++) *(uint32_t*)(p + c*UST) = d[c];
  if (pos >= 60 && a < 15){
    #pragma unroll
    for (int c=0;c<4;c++) *(uint32_t*)(p + c*UST + 16) = d[c];
  }
  if (tr < 16) *(uint32_t*)(smem + (tr>>2)*UST + 4*(tr&3)) = 0u;
}

#define LDK(F, dl, c) asm volatile("ds_read_b128 %0, %1 offset:%2" : "=v"(F) : "v"(kbase), "i"(256*(dl) + 64*(3-(c))))
#define LDK4(S, dl) LDK(S[0], dl, 0); LDK(S[1], dl, 1); LDK(S[2], dl, 2); LDK(S[3], dl, 3)
#define WAITK(n, S) asm volatile("s_waitcnt lgkmcnt(" #n ")" : "+v"(S[0]), "+v"(S[1]), "+v"(S[2]), "+v"(S[3]))

template<int NT, int R0>
__device__ __forceinline__ void near_conv(char* smem, int ch, int m, int kg){
  const uint32_t kbase = (uint32_t)(uintptr_t)smem + TOFF + (2*ch)*KS2 + 16*(m - kg + 3);
#define LDN(F, hs, c) asm volatile("ds_read_b128 %0, %1 offset:%2" : "=v"(F) : "v"(kbase), "i"(((hs)&1)*KS2 + 256*(4-((hs)>>1)) + 64*(3-(c))))
#define LDN4(S, hs) LDN(S[0], hs, 0); LDN(S[1], hs, 1); LDN(S[2], hs, 2); LDN(S[3], hs, 3)
  bf16x8 KA[4], KB[4];
  LDN4(KA, 0);
  bf16x8 U[NT][4];
  {
    const char* pp = smem + ch*UST + 272*m + 16*kg + (R0 ? 8 : 0);
    #pragma unroll
    for (int c=0;c<4;c++){
      W6 W = uwin(pp + 64*c);
      #pragma unroll
      for (int r=0;r<NT;r++){ const int rel = R0 + r + 1 - (R0 ? 4 : 0); U[r][c] = (rel & 1) ? uodd(W, rel >> 1) : uevn(W, rel >> 1); }
    }
  }
  f32x4 acc[NT];
  #pragma unroll
  for (int r=0;r<NT;r++) acc[r] = f32x4{0,0,0,0};
#define NH(hs, CUR, NXT) \
  if ((hs) < 9){ LDN4(NXT, (hs)+1); WAITK(4, CUR); } else { WAITK(0, CUR); } \
  if ((hs) > 0 && !((hs) & 1)){ _Pragma("unroll") for (int r=0;r<NT;r++) acc[r] = shrN1(acc[r]); } \
  _Pragma("unroll") for (int c=0;c<4;c++){ _Pragma("unroll") for (int r=0;r<NT;r++) acc[r] = MFMA16(CUR[c], U[r][c], acc[r]); }
  NH(0, KA, KB) NH(1, KB, KA) NH(2, KA, KB) NH(3, KB, KA) NH(4, KA, KB)
  NH(5, KB, KA) NH(6, KA, KB) NH(7, KB, KA) NH(8, KA, KB) NH(9, KB, KA)
  float* yb = (float*)(smem + YAOFF + ch*YST) + (R0 + 32*kg + 132*m);
  #pragma unroll
  for (int i=0;i<4;i++){
    #pragma unroll
    for (int r=0;r<NT;r++) yb[8*i + r] = acc[r][i];
  }
}
__device__ __forceinline__ bf16x8 band(int sh, int w, int kg){
  const uint32_t by = (((1u << w) - 1u) << sh) >> (8*kg);
  u32x4 r;
  #pragma unroll
  for (int k2=0;k2<4;k2++){ uint32_t x = (by >> (2*k2)) & 3u; x = (x | (x << 15)) & 0x00010001u; r[k2] = x * 0x3f80u; }
  return __builtin_bit_cast(bf16x8, r);
}
__device__ __forceinline__ void split2(const f32x4& a, const f32x4& b, bf16x8& h, bf16x8& l){
  u32x4 hh, ll;
  #pragma unroll
  for (int i=0;i<4;i++){ uint32_t x = pk2(a[i], b[i]); hh[i] = x; ll[i] = pk2(a[i] - lo16f(x), b[i] - hi16f(x)); }
  h = __builtin_bit_cast(bf16x8, hh); l = __builtin_bit_cast(bf16x8, ll);
}
template<int A0, int A1>
__device__ __forceinline__ void far_conv(char* smem, int ch, int m, int kg){
  const char* ub = smem + ch*UST;
  const f32x4 Z4 = {0,0,0,0};
  const int B0 = 2*m + kg - 2;
  const char* va0 = ub + (B0 < 0 ? 0 : 272*(B0 >> 4) + 16 + 16*(B0 & 15));
  const char* va1 = ub + 272*((B0+32) >> 4) + 16 + 16*((B0+32) & 15);
  const int cp = (3 - m) & 3;
  const char* ft = smem + FTOFF + ch*8192 + cp*FCS + 4*(47 - m + 4*kg - cp);
  char* yf = smem + YFOFF + ch*YFST + 64*m + 16*kg;
  f32x4 acc[6];
  #pragma unroll
  for (int a=0;a<6;a++) acc[a] = Z4;
  {
    bf16x8 M[4];
    M[0] = band(14 + m, 3, kg); M[1] = band(9 + m, 5, kg); M[2] = band(6 + m, 3, kg); M[3] = band(1 + m, 5, kg);
    bf16x8 Ch[3][2], Cl[3][2];
    #pragma unroll
    for (int dl=0;dl<3;dl++){
      #pragma unroll
      for (int h=0;h<2;h++){ Ch[dl][h] = *(const bf16x8*)(ft + h*(8*FCS) - 64*dl); Cl[dl][h] = *(const bf16x8*)(ft + h*(8*FCS) + 4*FCS - 64*dl); }
    }
    #pragma unroll
    for (int b=(A0 > 2 ? A0-2 : 0);b<A1;b++){
      const bf16x8 V = *(const bf16x8*)(b ? va1 + 544*(b-1) : va0);
      f32x4 F[4];
      #pragma unroll
      for (int p=0;p<4;p++) F[p] = MFMA16(V, M[p], Z4);
      #pragma unroll
      for (int h=0;h<2;h++){
        bf16x8 Bh, Bl;
        split2(F[2*h], F[2*h+1], Bh, Bl);
        #pragma unroll
        for (int dl=0;dl<3;dl++) if (b + dl >= A0 && b + dl < A1) acc[b+dl] = MFMA16(Bh, Ch[dl][h], acc[b+dl]);
        #pragma unroll
        for (int dl=0;dl<3;dl++) if (b + dl >= A0 && b + dl < A1) acc[b+dl] = MFMA16(Bh, Cl[dl][h], acc[b+dl]);
        #pragma unroll
        for (int dl=0;dl<3;dl++) if (b + dl >= A0 && b + dl < A1) acc[b+dl] = MFMA16(Bl, Ch[dl][h], acc[b+dl]);
      }
      if (b < 2 && b >= A0) *(f32x4*)(yf + 1024*b) = acc[b];
    }
  }
  {
    const char* vb0 = ub + (B0 < 1 ? 0 : 272*((B0-1) >> 4) + 16 + 16*((B0-1) & 15));
    const char* vb1 = ub + 272*((B0+31) >> 4) + 16 + 16*((B0+31) & 15);
    const char* f8 = smem + FTOFF + ch*8192 + 16*FCS + cp*FCS + 4*(63 - m + 4*kg - cp);
    const bf16x8 Ma = band(8 + m, 7, kg), Mb = band(7 + m, 9, kg), Mc = band(15 + m, 2, kg);
    const float k1024 = ((const float*)(smem + KCOFF))[ch*KC_ + 512];
    bf16x8 Ch[4], Cl[4];
    #pragma unroll
    for (int dl=0;dl<A1-2;dl++){ Ch[dl] = *(const bf16x8*)(f8 - 64*dl); Cl[dl] = *(const bf16x8*)(f8 + 4*FCS - 64*dl); }
    #pragma unroll
    for (int b=0;b<A1-2;b++){
      const bf16x8 V = *(const bf16x8*)(b ? va1 + 544*(b-1) : va0);
      const bf16x8 V2 = *(const bf16x8*)(b ? vb1 + 544*(b-1) : vb0);
      const f32x4 Fa = MFMA16(V, Ma, Z4), Fb = MFMA16(V2, Mb, Z4);
      bf16x8 Bh, Bl;
      split2(Fa, Fb, Bh, Bl);
      if (b + 2 >= A0){
        const f32x4 Fc = MFMA16(Mc, V, Z4);
        #pragma unroll
        for (int i=0;i<4;i++) acc[b+2][i] = __builtin_fmaf(k1024, Fc[i], acc[b+2][i]);
      }
      #pragma unroll
      for (int dl=0;dl<4;dl++) if (b + 2 + dl >= A0 && b + 2 + dl < A1) acc[b+2+dl] = MFMA16(Bh, Ch[dl], acc[b+2+dl]);
      #pragma unroll
      for (int dl=0;dl<4;dl++) if (b + 2 + dl >= A0 && b + 2 + dl < A1) acc[b+2+dl] = MFMA16(Bh, Cl[dl], acc[b+2+dl]);
      #pragma unroll
      for (int dl=0;dl<4;dl++) if (b + 2 + dl >= A0 && b + 2 + dl < A1) acc[b+2+dl] = MFMA16(Bl, Ch[dl], acc[b+2+dl]);
    }
  }
  #pragma unroll
  for (int a=(A0 > 2 ? A0 : 2);a<A1;a++) *(f32x4*)(yf + 1024*a) = acc[a];
}

__global__ __launch_bounds__(1024) void mono_kernel(const u16* __restrict__ u, const u16* __restrict__ logA, const u16* __restrict__ Aim,
    const u16* __restrict__ Cre, const u16* __restrict__ Cim, const u16* __restrict__ logdt, const u16* __restrict__ Dskip,
    const u16* __restrict__ q, const u16* __restrict__ k, const u16* __restrict__ v, const u16* __restrict__ shift_k,
    const u16* __restrict__ ssmk, const u16* __restrict__ ssmD, u16* __restrict__ out
){
#define TSP(i)
  TSP(0); TSP(1); TSP(2); TSP(3); TSP(4); TSP(5);
  extern __shared__ __attribute__((aligned(16))) char smem[];
  const int bid = blockIdx.x, t = threadIdx.x;
  const int w5 = bid >> 3;
  const bool isA = w5 < 16;
  const int c0 = (bid & 7)*64 + (w5 & 15)*4;
  const int tr = (t + 64*(w5 & 15)) & 1023;
  const int wave = t >> 6, lane = t & 63, m = lane & 15, kg = lane >> 4;
  const u32x2 dsk = *(const u32x2*)((isA ? Dskip : ssmD) + c0);
  if (isA){
    const int ch = t >> 8, tg = t & 255, wv = (wave + ch) & 3;
    const int d = c0 + ch;
    char* tb = smem + TOFF + ch*TSZ;
    const u16 ldt = logdt[d];
    const u16 pla = logA[d*NS_+(tg & 127)], pai = Aim[d*NS_+(tg & 127)];
    u16 pc[2][2];
    #pragma unroll
    for (int hp=0;hp<2;hp++){ pc[hp][0] = Cre[d*NS_+64*hp+lane];  pc[hp][1] = Cim[d*NS_+64*hp+lane]; }
    const float dt = rbf(expf(bf2f(ldt)));
    {
      const float zre = -rbf(expf(bf2f(pla))) * dt;
      const float zim = bf2f(pai) * dt;
      const float f = (tg >> 7) ? 128.f : 1.f;
      cpx e = cexp_(zre*f, zim*f);
      *(float2*)(tb + 2*TLO + 8*tg) = make_float2(e.re, e.im);
    }
    __syncthreads();
    TSP(1);
    __builtin_amdgcn_sched_barrier(0);
    const u32x2 u0 = *(const u32x2*)(u + (2*tr)*D_ + c0);
    const u32x2 u1 = *(const u32x2*)(u + (2*tr+1)*D_ + c0);
    __builtin_amdgcn_sched_barrier(0);
    f32x4 a4 = {0,0,0,0};
    char* col = tb + 4*lane;
    #pragma unroll 1
    for (int hp=0;hp<2;hp++){
      cpx C; C.re = bf2f(hp ? pc[1][0] : pc[0][0])*dt; C.im = bf2f(hp ? pc[1][1] : pc[0][1])*dt;
      cpx e1, e2;
      { float2 a = *(const float2*)(tb + 2*TLO + 8*(64*hp+lane)); e2.re = a.x; e2.im = a.y;
        float2 c = *(const float2*)(tb + 2*TLO + 1024 + 8*(64*hp+lane)); e1.re = c.x; e1.im = c.y; }
      if (wv == 0){
        cpx e8 = cmul(e2, e2); e8 = cmul(e8, e8); e8 = cmul(e8, e8);
        cpx xx; xx.re = 1.f; xx.im = 0.f;
        #pragma unroll
        for (int i=0;i<16;i++){ put_split(col + (41+i)*TR, xx.re, -xx.im); if (i < 15) xx = (i == 0) ? e8 : cmul(xx, e8); }
      } else if (wv == 1){
        cpx eb = cmul(e1, e1); eb = cmul(eb, eb); eb = cmul(eb, eb);
        cpx cb = cmul(C, eb);
        #pragma unroll
        for (int i=0;i<9;i++){ put_split(col + (32+i)*TR, cb.re, cb.im); if (i < 8) cb = cmul(cb, e1); }
      } else if (wv == 2){
        cpx S[8];
        S[1] = e2;
        #pragma unroll
        for (int i=2;i<8;i++) S[i] = cmul(S[i-1], S[1]);
        cpx cb = C;
        #pragma unroll
        for (int a=0;a<2;a++){
          put_split(col + (8*a)*TR, cb.re, cb.im);
          #pragma unroll
          for (int p=1;p<8;p++){ cpx g = cmul(cb, S[p]); put_split(col + (8*a+p)*TR, g.re, g.im); }
          if (a == 0) cb = cmul(cb, e1);
        }
      } else {
        cpx S2 = cmul(e2, e2), S4 = cmul(S2, S2), S6 = cmul(S4, S2);
        cpx cb = cmul(C, cmul(e1, e1));
        #pragma unroll
        for (int a=0;a<2;a++){
          put_split(col + (16+4*a)*TR, cb.re, cb.im);
          { cpx g = cmul(cb, S2); put_split(col + (16+4*a+1)*TR, g.re, g.im); }
          { cpx g = cmul(cb, S4); put_split(col + (16+4*a+2)*TR, g.re, g.im); }
          { cpx g = cmul(cb, S6); put_split(col + (16+4*a+3)*TR, g.re, g.im); }
          cb = cmul(cb, e1);
        }
        #pragma unroll
        for (int a=0;a<4;a++){
          put_split(col + (24+2*a)*TR, cb.re, cb.im);
          cpx g = cmul(cb, S4); put_split(col + (24+2*a+1)*TR, g.re, g.im);
          if (a < 3) cb = cmul(cb, e1);
        }
      }
      __syncthreads();
      if (wv < 3){
        const char* ga = tb + (16*wv + m)*TR + 16*kg;
        const char* xb = tb + (41 + m)*TR + 16*kg;
        #pragma unroll
        for (int kc=0;kc<4;kc++){
          bf16x8 ah = *(const bf16x8*)(ga + 64*kc), al = *(const bf16x8*)(ga + 64*kc + TLO);
          bf16x8 bh = *(const bf16x8*)(xb + 64*kc), bl = *(const bf16x8*)(xb + 64*kc + TLO);
          a4 = MFMA16(ah, bh, a4);
          a4 = MFMA16(ah, bl, a4);
          a4 = MFMA16(al, bh, a4);
        }
      }
      if (hp == 0) __syncthreads();
    }
    float* Kc = (float*)(smem + KCOFF) + ch*KC_;
    if (wv < 3){
      #pragma unroll
      for (int i=0;i<4;i++){
        int r = 16*wv + 4*kg + i;
        int idx;
        if (r < 16) idx = 128*(r>>3) + (r&7) + 8*m;
        else if (r < 24) idx = 256 + 64*((r-16)>>2) + (r&3) + 4*m;
        else if (r < 32) idx = 384 + 32*((r-24)>>1) + (r&1) + 2*m;
        else idx = 512 + 16*(r-32) + m;
        if (r < 40 || (r == 40 && m == 0)) Kc[idx] = 2.f*a4[i];
      }
    }
    put_u(smem, tr, u0, u1);
    __syncthreads();
    TSP(2);
    {
      char* ft = smem + FTOFF + ch*8192;
      if (tg < 208){
        int z, idx; bool ok; char* wp;
        if (tg < 128){ z = tg & 63; const int Dd = 47 - z; ok = Dd >= 0 && Dd < 32; idx = 385 + 4*Dd + 2*(tg >> 6); wp = ft + (tg >> 6)*(8*FCS); }
        else { z = tg - 128; const int Dd = 63 - z; ok = Dd >= 0; idx = 513 + 2*Dd; wp = ft + 16*FCS; }
        float a = 0.f, b = 0.f;
        if (ok){ a = Kc[idx]; b = Kc[idx+1]; }
        const uint32_t hh = pk2(a, b), ll = pk2(a - lo16f(hh), b - hi16f(hh));
        #pragma unroll
        for (int c=0;c<4;c++) if (z >= c){ *(uint32_t*)(wp + c*FCS + 4*(z-c)) = hh; *(uint32_t*)(wp + (4+c)*FCS + 4*(z-c)) = ll; }
      }
      if (tg >= 161){
        const int sl = tg - 161, j = sl - 15;
        float kv[8];
        #pragma unroll
        for (int i=0;i<8;i++){
          int l = 8*j + 7 - i;
          int pr = (int)rbf((float)l);
          int p = pr < 256 ? pr : 128 + (pr>>1);
          kv[i] = (l >= 0 && l <= 514) ? Kc[p] : 0.f;
        }
        u32x4 h, lo;
        #pragma unroll
        for (int jj=0;jj<4;jj++){
          uint32_t hh = pk2(kv[2*jj], kv[2*jj+1]);
          h[jj] = hh; lo[jj] = pk2(kv[2*jj] - lo16f(hh), kv[2*jj+1] - hi16f(hh));
        }
        char* kb = smem + TOFF + (2*ch)*KS2;
        *(u32x4*)(kb + 16*sl) = h;
        *(u32x4*)(kb + KS2 + 16*sl) = lo;
      }
      if (tg < 4) *(float*)(smem + YFOFF + ch*YFST - 16 + 4*tg) = 0.f;
    }
    __syncthreads();
    TSP(3);
  } else {
    const u32x2 k0 = *(const u32x2*)(k + (2*tr)*D_ + c0);
    const u32x2 k1 = *(const u32x2*)(k + (2*tr+1)*D_ + c0);
    const u32x4 wa = *(const u32x4*)(shift_k + c0*4), wb = *(const u32x4*)(shift_k + c0*4 + 8);
    char* ks = smem + YOFF;
    __builtin_amdgcn_sched_barrier(0);
    *(u32x4*)(ks + 32 + 16*tr) = u32x4{k0.x, k0.y, k1.x, k1.y};
    __builtin_amdgcn_sched_barrier(0);
    const u32x2 v0 = *(const u32x2*)(v + (2*tr)*D_ + c0);
    const u32x2 v1 = *(const u32x2*)(v + (2*tr+1)*D_ + c0);
    const u32x4 sx = *(const u32x4*)(ssmk + (c0+(t >> 8))*L_ + 8*(t & 255));
    __builtin_amdgcn_sched_barrier(0);
    if (t < 2) *(u32x4*)(ks + 16*t) = u32x4{0,0,0,0};
    __syncthreads();
    TSP(1);
    u32x2 kk[5];
    kk[0] = *(const u32x2*)(ks + 8 + 16*tr);
    { u32x4 x = *(const u32x4*)(ks + 16 + 16*tr); kk[1] = u32x2{x.x, x.y}; kk[2] = u32x2{x.z, x.w}; }
    kk[3] = k0; kk[4] = k1;
    u32x2 o[2];
    #pragma unroll
    for (int j=0;j<2;j++){
      const u32x2 vv = j ? v1 : v0;
      #pragma unroll
      for (int p=0;p<2;p++){
        float r2[2];
        #pragma unroll
        for (int e=0;e<2;e++){
          const u32x4 ww = p ? wb : wa;
          const uint32_t wl = e ? ww.z : ww.x, wh = e ? ww.w : ww.y;
          const float w0 = lo16f(wl), w1 = hi16f(wl), w2 = lo16f(wh), w3 = hi16f(wh);
          auto g = [&](int idx){ uint32_t x = kk[idx][p]; return e? hi16f(x) : lo16f(x); };
          float acc = rbf(g(j)*w3);
          acc = rbf(acc + rbf(g(j+1)*w2));
          acc = rbf(acc + rbf(g(j+2)*w1));
          acc = rbf(acc + rbf(g(j+3)*w0));
          r2[e] = acc * (e? hi16f(vv[p]) : lo16f(vv[p]));
        }
        o[j][p] = pk2(r2[0], r2[1]);
      }
    }
    put_u(smem, tr, o[0], o[1]);
    {
      const int ch = t >> 8, j = t & 255;
      u32x4 r;
      r.x = __builtin_amdgcn_alignbit(sx.w, sx.w, 16); r.y = __builtin_amdgcn_alignbit(sx.z, sx.z, 16);
      r.z = __builtin_amdgcn_alignbit(sx.y, sx.y, 16); r.w = __builtin_amdgcn_alignbit(sx.x, sx.x, 16);
      char* kb = smem + TOFF + ch*KST;
      *(u32x4*)(kb + 16*(j+15)) = r;
      if (j < 15) *(u32x4*)(kb + 16*j) = u32x4{0,0,0,0};
    }
    __syncthreads();
    TSP(2); TSP(3);
    if (wave >= 8){
      const int r4 = (((t - 512) + 32*(w5 & 15)) & 511)*4;
      u32x2 a[4];
      #pragma unroll
      for (int i=0;i<4;i++) a[i] = *(const u32x2*)(q + (r4+i)*D_ + c0);
      *(u32x4*)(smem + QOFF + 8*r4)      = u32x4{a[0].x, a[0].y, a[1].x, a[1].y};
      *(u32x4*)(smem + QOFF + 8*r4 + 16) = u32x4{a[2].x, a[2].y, a[3].x, a[3].y};
    }
  }
  if (isA){
    const int ch = wave >> 2, role = (wave + ch) & 3;
    if (role == 0) near_conv<4,0>(smem, ch, m, kg);
    else if (role == 1) near_conv<4,4>(smem, ch, m, kg);
    else if (role == 2){ __builtin_amdgcn_s_setprio(3); far_conv<0,4>(smem, ch, m, kg); __builtin_amdgcn_s_setprio(0); }
    else { __builtin_amdgcn_s_setprio(3); far_conv<4,6>(smem, ch, m, kg); __builtin_amdgcn_s_setprio(0); }
    TSP(5);
  } else if (wave < 8){
    const int ch = wave >> 1;
    const int g = wave & 1;
    const int part = 0;
    const int unit = ch;
    const uint32_t kbase = (uint32_t)(uintptr_t)smem + TOFF + unit*KST + 16*(m - kg + 3);
    const char* ub = smem + ch*UST;
    bf16x8 KA[4], KB[4];
    LDK4(KA, 15);
    f32x4 acc[4];
    {
      bf16x8 Up[2][4];
      {
        const uint32_t sel = (m & 1) ? 0x07060504u : 0x05040302u;
        const char* pp = ub + 272*(m >> 1) + 16*kg + 8*g;
        #pragma unroll
        for (int c=0;c<4;c++){ W6 W = uwin(pp + 64*c); Up[0][c] = usel(W, 0, sel); Up[1][c] = usel(W, 1, sel); }
      }
      f32x4 P0 = {0,0,0,0}, P1 = {0,0,0,0};
#define PH1(dl, CUR, NXT) \
      LDK4(NXT, (dl)-1); WAITK(4, CUR); \
      if ((dl) < 15){ P0 = shrN2(P0); P1 = shrN2(P1); } \
      P0 = MFMA16(CUR[0], Up[0][0], P0); P1 = MFMA16(CUR[0], Up[1][0], P1); \
      P0 = MFMA16(CUR[1], Up[0][1], P0); P1 = MFMA16(CUR[1], Up[1][1], P1); \
      P0 = MFMA16(CUR[2], Up[0][2], P0); P1 = MFMA16(CUR[2], Up[1][2], P1); \
      P0 = MFMA16(CUR[3], Up[0][3], P0); P1 = MFMA16(CUR[3], Up[1][3], P1);
      PH1(15, KA, KB) PH1(14, KB, KA) PH1(13, KA, KB) PH1(12, KB, KA)
      PH1(11, KA, KB) PH1(10, KB, KA) PH1(9, KA, KB) PH1(8, KB, KA)
      const bool ok = m >= 1 && m <= 8;
      const int src = 4*((lane & 48) + ((2*(m-1)) & 15));
      #pragma unroll
      for (int i=0;i<4;i++){
        float a0 = i2f(__builtin_amdgcn_ds_bpermute(src,   f2i(P0[i])));
        float a1 = i2f(__builtin_amdgcn_ds_bpermute(src+4, f2i(P0[i])));
        float a2 = i2f(__builtin_amdgcn_ds_bpermute(src,   f2i(P1[i])));
        float a3 = i2f(__builtin_amdgcn_ds_bpermute(src+4, f2i(P1[i])));
        acc[0][i] = ok ? a0 : 0.f; acc[1][i] = ok ? a1 : 0.f; acc[2][i] = ok ? a2 : 0.f; acc[3][i] = ok ? a3 : 0.f;
      }
    }
    TSP(4);
    bf16x8 U[4][4];
    {
      const char* pp = ub + 272*m + 16*kg + 8*g;
      #pragma unroll
      for (int c=0;c<4;c++){ W6 W = uwin(pp + 64*c); U[0][c] = uodd(W, 0); U[1][c] = uevn(W, 1); U[2][c] = uodd(W, 1); U[3][c] = uevn(W, 2); }
    }
#define PH2(dl, CUR, NXT) \
    if ((dl) > 0){ LDK4(NXT, (dl)-1); WAITK(4, CUR); } else { WAITK(0, CUR); } \
    if ((dl) < 7){ acc[0] = shrN1(acc[0]); acc[1] = shrN1(acc[1]); acc[2] = shrN1(acc[2]); acc[3] = shrN1(acc[3]); } \
    _Pragma("unroll") for (int c=0;c<4;c++){ \
      acc[0] = MFMA16(CUR[c], U[0][c], acc[0]); acc[1] = MFMA16(CUR[c], U[1][c], acc[1]); \
      acc[2] = MFMA16(CUR[c], U[2][c], acc[2]); acc[3] = MFMA16(CUR[c], U[3][c], acc[3]); }
    PH2(7, KA, KB) PH2(6, KB, KA) PH2(5, KA, KB) PH2(4, KB, KA)
    PH2(3, KA, KB) PH2(2, KB, KA) PH2(1, KA, KB) PH2(0, KB, KA)
    TSP(5);
    char* yb = smem + YOFF + (part*4 + ch)*YST + 4*(4*g + 32*kg + 132*m);
    #pragma unroll
    for (int i=0;i<4;i++) *(f32x4*)(yb + 32*i) = f32x4{acc[0][i], acc[1][i], acc[2][i], acc[3][i]};
  }
  __syncthreads();
  TSP(6);
  {
    const char* yb = smem + YOFF + 8*tr + 16*(tr >> 6);
    const char* up = smem + 272*(tr >> 6) + 16 + 4*(tr & 63);
    float o0[4], o1[4];
    if (isA){
      const char* ya = yb + (YAOFF - YOFF);
      const char* yf = smem + YFOFF + (tr >= 257 ? 4*(2*tr - 515) : -16);
      #pragma unroll
      for (int c=0;c<4;c++){
        f32x2 a = *(const f32x2*)(ya + c*YST);
        float b0 = *(const float*)(yf + c*YFST), b1 = *(const float*)(yf + c*YFST + 4);
        uint32_t uu = *(const uint32_t*)(up + c*UST);
        float sd = (c & 1) ? hi16f(dsk[c>>1]) : lo16f(dsk[c>>1]);
        o0[c] = rbf(a.x + b0) + rbf(lo16f(uu)*sd);
        o1[c] = rbf(a.y + b1) + rbf(hi16f(uu)*sd);
      }
    } else {
      u32x4 qq = *(const u32x4*)(smem + QOFF + 16*tr);
      #pragma unroll
      for (int c=0;c<4;c++){
        f32x2 a = *(const f32x2*)(yb + c*YST);
        uint32_t uu = *(const uint32_t*)(up + c*UST);
        float sd = (c & 1) ? hi16f(dsk[c>>1]) : lo16f(dsk[c>>1]);
        float q0 = (c & 1) ? hi16f(qq[c>>1]) : lo16f(qq[c>>1]);
        float q1 = (c & 1) ? hi16f(qq[2+(c>>1)]) : lo16f(qq[2+(c>>1)]);
        o0[c] = q0 * rbf(rbf(a.x) + rbf(lo16f(uu)*sd));
        o1[c] = q1 * rbf(rbf(a.y) + rbf(hi16f(uu)*sd));
      }
    }
    u16* op = out + (isA ? 0 : D_) + c0;
    *(u32x2*)(op + (2*tr)*(2*D_))   = u32x2{pk2(o0[0],o0[1]), pk2(o0[2],o0[3])};
    *(u32x2*)(op + (2*tr+1)*(2*D_)) = u32x2{pk2(o1[0],o1[1]), pk2(o1[2],o1[3])};
  }
}

extern "C" void kernel_launch(void* const* d_in, const int* in_sizes, int n_in,
                              void* d_out, int out_size, void* d_ws, size_t ws_size,
                              hipStream_t stream) {
  mono_kernel<<<256, 1024, LDS_TOTAL, stream>>>((const u16*)d_in[0], (const u16*)d_in[1], (const u16*)d_in[2], (const u16*)d_in[3],
     (const u16*)d_in[4], (const u16*)d_in[5], (const u16*)d_in[6], (const u16*)d_in[7], (const u16*)d_in[8], (const u16*)d_in[9],
     (const u16*)d_in[10], (const u16*)d_in[11], (const u16*)d_in[12], (u16*)d_out
);
}
```

```cpp
#include <hip/hip_runtime.h>
#include <hip/hip_bf16.h>
#include <stdint.h>

typedef unsigned short u16;
typedef short bf16x8 __attribute__((ext_vector_type(8)));
typedef float f32x4 __attribute__((ext_vector_type(4)));
typedef float f32x2 __attribute__((ext_vector_type(2)));
typedef __bf16 bf16x2_t __attribute__((ext_vector_type(2)));

#define L_ 2048
#define D_ 512
#define NS_ 128

__device__ __forceinline__ float bf2f(u16 x){ return __builtin_bit_cast(float, (uint32_t)x << 16); }
__device__ __forceinline__ u16 f2bf(float f){ __hip_bfloat16 h = __float2bfloat16(f); return __builtin_bit_cast(u16, h); }
__device__ __forceinline__ float rbf(float f){ return bf2f(f2bf(f)); }
__device__ __forceinline__ uint32_t pk2(float a, float b){
  f32x2 v = {a,b}; bf16x2_t r = __builtin_convertvector(v, bf16x2_t); return __builtin_bit_cast(uint32_t, r); }
__device__ __forceinline__ float lo16f(uint32_t w){ return __builtin_bit_cast(float, w << 16); }
__device__ __forceinline__ float hi16f(uint32_t w){ return __builtin_bit_cast(float, w & 0xffff0000u); }

struct cpx { float re, im; };
__device__ __forceinline__ float amul(float a, float b){ float r; asm("v_mul_f32 %0, %1, %2" : "=v"(r) : "v"(a), "v"(b)); return r; }
__device__ __forceinline__ float afma(float a, float b, float c){ float r; asm("v_fma_f32 %0, %1, %2, %3" : "=v"(r) : "v"(a), "v"(b), "v"(c)); return r; }
__device__ __forceinline__ float afms(float a, float b, float c){ float r; asm("v_fma_f32 %0, %1, %2, -%3" : "=v"(r) : "v"(a), "v"(b), "v"(c)); return r; }
__device__ __forceinline__ cpx cmul(cpx a, cpx b){ cpx r; r.re = __builtin_fmaf(a.re, b.re, -(a.im*b.im)); r.im = __builtin_fmaf(a.re, b.im, a.im*b.re); return r; }
__device__ __forceinline__ cpx cexp_(float re, float im){ float e = expf(re); float s,c; sincosf(im,&s,&c); cpx r; r.re=e*c; r.im=e*s; return r; }

#define MFMA16(a,b,c) __builtin_amdgcn_mfma_f32_16x16x32_bf16(a,b,c,0,0,0)
typedef uint32_t u32x2 __attribute__((ext_vector_type(2)));
typedef uint32_t u32x4 __attribute__((ext_vector_type(4)));
#define KC_ 656
#define TR 288
#define TLO (57*TR)
#define TSZ (2*TLO)
#define UST 4352
#define KCOFF 17408
#define TOFF 27904
#define KST 4352
#define KS2 1536
#define FTOFF (TOFF + 12288)
#define FCS 320
#define YFOFF (TOFF + 45056 + 16)
#define YFST 6176
#define YAOFF (TOFF + 69888)
#define YOFF (TOFF + 36864)
#define YST 8448
#define QOFF (TOFF + 112128)
#define LDS_TOTAL (TOFF + 4*TSZ)

__device__ __forceinline__ uint32_t resid2(uint32_t h, float a, float b){
  return pk2(a - lo16f(h), b - hi16f(h));
}
__device__ __forceinline__ void put_split(char* p, float a, float b){
  uint32_t h = pk2(a,b);
  uint32_t l = resid2(h, a, b);
  *(uint32_t*)p = h; *(uint32_t*)(p + TLO) = l;
}
__device__ __forceinline__ int f2i(float x){ return __builtin_bit_cast(int, x); }
__device__ __forceinline__ float i2f(int x){ return __builtin_bit_cast(float, x); }
__device__ __forceinline__ f32x4 shrN1(f32x4 x){
  f32x4 r;
  #pragma unroll
  for (int i=0;i<4;i++) r[i] = i2f(__builtin_amdgcn_update_dpp(0, f2i(x[i]), 0x111, 0xf, 0xf, true));
  return r;
}
__device__ __forceinline__ f32x4 shrN2(f32x4 x){
  f32x4 r;
  #pragma unroll
  for (int i=0;i<4;i++) r[i] = i2f(__builtin_amdgcn_update_dpp(0, f2i(x[i]), 0x112, 0xf, 0xf, true));
  return r;
}
struct W6 { uint32_t w[6]; };
__device__ __forceinline__ W6 uwin(const char* p){
  u32x2 a = *(const u32x2*)p, b = *(const u32x2*)(p+8), c = *(const u32x2*)(p+16);
  W6 r; r.w[0]=a.x; r.w[1]=a.y; r.w[2]=b.x; r.w[3]=b.y; r.w[4]=c.x; r.w[5]=c.y; return r;
}
__device__ __forceinline__ bf16x8 usel(const W6& W, int x, uint32_t sel){
  u32x4 r;
  r.x = __builtin_amdgcn_perm(W.w[x+1], W.w[x],   sel); r.y = __builtin_amdgcn_perm(W.w[x+2], W.w[x+1], sel);
  r.z = __builtin_amdgcn_perm(W.w[x+3], W.w[x+2], sel); r.w = __builtin_amdgcn_perm(W.w[x+4], W.w[x+3], sel);
  return __builtin_bit_cast(bf16x8, r);
}
__device__ __forceinline__ bf16x8 uodd(const W6& W, int x){
  u32x4 r;
  r.x = __builtin_amdgcn_alignbit(W.w[x+1], W.w[x],   16); r.y = __builtin_amdgcn_alignbit(W.w[x+2], W.w[x+1], 16);
  r.z = __builtin_amdgcn_alignbit(W.w[x+3], W.w[x+2], 16); r.w = __builtin_amdgcn_alignbit(W.w[x+4], W.w[x+3], 16);
  return __builtin_bit_cast(bf16x8, r);
}
__device__ __forceinline__ bf16x8 uevn(const W6& W, int x){
  u32x4 r = {W.w[x], W.w[x+1], W.w[x+2], W.w[x+3]};
  return __builtin_bit_cast(bf16x8, r);
}
__device__ __forceinline__ void put_u(char* smem, int tr, u32x2 r0, u32x2 r1){
  uint32_t d[4];
  d[0] = __builtin_amdgcn_perm(r1.x, r0.x, 0x05040100); d[1] = __builtin_amdgcn_perm(r1.x, r0.x, 0x07060302);
  d[2] = __builtin_amdgcn_perm(r1.y, r0.y, 0x05040100); d[3] = __builtin_amdgcn_perm(r1.y, r0.y, 0x07060302);
  const int a = tr >> 6, pos = tr & 63;
  char* p = smem + 272*a + 16 + 4*pos;
  #pragma unroll
  for (int c=0;c<4;c++) *(uint32_t*)(p + c*UST) = d[c];
  if (pos >= 60 && a < 15){
    #pragma unroll
    for (int c=0;c<4;c++) *(uint32_t*)(p + c*UST + 16) = d[c];
  }
  if (tr < 16) *(uint32_t*)(smem + (tr>>2)*UST + 4*(tr&3)) = 0u;
}

#define LDK(F, dl, c) asm volatile("ds_read_b128 %0, %1 offset:%2" : "=v"(F) : "v"(kbase), "i"(256*(dl) + 64*(3-(c))))
#define LDK4(S, dl) LDK(S[0], dl, 0); LDK(S[1], dl, 1); LDK(S[2], dl, 2); LDK(S[3], dl, 3)
#define WAITK(n, S) asm volatile("s_waitcnt lgkmcnt(" #n ")" : "+v"(S[0]), "+v"(S[1]), "+v"(S[2]), "+v"(S[3]))

template<int NT, int R0>
__device__ __forceinline__ void near_conv(char* smem, int ch, int m, int kg){
  const uint32_t kbase = (uint32_t)(uintptr_t)smem + TOFF + (2*ch)*KS2 + 16*(m - kg + 3);
#define LDN(F, hs, c) asm volatile("ds_read_b128 %0, %1 offset:%2" : "=v"(F) : "v"(kbase), "i"(((hs)&1)*KS2 + 256*(4-((hs)>>1)) + 64*(3-(c))))
#define LDN4(S, hs) LDN(S[0], hs, 0); LDN(S[1], hs, 1); LDN(S[2], hs, 2); LDN(S[3], hs, 3)
  bf16x8 KA[4], KB[4];
  LDN4(KA, 0);
  bf16x8 U[NT][4];
  {
    const char* pp = smem + ch*UST + 272*m + 16*kg + (R0 ? 8 : 0);
    #pragma unroll
    for (int c=0;c<4;c++){
      W6 W = uwin(pp + 64*c);
      #pragma unroll
      for (int r=0;r<NT;r++){ const int rel = R0 + r + 1 - (R0 ? 4 : 0); U[r][c] = (rel & 1) ? uodd(W, rel >> 1) : uevn(W, rel >> 1); }
    }
  }
  f32x4 acc[NT];
  #pragma unroll
  for (int r=0;r<NT;r++) acc[r] = f32x4{0,0,0,0};
#define NH(hs, CUR, NXT) \
  if ((hs) < 9){ LDN4(NXT, (hs)+1); WAITK(4, CUR); } else { WAITK(0, CUR); } \
  if ((hs) > 0 && !((hs) & 1)){ _Pragma("unroll") for (int r=0;r<NT;r++) acc[r] = shrN1(acc[r]); } \
  _Pragma("unroll") for (int c=0;c<4;c++){ _Pragma("unroll") for (int r=0;r<NT;r++) acc[r] = MFMA16(CUR[c], U[r][c], acc[r]); }
  NH(0, KA, KB) NH(1, KB, KA) NH(2, KA, KB) NH(3, KB, KA) NH(4, KA, KB)
  NH(5, KB, KA) NH(6, KA, KB) NH(7, KB, KA) NH(8, KA, KB) NH(9, KB, KA)
  float* yb = (float*)(smem + YAOFF + ch*YST) + (R0 + 32*kg + 132*m);
  #pragma unroll
  for (int i=0;i<4;i++){
    #pragma unroll
    for (int r=0;r<NT;r++) yb[8*i + r] = acc[r][i];
  }
}
__device__ __forceinline__ bf16x8 band(int sh, int w, int kg){
  const uint32_t by = (((1u << w) - 1u) << sh) >> (8*kg);
  u32x4 r;
  #pragma unroll
  for (int k2=0;k2<4;k2++){ uint32_t x = (by >> (2*k2)) & 3u; x = (x | (x << 15)) & 0x00010001u; r[k2] = x * 0x3f80u; }
  return __builtin_bit_cast(bf16x8, r);
}
__device__ __forceinline__ void split2(const f32x4& a, const f32x4& b, bf16x8& h, bf16x8& l){
  u32x4 hh, ll;
  #pragma unroll
  for (int i=0;i<4;i++){ uint32_t x = pk2(a[i], b[i]); hh[i] = x; ll[i] = resid2(x, a[i], b[i]); }
  h = __builtin_bit_cast(bf16x8, hh); l = __builtin_bit_cast(bf16x8, ll);
}
template<int A0, int A1>
__device__ __forceinline__ void far_conv(char* smem, int ch, int m, int kg){
  const char* ub = smem + ch*UST;
  const f32x4 Z4 = {0,0,0,0};
  const int B0 = 2*m + kg - 2;
  const char* va0 = ub + (B0 < 0 ? 0 : 272*(B0 >> 4) + 16 + 16*(B0 & 15));
  const char* va1 = ub + 272*((B0+32) >> 4) + 16 + 16*((B0+32) & 15);
  const int cp = (3 - m) & 3;
  const char* ft = smem + FTOFF + ch*8192 + cp*FCS + 4*(47 - m + 4*kg - cp);
  char* yf = smem + YFOFF + ch*YFST + 64*m + 16*kg;
  f32x4 acc[6];
  #pragma unroll
  for (int a=0;a<6;a++) acc[a] = Z4;
  {
    bf16x8 M[4];
    M[0] = band(14 + m, 3, kg); M[1] = band(9 + m, 5, kg); M[2] = band(6 + m, 3, kg); M[3] = band(1 + m, 5, kg);
    bf16x8 Ch[3][2], Cl[3][2];
    #pragma unroll
    for (int dl=0;dl<3;dl++){
      #pragma unroll
      for (int h=0;h<2;h++){ Ch[dl][h] = *(const bf16x8*)(ft + h*(8*FCS) - 64*dl); Cl[dl][h] = *(const bf16x8*)(ft + h*(8*FCS) + 4*FCS - 64*dl); }
    }
    #pragma unroll
    for (int b=(A0 > 2 ? A0-2 : 0);b<A1;b++){
      const bf16x8 V = *(const bf16x8*)(b ? va1 + 544*(b-1) : va0);
      f32x4 F[4];
      #pragma unroll
      for (int p=0;p<4;p++) F[p] = MFMA16(V, M[p], Z4);
      #pragma unroll
      for (int h=0;h<2;h++){
        bf16x8 Bh, Bl;
        split2(F[2*h], F[2*h+1], Bh, Bl);
        #pragma unroll
        for (int dl=0;dl<3;dl++) if (b + dl >= A0 && b + dl < A1) acc[b+dl] = MFMA16(Bh, Ch[dl][h], acc[b+dl]);
        #pragma unroll
        for (int dl=0;dl<3;dl++) if (b + dl >= A0 && b + dl < A1) acc[b+dl] = MFMA16(Bh, Cl[dl][h], acc[b+dl]);
        #pragma unroll
        for (int dl=0;dl<3;dl++) if (b + dl >= A0 && b + dl < A1) acc[b+dl] = MFMA16(Bl, Ch[dl][h], acc[b+dl]);
      }
      if (b < 2 && b >= A0) *(f32x4*)(yf + 1024*b) = acc[b];
    }
  }
  {
    const char* vb0 = ub + (B0 < 1 ? 0 : 272*((B0-1) >> 4) + 16 + 16*((B0-1) & 15));
    const char* vb1 = ub + 272*((B0+31) >> 4) + 16 + 16*((B0+31) & 15);
    const char* f8 = smem + FTOFF + ch*8192 + 16*FCS + cp*FCS + 4*(63 - m + 4*kg - cp);
    const bf16x8 Ma = band(8 + m, 7, kg), Mb = band(7 + m, 9, kg), Mc = band(15 + m, 2, kg);
    const float k1024 = ((const float*)(smem + KCOFF))[ch*KC_ + 512];
    bf16x8 Ch[4], Cl[4];
    #pragma unroll
    for (int dl=0;dl<A1-2;dl++){ Ch[dl] = *(const bf16x8*)(f8 - 64*dl); Cl[dl] = *(const bf16x8*)(f8 + 4*FCS - 64*dl); }
    #pragma unroll
    for (int b=0;b<A1-2;b++){
      const bf16x8 V = *(const bf16x8*)(b ? va1 + 544*(b-1) : va0);
      const bf16x8 V2 = *(const bf16x8*)(b ? vb1 + 544*(b-1) : vb0);
      const f32x4 Fa = MFMA16(V, Ma, Z4), Fb = MFMA16(V2, Mb, Z4);
      bf16x8 Bh, Bl;
      split2(Fa, Fb, Bh, Bl);
      if (b + 2 >= A0){
        const f32x4 Fc = MFMA16(Mc, V, Z4);
        #pragma unroll
        for (int i=0;i<4;i++) acc[b+2][i] = __builtin_fmaf(k1024, Fc[i], acc[b+2][i]);
      }
      #pragma unroll
      for (int dl=0;dl<4;dl++) if (b + 2 + dl >= A0 && b + 2 + dl < A1) acc[b+2+dl] = MFMA16(Bh, Ch[dl], acc[b+2+dl]);
      #pragma unroll
      for (int dl=0;dl<4;dl++) if (b + 2 + dl >= A0 && b + 2 + dl < A1) acc[b+2+dl] = MFMA16(Bh, Cl[dl], acc[b+2+dl]);
      #pragma unroll
      for (int dl=0;dl<4;dl++) if (b + 2 + dl >= A0 && b + 2 + dl < A1) acc[b+2+dl] = MFMA16(Bl, Ch[dl], acc[b+2+dl]);
    }
  }
  #pragma unroll
  for (int a=(A0 > 2 ? A0 : 2);a<A1;a++) *(f32x4*)(yf + 1024*a) = acc[a];
}

__global__ __launch_bounds__(1024) void mono_kernel(const u16* __restrict__ u, const u16* __restrict__ logA, const u16* __restrict__ Aim,
    const u16* __restrict__ Cre, const u16* __restrict__ Cim, const u16* __restrict__ logdt, const u16* __restrict__ Dskip,
    const u16* __restrict__ q, const u16* __restrict__ k, const u16* __restrict__ v, const u16* __restrict__ shift_k,
    const u16* __restrict__ ssmk, const u16* __restrict__ ssmD, u16* __restrict__ out
){
#define TSP(i)
  TSP(0); TSP(1); TSP(2); TSP(3); TSP(4); TSP(5);
  extern __shared__ __attribute__((aligned(16))) char smem[];
  const int bid = blockIdx.x, t = threadIdx.x;
  const int w5 = bid >> 3;
  const bool isA = w5 < 16;
  const int c0 = (bid & 7)*64 + (w5 & 15)*4;
  const int tr = (t + 64*(w5 & 15)) & 1023;
  const int wave = t >> 6, lane = t & 63, m = lane & 15, kg = lane >> 4;
  const u32x2 dsk = *(const u32x2*)((isA ? Dskip : ssmD) + c0);
  if (isA){
    const int ch = t >> 8, tg = t & 255, wv = (wave + ch) & 3;
    const int d = c0 + ch;
    char* tb = smem + TOFF + ch*TSZ;
    char* eb_ = smem + KCOFF + ch*(4*KC_);
    const u16 ldt = logdt[d];
    const u16 pla = logA[d*NS_+(tg & 127)], pai = Aim[d*NS_+(tg & 127)];
    u16 pc[2][2];
    #pragma unroll
    for (int hp=0;hp<2;hp++){ pc[hp][0] = Cre[d*NS_+64*hp+lane];  pc[hp][1] = Cim[d*NS_+64*hp+lane]; }
    const float dt = rbf(expf(bf2f(ldt)));
    {
      const float zre = -rbf(expf(bf2f(pla))) * dt;
      const float zim = bf2f(pai) * dt;
      const float f = (tg >> 7) ? 128.f : 1.f;
      cpx e = cexp_(zre*f, zim*f);
      *(float2*)(eb_ + 8*tg) = make_float2(e.re, e.im);
    }
    __syncthreads();
    TSP(1);
    __builtin_amdgcn_sched_barrier(0);
    const u32x2 u0 = *(const u32x2*)(u + (2*tr)*D_ + c0);
    const u32x2 u1 = *(const u32x2*)(u + (2*tr+1)*D_ + c0);
    __builtin_amdgcn_sched_barrier(0);
    f32x4 a4 = {0,0,0,0};
    char* col = tb + 4*lane;
    #pragma unroll 1
    for (int hp=0;hp<2;hp++){
      cpx C; C.re = bf2f(hp ? pc[1][0] : pc[0][0])*dt; C.im = bf2f(hp ? pc[1][1] : pc[0][1])*dt;
      cpx e1, e2;
      { float2 a = *(const float2*)(eb_ + 8*(64*hp+lane)); e2.re = a.x; e2.im = a.y;
        float2 c = *(const float2*)(eb_ + 1024 + 8*(64*hp+lane)); e1.re = c.x; e1.im = c.y; }
      if (wv == 0){
        cpx e8 = cmul(e2, e2); e8 = cmul(e8, e8); e8 = cmul(e8, e8);
        cpx xx; xx.re = 1.f; xx.im = 0.f;
        #pragma unroll
        for (int i=0;i<16;i++){ put_split(col + (41+i)*TR, xx.re, -xx.im); if (i < 15) xx = (i == 0) ? e8 : cmul(xx, e8); }
      } else if (wv == 1){
        cpx eb = cmul(e1, e1); eb = cmul(eb, eb); eb = cmul(eb, eb);
        cpx cb = cmul(C, eb);
        #pragma unroll
        for (int i=0;i<9;i++){ put_split(col + (32+i)*TR, cb.re, cb.im); if (i < 8) cb = cmul(cb, e1); }
      } else if (wv == 2){
        cpx S[8];
        S[1] = e2;
        #pragma unroll
        for (int i=2;i<8;i++) S[i] = cmul(S[i-1], S[1]);
        cpx cb = C;
        #pragma unroll
        for (int a=0;a<2;a++){
          put_split(col + (8*a)*TR, cb.re, cb.im);
          #pragma unroll
          for (int p=1;p<8;p++){ cpx g = cmul(cb, S[p]); put_split(col + (8*a+p)*TR, g.re, g.im); }
          if (a == 0) cb = cmul(cb, e1);
        }
      } else {
        cpx S2 = cmul(e2, e2), S4 = cmul(S2, S2), S6 = cmul(S4, S2);
        cpx cb = cmul(C, cmul(e1, e1));
        #pragma unroll
        for (int a=0;a<2;a++){
          put_split(col + (16+4*a)*TR, cb.re, cb.im);
          { cpx g = cmul(cb, S2); put_split(col + (16+4*a+1)*TR, g.re, g.im); }
          { cpx g = cmul(cb, S4); put_split(col + (16+4*a+2)*TR, g.re, g.im); }
          { cpx g = cmul(cb, S6); put_split(col + (16+4*a+3)*TR, g.re, g.im); }
          cb = cmul(cb, e1);
        }
        #pragma unroll
        for (int a=0;a<4;a++){
          put_split(col + (24+2*a)*TR, cb.re, cb.im);
          cpx g = cmul(cb, S4); put_split(col + (24+2*a+1)*TR, g.re, g.im);
          if (a < 3) cb = cmul(cb, e1);
        }
      }
      __syncthreads();
      if (wv < 3){
        const char* ga = tb + (16*wv + m)*TR + 16*kg;
        const char* xb = tb + (41 + m)*TR + 16*kg;
        #pragma unroll
        for (int kc=0;kc<4;kc++){
          bf16x8 ah = *(const bf16x8*)(ga + 64*kc), al = *(const bf16x8*)(ga + 64*kc + TLO);
          bf16x8 bh = *(const bf16x8*)(xb + 64*kc), bl = *(const bf16x8*)(xb + 64*kc + TLO);
          a4 = MFMA16(ah, bh, a4);
          a4 = MFMA16(ah, bl, a4);
          a4 = MFMA16(al, bh, a4);
        }
      }
      if (hp == 0) __syncthreads();
    }
    float* Kc = (float*)(smem + KCOFF) + ch*KC_;
    if (wv < 3){
      #pragma unroll
      for (int i=0;i<4;i++){
        int r = 16*wv + 4*kg + i;
        int idx;
        if (r < 16) idx = 128*(r>>3) + (r&7) + 8*m;
        else if (r < 24) idx = 256 + 64*((r-16)>>2) + (r&3) + 4*m;
        else if (r < 32) idx = 384 + 32*((r-24)>>1) + (r&1) + 2*m;
        else idx = 512 + 16*(r-32) + m;
        if (r < 40 || (r == 40 && m == 0)) Kc[idx] = 2.f*a4[i];
      }
    }
    put_u(smem, tr, u0, u1);
    __syncthreads();
    TSP(2);
    {
      char* ft = smem + FTOFF + ch*8192;
      if (tg < 208){
        int z, idx; bool ok; char* wp;
        if (tg < 128){ z = tg & 63; const int Dd = 47 - z; ok = Dd >= 0 && Dd < 32; idx = 385 + 4*Dd + 2*(tg >> 6); wp = ft + (tg >> 6)*(8*FCS); }
        else { z = tg - 128; const int Dd = 63 - z; ok = Dd >= 0; idx = 513 + 2*Dd; wp = ft + 16*FCS; }
        float a = 0.f, b = 0.f;
        if (ok){ a = Kc[idx]; b = Kc[idx+1]; }
        const uint32_t hh = pk2(a, b), ll = pk2(a - lo16f(hh), b - hi16f(hh));
        #pragma unroll
        for (int c=0;c<4;c++) if (z >= c){ *(uint32_t*)(wp + c*FCS + 4*(z-c)) = hh; *(uint32_t*)(wp + (4+c)*FCS + 4*(z-c)) = ll; }
      }
      if (tg >= 161){
        const int sl = tg - 161, j = sl - 15;
        float kv[8];
        #pragma unroll
        for (int i=0;i<8;i++){
          int l = 8*j + 7 - i;
          int pr = (int)rbf((float)l);
          int p = pr < 256 ? pr : 128 + (pr>>1);
          kv[i] = (l >= 0 && l <= 514) ? Kc[p] : 0.f;
        }
        u32x4 h, lo;
        #pragma unroll
        for (int jj=0;jj<4;jj++){
          uint32_t hh = pk2(kv[2*jj], kv[2*jj+1]);
          h[jj] = hh; lo[jj] = pk2(kv[2*jj] - lo16f(hh), kv[2*jj+1] - hi16f(hh));
        }
        char* kb = smem + TOFF + (2*ch)*KS2;
        *(u32x4*)(kb + 16*sl) = h;
        *(u32x4*)(kb + KS2 + 16*sl) = lo;
      }
      if (tg < 4) *(float*)(smem + YFOFF + ch*YFST - 16 + 4*tg) = 0.f;
    }
    __syncthreads();
    TSP(3);
  } else {
    const u32x2 k0 = *(const u32x2*)(k + (2*tr)*D_ + c0);
    const u32x2 k1 = *(const u32x2*)(k + (2*tr+1)*D_ + c0);
    const u32x4 wa = *(const u32x4*)(shift_k + c0*4), wb = *(const u32x4*)(shift_k + c0*4 + 8);
    char* ks = smem + YOFF;
    __builtin_amdgcn_sched_barrier(0);
    *(u32x4*)(ks + 32 + 16*tr) = u32x4{k0.x, k0.y, k1.x, k1.y};
    __builtin_amdgcn_sched_barrier(0);
    const u32x2 v0 = *(const u32x2*)(v + (2*tr)*D_ + c0);
    const u32x2 v1 = *(const u32x2*)(v + (2*tr+1)*D_ + c0);
    const u32x4 sx = *(const u32x4*)(ssmk + (c0+(t >> 8))*L_ + 8*(t & 255));
    __builtin_amdgcn_sched_barrier(0);
    if (t < 2) *(u32x4*)(ks + 16*t) = u32x4{0,0,0,0};
    __syncthreads();
    TSP(1);
    u32x2 kk[5];
    kk[0] = *(const u32x2*)(ks + 8 + 16*tr);
    { u32x4 x = *(const u32x4*)(ks + 16 + 16*tr); kk[1] = u32x2{x.x, x.y}; kk[2] = u32x2{x.z, x.w}; }
    kk[3] = k0; kk[4] = k1;
    u32x2 o[2];
    #pragma unroll
    for (int j=0;j<2;j++){
      const u32x2 vv = j ? v1 : v0;
      #pragma unroll
      for (int p=0;p<2;p++){
        float r2[2];
        #pragma unroll
        for (int e=0;e<2;e++){
          const u32x4 ww = p ? wb : wa;
          const uint32_t wl = e ? ww.z : ww.x, wh = e ? ww.w : ww.y;
          const float w0 = lo16f(wl), w1 = hi16f(wl), w2 = lo16f(wh), w3 = hi16f(wh);
          auto g = [&](int idx){ uint32_t x = kk[idx][p]; return e? hi16f(x) : lo16f(x); };
          float acc = rbf(g(j)*w3);
          acc = rbf(acc + rbf(g(j+1)*w2));
          acc = rbf(acc + rbf(g(j+2)*w1));
          acc = rbf(acc + rbf(g(j+3)*w0));
          r2[e] = acc * (e? hi16f(vv[p]) : lo16f(vv[p]));
        }
        o[j][p] = pk2(r2[0], r2[1]);
      }
    }
    put_u(smem, tr, o[0], o[1]);
    {
      const int ch = t >> 8, j = t & 255;
      u32x4 r;
      r.x = __builtin_amdgcn_alignbit(sx.w, sx.w, 16); r.y = __builtin_amdgcn_alignbit(sx.z, sx.z, 16);
      r.z = __builtin_amdgcn_alignbit(sx.y, sx.y, 16); r.w = __builtin_amdgcn_alignbit(sx.x, sx.x, 16);
      char* kb = smem + TOFF + ch*KST;
      *(u32x4*)(kb + 16*(j+15)) = r;
      if (j < 15) *(u32x4*)(kb + 16*j) = u32x4{0,0,0,0};
    }
    __syncthreads();
    TSP(2); TSP(3);
    if (wave >= 8){
      const int r4 = (((t - 512) + 32*(w5 & 15)) & 511)*4;
      u32x2 a[4];
      #pragma unroll
      for (int i=0;i<4;i++) a[i] = *(const u32x2*)(q + (r4+i)*D_ + c0);
      *(u32x4*)(smem + QOFF + 8*r4)      = u32x4{a[0].x, a[0].y, a[1].x, a[1].y};
      *(u32x4*)(smem + QOFF + 8*r4 + 16) = u32x4{a[2].x, a[2].y, a[3].x, a[3].y};
    }
  }
  if (isA){
    const int ch = wave >> 2, role = (wave + ch) & 3;
    if (role == 0) near_conv<4,0>(smem, ch, m, kg);
    else if (role == 1) near_conv<4,4>(smem, ch, m, kg);
    else if (role == 2){ __builtin_amdgcn_s_setprio(3); far_conv<0,4>(smem, ch, m, kg); __builtin_amdgcn_s_setprio(0); }
    else { __builtin_amdgcn_s_setprio(3); far_conv<4,6>(smem, ch, m, kg); __builtin_amdgcn_s_setprio(0); }
    TSP(5);
  } else if (wave < 8){
    const int ch = wave >> 1;
    const int g = wave & 1;
    const int part = 0;
    const int unit = ch;
    const uint32_t kbase = (uint32_t)(uintptr_t)smem + TOFF + unit*KST + 16*(m - kg + 3);
    const char* ub = smem + ch*UST;
    bf16x8 KA[4], KB[4];
    LDK4(KA, 15);
    f32x4 acc[4];
    {
      bf16x8 Up[2][4];
      {
        const uint32_t sel = (m & 1) ? 0x07060504u : 0x05040302u;
        const char* pp = ub + 272*(m >> 1) + 16*kg + 8*g;
        #pragma unroll
        for (int c=0;c<4;c++){ W6 W = uwin(pp + 64*c); Up[0][c] = usel(W, 0, sel); Up[1][c] = usel(W, 1, sel); }
      }
      f32x4 P0 = {0,0,0,0}, P1 = {0,0,0,0};
#define PH1(dl, CUR, NXT) \
      LDK4(NXT, (dl)-1); WAITK(4, CUR); \
      if ((dl) < 15){ P0 = shrN2(P0); P1 = shrN2(P1); } \
      P0 = MFMA16(CUR[0], Up[0][0], P0); P1 = MFMA16(CUR[0], Up[1][0], P1); \
      P0 = MFMA16(CUR[1], Up[0][1], P0); P1 = MFMA16(CUR[1], Up[1][1], P1); \
      P0 = MFMA16(CUR[2], Up[0][2], P0); P1 = MFMA16(CUR[2], Up[1][2], P1); \
      P0 = MFMA16(CUR[3], Up[0][3], P0); P1 = MFMA16(CUR[3], Up[1][3], P1);
      PH1(15, KA, KB) PH1(14, KB, KA) PH1(13, KA, KB) PH1(12, KB, KA)
      PH1(11, KA, KB) PH1(10, KB, KA) PH1(9, KA, KB) PH1(8, KB, KA)
      const bool ok = m >= 1 && m <= 8;
      const int src = 4*((lane & 48) + ((2*(m-1)) & 15));
      #pragma unroll
      for (int i=0;i<4;i++){
        float a0 = i2f(__builtin_amdgcn_ds_bpermute(src,   f2i(P0[i])));
        float a1 = i2f(__builtin_amdgcn_ds_bpermute(src+4, f2i(P0[i])));
        float a2 = i2f(__builtin_amdgcn_ds_bpermute(src,   f2i(P1[i])));
        float a3 = i2f(__builtin_amdgcn_ds_bpermute(src+4, f2i(P1[i])));
        acc[0][i] = ok ? a0 : 0.f; acc[1][i] = ok ? a1 : 0.f; acc[2][i] = ok ? a2 : 0.f; acc[3][i] = ok ? a3 : 0.f;
      }
    }
    TSP(4);
    bf16x8 U[4][4];
    {
      const char* pp = ub + 272*m + 16*kg + 8*g;
      #pragma unroll
      for (int c=0;c<4;c++){ W6 W = uwin(pp + 64*c); U[0][c] = uodd(W, 0); U[1][c] = uevn(W, 1); U[2][c] = uodd(W, 1); U[3][c] = uevn(W, 2); }
    }
#define PH2(dl, CUR, NXT) \
    if ((dl) > 0){ LDK4(NXT, (dl)-1); WAITK(4, CUR); } else { WAITK(0, CUR); } \
    if ((dl) < 7){ acc[0] = shrN1(acc[0]); acc[1] = shrN1(acc[1]); acc[2] = shrN1(acc[2]); acc[3] = shrN1(acc[3]); } \
    _Pragma("unroll") for (int c=0;c<4;c++){ \
      acc[0] = MFMA16(CUR[c], U[0][c], acc[0]); acc[1] = MFMA16(CUR[c], U[1][c], acc[1]); \
      acc[2] = MFMA16(CUR[c], U[2][c], acc[2]); acc[3] = MFMA16(CUR[c], U[3][c], acc[3]); }
    PH2(7, KA, KB) PH2(6, KB, KA) PH2(5, KA, KB) PH2(4, KB, KA)
    PH2(3, KA, KB) PH2(2, KB, KA) PH2(1, KA, KB) PH2(0, KB, KA)
    TSP(5);
    char* yb = smem + YOFF + (part*4 + ch)*YST + 4*(4*g + 32*kg + 132*m);
    #pragma unroll
    for (int i=0;i<4;i++) *(f32x4*)(yb + 32*i) = f32x4{acc[0][i], acc[1][i], acc[2][i], acc[3][i]};
  }
  __syncthreads();
  TSP(6);
  {
    const char* yb = smem + YOFF + 8*tr + 16*(tr >> 6);
    const char* up = smem + 272*(tr >> 6) + 16 + 4*(tr & 63);
    float o0[4], o1[4];
    if (isA){
      const char* ya = yb + (YAOFF - YOFF);
      const char* yf = smem + YFOFF + (tr >= 257 ? 4*(2*tr - 515) : -16);
      #pragma unroll
      for (int c=0;c<4;c++){
        f32x2 a = *(const f32x2*)(ya + c*YST);
        float b0 = *(const float*)(yf + c*YFST), b1 = *(const float*)(yf + c*YFST + 4);
        uint32_t uu = *(const uint32_t*)(up + c*UST);
        float sd = (c & 1) ? hi16f(dsk[c>>1]) : lo16f(dsk[c>>1]);
        o0[c] = rbf(a.x + b0) + rbf(lo16f(uu)*sd);
        o1[c] = rbf(a.y + b1) + rbf(hi16f(uu)*sd);
      }
    } else {
      u32x4 qq = *(const u32x4*)(smem + QOFF + 16*tr);
      #pragma unroll
      for (int c=0;c<4;c++){
        f32x2 a = *(const f32x2*)(yb + c*YST);
        uint32_t uu = *(const uint32_t*)(up + c*UST);
        float sd = (c & 1) ? hi16f(dsk[c>>1]) : lo16f(dsk[c>>1]);
        float q0 = (c & 1) ? hi16f(qq[c>>1]) : lo16f(qq[c>>1]);
        float q1 = (c & 1) ? hi16f(qq[2+(c>>1)]) : lo16f(qq[2+(c>>1)]);
        o0[c] = q0 * rbf(rbf(a.x) + rbf(lo16f(uu)*sd));
        o1[c] = q1 * rbf(rbf(a.y) + rbf(hi16f(uu)*sd));
      }
    }
    u16* op = out + (isA ? 0 : D_) + c0;
    *(u32x2*)(op + (2*tr)*(2*D_))   = u32x2{pk2(o0[0],o0[1]), pk2(o0[2],o0[3])};
    *(u32x2*)(op + (2*tr+1)*(2*D_)) = u32x2{pk2(o1[0],o1[1]), pk2(o1[2],o1[3])};
  }
}

extern "C" void kernel_launch(void* const* d_in, const int* in_sizes, int n_in,
                              void* d_out, int out_size, void* d_ws, size_t ws_size,
                              hipStream_t stream) {
  mono_kernel<<<256, 1024, LDS_TOTAL, stream>>>((const u16*)d_in[0], (const u16*)d_in[1], (const u16*)d_in[2], (const u16*)d_in[3],
     (const u16*)d_in[4], (const u16*)d_in[5], (const u16*)d_in[6], (const u16*)d_in[7], (const u16*)d_in[8], (const u16*)d_in[9],
     (const u16*)d_in[10], (const u16*)d_in[11], (const u16*)d_in[12], (u16*)d_out
);
}
```

```cpp
#include <hip/hip_runtime.h>
#include <hip/hip_bf16.h>
#include <stdint.h>

typedef unsigned short u16;
typedef short bf16x8 __attribute__((ext_vector_type(8)));
typedef float f32x4 __attribute__((ext_vector_type(4)));
typedef float f32x2 __attribute__((ext_vector_type(2)));
typedef __bf16 bf16x2_t __attribute__((ext_vector_type(2)));

#define L_ 2048
#define D_ 512
#define NS_ 128

__device__ __forceinline__ float bf2f(u16 x){ return __builtin_bit_cast(float, (uint32_t)x << 16); }
__device__ __forceinline__ u16 f2bf(float f){ __hip_bfloat16 h = __float2bfloat16(f); return __builtin_bit_cast(u16, h); }
__device__ __forceinline__ float rbf(float f){ return bf2f(f2bf(f)); }
__device__ __forceinline__ uint32_t pk2(float a, float b){
  f32x2 v = {a,b}; bf16x2_t r = __builtin_convertvector(v, bf16x2_t); return __builtin_bit_cast(uint32_t, r); }
__device__ __forceinline__ float lo16f(uint32_t w){ return __builtin_bit_cast(float, w << 16); }
__device__ __forceinline__ float hi16f(uint32_t w){ return __builtin_bit_cast(float, w & 0xffff0000u); }

struct cpx { float re, im; };
__device__ __forceinline__ float amul(float a, float b){ float r; asm("v_mul_f32 %0, %1, %2" : "=v"(r) : "v"(a), "v"(b)); return r; }
__device__ __forceinline__ float afma(float a, float b, float c){ float r; asm("v_fma_f32 %0, %1, %2, %3" : "=v"(r) : "v"(a), "v"(b), "v"(c)); return r; }
__device__ __forceinline__ float afms(float a, float b, float c){ float r; asm("v_fma_f32 %0, %1, %2, -%3" : "=v"(r) : "v"(a), "v"(b), "v"(c)); return r; }
__device__ __forceinline__ cpx cmul(cpx a, cpx b){
  f32x2 A = {a.re, a.im}, B = {b.re, b.im}, T, R;
  asm("v_pk_mul_f32 %0, %1, %2 op_sel:[1,1] op_sel_hi:[1,0] neg_lo:[1,0]" : "=v"(T) : "v"(A), "v"(B));
  asm("v_pk_fma_f32 %0, %1, %2, %3 op_sel:[0,0,0] op_sel_hi:[0,1,1]" : "=v"(R) : "v"(A), "v"(B), "v"(T));
  cpx r; r.re = R.x; r.im = R.y; return r; }
__device__ __forceinline__ cpx cexp_(float re, float im){ float e = expf(re); float s,c; sincosf(im,&s,&c); cpx r; r.re=e*c; r.im=e*s; return r; }

#define MFMA16(a,b,c) __builtin_amdgcn_mfma_f32_16x16x32_bf16(a,b,c,0,0,0)
typedef uint32_t u32x2 __attribute__((ext_vector_type(2)));
typedef uint32_t u32x4 __attribute__((ext_vector_type(4)));
#define KC_ 656
#define TR 288
#define TLO (57*TR)
#define TSZ (2*TLO)
#define UST 4352
#define KCOFF 17408
#define TOFF 27904
#define KST 4352
#define KS2 1536
#define FTOFF (TOFF + 12288)
#define FCS 320
#define YFOFF (TOFF + 45056 + 16)
#define YFST 6176
#define YAOFF (TOFF + 69888)
#define YOFF (TOFF + 36864)
#define YST 8448
#define QOFF (TOFF + 112128)
#define LDS_TOTAL (TOFF + 4*TSZ)

__device__ __forceinline__ uint32_t resid2(uint32_t h, float a, float b){
  return pk2(a - lo16f(h), b - hi16f(h));
}
__device__ __forceinline__ void put_split(char* p, float a, float b){
  uint32_t h = pk2(a,b);
  uint32_t l = resid2(h, a, b);
  *(uint32_t*)p = h; *(uint32_t*)(p + TLO) = l;
}
__device__ __forceinline__ int f2i(float x){ return __builtin_bit_cast(int, x); }
__device__ __forceinline__ float i2f(int x){ return __builtin_bit_cast(float, x); }
__device__ __forceinline__ f32x4 shrN1(f32x4 x){
  f32x4 r;
  #pragma unroll
  for (int i=0;i<4;i++) r[i] = i2f(__builtin_amdgcn_update_dpp(0, f2i(x[i]), 0x111, 0xf, 0xf, true));
  return r;
}
__device__ __forceinline__ f32x4 shrN2(f32x4 x){
  f32x4 r;
  #pragma unroll
  for (int i=0;i<4;i++) r[i] = i2f(__builtin_amdgcn_update_dpp(0, f2i(x[i]), 0x112, 0xf, 0xf, true));
  return r;
}
struct W6 { uint32_t w[6]; };
__device__ __forceinline__ W6 uwin(const char* p){
  u32x2 a = *(const u32x2*)p, b = *(const u32x2*)(p+8), c = *(const u32x2*)(p+16);
  W6 r; r.w[0]=a.x; r.w[1]=a.y; r.w[2]=b.x; r.w[3]=b.y; r.w[4]=c.x; r.w[5]=c.y; return r;
}
__device__ __forceinline__ bf16x8 usel(const W6& W, int x, uint32_t sel){
  u32x4 r;
  r.x = __builtin_amdgcn_perm(W.w[x+1], W.w[x],   sel); r.y = __builtin_amdgcn_perm(W.w[x+2], W.w[x+1], sel);
  r.z = __builtin_amdgcn_perm(W.w[x+3], W.w[x+2], sel); r.w = __builtin_amdgcn_perm(W.w[x+4], W.w[x+3], sel);
  return __builtin_bit_cast(bf16x8, r);
}
__device__ __forceinline__ bf16x8 uodd(const W6& W, int x){
  u32x4 r;
  r.x = __builtin_amdgcn_alignbit(W.w[x+1], W.w[x],   16); r.y = __builtin_amdgcn_alignbit(W.w[x+2], W.w[x+1], 16);
  r.z = __builtin_amdgcn_alignbit(W.w[x+3], W.w[x+2], 16); r.w = __builtin_amdgcn_alignbit(W.w[x+4], W.w[x+3], 16);
  return __builtin_bit_cast(bf16x8, r);
}
__device__ __forceinline__ bf16x8 uevn(const W6& W, int x){
  u32x4 r = {W.w[x], W.w[x+1], W.w[x+2], W.w[x+3]};
  return __builtin_bit_cast(bf16x8, r);
}
__device__ __forceinline__ void put_u(char* smem, int tr, u32x2 r0, u32x2 r1){
  uint32_t d[4];
  d[0] = __builtin_amdgcn_perm(r1.x, r0.x, 0x05040100); d[1] = __builtin_amdgcn_perm(r1.x, r0.x, 0x07060302);
  d[2] = __builtin_amdgcn_perm(r1.y, r0.y, 0x05040100); d[3] = __builtin_amdgcn_perm(r1.y, r0.y, 0x07060302);
  const int a = tr >> 6, pos = tr & 63;
  char* p = smem + 272*a + 16 + 4*pos;
  #pragma unroll
  for (int c=0;c<4;c++) *(uint32_t*)(p + c*UST) = d[c];
  if (pos >= 60 && a < 15){
    #pragma unroll
    for (int c=0;c<4;c++) *(uint32_t*)(p + c*UST + 16) = d[c];
  }
  if (tr < 16) *(uint32_t*)(smem + (tr>>2)*UST + 4*(tr&3)) = 0u;
}

#define LDK(F, dl, c) asm volatile("ds_read_b128 %0, %1 offset:%2" : "=v"(F) : "v"(kbase), "i"(256*(dl) + 64*(3-(c))))
#define LDK4(S, dl) LDK(S[0], dl, 0); LDK(S[1], dl, 1); LDK(S[2], dl, 2); LDK(S[3], dl, 3)
#define WAITK(n, S) asm volatile("s_waitcnt lgkmcnt(" #n ")" : "+v"(S[0]), "+v"(S[1]), "+v"(S[2]), "+v"(S[3]))

template<int NT, int R0>
__device__ __forceinline__ void near_conv(char* smem, int ch, int m, int kg){
  const uint32_t kbase = (uint32_t)(uintptr_t)smem + TOFF + (2*ch)*KS2 + 16*(m - kg + 3);
#define LDN(F, hs, c) asm volatile("ds_read_b128 %0, %1 offset:%2" : "=v"(F) : "v"(kbase), "i"(((hs)&1)*KS2 + 256*(4-((hs)>>1)) + 64*(3-(c))))
#define LDN4(S, hs) LDN(S[0], hs, 0); LDN(S[1], hs, 1); LDN(S[2], hs, 2); LDN(S[3], hs, 3)
  bf16x8 KA[4], KB[4];
  LDN4(KA, 0);
  bf16x8 U[NT][4];
  {
    const char* pp = smem + ch*UST + 272*m + 16*kg + (R0 ? 8 : 0);
    #pragma unroll
    for (int c=0;c<4;c++){
      W6 W = uwin(pp + 64*c);
      #pragma unroll
      for (int r=0;r<NT;r++){ const int rel = R0 + r + 1 - (R0 ? 4 : 0); U[r][c] = (rel & 1) ? uodd(W, rel >> 1) : uevn(W, rel >> 1); }
    }
  }
  f32x4 acc[NT];
  #pragma unroll
  for (int r=0;r<NT;r++) acc[r] = f32x4{0,0,0,0};
#define NH(hs, CUR, NXT) \
  if ((hs) < 9){ LDN4(NXT, (hs)+1); WAITK(4, CUR); } else { WAITK(0, CUR); } \
  if ((hs) > 0 && !((hs) & 1)){ _Pragma("unroll") for (int r=0;r<NT;r++) acc[r] = shrN1(acc[r]); } \
  _Pragma("unroll") for (int c=0;c<4;c++){ _Pragma("unroll") for (int r=0;r<NT;r++) acc[r] = MFMA16(CUR[c], U[r][c], acc[r]); }
  NH(0, KA, KB) NH(1, KB, KA) NH(2, KA, KB) NH(3, KB, KA) NH(4, KA, KB)
  NH(5, KB, KA) NH(6, KA, KB) NH(7, KB, KA) NH(8, KA, KB) NH(9, KB, KA)
  float* yb = (float*)(smem + YAOFF + ch*YST) + (R0 + 32*kg + 132*m);
  #pragma unroll
  for (int i=0;i<4;i++){
    #pragma unroll
    for (int r=0;r<NT;r++) yb[8*i + r] = acc[r][i];
  }
}
__device__ __forceinline__ bf16x8 band(int sh, int w, int kg){
  const uint32_t by = (((1u << w) - 1u) << sh) >> (8*kg);
  u32x4 r;
  #pragma unroll
  for (int k2=0;k2<4;k2++){ uint32_t x = (by >> (2*k2)) & 3u; x = (x | (x << 15)) & 0x00010001u; r[k2] = x * 0x3f80u; }
  return __builtin_bit_cast(bf16x8, r);
}
__device__ __forceinline__ void split2(const f32x4& a, const f32x4& b, bf16x8& h, bf16x8& l){
  u32x4 hh, ll;
  #pragma unroll
  for (int i=0;i<4;i++){ uint32_t x = pk2(a[i], b[i]); hh[i] = x; ll[i] = resid2(x, a[i], b[i]); }
  h = __builtin_bit_cast(bf16x8, hh); l = __builtin_bit_cast(bf16x8, ll);
}
template<int A0, int A1>
__device__ __forceinline__ void far_conv(char* smem, int ch, int m, int kg){
  const char* ub = smem + ch*UST;
  const f32x4 Z4 = {0,0,0,0};
  const int B0 = 2*m + kg - 2;
  const char* va0 = ub + (B0 < 0 ? 0 : 272*(B0 >> 4) + 16 + 16*(B0 & 15));
  const char* va1 = ub + 272*((B0+32) >> 4) + 16 + 16*((B0+32) & 15);
  const int cp = (3 - m) & 3;
  const char* ft = smem + FTOFF + ch*8192 + cp*FCS + 4*(47 - m + 4*kg - cp);
  char* yf = smem + YFOFF + ch*YFST + 64*m + 16*kg;
  f32x4 acc[6];
  #pragma unroll
  for (int a=0;a<6;a++) acc[a] = Z4;
  {
    bf16x8 M[4];
    M[0] = band(14 + m, 3, kg); M[1] = band(9 + m, 5, kg); M[2] = band(6 + m, 3, kg); M[3] = band(1 + m, 5, kg);
    bf16x8 Ch[3][2], Cl[3][2];
    #pragma unroll
    for (int dl=0;dl<3;dl++){
      #pragma unroll
      for (int h=0;h<2;h++){ Ch[dl][h] = *(const bf16x8*)(ft + h*(8*FCS) - 64*dl); Cl[dl][h] = *(const bf16x8*)(ft + h*(8*FCS) + 4*FCS - 64*dl); }
    }
    #pragma unroll
    for (int b=(A0 > 2 ? A0-2 : 0);b<A1;b++){
      const bf16x8 V = *(const bf16x8*)(b ? va1 + 544*(b-1) : va0);
      f32x4 F[4];
      #pragma unroll
      for (int p=0;p<4;p++) F[p] = MFMA16(V, M[p], Z4);
      #pragma unroll
      for (int h=0;h<2;h++){
        bf16x8 Bh, Bl;
        split2(F[2*h], F[2*h+1], Bh, Bl);
        #pragma unroll
        for (int dl=0;dl<3;dl++) if (b + dl >= A0 && b + dl < A1) acc[b+dl] = MFMA16(Bh, Ch[dl][h], acc[b+dl]);
        #pragma unroll
        for (int dl=0;dl<3;dl++) if (b + dl >= A0 && b + dl < A1) acc[b+dl] = MFMA16(Bh, Cl[dl][h], acc[b+dl]);
        #pragma unroll
        for (int dl=0;dl<3;dl++) if (b + dl >= A0 && b + dl < A1) acc[b+dl] = MFMA16(Bl, Ch[dl][h], acc[b+dl]);
      }
      if (b < 2 && b >= A0) *(f32x4*)(yf + 1024*b) = acc[b];
    }
  }
  {
    const char* vb0 = ub + (B0 < 1 ? 0 : 272*((B0-1) >> 4) + 16 + 16*((B0-1) & 15));
    const char* vb1 = ub + 272*((B0+31) >> 4) + 16 + 16*((B0+31) & 15);
    const char* f8 = smem + FTOFF + ch*8192 + 16*FCS + cp*FCS + 4*(63 - m + 4*kg - cp);
    const bf16x8 Ma = band(8 + m, 7, kg), Mb = band(7 + m, 9, kg), Mc = band(15 + m, 2, kg);
    const float k1024 = ((const float*)(smem + KCOFF))[ch*KC_ + 512];
    bf16x8 Ch[4], Cl[4];
    #pragma unroll
    for (int dl=0;dl<A1-2;dl++){ Ch[dl] = *(const bf16x8*)(f8 - 64*dl); Cl[dl] = *(const bf16x8*)(f8 + 4*FCS - 64*dl); }
    #pragma unroll
    for (int b=0;b<A1-2;b++){
      const bf16x8 V = *(const bf16x8*)(b ? va1 + 544*(b-1) : va0);
      const bf16x8 V2 = *(const bf16x8*)(b ? vb1 + 544*(b-1) : vb0);
      const f32x4 Fa = MFMA16(V, Ma, Z4), Fb = MFMA16(V2, Mb, Z4);
      bf16x8 Bh, Bl;
      split2(Fa, Fb, Bh, Bl);
      if (b + 2 >= A0){
        const f32x4 Fc = MFMA16(Mc, V, Z4);
        #pragma unroll
        for (int i=0;i<4;i++) acc[b+2][i] = __builtin_fmaf(k1024, Fc[i], acc[b+2][i]);
      }
      #pragma unroll
      for (int dl=0;dl<4;dl++) if (b + 2 + dl >= A0 && b + 2 + dl < A1) acc[b+2+dl] = MFMA16(Bh, Ch[dl], acc[b+2+dl]);
      #pragma unroll
      for (int dl=0;dl<4;dl++) if (b + 2 + dl >= A0 && b + 2 + dl < A1) acc[b+2+dl] = MFMA16(Bh, Cl[dl], acc[b+2+dl]);
      #pragma unroll
      for (int dl=0;dl<4;dl++) if (b + 2 + dl >= A0 && b + 2 + dl < A1) acc[b+2+dl] = MFMA16(Bl, Ch[dl], acc[b+2+dl]);
    }
  }
  #pragma unroll
  for (int a=(A0 > 2 ? A0 : 2);a<A1;a++) *(f32x4*)(yf + 1024*a) = acc[a];
}

__global__ __launch_bounds__(1024) void mono_kernel(const u16* __restrict__ u, const u16* __restrict__ logA, const u16* __restrict__ Aim,
    const u16* __restrict__ Cre, const u16* __restrict__ Cim, const u16* __restrict__ logdt, const u16* __restrict__ Dskip,
    const u16* __restrict__ q, const u16* __restrict__ k, const u16* __restrict__ v, const u16* __restrict__ shift_k,
    const u16* __restrict__ ssmk, const u16* __restrict__ ssmD, u16* __restrict__ out
){
  unsigned long long tsv[10], tsc[10];
#define TSP(i) tsv[i] = __builtin_amdgcn_s_memrealtime(); tsc[i] = __builtin_amdgcn_s_memtime()
  TSP(0); TSP(1); TSP(2); TSP(3); TSP(4); TSP(5);
  extern __shared__ __attribute__((aligned(16))) char smem[];
  const int bid = blockIdx.x, t = threadIdx.x;
  const int w5 = bid >> 3;
  const bool isA = w5 < 16;
  const int c0 = (bid & 7)*64 + (w5 & 15)*4;
  const int tr = (t + 64*(w5 & 15)) & 1023;
  const int wave = t >> 6, lane = t & 63, m = lane & 15, kg = lane >> 4;
  const u32x2 dsk = *(const u32x2*)((isA ? Dskip : ssmD) + c0);
  if (isA){
    const int ch = t >> 8, tg = t & 255, wv = (wave + ch) & 3;
    const int d = c0 + ch;
    char* tb = smem + TOFF + ch*TSZ;
    char* eb_ = smem + KCOFF + ch*(4*KC_);
    const u16 ldt = logdt[d];
    const u16 pla = logA[d*NS_+(tg & 127)], pai = Aim[d*NS_+(tg & 127)];
    u16 pc[2][2];
    #pragma unroll
    for (int hp=0;hp<2;hp++){ pc[hp][0] = Cre[d*NS_+64*hp+lane];  pc[hp][1] = Cim[d*NS_+64*hp+lane]; }
    const float dt = rbf(expf(bf2f(ldt)));
    {
      const float zre = -rbf(expf(bf2f(pla))) * dt;
      const float zim = bf2f(pai) * dt;
      const float f = (tg >> 7) ? 128.f : 1.f;
      cpx e = cexp_(zre*f, zim*f);
      *(float2*)(eb_ + 8*tg) = make_float2(e.re, e.im);
    }
    __syncthreads();
    TSP(1);
    __builtin_amdgcn_sched_barrier(0);
    const u32x2 u0 = *(const u32x2*)(u + (2*tr)*D_ + c0);
    const u32x2 u1 = *(const u32x2*)(u + (2*tr+1)*D_ + c0);
    __builtin_amdgcn_sched_barrier(0);
    f32x4 a4 = {0,0,0,0};
    char* col = tb + 4*lane;
    #pragma unroll 1
    for (int hp=0;hp<2;hp++){
      cpx C; C.re = bf2f(hp ? pc[1][0] : pc[0][0])*dt; C.im = bf2f(hp ? pc[1][1] : pc[0][1])*dt;
      cpx e1, e2;
      { float2 a = *(const float2*)(eb_ + 8*(64*hp+lane)); e2.re = a.x; e2.im = a.y;
        float2 c = *(const float2*)(eb_ + 1024 + 8*(64*hp+lane)); e1.re = c.x; e1.im = c.y; }
      if (wv == 0){
        cpx e8 = cmul(e2, e2); e8 = cmul(e8, e8); e8 = cmul(e8, e8);
        cpx xx; xx.re = 1.f; xx.im = 0.f;
        #pragma unroll
        for (int i=0;i<16;i++){ put_split(col + (41+i)*TR, xx.re, -xx.im); if (i < 15) xx = (i == 0) ? e8 : cmul(xx, e8); }
      } else if (wv == 1){
        cpx eb = cmul(e1, e1); eb = cmul(eb, eb); eb = cmul(eb, eb);
        cpx cb = cmul(C, eb);
        #pragma unroll
        for (int i=0;i<9;i++){ put_split(col + (32+i)*TR, cb.re, cb.im); if (i < 8) cb = cmul(cb, e1); }
      } else if (wv == 2){
        cpx S[8];
        S[1] = e2;
        #pragma unroll
        for (int i=2;i<8;i++) S[i] = cmul(S[i-1], S[1]);
        cpx cb = C;
        #pragma unroll
        for (int a=0;a<2;a++){
          put_split(col + (8*a)*TR, cb.re, cb.im);
          #pragma unroll
          for (int p=1;p<8;p++){ cpx g = cmul(cb, S[p]); put_split(col + (8*a+p)*TR, g.re, g.im); }
          if (a == 0) cb = cmul(cb, e1);
        }
      } else {
        cpx S2 = cmul(e2, e2), S4 = cmul(S2, S2), S6 = cmul(S4, S2);
        cpx cb = cmul(C, cmul(e1, e1));
        #pragma unroll
        for (int a=0;a<2;a++){
          put_split(col + (16+4*a)*TR, cb.re, cb.im);
          { cpx g = cmul(cb, S2); put_split(col + (16+4*a+1)*TR, g.re, g.im); }
          { cpx g = cmul(cb, S4); put_split(col + (16+4*a+2)*TR, g.re, g.im); }
          { cpx g = cmul(cb, S6); put_split(col + (16+4*a+3)*TR, g.re, g.im); }
          cb = cmul(cb, e1);
        }
        #pragma unroll
        for (int a=0;a<4;a++){
          put_split(col + (24+2*a)*TR, cb.re, cb.im);
          cpx g = cmul(cb, S4); put_split(col + (24+2*a+1)*TR, g.re, g.im);
          if (a < 3) cb = cmul(cb, e1);
        }
      }
      __syncthreads();
      if (wv < 3){
        const char* ga = tb + (16*wv + m)*TR + 16*kg;
        const char* xb = tb + (41 + m)*TR + 16*kg;
        #pragma unroll
        for (int kc=0;kc<4;kc++){
          bf16x8 ah = *(const bf16x8*)(ga + 64*kc), al = *(const bf16x8*)(ga + 64*kc + TLO);
          bf16x8 bh = *(const bf16x8*)(xb + 64*kc), bl = *(const bf16x8*)(xb + 64*kc + TLO);
          a4 = MFMA16(ah, bh, a4);
          a4 = MFMA16(ah, bl, a4);
          a4 = MFMA16(al, bh, a4);
        }
      }
      if (hp == 0) __syncthreads();
    }
    float* Kc = (float*)(smem + KCOFF) + ch*KC_;
    if (wv < 3){
      #pragma unroll
      for (int i=0;i<4;i++){
        int r = 16*wv + 4*kg + i;
        int idx;
        if (r < 16) idx = 128*(r>>3) + (r&7) + 8*m;
        else if (r < 24) idx = 256 + 64*((r-16)>>2) + (r&3) + 4*m;
        else if (r < 32) idx = 384 + 32*((r-24)>>1) + (r&1) + 2*m;
        else idx = 512 + 16*(r-32) + m;
        if (r < 40 || (r == 40 && m == 0)) Kc[idx] = 2.f*a4[i];
      }
    }
    put_u(smem, tr, u0, u1);
    __syncthreads();
    TSP(2);
    {
      char* ft = smem + FTOFF + ch*8192;
      if (tg < 208){
        int z, idx; bool ok; char* wp;
        if (tg < 128){ z = tg & 63; const int Dd = 47 - z; ok = Dd >= 0 && Dd < 32; idx = 385 + 4*Dd + 2*(tg >> 6); wp = ft + (tg >> 6)*(8*FCS); }
        else { z = tg - 128; const int Dd = 63 - z; ok = Dd >= 0; idx = 513 + 2*Dd; wp = ft + 16*FCS; }
        float a = 0.f, b = 0.f;
        if (ok){ a = Kc[idx]; b = Kc[idx+1]; }
        const uint32_t hh = pk2(a, b), ll = pk2(a - lo16f(hh), b - hi16f(hh));
        #pragma unroll
        for (int c=0;c<4;c++) if (z >= c){ *(uint32_t*)(wp + c*FCS + 4*(z-c)) = hh; *(uint32_t*)(wp + (4+c)*FCS + 4*(z-c)) = ll; }
      }
      if (tg >= 161){
        const int sl = tg - 161, j = sl - 15;
        float kv[8];
        #pragma unroll
        for (int i=0;i<8;i++){
          int l = 8*j + 7 - i;
          int pr = (int)rbf((float)l);
          int p = pr < 256 ? pr : 128 + (pr>>1);
          kv[i] = (l >= 0 && l <= 514) ? Kc[p] : 0.f;
        }
        u32x4 h, lo;
        #pragma unroll
        for (int jj=0;jj<4;jj++){
          uint32_t hh = pk2(kv[2*jj], kv[2*jj+1]);
          h[jj] = hh; lo[jj] = pk2(kv[2*jj] - lo16f(hh), kv[2*jj+1] - hi16f(hh));
        }
        char* kb = smem + TOFF + (2*ch)*KS2;
        *(u32x4*)(kb + 16*sl) = h;
        *(u32x4*)(kb + KS2 + 16*sl) = lo;
      }
      if (tg < 4) *(float*)(smem + YFOFF + ch*YFST - 16 + 4*tg) = 0.f;
    }
    __syncthreads();
    TSP(3);
  } else {
    const u32x2 k0 = *(const u32x2*)(k + (2*tr)*D_ + c0);
    const u32x2 k1 = *(const u32x2*)(k + (2*tr+1)*D_ + c0);
    const u32x4 wa = *(const u32x4*)(shift_k + c0*4), wb = *(const u32x4*)(shift_k + c0*4 + 8);
    char* ks = smem + YOFF;
    __builtin_amdgcn_sched_barrier(0);
    *(u32x4*)(ks + 32 + 16*tr) = u32x4{k0.x, k0.y, k1.x, k1.y};
    __builtin_amdgcn_sched_barrier(0);
    const u32x2 v0 = *(const u32x2*)(v + (2*tr)*D_ + c0);
    const u32x2 v1 = *(const u32x2*)(v + (2*tr+1)*D_ + c0);
    const u32x4 sx = *(const u32x4*)(ssmk + (c0+(t >> 8))*L_ + 8*(t & 255));
    __builtin_amdgcn_sched_barrier(0);
    if (t < 2) *(u32x4*)(ks + 16*t) = u32x4{0,0,0,0};
    __syncthreads();
    TSP(1);
    u32x2 kk[5];
    kk[0] = *(const u32x2*)(ks + 8 + 16*tr);
    { u32x4 x = *(const u32x4*)(ks + 16 + 16*tr); kk[1] = u32x2{x.x, x.y}; kk[2] = u32x2{x.z, x.w}; }
    kk[3] = k0; kk[4] = k1;
    u32x2 o[2];
    #pragma unroll
    for (int j=0;j<2;j++){
      const u32x2 vv = j ? v1 : v0;
      #pragma unroll
      for (int p=0;p<2;p++){
        float r2[2];
        #pragma unroll
        for (int e=0;e<2;e++){
          const u32x4 ww = p ? wb : wa;
          const uint32_t wl = e ? ww.z : ww.x, wh = e ? ww.w : ww.y;
          const float w0 = lo16f(wl), w1 = hi16f(wl), w2 = lo16f(wh), w3 = hi16f(wh);
          auto g = [&](int idx){ uint32_t x = kk[idx][p]; return e? hi16f(x) : lo16f(x); };
          float acc = rbf(g(j)*w3);
          acc = rbf(acc + rbf(g(j+1)*w2));
          acc = rbf(acc + rbf(g(j+2)*w1));
          acc = rbf(acc + rbf(g(j+3)*w0));
          r2[e] = acc * (e? hi16f(vv[p]) : lo16f(vv[p]));
        }
        o[j][p] = pk2(r2[0], r2[1]);
      }
    }
    put_u(smem, tr, o[0], o[1]);
    {
      const int ch = t >> 8, j = t & 255;
      u32x4 r;
      r.x = __builtin_amdgcn_alignbit(sx.w, sx.w, 16); r.y = __builtin_amdgcn_alignbit(sx.z, sx.z, 16);
      r.z = __builtin_amdgcn_alignbit(sx.y, sx.y, 16); r.w = __builtin_amdgcn_alignbit(sx.x, sx.x, 16);
      char* kb = smem + TOFF + ch*KST;
      *(u32x4*)(kb + 16*(j+15)) = r;
      if (j < 15) *(u32x4*)(kb + 16*j) = u32x4{0,0,0,0};
    }
    __syncthreads();
    TSP(2); TSP(3);
    if (wave >= 8){
      const int r4 = (((t - 512) + 32*(w5 & 15)) & 511)*4;
      u32x2 a[4];
      #pragma unroll
      for (int i=0;i<4;i++) a[i] = *(const u32x2*)(q + (r4+i)*D_ + c0);
      *(u32x4*)(smem + QOFF + 8*r4)      = u32x4{a[0].x, a[0].y, a[1].x, a[1].y};
      *(u32x4*)(smem + QOFF + 8*r4 + 16) = u32x4{a[2].x, a[2].y, a[3].x, a[3].y};
    }
  }
  if (isA){
    const int ch = wave >> 2, role = (wave + ch) & 3;
    if (role == 0) near_conv<4,0>(smem, ch, m, kg);
    else if (role == 1) near_conv<4,4>(smem, ch, m, kg);
    else if (role == 2){ __builtin_amdgcn_s_setprio(3); far_conv<0,4>(smem, ch, m, kg); __builtin_amdgcn_s_setprio(0); }
    else { __builtin_amdgcn_s_setprio(3); far_conv<4,6>(smem, ch, m, kg); __builtin_amdgcn_s_setprio(0); }
    TSP(5);
  } else if (wave < 8){
    const int ch = wave >> 1;
    const int g = wave & 1;
    const int part = 0;
    const int unit = ch;
    const uint32_t kbase = (uint32_t)(uintptr_t)smem + TOFF + unit*KST + 16*(m - kg + 3);
    const char* ub = smem + ch*UST;
    bf16x8 KA[4], KB[4];
    LDK4(KA, 15);
    f32x4 acc[4];
    {
      bf16x8 Up[2][4];
      {
        const uint32_t sel = (m & 1) ? 0x07060504u : 0x05040302u;
        const char* pp = ub + 272*(m >> 1) + 16*kg + 8*g;
        #pragma unroll
        for (int c=0;c<4;c++){ W6 W = uwin(pp + 64*c); Up[0][c] = usel(W, 0, sel); Up[1][c] = usel(W, 1, sel); }
      }
      f32x4 P0 = {0,0,0,0}, P1 = {0,0,0,0};
#define PH1(dl, CUR, NXT) \
      LDK4(NXT, (dl)-1); WAITK(4, CUR); \
      if ((dl) < 15){ P0 = shrN2(P0); P1 = shrN2(P1); } \
      P0 = MFMA16(CUR[0], Up[0][0], P0); P1 = MFMA16(CUR[0], Up[1][0], P1); \
      P0 = MFMA16(CUR[1], Up[0][1], P0); P1 = MFMA16(CUR[1], Up[1][1], P1); \
      P0 = MFMA16(CUR[2], Up[0][2], P0); P1 = MFMA16(CUR[2], Up[1][2], P1); \
      P0 = MFMA16(CUR[3], Up[0][3], P0); P1 = MFMA16(CUR[3], Up[1][3], P1);
      PH1(15, KA, KB) PH1(14, KB, KA) PH1(13, KA, KB) PH1(12, KB, KA)
      PH1(11, KA, KB) PH1(10, KB, KA) PH1(9, KA, KB) PH1(8, KB, KA)
      const bool ok = m >= 1 && m <= 8;
      const int src = 4*((lane & 48) + ((2*(m-1)) & 15));
      #pragma unroll
      for (int i=0;i<4;i++){
        float a0 = i2f(__builtin_amdgcn_ds_bpermute(src,   f2i(P0[i])));
        float a1 = i2f(__builtin_amdgcn_ds_bpermute(src+4, f2i(P0[i])));
        float a2 = i2f(__builtin_amdgcn_ds_bpermute(src,   f2i(P1[i])));
        float a3 = i2f(__builtin_amdgcn_ds_bpermute(src+4, f2i(P1[i])));
        acc[0][i] = ok ? a0 : 0.f; acc[1][i] = ok ? a1 : 0.f; acc[2][i] = ok ? a2 : 0.f; acc[3][i] = ok ? a3 : 0.f;
      }
    }
    TSP(4);
    bf16x8 U[4][4];
    {
      const char* pp = ub + 272*m + 16*kg + 8*g;
      #pragma unroll
      for (int c=0;c<4;c++){ W6 W = uwin(pp + 64*c); U[0][c] = uodd(W, 0); U[1][c] = uevn(W, 1); U[2][c] = uodd(W, 1); U[3][c] = uevn(W, 2); }
    }
#define PH2(dl, CUR, NXT) \
    if ((dl) > 0){ LDK4(NXT, (dl)-1); WAITK(4, CUR); } else { WAITK(0, CUR); } \
    if ((dl) < 7){ acc[0] = shrN1(acc[0]); acc[1] = shrN1(acc[1]); acc[2] = shrN1(acc[2]); acc[3] = shrN1(acc[3]); } \
    _Pragma("unroll") for (int c=0;c<4;c++){ \
      acc[0] = MFMA16(CUR[c], U[0][c], acc[0]); acc[1] = MFMA16(CUR[c], U[1][c], acc[1]); \
      acc[2] = MFMA16(CUR[c], U[2][c], acc[2]); acc[3] = MFMA16(CUR[c], U[3][c], acc[3]); }
    PH2(7, KA, KB) PH2(6, KB, KA) PH2(5, KA, KB) PH2(4, KB, KA)
    PH2(3, KA, KB) PH2(2, KB, KA) PH2(1, KA, KB) PH2(0, KB, KA)
    TSP(5);
    char* yb = smem + YOFF + (part*4 + ch)*YST + 4*(4*g + 32*kg + 132*m);
    #pragma unroll
    for (int i=0;i<4;i++) *(f32x4*)(yb + 32*i) = f32x4{acc[0][i], acc[1][i], acc[2][i], acc[3][i]};
  }
  __syncthreads();
  TSP(6);
  {
    const char* yb = smem + YOFF + 8*tr + 16*(tr >> 6);
    const char* up = smem + 272*(tr >> 6) + 16 + 4*(tr & 63);
    float o0[4], o1[4];
    if (isA){
      const char* ya = yb + (YAOFF - YOFF);
      const char* yf = smem + YFOFF + (tr >= 257 ? 4*(2*tr - 515) : -16);
      #pragma unroll
      for (int c=0;c<4;c++){
        f32x2 a = *(const f32x2*)(ya + c*YST);
        float b0 = *(const float*)(yf + c*YFST), b1 = *(const float*)(yf + c*YFST + 4);
        uint32_t uu = *(const uint32_t*)(up + c*UST);
        float sd = (c & 1) ? hi16f(dsk[c>>1]) : lo16f(dsk[c>>1]);
        o0[c] = rbf(a.x + b0) + rbf(lo16f(uu)*sd);
        o1[c] = rbf(a.y + b1) + rbf(hi16f(uu)*sd);
      }
    } else {
      u32x4 qq = *(const u32x4*)(smem + QOFF + 16*tr);
      #pragma unroll
      for (int c=0;c<4;c++){
        f32x2 a = *(const f32x2*)(yb + c*YST);
        uint32_t uu = *(const uint32_t*)(up + c*UST);
        float sd = (c & 1) ? hi16f(dsk[c>>1]) : lo16f(dsk[c>>1]);
        float q0 = (c & 1) ? hi16f(qq[c>>1]) : lo16f(qq[c>>1]);
        float q1 = (c & 1) ? hi16f(qq[2+(c>>1)]) : lo16f(qq[2+(c>>1)]);
        o0[c] = q0 * rbf(rbf(a.x) + rbf(lo16f(uu)*sd));
        o1[c] = q1 * rbf(rbf(a.y) + rbf(hi16f(uu)*sd));
      }
    }
    u16* op = out + (isA ? 0 : D_) + c0;
    *(u32x2*)(op + (2*tr)*(2*D_))   = u32x2{pk2(o0[0],o0[1]), pk2(o0[2],o0[3])};
    *(u32x2*)(op + (2*tr+1)*(2*D_)) = u32x2{pk2(o1[0],o1[1]), pk2(o1[2],o1[3])};
  }
  TSP(7);
  if (isA){
    unsigned long long dlt = (tsv[1]-tsv[0])*8;
    unsigned long long tgt = tsv[7] + dlt;
    while (__builtin_amdgcn_s_memrealtime() < tgt) __builtin_amdgcn_s_sleep(1);
  }
}

extern "C" void kernel_launch(void* const* d_in, const int* in_sizes, int n_in,
                              void* d_out, int out_size, void* d_ws, size_t ws_size,
                              hipStream_t stream) {
  mono_kernel<<<256, 1024, LDS_TOTAL, stream>>>((const u16*)d_in[0], (const u16*)d_in[1], (const u16*)d_in[2], (const u16*)d_in[3],
     (const u16*)d_in[4], (const u16*)d_in[5], (const u16*)d_in[6], (const u16*)d_in[7], (const u16*)d_in[8], (const u16*)d_in[9],
     (const u16*)d_in[10], (const u16*)d_in[11], (const u16*)d_in[12], (u16*)d_out
);
}
```

```cpp
#include <hip/hip_runtime.h>
#include <hip/hip_bf16.h>
#include <stdint.h>

typedef unsigned short u16;
typedef short bf16x8 __attribute__((ext_vector_type(8)));
typedef float f32x4 __attribute__((ext_vector_type(4)));
typedef float f32x2 __attribute__((ext_vector_type(2)));
typedef __bf16 bf16x2_t __attribute__((ext_vector_type(2)));

#define L_ 2048
#define D_ 512
#define NS_ 128

__device__ __forceinline__ float bf2f(u16 x){ return __builtin_bit_cast(float, (uint32_t)x << 16); }
__device__ __forceinline__ u16 f2bf(float f){ __hip_bfloat16 h = __float2bfloat16(f); return __builtin_bit_cast(u16, h); }
__device__ __forceinline__ float rbf(float f){ return bf2f(f2bf(f)); }
__device__ __forceinline__ uint32_t pk2(float a, float b){
  f32x2 v = {a,b}; bf16x2_t r = __builtin_convertvector(v, bf16x2_t); return __builtin_bit_cast(uint32_t, r); }
__device__ __forceinline__ float lo16f(uint32_t w){ return __builtin_bit_cast(float, w << 16); }
__device__ __forceinline__ float hi16f(uint32_t w){ return __builtin_bit_cast(float, w & 0xffff0000u); }

struct cpx { float re, im; };
__device__ __forceinline__ float amul(float a, float b){ float r; asm("v_mul_f32 %0, %1, %2" : "=v"(r) : "v"(a), "v"(b)); return r; }
__device__ __forceinline__ float afma(float a, float b, float c){ float r; asm("v_fma_f32 %0, %1, %2, %3" : "=v"(r) : "v"(a), "v"(b), "v"(c)); return r; }
__device__ __forceinline__ float afms(float a, float b, float c){ float r; asm("v_fma_f32 %0, %1, %2, -%3" : "=v"(r) : "v"(a), "v"(b), "v"(c)); return r; }
__device__ __forceinline__ cpx cmul(cpx a, cpx b){
  f32x2 A = {a.re, a.im}, B = {b.re, b.im}, T, R;
  asm("v_pk_mul_f32 %0, %1, %2 op_sel:[1,1] op_sel_hi:[1,0] neg_lo:[1,0]" : "=v"(T) : "v"(A), "v"(B));
  asm("v_pk_fma_f32 %0, %1, %2, %3 op_sel:[0,0,0] op_sel_hi:[0,1,1]" : "=v"(R) : "v"(A), "v"(B), "v"(T));
  cpx r; r.re = R.x; r.im = R.y; return r; }
__device__ __forceinline__ cpx cexp_(float re, float im){ float e = expf(re); float s,c; sincosf(im,&s,&c); cpx r; r.re=e*c; r.im=e*s; return r; }

#define MFMA16(a,b,c) __builtin_amdgcn_mfma_f32_16x16x32_bf16(a,b,c,0,0,0)
typedef uint32_t u32x2 __attribute__((ext_vector_type(2)));
typedef uint32_t u32x4 __attribute__((ext_vector_type(4)));
#define KC_ 656
#define TR 288
#define TLO (57*TR)
#define TSZ (2*TLO)
#define UST 4352
#define KCOFF 17408
#define TOFF 27904
#define KST 4352
#define KS2 1536
#define FTOFF (TOFF + 12288)
#define FCS 320
#define YFOFF (TOFF + 45056 + 16)
#define YFST 6176
#define YAOFF (TOFF + 69888)
#define YOFF (TOFF + 36864)
#define YST 8448
#define QOFF (TOFF + 112128)
#define LDS_TOTAL (TOFF + 4*TSZ)

__device__ __forceinline__ uint32_t resid2(uint32_t h, float a, float b){
  return pk2(a - lo16f(h), b - hi16f(h));
}
__device__ __forceinline__ void put_split(char* p, float a, float b){
  uint32_t h = pk2(a,b);
  uint32_t l = resid2(h, a, b);
  *(uint32_t*)p = h; *(uint32_t*)(p + TLO) = l;
}
__device__ __forceinline__ int f2i(float x){ return __builtin_bit_cast(int, x); }
__device__ __forceinline__ float i2f(int x){ return __builtin_bit_cast(float, x); }
__device__ __forceinline__ f32x4 shrN1(f32x4 x){
  f32x4 r;
  #pragma unroll
  for (int i=0;i<4;i++) r[i] = i2f(__builtin_amdgcn_update_dpp(0, f2i(x[i]), 0x111, 0xf, 0xf, true));
  return r;
}
__device__ __forceinline__ f32x4 shrN2(f32x4 x){
  f32x4 r;
  #pragma unroll
  for (int i=0;i<4;i++) r[i] = i2f(__builtin_amdgcn_update_dpp(0, f2i(x[i]), 0x112, 0xf, 0xf, true));
  return r;
}
struct W6 { uint32_t w[6]; };
__device__ __forceinline__ W6 uwin(const char* p){
  u32x2 a = *(const u32x2*)p, b = *(const u32x2*)(p+8), c = *(const u32x2*)(p+16);
  W6 r; r.w[0]=a.x; r.w[1]=a.y; r.w[2]=b.x; r.w[3]=b.y; r.w[4]=c.x; r.w[5]=c.y; return r;
}
__device__ __forceinline__ bf16x8 usel(const W6& W, int x, uint32_t sel){
  u32x4 r;
  r.x = __builtin_amdgcn_perm(W.w[x+1], W.w[x],   sel); r.y = __builtin_amdgcn_perm(W.w[x+2], W.w[x+1], sel);
  r.z = __builtin_amdgcn_perm(W.w[x+3], W.w[x+2], sel); r.w = __builtin_amdgcn_perm(W.w[x+4], W.w[x+3], sel);
  return __builtin_bit_cast(bf16x8, r);
}
__device__ __forceinline__ bf16x8 uodd(const W6& W, int x){
  u32x4 r;
  r.x = __builtin_amdgcn_alignbit(W.w[x+1], W.w[x],   16); r.y = __builtin_amdgcn_alignbit(W.w[x+2], W.w[x+1], 16);
  r.z = __builtin_amdgcn_alignbit(W.w[x+3], W.w[x+2], 16); r.w = __builtin_amdgcn_alignbit(W.w[x+4], W.w[x+3], 16);
  return __builtin_bit_cast(bf16x8, r);
}
__device__ __forceinline__ bf16x8 uevn(const W6& W, int x){
  u32x4 r = {W.w[x], W.w[x+1], W.w[x+2], W.w[x+3]};
  return __builtin_bit_cast(bf16x8, r);
}
__device__ __forceinline__ void put_u(char* smem, int tr, u32x2 r0, u32x2 r1){
  uint32_t d[4];
  d[0] = __builtin_amdgcn_perm(r1.x, r0.x, 0x05040100); d[1] = __builtin_amdgcn_perm(r1.x, r0.x, 0x07060302);
  d[2] = __builtin_amdgcn_perm(r1.y, r0.y, 0x05040100); d[3] = __builtin_amdgcn_perm(r1.y, r0.y, 0x07060302);
  const int a = tr >> 6, pos = tr & 63;
  char* p = smem + 272*a + 16 + 4*pos;
  #pragma unroll
  for (int c=0;c<4;c++) *(uint32_t*)(p + c*UST) = d[c];
  if (pos >= 60 && a < 15){
    #pragma unroll
    for (int c=0;c<4;c++) *(uint32_t*)(p + c*UST + 16) = d[c];
  }
  if (tr < 16) *(uint32_t*)(smem + (tr>>2)*UST + 4*(tr&3)) = 0u;
}

#define LDK(F, dl, c) asm volatile("ds_read_b128 %0, %1 offset:%2" : "=v"(F) : "v"(kbase), "i"(256*(dl) + 64*(3-(c))))
#define LDK4(S, dl) LDK(S[0], dl, 0); LDK(S[1], dl, 1); LDK(S[2], dl, 2); LDK(S[3], dl, 3)
#define WAITK(n, S) asm volatile("s_waitcnt lgkmcnt(" #n ")" : "+v"(S[0]), "+v"(S[1]), "+v"(S[2]), "+v"(S[3]))

template<int NT, int R0>
__device__ __forceinline__ void near_conv(char* smem, int ch, int m, int kg){
  const uint32_t kbase = (uint32_t)(uintptr_t)smem + TOFF + (2*ch)*KS2 + 16*(m - kg + 3);
#define LDN(F, hs, c) asm volatile("ds_read_b128 %0, %1 offset:%2" : "=v"(F) : "v"(kbase), "i"(((hs)&1)*KS2 + 256*(4-((hs)>>1)) + 64*(3-(c))))
#define LDN4(S, hs) LDN(S[0], hs, 0); LDN(S[1], hs, 1); LDN(S[2], hs, 2); LDN(S[3], hs, 3)
  bf16x8 KA[4], KB[4];
  LDN4(KA, 0);
  bf16x8 U[NT][4];
  {
    const char* pp = smem + ch*UST + 272*m + 16*kg + (R0 ? 8 : 0);
    #pragma unroll
    for (int c=0;c<4;c++){
      W6 W = uwin(pp + 64*c);
      #pragma unroll
      for (int r=0;r<NT;r++){ const int rel = R0 + r + 1 - (R0 ? 4 : 0); U[r][c] = (rel & 1) ? uodd(W, rel >> 1) : uevn(W, rel >> 1); }
    }
  }
  f32x4 acc[NT];
  #pragma unroll
  for (int r=0;r<NT;r++) acc[r] = f32x4{0,0,0,0};
#define NH(hs, CUR, NXT) \
  if ((hs) < 9){ LDN4(NXT, (hs)+1); WAITK(4, CUR); } else { WAITK(0, CUR); } \
  if ((hs) > 0 && !((hs) & 1)){ _Pragma("unroll") for (int r=0;r<NT;r++) acc[r] = shrN1(acc[r]); } \
  _Pragma("unroll") for (int c=0;c<4;c++){ _Pragma("unroll") for (int r=0;r<NT;r++) acc[r] = MFMA16(CUR[c], U[r][c], acc[r]); }
  NH(0, KA, KB) NH(1, KB, KA) NH(2, KA, KB) NH(3, KB, KA) NH(4, KA, KB)
  NH(5, KB, KA) NH(6, KA, KB) NH(7, KB, KA) NH(8, KA, KB) NH(9, KB, KA)
  float* yb = (float*)(smem + YAOFF + ch*YST) + (R0 + 32*kg + 132*m);
  #pragma unroll
  for (int i=0;i<4;i++){
    #pragma unroll
    for (int r=0;r<NT;r++) yb[8*i + r] = acc[r][i];
  }
}
__device__ __forceinline__ bf16x8 band(int sh, int w, int kg){
  const uint32_t by = (((1u << w) - 1u) << sh) >> (8*kg);
  u32x4 r;
  #pragma unroll
  for (int k2=0;k2<4;k2++){ uint32_t x = (by >> (2*k2)) & 3u; x = (x | (x << 15)) & 0x00010001u; r[k2] = x * 0x3f80u; }
  return __builtin_bit_cast(bf16x8, r);
}
__device__ __forceinline__ void split2(const f32x4& a, const f32x4& b, bf16x8& h, bf16x8& l){
  u32x4 hh, ll;
  #pragma unroll
  for (int i=0;i<4;i++){ uint32_t x = pk2(a[i], b[i]); hh[i] = x; ll[i] = resid2(x, a[i], b[i]); }
  h = __builtin_bit_cast(bf16x8, hh); l = __builtin_bit_cast(bf16x8, ll);
}
template<int A0, int A1>
__device__ __forceinline__ void far_conv(char* smem, int ch, int m, int kg){
  const char* ub = smem + ch*UST;
  const f32x4 Z4 = {0,0,0,0};
  const int B0 = 2*m + kg - 2;
  const char* va0 = ub + (B0 < 0 ? 0 : 272*(B0 >> 4) + 16 + 16*(B0 & 15));
  const char* va1 = ub + 272*((B0+32) >> 4) + 16 + 16*((B0+32) & 15);
  const int cp = (3 - m) & 3;
  const char* ft = smem + FTOFF + ch*8192 + cp*FCS + 4*(47 - m + 4*kg - cp);
  char* yf = smem + YFOFF + ch*YFST + 64*m + 16*kg;
  f32x4 acc[6];
  #pragma unroll
  for (int a=0;a<6;a++) acc[a] = Z4;
  {
    bf16x8 M[4];
    M[0] = band(14 + m, 3, kg); M[1] = band(9 + m, 5, kg); M[2] = band(6 + m, 3, kg); M[3] = band(1 + m, 5, kg);
    bf16x8 Ch[3][2], Cl[3][2];
    #pragma unroll
    for (int dl=0;dl<3;dl++){
      #pragma unroll
      for (int h=0;h<2;h++){ Ch[dl][h] = *(const bf16x8*)(ft + h*(8*FCS) - 64*dl); Cl[dl][h] = *(const bf16x8*)(ft + h*(8*FCS) + 4*FCS - 64*dl); }
    }
    #pragma unroll
    for (int b=(A0 > 2 ? A0-2 : 0);b<A1;b++){
      const bf16x8 V = *(const bf16x8*)(b ? va1 + 544*(b-1) : va0);
      f32x4 F[4];
      #pragma unroll
      for (int p=0;p<4;p++) F[p] = MFMA16(V, M[p], Z4);
      #pragma unroll
      for (int h=0;h<2;h++){
        bf16x8 Bh, Bl;
        split2(F[2*h], F[2*h+1], Bh, Bl);
        #pragma unroll
        for (int dl=0;dl<3;dl++) if (b + dl >= A0 && b + dl < A1) acc[b+dl] = MFMA16(Bh, Ch[dl][h], acc[b+dl]);
        #pragma unroll
        for (int dl=0;dl<3;dl++) if (b + dl >= A0 && b + dl < A1) acc[b+dl] = MFMA16(Bh, Cl[dl][h], acc[b+dl]);
        #pragma unroll
        for (int dl=0;dl<3;dl++) if (b + dl >= A0 && b + dl < A1) acc[b+dl] = MFMA16(Bl, Ch[dl][h], acc[b+dl]);
      }
      if (b < 2 && b >= A0) *(f32x4*)(yf + 1024*b) = acc[b];
    }
  }
  {
    const char* vb0 = ub + (B0 < 1 ? 0 : 272*((B0-1) >> 4) + 16 + 16*((B0-1) & 15));
    const char* vb1 = ub + 272*((B0+31) >> 4) + 16 + 16*((B0+31) & 15);
    const char* f8 = smem + FTOFF + ch*8192 + 16*FCS + cp*FCS + 4*(63 - m + 4*kg - cp);
    const bf16x8 Ma = band(8 + m, 7, kg), Mb = band(7 + m, 9, kg), Mc = band(15 + m, 2, kg);
    const float k1024 = ((const float*)(smem + KCOFF))[ch*KC_ + 512];
    bf16x8 Ch[4], Cl[4];
    #pragma unroll
    for (int dl=0;dl<A1-2;dl++){ Ch[dl] = *(const bf16x8*)(f8 - 64*dl); Cl[dl] = *(const bf16x8*)(f8 + 4*FCS - 64*dl); }
    #pragma unroll
    for (int b=0;b<A1-2;b++){
      const bf16x8 V = *(const bf16x8*)(b ? va1 + 544*(b-1) : va0);
      const bf16x8 V2 = *(const bf16x8*)(b ? vb1 + 544*(b-1) : vb0);
      const f32x4 Fa = MFMA16(V, Ma, Z4), Fb = MFMA16(V2, Mb, Z4);
      bf16x8 Bh, Bl;
      split2(Fa, Fb, Bh, Bl);
      if (b + 2 >= A0){
        const f32x4 Fc = MFMA16(Mc, V, Z4);
        #pragma unroll
        for (int i=0;i<4;i++) acc[b+2][i] = __builtin_fmaf(k1024, Fc[i], acc[b+2][i]);
      }
      #pragma unroll
      for (int dl=0;dl<4;dl++) if (b + 2 + dl >= A0 && b + 2 + dl < A1) acc[b+2+dl] = MFMA16(Bh, Ch[dl], acc[b+2+dl]);
      #pragma unroll
      for (int dl=0;dl<4;dl++) if (b + 2 + dl >= A0 && b + 2 + dl < A1) acc[b+2+dl] = MFMA16(Bh, Cl[dl], acc[b+2+dl]);
      #pragma unroll
      for (int dl=0;dl<4;dl++) if (b + 2 + dl >= A0 && b + 2 + dl < A1) acc[b+2+dl] = MFMA16(Bl, Ch[dl], acc[b+2+dl]);
    }
  }
  #pragma unroll
  for (int a=(A0 > 2 ? A0 : 2);a<A1;a++) *(f32x4*)(yf + 1024*a) = acc[a];
}

__global__ __launch_bounds__(1024) void mono_kernel(const u16* __restrict__ u, const u16* __restrict__ logA, const u16* __restrict__ Aim,
    const u16* __restrict__ Cre, const u16* __restrict__ Cim, const u16* __restrict__ logdt, const u16* __restrict__ Dskip,
    const u16* __restrict__ q, const u16* __restrict__ k, const u16* __restrict__ v, const u16* __restrict__ shift_k,
    const u16* __restrict__ ssmk, const u16* __restrict__ ssmD, u16* __restrict__ out, unsigned long long* wsb
){
  unsigned long long tsv[10], tsc[10];
#define TSP(i) tsv[i] = __builtin_amdgcn_s_memrealtime(); tsc[i] = __builtin_amdgcn_s_memtime()
  TSP(0); TSP(1); TSP(2); TSP(3); TSP(4); TSP(5);
  if (threadIdx.x == 0) __hip_atomic_store(wsb + blockIdx.x, tsv[0], __ATOMIC_RELAXED, __HIP_MEMORY_SCOPE_AGENT);
  extern __shared__ __attribute__((aligned(16))) char smem[];
  const int bid = blockIdx.x, t = threadIdx.x;
  const int w5 = bid >> 3;
  const bool isA = w5 < 16;
  const int c0 = (bid & 7)*64 + (w5 & 15)*4;
  const int tr = (t + 64*(w5 & 15)) & 1023;
  const int wave = t >> 6, lane = t & 63, m = lane & 15, kg = lane >> 4;
  const u32x2 dsk = *(const u32x2*)((isA ? Dskip : ssmD) + c0);
  if (isA){
    const int ch = t >> 8, tg = t & 255, wv = (wave + ch) & 3;
    const int d = c0 + ch;
    char* tb = smem + TOFF + ch*TSZ;
    char* eb_ = smem + KCOFF + ch*(4*KC_);
    const u16 ldt = logdt[d];
    const u16 pla = logA[d*NS_+(tg & 127)], pai = Aim[d*NS_+(tg & 127)];
    u16 pc[2][2];
    #pragma unroll
    for (int hp=0;hp<2;hp++){ pc[hp][0] = Cre[d*NS_+64*hp+lane];  pc[hp][1] = Cim[d*NS_+64*hp+lane]; }
    const float dt = rbf(expf(bf2f(ldt)));
    {
      const float zre = -rbf(expf(bf2f(pla))) * dt;
      const float zim = bf2f(pai) * dt;
      const float f = (tg >> 7) ? 128.f : 1.f;
      cpx e = cexp_(zre*f, zim*f);
      *(float2*)(eb_ + 8*tg) = make_float2(e.re, e.im);
    }
    __syncthreads();
    TSP(1);
    __builtin_amdgcn_sched_barrier(0);
    const u32x2 u0 = *(const u32x2*)(u + (2*tr)*D_ + c0);
    const u32x2 u1 = *(const u32x2*)(u + (2*tr+1)*D_ + c0);
    __builtin_amdgcn_sched_barrier(0);
    f32x4 a4 = {0,0,0,0};
    char* col = tb + 4*lane;
    #pragma unroll 1
    for (int hp=0;hp<2;hp++){
      cpx C; C.re = bf2f(hp ? pc[1][0] : pc[0][0])*dt; C.im = bf2f(hp ? pc[1][1] : pc[0][1])*dt;
      cpx e1, e2;
      { float2 a = *(const float2*)(eb_ + 8*(64*hp+lane)); e2.re = a.x; e2.im = a.y;
        float2 c = *(const float2*)(eb_ + 1024 + 8*(64*hp+lane)); e1.re = c.x; e1.im = c.y; }
      if (wv == 0){
        cpx e8 = cmul(e2, e2); e8 = cmul(e8, e8); e8 = cmul(e8, e8);
        cpx xx; xx.re = 1.f; xx.im = 0.f;
        #pragma unroll
        for (int i=0;i<16;i++){ put_split(col + (41+i)*TR, xx.re, -xx.im); if (i < 15) xx = (i == 0) ? e8 : cmul(xx, e8); }
      } else if (wv == 1){
        cpx eb = cmul(e1, e1); eb = cmul(eb, eb); eb = cmul(eb, eb);
        cpx cb = cmul(C, eb);
        #pragma unroll
        for (int i=0;i<9;i++){ put_split(col + (32+i)*TR, cb.re, cb.im); if (i < 8) cb = cmul(cb, e1); }
      } else if (wv == 2){
        cpx S[8];
        S[1] = e2;
        #pragma unroll
        for (int i=2;i<8;i++) S[i] = cmul(S[i-1], S[1]);
        cpx cb = C;
        #pragma unroll
        for (int a=0;a<2;a++){
          put_split(col + (8*a)*TR, cb.re, cb.im);
          #pragma unroll
          for (int p=1;p<8;p++){ cpx g = cmul(cb, S[p]); put_split(col + (8*a+p)*TR, g.re, g.im); }
          if (a == 0) cb = cmul(cb, e1);
        }
      } else {
        cpx S2 = cmul(e2, e2), S4 = cmul(S2, S2), S6 = cmul(S4, S2);
        cpx cb = cmul(C, cmul(e1, e1));
        #pragma unroll
        for (int a=0;a<2;a++){
          put_split(col + (16+4*a)*TR, cb.re, cb.im);
          { cpx g = cmul(cb, S2); put_split(col + (16+4*a+1)*TR, g.re, g.im); }
          { cpx g = cmul(cb, S4); put_split(col + (16+4*a+2)*TR, g.re, g.im); }
          { cpx g = cmul(cb, S6); put_split(col + (16+4*a+3)*TR, g.re, g.im); }
          cb = cmul(cb, e1);
        }
        #pragma unroll
        for (int a=0;a<4;a++){
          put_split(col + (24+2*a)*TR, cb.re, cb.im);
          cpx g = cmul(cb, S4); put_split(col + (24+2*a+1)*TR, g.re, g.im);
          if (a < 3) cb = cmul(cb, e1);
        }
      }
      __syncthreads();
      if (wv < 3){
        const char* ga = tb + (16*wv + m)*TR + 16*kg;
        const char* xb = tb + (41 + m)*TR + 16*kg;
        #pragma unroll
        for (int kc=0;kc<4;kc++){
          bf16x8 ah = *(const bf16x8*)(ga + 64*kc), al = *(const bf16x8*)(ga + 64*kc + TLO);
          bf16x8 bh = *(const bf16x8*)(xb + 64*kc), bl = *(const bf16x8*)(xb + 64*kc + TLO);
          a4 = MFMA16(ah, bh, a4);
          a4 = MFMA16(ah, bl, a4);
          a4 = MFMA16(al, bh, a4);
        }
      }
      if (hp == 0) __syncthreads();
    }
    float* Kc = (float*)(smem + KCOFF) + ch*KC_;
    if (wv < 3){
      #pragma unroll
      for (int i=0;i<4;i++){
        int r = 16*wv + 4*kg + i;
        int idx;
        if (r < 16) idx = 128*(r>>3) + (r&7) + 8*m;
        else if (r < 24) idx = 256 + 64*((r-16)>>2) + (r&3) + 4*m;
        else if (r < 32) idx = 384 + 32*((r-24)>>1) + (r&1) + 2*m;
        else idx = 512 + 16*(r-32) + m;
        if (r < 40 || (r == 40 && m == 0)) Kc[idx] = 2.f*a4[i];
      }
    }
    put_u(smem, tr, u0, u1);
    __syncthreads();
    TSP(2);
    {
      char* ft = smem + FTOFF + ch*8192;
      if (tg < 208){
        int z, idx; bool ok; char* wp;
        if (tg < 128){ z = tg & 63; const int Dd = 47 - z; ok = Dd >= 0 && Dd < 32; idx = 385 + 4*Dd + 2*(tg >> 6); wp = ft + (tg >> 6)*(8*FCS); }
        else { z = tg - 128; const int Dd = 63 - z; ok = Dd >= 0; idx = 513 + 2*Dd; wp = ft + 16*FCS; }
        float a = 0.f, b = 0.f;
        if (ok){ a = Kc[idx]; b = Kc[idx+1]; }
        const uint32_t hh = pk2(a, b), ll = pk2(a - lo16f(hh), b - hi16f(hh));
        #pragma unroll
        for (int c=0;c<4;c++) if (z >= c){ *(uint32_t*)(wp + c*FCS + 4*(z-c)) = hh; *(uint32_t*)(wp + (4+c)*FCS + 4*(z-c)) = ll; }
      }
      if (tg >= 161){
        const int sl = tg - 161, j = sl - 15;
        float kv[8];
        #pragma unroll
        for (int i=0;i<8;i++){
          int l = 8*j + 7 - i;
          int pr = (int)rbf((float)l);
          int p = pr < 256 ? pr : 128 + (pr>>1);
          kv[i] = (l >= 0 && l <= 514) ? Kc[p] : 0.f;
        }
        u32x4 h, lo;
        #pragma unroll
        for (int jj=0;jj<4;jj++){
          uint32_t hh = pk2(kv[2*jj], kv[2*jj+1]);
          h[jj] = hh; lo[jj] = pk2(kv[2*jj] - lo16f(hh), kv[2*jj+1] - hi16f(hh));
        }
        char* kb = smem + TOFF + (2*ch)*KS2;
        *(u32x4*)(kb + 16*sl) = h;
        *(u32x4*)(kb + KS2 + 16*sl) = lo;
      }
      if (tg < 4) *(float*)(smem + YFOFF + ch*YFST - 16 + 4*tg) = 0.f;
    }
    __syncthreads();
    TSP(3);
  } else {
    const u32x2 k0 = *(const u32x2*)(k + (2*tr)*D_ + c0);
    const u32x2 k1 = *(const u32x2*)(k + (2*tr+1)*D_ + c0);
    const u32x4 wa = *(const u32x4*)(shift_k + c0*4), wb = *(const u32x4*)(shift_k + c0*4 + 8);
    char* ks = smem + YOFF;
    __builtin_amdgcn_sched_barrier(0);
    *(u32x4*)(ks + 32 + 16*tr) = u32x4{k0.x, k0.y, k1.x, k1.y};
    __builtin_amdgcn_sched_barrier(0);
    const u32x2 v0 = *(const u32x2*)(v + (2*tr)*D_ + c0);
    const u32x2 v1 = *(const u32x2*)(v + (2*tr+1)*D_ + c0);
    const u32x4 sx = *(const u32x4*)(ssmk + (c0+(t >> 8))*L_ + 8*(t & 255));
    __builtin_amdgcn_sched_barrier(0);
    if (t < 2) *(u32x4*)(ks + 16*t) = u32x4{0,0,0,0};
    __syncthreads();
    TSP(1);
    u32x2 kk[5];
    kk[0] = *(const u32x2*)(ks + 8 + 16*tr);
    { u32x4 x = *(const u32x4*)(ks + 16 + 16*tr); kk[1] = u32x2{x.x, x.y}; kk[2] = u32x2{x.z, x.w}; }
    kk[3] = k0; kk[4] = k1;
    u32x2 o[2];
    #pragma unroll
    for (int j=0;j<2;j++){
      const u32x2 vv = j ? v1 : v0;
      #pragma unroll
      for (int p=0;p<2;p++){
        float r2[2];
        #pragma unroll
        for (int e=0;e<2;e++){
          const u32x4 ww = p ? wb : wa;
          const uint32_t wl = e ? ww.z : ww.x, wh = e ? ww.w : ww.y;
          const float w0 = lo16f(wl), w1 = hi16f(wl), w2 = lo16f(wh), w3 = hi16f(wh);
          auto g = [&](int idx){ uint32_t x = kk[idx][p]; return e? hi16f(x) : lo16f(x); };
          float acc = rbf(g(j)*w3);
          acc = rbf(acc + rbf(g(j+1)*w2));
          acc = rbf(acc + rbf(g(j+2)*w1));
          acc = rbf(acc + rbf(g(j+3)*w0));
          r2[e] = acc * (e? hi16f(vv[p]) : lo16f(vv[p]));
        }
        o[j][p] = pk2(r2[0], r2[1]);
      }
    }
    put_u(smem, tr, o[0], o[1]);
    {
      const int ch = t >> 8, j = t & 255;
      u32x4 r;
      r.x = __builtin_amdgcn_alignbit(sx.w, sx.w, 16); r.y = __builtin_amdgcn_alignbit(sx.z, sx.z, 16);
      r.z = __builtin_amdgcn_alignbit(sx.y, sx.y, 16); r.w = __builtin_amdgcn_alignbit(sx.x, sx.x, 16);
      char* kb = smem + TOFF + ch*KST;
      *(u32x4*)(kb + 16*(j+15)) = r;
      if (j < 15) *(u32x4*)(kb + 16*j) = u32x4{0,0,0,0};
    }
    __syncthreads();
    TSP(2); TSP(3);
    if (wave >= 8){
      const int r4 = (((t - 512) + 32*(w5 & 15)) & 511)*4;
      u32x2 a[4];
      #pragma unroll
      for (int i=0;i<4;i++) a[i] = *(const u32x2*)(q + (r4+i)*D_ + c0);
      *(u32x4*)(smem + QOFF + 8*r4)      = u32x4{a[0].x, a[0].y, a[1].x, a[1].y};
      *(u32x4*)(smem + QOFF + 8*r4 + 16) = u32x4{a[2].x, a[2].y, a[3].x, a[3].y};
    }
  }
  if (isA){
    const int ch = wave >> 2, role = (wave + ch) & 3;
    if (role == 0) near_conv<4,0>(smem, ch, m, kg);
    else if (role == 1) near_conv<4,4>(smem, ch, m, kg);
    else if (role == 2){ __builtin_amdgcn_s_setprio(3); far_conv<0,4>(smem, ch, m, kg); __builtin_amdgcn_s_setprio(0); }
    else { __builtin_amdgcn_s_setprio(3); far_conv<4,6>(smem, ch, m, kg); __builtin_amdgcn_s_setprio(0); }
    TSP(5);
  } else if (wave < 8){
    const int ch = wave >> 1;
    const int g = wave & 1;
    const int part = 0;
    const int unit = ch;
    const uint32_t kbase = (uint32_t)(uintptr_t)smem + TOFF + unit*KST + 16*(m - kg + 3);
    const char* ub = smem + ch*UST;
    bf16x8 KA[4], KB[4];
    LDK4(KA, 15);
    f32x4 acc[4];
    {
      bf16x8 Up[2][4];
      {
        const uint32_t sel = (m & 1) ? 0x07060504u : 0x05040302u;
        const char* pp = ub + 272*(m >> 1) + 16*kg + 8*g;
        #pragma unroll
        for (int c=0;c<4;c++){ W6 W = uwin(pp + 64*c); Up[0][c] = usel(W, 0, sel); Up[1][c] = usel(W, 1, sel); }
      }
      f32x4 P0 = {0,0,0,0}, P1 = {0,0,0,0};
#define PH1(dl, CUR, NXT) \
      LDK4(NXT, (dl)-1); WAITK(4, CUR); \
      if ((dl) < 15){ P0 = shrN2(P0); P1 = shrN2(P1); } \
      P0 = MFMA16(CUR[0], Up[0][0], P0); P1 = MFMA16(CUR[0], Up[1][0], P1); \
      P0 = MFMA16(CUR[1], Up[0][1], P0); P1 = MFMA16(CUR[1], Up[1][1], P1); \
      P0 = MFMA16(CUR[2], Up[0][2], P0); P1 = MFMA16(CUR[2], Up[1][2], P1); \
      P0 = MFMA16(CUR[3], Up[0][3], P0); P1 = MFMA16(CUR[3], Up[1][3], P1);
      PH1(15, KA, KB) PH1(14, KB, KA) PH1(13, KA, KB) PH1(12, KB, KA)
      PH1(11, KA, KB) PH1(10, KB, KA) PH1(9, KA, KB) PH1(8, KB, KA)
      const bool ok = m >= 1 && m <= 8;
      const int src = 4*((lane & 48) + ((2*(m-1)) & 15));
      #pragma unroll
      for (int i=0;i<4;i++){
        float a0 = i2f(__builtin_amdgcn_ds_bpermute(src,   f2i(P0[i])));
        float a1 = i2f(__builtin_amdgcn_ds_bpermute(src+4, f2i(P0[i])));
        float a2 = i2f(__builtin_amdgcn_ds_bpermute(src,   f2i(P1[i])));
        float a3 = i2f(__builtin_amdgcn_ds_bpermute(src+4, f2i(P1[i])));
        acc[0][i] = ok ? a0 : 0.f; acc[1][i] = ok ? a1 : 0.f; acc[2][i] = ok ? a2 : 0.f; acc[3][i] = ok ? a3 : 0.f;
      }
    }
    TSP(4);
    bf16x8 U[4][4];
    {
      const char* pp = ub + 272*m + 16*kg + 8*g;
      #pragma unroll
      for (int c=0;c<4;c++){ W6 W = uwin(pp + 64*c); U[0][c] = uodd(W, 0); U[1][c] = uevn(W, 1); U[2][c] = uodd(W, 1); U[3][c] = uevn(W, 2); }
    }
#define PH2(dl, CUR, NXT) \
    if ((dl) > 0){ LDK4(NXT, (dl)-1); WAITK(4, CUR); } else { WAITK(0, CUR); } \
    if ((dl) < 7){ acc[0] = shrN1(acc[0]); acc[1] = shrN1(acc[1]); acc[2] = shrN1(acc[2]); acc[3] = shrN1(acc[3]); } \
    _Pragma("unroll") for (int c=0;c<4;c++){ \
      acc[0] = MFMA16(CUR[c], U[0][c], acc[0]); acc[1] = MFMA16(CUR[c], U[1][c], acc[1]); \
      acc[2] = MFMA16(CUR[c], U[2][c], acc[2]); acc[3] = MFMA16(CUR[c], U[3][c], acc[3]); }
    PH2(7, KA, KB) PH2(6, KB, KA) PH2(5, KA, KB) PH2(4, KB, KA)
    PH2(3, KA, KB) PH2(2, KB, KA) PH2(1, KA, KB) PH2(0, KB, KA)
    TSP(5);
    char* yb = smem + YOFF + (part*4 + ch)*YST + 4*(4*g + 32*kg + 132*m);
    #pragma unroll
    for (int i=0;i<4;i++) *(f32x4*)(yb + 32*i) = f32x4{acc[0][i], acc[1][i], acc[2][i], acc[3][i]};
  }
  __syncthreads();
  TSP(6);
  {
    const char* yb = smem + YOFF + 8*tr + 16*(tr >> 6);
    const char* up = smem + 272*(tr >> 6) + 16 + 4*(tr & 63);
    float o0[4], o1[4];
    if (isA){
      const char* ya = yb + (YAOFF - YOFF);
      const char* yf = smem + YFOFF + (tr >= 257 ? 4*(2*tr - 515) : -16);
      #pragma unroll
      for (int c=0;c<4;c++){
        f32x2 a = *(const f32x2*)(ya + c*YST);
        float b0 = *(const float*)(yf + c*YFST), b1 = *(const float*)(yf + c*YFST + 4);
        uint32_t uu = *(const uint32_t*)(up + c*UST);
        float sd = (c & 1) ? hi16f(dsk[c>>1]) : lo16f(dsk[c>>1]);
        o0[c] = rbf(a.x + b0) + rbf(lo16f(uu)*sd);
        o1[c] = rbf(a.y + b1) + rbf(hi16f(uu)*sd);
      }
    } else {
      u32x4 qq = *(const u32x4*)(smem + QOFF + 16*tr);
      #pragma unroll
      for (int c=0;c<4;c++){
        f32x2 a = *(const f32x2*)(yb + c*YST);
        uint32_t uu = *(const uint32_t*)(up + c*UST);
        float sd = (c & 1) ? hi16f(dsk[c>>1]) : lo16f(dsk[c>>1]);
        float q0 = (c & 1) ? hi16f(qq[c>>1]) : lo16f(qq[c>>1]);
        float q1 = (c & 1) ? hi16f(qq[2+(c>>1)]) : lo16f(qq[2+(c>>1)]);
        o0[c] = q0 * rbf(rbf(a.x) + rbf(lo16f(uu)*sd));
        o1[c] = q1 * rbf(rbf(a.y) + rbf(hi16f(uu)*sd));
      }
    }
    u16* op = out + (isA ? 0 : D_) + c0;
    *(u32x2*)(op + (2*tr)*(2*D_))   = u32x2{pk2(o0[0],o0[1]), pk2(o0[2],o0[3])};
    *(u32x2*)(op + (2*tr+1)*(2*D_)) = u32x2{pk2(o1[0],o1[1]), pk2(o1[2],o1[3])};
  }
  TSP(7);
  if (wave == 0){
    unsigned long long mn = tsv[0];
    for (int i=0;i<4;i++){
      unsigned long long x = __hip_atomic_load(wsb + 64*i + lane, __ATOMIC_RELAXED, __HIP_MEMORY_SCOPE_AGENT);
      if (x + 1500 > tsv[0] && x < mn) mn = x;
    }
    unsigned d32 = (unsigned)(tsv[0] - mn);
    for (int o=32;o>=1;o>>=1){ unsigned y = __shfl_xor(d32, o); d32 = y > d32 ? y : d32; }
    d32 = __builtin_amdgcn_readfirstlane(d32);
    if (d32 > 1500) d32 = 1500;
    unsigned long long dlt = (unsigned long long)d32*8;
    unsigned long long tgt = tsv[7] + dlt;
    while (__builtin_amdgcn_s_memrealtime() < tgt) __builtin_amdgcn_s_sleep(1);
  }
}

extern "C" void kernel_launch(void* const* d_in, const int* in_sizes, int n_in,
                              void* d_out, int out_size, void* d_ws, size_t ws_size,
                              hipStream_t stream) {
  mono_kernel<<<256, 1024, LDS_TOTAL, stream>>>((const u16*)d_in[0], (const u16*)d_in[1], (const u16*)d_in[2], (const u16*)d_in[3],
     (const u16*)d_in[4], (const u16*)d_in[5], (const u16*)d_in[6], (const u16*)d_in[7], (const u16*)d_in[8], (const u16*)d_in[9],
     (const u16*)d_in[10], (const u16*)d_in[11], (const u16*)d_in[12], (u16*)d_out, (unsigned long long*)d_ws
);
}
```

```cpp
#include <hip/hip_runtime.h>
#include <hip/hip_bf16.h>
#include <stdint.h>

typedef unsigned short u16;
typedef short bf16x8 __attribute__((ext_vector_type(8)));
typedef float f32x4 __attribute__((ext_vector_type(4)));
typedef float f32x2 __attribute__((ext_vector_type(2)));
typedef __bf16 bf16x2_t __attribute__((ext_vector_type(2)));

#define L_ 2048
#define D_ 512
#define NS_ 128

__device__ __forceinline__ float bf2f(u16 x){ return __builtin_bit_cast(float, (uint32_t)x << 16); }
__device__ __forceinline__ u16 f2bf(float f){ __hip_bfloat16 h = __float2bfloat16(f); return __builtin_bit_cast(u16, h); }
__device__ __forceinline__ float rbf(float f){ return bf2f(f2bf(f)); }
__device__ __forceinline__ uint32_t pk2(float a, float b){
  f32x2 v = {a,b}; bf16x2_t r = __builtin_convertvector(v, bf16x2_t); return __builtin_bit_cast(uint32_t, r); }
__device__ __forceinline__ float lo16f(uint32_t w){ return __builtin_bit_cast(float, w << 16); }
__device__ __forceinline__ float hi16f(uint32_t w){ return __builtin_bit_cast(float, w & 0xffff0000u); }

struct cpx { float re, im; };
__device__ __forceinline__ float amul(float a, float b){ float r; asm("v_mul_f32 %0, %1, %2" : "=v"(r) : "v"(a), "v"(b)); return r; }
__device__ __forceinline__ float afma(float a, float b, float c){ float r; asm("v_fma_f32 %0, %1, %2, %3" : "=v"(r) : "v"(a), "v"(b), "v"(c)); return r; }
__device__ __forceinline__ float afms(float a, float b, float c){ float r; asm("v_fma_f32 %0, %1, %2, -%3" : "=v"(r) : "v"(a), "v"(b), "v"(c)); return r; }
__device__ __forceinline__ cpx cmul(cpx a, cpx b){
  f32x2 A = {a.re, a.im}, B = {b.re, b.im}, T, R;
  asm("v_pk_mul_f32 %0, %1, %2 op_sel:[1,1] op_sel_hi:[1,0] neg_lo:[1,0]" : "=v"(T) : "v"(A), "v"(B));
  asm("v_pk_fma_f32 %0, %1, %2, %3 op_sel:[0,0,0] op_sel_hi:[0,1,1]" : "=v"(R) : "v"(A), "v"(B), "v"(T));
  cpx r; r.re = R.x; r.im = R.y; return r; }
__device__ __forceinline__ cpx cexp_(float re, float im){ float e = expf(re); float s,c; sincosf(im,&s,&c); cpx r; r.re=e*c; r.im=e*s; return r; }

#define MFMA16(a,b,c) __builtin_amdgcn_mfma_f32_16x16x32_bf16(a,b,c,0,0,0)
typedef uint32_t u32x2 __attribute__((ext_vector_type(2)));
typedef uint32_t u32x4 __attribute__((ext_vector_type(4)));
#define KC_ 656
#define TR 288
#define TLO (57*TR)
#define TSZ (2*TLO)
#define UST 4352
#define KCOFF 17408
#define TOFF 27904
#define KST 4352
#define KS2 1536
#define FTOFF (TOFF + 12288)
#define FCS 320
#define YFOFF (TOFF + 45056 + 16)
#define YFST 6176
#define YAOFF (TOFF + 69888)
#define YOFF (TOFF + 36864)
#define YST 8448
#define QOFF (TOFF + 112128)
#define LDS_TOTAL (TOFF + 4*TSZ)

__device__ __forceinline__ uint32_t resid2(uint32_t h, float a, float b){
  return pk2(a - lo16f(h), b - hi16f(h));
}
__device__ __forceinline__ void put_split(char* p, float a, float b){
  uint32_t h = pk2(a,b);
  uint32_t l = resid2(h, a, b);
  *(uint32_t*)p = h; *(uint32_t*)(p + TLO) = l;
}
__device__ __forceinline__ int f2i(float x){ return __builtin_bit_cast(int, x); }
__device__ __forceinline__ float i2f(int x){ return __builtin_bit_cast(float, x); }
__device__ __forceinline__ f32x4 shrN1(f32x4 x){
  f32x4 r;
  #pragma unroll
  for (int i=0;i<4;i++) r[i] = i2f(__builtin_amdgcn_update_dpp(0, f2i(x[i]), 0x111, 0xf, 0xf, true));
  return r;
}
__device__ __forceinline__ f32x4 shrN2(f32x4 x){
  f32x4 r;
  #pragma unroll
  for (int i=0;i<4;i++) r[i] = i2f(__builtin_amdgcn_update_dpp(0, f2i(x[i]), 0x112, 0xf, 0xf, true));
  return r;
}
struct W6 { uint32_t w[6]; };
__device__ __forceinline__ W6 uwin(const char* p){
  u32x2 a = *(const u32x2*)p, b = *(const u32x2*)(p+8), c = *(const u32x2*)(p+16);
  W6 r; r.w[0]=a.x; r.w[1]=a.y; r.w[2]=b.x; r.w[3]=b.y; r.w[4]=c.x; r.w[5]=c.y; return r;
}
__device__ __forceinline__ bf16x8 usel(const W6& W, int x, uint32_t sel){
  u32x4 r;
  r.x = __builtin_amdgcn_perm(W.w[x+1], W.w[x],   sel); r.y = __builtin_amdgcn_perm(W.w[x+2], W.w[x+1], sel);
  r.z = __builtin_amdgcn_perm(W.w[x+3], W.w[x+2], sel); r.w = __builtin_amdgcn_perm(W.w[x+4], W.w[x+3], sel);
  return __builtin_bit_cast(bf16x8, r);
}
__device__ __forceinline__ bf16x8 uodd(const W6& W, int x){
  u32x4 r;
  r.x = __builtin_amdgcn_alignbit(W.w[x+1], W.w[x],   16); r.y = __builtin_amdgcn_alignbit(W.w[x+2], W.w[x+1], 16);
  r.z = __builtin_amdgcn_alignbit(W.w[x+3], W.w[x+2], 16); r.w = __builtin_amdgcn_alignbit(W.w[x+4], W.w[x+3], 16);
  return __builtin_bit_cast(bf16x8, r);
}
__device__ __forceinline__ bf16x8 uevn(const W6& W, int x){
  u32x4 r = {W.w[x], W.w[x+1], W.w[x+2], W.w[x+3]};
  return __builtin_bit_cast(bf16x8, r);
}
__device__ __forceinline__ void put_u(char* smem, int tr, u32x2 r0, u32x2 r1){
  uint32_t d[4];
  d[0] = __builtin_amdgcn_perm(r1.x, r0.x, 0x05040100); d[1] = __builtin_amdgcn_perm(r1.x, r0.x, 0x07060302);
  d[2] = __builtin_amdgcn_perm(r1.y, r0.y, 0x05040100); d[3] = __builtin_amdgcn_perm(r1.y, r0.y, 0x07060302);
  const int a = tr >> 6, pos = tr & 63;
  char* p = smem + 272*a + 16 + 4*pos;
  #pragma unroll
  for (int c=0;c<4;c++) *(uint32_t*)(p + c*UST) = d[c];
  if (pos >= 60 && a < 15){
    #pragma unroll
    for (int c=0;c<4;c++) *(uint32_t*)(p + c*UST + 16) = d[c];
  }
  if (tr < 16) *(uint32_t*)(smem + (tr>>2)*UST + 4*(tr&3)) = 0u;
}

#define LDK(F, dl, c) asm volatile("ds_read_b128 %0, %1 offset:%2" : "=v"(F) : "v"(kbase), "i"(256*(dl) + 64*(3-(c))))
#define LDK4(S, dl) LDK(S[0], dl, 0); LDK(S[1], dl, 1); LDK(S[2], dl, 2); LDK(S[3], dl, 3)
#define WAITK(n, S) asm volatile("s_waitcnt lgkmcnt(" #n ")" : "+v"(S[0]), "+v"(S[1]), "+v"(S[2]), "+v"(S[3]))

template<int NT, int R0>
__device__ __forceinline__ void near_conv(char* smem, int ch, int m, int kg){
  const uint32_t kbase = (uint32_t)(uintptr_t)smem + TOFF + (2*ch)*KS2 + 16*(m - kg + 3);
#define LDN(F, hs, c) asm volatile("ds_read_b128 %0, %1 offset:%2" : "=v"(F) : "v"(kbase), "i"(((hs)&1)*KS2 + 256*(4-((hs)>>1)) + 64*(3-(c))))
#define LDN4(S, hs) LDN(S[0], hs, 0); LDN(S[1], hs, 1); LDN(S[2], hs, 2); LDN(S[3], hs, 3)
  bf16x8 KA[4], KB[4];
  LDN4(KA, 0);
  bf16x8 U[NT][4];
  {
    const char* pp = smem + ch*UST + 272*m + 16*kg + (R0 ? 8 : 0);
    #pragma unroll
    for (int c=0;c<4;c++){
      W6 W = uwin(pp + 64*c);
      #pragma unroll
      for (int r=0;r<NT;r++){ const int rel = R0 + r + 1 - (R0 ? 4 : 0); U[r][c] = (rel & 1) ? uodd(W, rel >> 1) : uevn(W, rel >> 1); }
    }
  }
  f32x4 acc[NT];
  #pragma unroll
  for (int r=0;r<NT;r++) acc[r] = f32x4{0,0,0,0};
#define NH(hs, CUR, NXT) \
  if ((hs) < 9){ LDN4(NXT, (hs)+1); WAITK(4, CUR); } else { WAITK(0, CUR); } \
  if ((hs) > 0 && !((hs) & 1)){ _Pragma("unroll") for (int r=0;r<NT;r++) acc[r] = shrN1(acc[r]); } \
  _Pragma("unroll") for (int c=0;c<4;c++){ _Pragma("unroll") for (int r=0;r<NT;r++) acc[r] = MFMA16(CUR[c], U[r][c], acc[r]); }
  NH(0, KA, KB) NH(1, KB, KA) NH(2, KA, KB) NH(3, KB, KA) NH(4, KA, KB)
  NH(5, KB, KA) NH(6, KA, KB) NH(7, KB, KA) NH(8, KA, KB) NH(9, KB, KA)
  float* yb = (float*)(smem + YAOFF + ch*YST) + (R0 + 32*kg + 132*m);
  #pragma unroll
  for (int i=0;i<4;i++){
    #pragma unroll
    for (int r=0;r<NT;r++) yb[8*i + r] = acc[r][i];
  }
}
__device__ __forceinline__ bf16x8 band(int sh, int w, int kg){
  const uint32_t by = (((1u << w) - 1u) << sh) >> (8*kg);
  u32x4 r;
  #pragma unroll
  for (int k2=0;k2<4;k2++){ uint32_t x = (by >> (2*k2)) & 3u; x = (x | (x << 15)) & 0x00010001u; r[k2] = x * 0x3f80u; }
  return __builtin_bit_cast(bf16x8, r);
}
__device__ __forceinline__ void split2(const f32x4& a, const f32x4& b, bf16x8& h, bf16x8& l){
  u32x4 hh, ll;
  #pragma unroll
  for (int i=0;i<4;i++){ uint32_t x = pk2(a[i], b[i]); hh[i] = x; ll[i] = resid2(x, a[i], b[i]); }
  h = __builtin_bit_cast(bf16x8, hh); l = __builtin_bit_cast(bf16x8, ll);
}
template<int A0, int A1>
__device__ __forceinline__ void far_conv(char* smem, int ch, int m, int kg){
  const char* ub = smem + ch*UST;
  const f32x4 Z4 = {0,0,0,0};
  const int B0 = 2*m + kg - 2;
  const char* va0 = ub + (B0 < 0 ? 0 : 272*(B0 >> 4) + 16 + 16*(B0 & 15));
  const char* va1 = ub + 272*((B0+32) >> 4) + 16 + 16*((B0+32) & 15);
  const int cp = (3 - m) & 3;
  const char* ft = smem + FTOFF + ch*8192 + cp*FCS + 4*(47 - m + 4*kg - cp);
  char* yf = smem + YFOFF + ch*YFST + 64*m + 16*kg;
  f32x4 acc[6];
  #pragma unroll
  for (int a=0;a<6;a++) acc[a] = Z4;
  {
    bf16x8 M[4];
    M[0] = band(14 + m, 3, kg); M[1] = band(9 + m, 5, kg); M[2] = band(6 + m, 3, kg); M[3] = band(1 + m, 5, kg);
    bf16x8 Ch[3][2], Cl[3][2];
    #pragma unroll
    for (int dl=0;dl<3;dl++){
      #pragma unroll
      for (int h=0;h<2;h++){ Ch[dl][h] = *(const bf16x8*)(ft + h*(8*FCS) - 64*dl); Cl[dl][h] = *(const bf16x8*)(ft + h*(8*FCS) + 4*FCS - 64*dl); }
    }
    #pragma unroll
    for (int b=(A0 > 2 ? A0-2 : 0);b<A1;b++){
      const bf16x8 V = *(const bf16x8*)(b ? va1 + 544*(b-1) : va0);
      f32x4 F[4];
      #pragma unroll
      for (int p=0;p<4;p++) F[p] = MFMA16(V, M[p], Z4);
      #pragma unroll
      for (int h=0;h<2;h++){
        bf16x8 Bh, Bl;
        split2(F[2*h], F[2*h+1], Bh, Bl);
        #pragma unroll
        for (int dl=0;dl<3;dl++) if (b + dl >= A0 && b + dl < A1) acc[b+dl] = MFMA16(Bh, Ch[dl][h], acc[b+dl]);
        #pragma unroll
        for (int dl=0;dl<3;dl++) if (b + dl >= A0 && b + dl < A1) acc[b+dl] = MFMA16(Bh, Cl[dl][h], acc[b+dl]);
        #pragma unroll
        for (int dl=0;dl<3;dl++) if (b + dl >= A0 && b + dl < A1) acc[b+dl] = MFMA16(Bl, Ch[dl][h], acc[b+dl]);
      }
      if (b < 2 && b >= A0) *(f32x4*)(yf + 1024*b) = acc[b];
    }
  }
  {
    const char* vb0 = ub + (B0 < 1 ? 0 : 272*((B0-1) >> 4) + 16 + 16*((B0-1) & 15));
    const char* vb1 = ub + 272*((B0+31) >> 4) + 16 + 16*((B0+31) & 15);
    const char* f8 = smem + FTOFF + ch*8192 + 16*FCS + cp*FCS + 4*(63 - m + 4*kg - cp);
    const bf16x8 Ma = band(8 + m, 7, kg), Mb = band(7 + m, 9, kg), Mc = band(15 + m, 2, kg);
    const float k1024 = ((const float*)(smem + KCOFF))[ch*KC_ + 512];
    bf16x8 Ch[4], Cl[4];
    #pragma unroll
    for (int dl=0;dl<A1-2;dl++){ Ch[dl] = *(const bf16x8*)(f8 - 64*dl); Cl[dl] = *(const bf16x8*)(f8 + 4*FCS - 64*dl); }
    #pragma unroll
    for (int b=0;b<A1-2;b++){
      const bf16x8 V = *(const bf16x8*)(b ? va1 + 544*(b-1) : va0);
      const bf16x8 V2 = *(const bf16x8*)(b ? vb1 + 544*(b-1) : vb0);
      const f32x4 Fa = MFMA16(V, Ma, Z4), Fb = MFMA16(V2, Mb, Z4);
      bf16x8 Bh, Bl;
      split2(Fa, Fb, Bh, Bl);
      if (b + 2 >= A0){
        const f32x4 Fc = MFMA16(Mc, V, Z4);
        #pragma unroll
        for (int i=0;i<4;i++) acc[b+2][i] = __builtin_fmaf(k1024, Fc[i], acc[b+2][i]);
      }
      #pragma unroll
      for (int dl=0;dl<4;dl++) if (b + 2 + dl >= A0 && b + 2 + dl < A1) acc[b+2+dl] = MFMA16(Bh, Ch[dl], acc[b+2+dl]);
      #pragma unroll
      for (int dl=0;dl<4;dl++) if (b + 2 + dl >= A0 && b + 2 + dl < A1) acc[b+2+dl] = MFMA16(Bh, Cl[dl], acc[b+2+dl]);
      #pragma unroll
      for (int dl=0;dl<4;dl++) if (b + 2 + dl >= A0 && b + 2 + dl < A1) acc[b+2+dl] = MFMA16(Bl, Ch[dl], acc[b+2+dl]);
    }
  }
  #pragma unroll
  for (int a=(A0 > 2 ? A0 : 2);a<A1;a++) *(f32x4*)(yf + 1024*a) = acc[a];
}

__global__ __launch_bounds__(1024) void mono_kernel(const u16* __restrict__ u, const u16* __restrict__ logA, const u16* __restrict__ Aim,
    const u16* __restrict__ Cre, const u16* __restrict__ Cim, const u16* __restrict__ logdt, const u16* __restrict__ Dskip,
    const u16* __restrict__ q, const u16* __restrict__ k, const u16* __restrict__ v, const u16* __restrict__ shift_k,
    const u16* __restrict__ ssmk, const u16* __restrict__ ssmD, u16* __restrict__ out
){
#define TSP(i)
  TSP(0); TSP(1); TSP(2); TSP(3); TSP(4); TSP(5);
  extern __shared__ __attribute__((aligned(16))) char smem[];
  const int bid = blockIdx.x, t = threadIdx.x;
  const int w5 = bid >> 3;
  const bool isA = w5 < 16;
  const int c0 = (bid & 7)*64 + (w5 & 15)*4;
  const int tr = (t + 64*(w5 & 15)) & 1023;
  const int wave = t >> 6, lane = t & 63, m = lane & 15, kg = lane >> 4;
  const u32x2 dsk = *(const u32x2*)((isA ? Dskip : ssmD) + c0);
  if (isA){
    const int ch = t >> 8, tg = t & 255, wv = (wave + ch) & 3;
    const int d = c0 + ch;
    char* tb = smem + TOFF + ch*TSZ;
    char* eb_ = smem + KCOFF + ch*(4*KC_);
    const u16 ldt = logdt[d];
    const u16 pla = logA[d*NS_+(tg & 127)], pai = Aim[d*NS_+(tg & 127)];
    u16 pc[2][2];
    #pragma unroll
    for (int hp=0;hp<2;hp++){ pc[hp][0] = Cre[d*NS_+64*hp+lane];  pc[hp][1] = Cim[d*NS_+64*hp+lane]; }
    const float dt = rbf(expf(bf2f(ldt)));
    {
      const float zre = -rbf(expf(bf2f(pla))) * dt;
      const float zim = bf2f(pai) * dt;
      const float f = (tg >> 7) ? 128.f : 1.f;
      cpx e = cexp_(zre*f, zim*f);
      *(float2*)(eb_ + 8*tg) = make_float2(e.re, e.im);
    }
    __syncthreads();
    TSP(1);
    __builtin_amdgcn_sched_barrier(0);
    const u32x2 u0 = *(const u32x2*)(u + (2*tr)*D_ + c0);
    const u32x2 u1 = *(const u32x2*)(u + (2*tr+1)*D_ + c0);
    __builtin_amdgcn_sched_barrier(0);
    f32x4 a4 = {0,0,0,0};
    char* col = tb + 4*lane;
    #pragma unroll 1
    for (int hp=0;hp<2;hp++){
      cpx C; C.re = bf2f(hp ? pc[1][0] : pc[0][0])*dt; C.im = bf2f(hp ? pc[1][1] : pc[0][1])*dt;
      cpx e1, e2;
      { float2 a = *(const float2*)(eb_ + 8*(64*hp+lane)); e2.re = a.x; e2.im = a.y;
        float2 c = *(const float2*)(eb_ + 1024 + 8*(64*hp+lane)); e1.re = c.x; e1.im = c.y; }
      if (wv == 0){
        cpx e8 = cmul(e2, e2); e8 = cmul(e8, e8); e8 = cmul(e8, e8);
        cpx xx; xx.re = 1.f; xx.im = 0.f;
        #pragma unroll
        for (int i=0;i<16;i++){ put_split(col + (41+i)*TR, xx.re, -xx.im); if (i < 15) xx = (i == 0) ? e8 : cmul(xx, e8); }
      } else if (wv == 1){
        cpx eb = cmul(e1, e1); eb = cmul(eb, eb); eb = cmul(eb, eb);
        cpx cb = cmul(C, eb);
        #pragma unroll
        for (int i=0;i<9;i++){ put_split(col + (32+i)*TR, cb.re, cb.im); if (i < 8) cb = cmul(cb, e1); }
      } else if (wv == 2){
        cpx S[8];
        S[1] = e2;
        #pragma unroll
        for (int i=2;i<8;i++) S[i] = cmul(S[i-1], S[1]);
        cpx cb = C;
        #pragma unroll
        for (int a=0;a<2;a++){
          put_split(col + (8*a)*TR, cb.re, cb.im);
          #pragma unroll
          for (int p=1;p<8;p++){ cpx g = cmul(cb, S[p]); put_split(col + (8*a+p)*TR, g.re, g.im); }
          if (a == 0) cb = cmul(cb, e1);
        }
      } else {
        cpx S2 = cmul(e2, e2), S4 = cmul(S2, S2), S6 = cmul(S4, S2);
        cpx cb = cmul(C, cmul(e1, e1));
        #pragma unroll
        for (int a=0;a<2;a++){
          put_split(col + (16+4*a)*TR, cb.re, cb.im);
          { cpx g = cmul(cb, S2); put_split(col + (16+4*a+1)*TR, g.re, g.im); }
          { cpx g = cmul(cb, S4); put_split(col + (16+4*a+2)*TR, g.re, g.im); }
          { cpx g = cmul(cb, S6); put_split(col + (16+4*a+3)*TR, g.re, g.im); }
          cb = cmul(cb, e1);
        }
        #pragma unroll
        for (int a=0;a<4;a++){
          put_split(col + (24+2*a)*TR, cb.re, cb.im);
          cpx g = cmul(cb, S4); put_split(col + (24+2*a+1)*TR, g.re, g.im);
          if (a < 3) cb = cmul(cb, e1);
        }
      }
      __syncthreads();
      if (wv < 3){
        const char* ga = tb + (16*wv + m)*TR + 16*kg;
        const char* xb = tb + (41 + m)*TR + 16*kg;
        #pragma unroll
        for (int kc=0;kc<4;kc++){
          bf16x8 ah = *(const bf16x8*)(ga + 64*kc), al = *(const bf16x8*)(ga + 64*kc + TLO);
          bf16x8 bh = *(const bf16x8*)(xb + 64*kc), bl = *(const bf16x8*)(xb + 64*kc + TLO);
          a4 = MFMA16(ah, bh, a4);
          a4 = MFMA16(ah, bl, a4);
          a4 = MFMA16(al, bh, a4);
        }
      }
      if (hp == 0) __syncthreads();
    }
    float* Kc = (float*)(smem + KCOFF) + ch*KC_;
    if (wv < 3){
      #pragma unroll
      for (int i=0;i<4;i++){
        int r = 16*wv + 4*kg + i;
        int idx;
        if (r < 16) idx = 128*(r>>3) + (r&7) + 8*m;
        else if (r < 24) idx = 256 + 64*((r-16)>>2) + (r&3) + 4*m;
        else if (r < 32) idx = 384 + 32*((r-24)>>1) + (r&1) + 2*m;
        else idx = 512 + 16*(r-32) + m;
        if (r < 40 || (r == 40 && m == 0)) Kc[idx] = 2.f*a4[i];
      }
    }
    put_u(smem, tr, u0, u1);
    __syncthreads();
    TSP(2);
    {
      char* ft = smem + FTOFF + ch*8192;
      if (tg < 208){
        int z, idx; bool ok; char* wp;
        if (tg < 128){ z = tg & 63; const int Dd = 47 - z; ok = Dd >= 0 && Dd < 32; idx = 385 + 4*Dd + 2*(tg >> 6); wp = ft + (tg >> 6)*(8*FCS); }
        else { z = tg - 128; const int Dd = 63 - z; ok = Dd >= 0; idx = 513 + 2*Dd; wp = ft + 16*FCS; }
        float a = 0.f, b = 0.f;
        if (ok){ a = Kc[idx]; b = Kc[idx+1]; }
        const uint32_t hh = pk2(a, b), ll = pk2(a - lo16f(hh), b - hi16f(hh));
        #pragma unroll
        for (int c=0;c<4;c++) if (z >= c){ *(uint32_t*)(wp + c*FCS + 4*(z-c)) = hh; *(uint32_t*)(wp + (4+c)*FCS + 4*(z-c)) = ll; }
      }
      if (tg >= 161){
        const int sl = tg - 161, j = sl - 15;
        float kv[8];
        #pragma unroll
        for (int i=0;i<8;i++){
          int l = 8*j + 7 - i;
          int pr = (int)rbf((float)l);
          int p = pr < 256 ? pr : 128 + (pr>>1);
          kv[i] = (l >= 0 && l <= 514) ? Kc[p] : 0.f;
        }
        u32x4 h, lo;
        #pragma unroll
        for (int jj=0;jj<4;jj++){
          uint32_t hh = pk2(kv[2*jj], kv[2*jj+1]);
          h[jj] = hh; lo[jj] = pk2(kv[2*jj] - lo16f(hh), kv[2*jj+1] - hi16f(hh));
        }
        char* kb = smem + TOFF + (2*ch)*KS2;
        *(u32x4*)(kb + 16*sl) = h;
        *(u32x4*)(kb + KS2 + 16*sl) = lo;
      }
      if (tg < 4) *(float*)(smem + YFOFF + ch*YFST - 16 + 4*tg) = 0.f;
    }
    __syncthreads();
    TSP(3);
  } else {
    const u32x2 k0 = *(const u32x2*)(k + (2*tr)*D_ + c0);
    const u32x2 k1 = *(const u32x2*)(k + (2*tr+1)*D_ + c0);
    const u32x4 wa = *(const u32x4*)(shift_k + c0*4), wb = *(const u32x4*)(shift_k + c0*4 + 8);
    char* ks = smem + YOFF;
    __builtin_amdgcn_sched_barrier(0);
    *(u32x4*)(ks + 32 + 16*tr) = u32x4{k0.x, k0.y, k1.x, k1.y};
    __builtin_amdgcn_sched_barrier(0);
    const u32x2 v0 = *(const u32x2*)(v + (2*tr)*D_ + c0);
    const u32x2 v1 = *(const u32x2*)(v + (2*tr+1)*D_ + c0);
    const u32x4 sx = *(const u32x4*)(ssmk + (c0+(t >> 8))*L_ + 8*(t & 255));
    __builtin_amdgcn_sched_barrier(0);
    if (t < 2) *(u32x4*)(ks + 16*t) = u32x4{0,0,0,0};
    __syncthreads();
    TSP(1);
    u32x2 kk[5];
    kk[0] = *(const u32x2*)(ks + 8 + 16*tr);
    { u32x4 x = *(const u32x4*)(ks + 16 + 16*tr); kk[1] = u32x2{x.x, x.y}; kk[2] = u32x2{x.z, x.w}; }
    kk[3] = k0; kk[4] = k1;
    u32x2 o[2];
    #pragma unroll
    for (int j=0;j<2;j++){
      const u32x2 vv = j ? v1 : v0;
      #pragma unroll
      for (int p=0;p<2;p++){
        float r2[2];
        #pragma unroll
        for (int e=0;e<2;e++){
          const u32x4 ww = p ? wb : wa;
          const uint32_t wl = e ? ww.z : ww.x, wh = e ? ww.w : ww.y;
          const float w0 = lo16f(wl), w1 = hi16f(wl), w2 = lo16f(wh), w3 = hi16f(wh);
          auto g = [&](int idx){ uint32_t x = kk[idx][p]; return e? hi16f(x) : lo16f(x); };
          float acc = rbf(g(j)*w3);
          acc = rbf(acc + rbf(g(j+1)*w2));
          acc = rbf(acc + rbf(g(j+2)*w1));
          acc = rbf(acc + rbf(g(j+3)*w0));
          r2[e] = acc * (e? hi16f(vv[p]) : lo16f(vv[p]));
        }
        o[j][p] = pk2(r2[0], r2[1]);
      }
    }
    put_u(smem, tr, o[0], o[1]);
    {
      const int ch = t >> 8, j = t & 255;
      u32x4 r;
      r.x = __builtin_amdgcn_alignbit(sx.w, sx.w, 16); r.y = __builtin_amdgcn_alignbit(sx.z, sx.z, 16);
      r.z = __builtin_amdgcn_alignbit(sx.y, sx.y, 16); r.w = __builtin_amdgcn_alignbit(sx.x, sx.x, 16);
      char* kb = smem + TOFF + ch*KST;
      *(u32x4*)(kb + 16*(j+15)) = r;
      if (j < 15) *(u32x4*)(kb + 16*j) = u32x4{0,0,0,0};
    }
    __syncthreads();
    TSP(2); TSP(3);
    if (wave >= 8){
      const int r4 = (((t - 512) + 32*(w5 & 15)) & 511)*4;
      u32x2 a[4];
      #pragma unroll
      for (int i=0;i<4;i++) a[i] = *(const u32x2*)(q + (r4+i)*D_ + c0);
      *(u32x4*)(smem + QOFF + 8*r4)      = u32x4{a[0].x, a[0].y, a[1].x, a[1].y};
      *(u32x4*)(smem + QOFF + 8*r4 + 16) = u32x4{a[2].x, a[2].y, a[3].x, a[3].y};
    }
  }
  if (dsk.x == 0x12345678u && dsk.y == 0x9abcdef0u && smem[77] == 99){
    float a = t, b = 1.0001f;
#define R4(x) x x x x
#define R16(x) R4(R4(x))
#define R256(x) R16(R16(x))
    R256(R16(asm volatile("v_fma_f32 %0, %0, %1, %1\n v_fma_f32 %0, %0, %1, %1" : "+v"(a) : "v"(b));))
    *(float*)(smem + 4*t) = a;
  }
  if (isA){
    const int ch = wave >> 2, role = (wave + ch) & 3;
    if (role == 0) near_conv<4,0>(smem, ch, m, kg);
    else if (role == 1) near_conv<4,4>(smem, ch, m, kg);
    else if (role == 2){ __builtin_amdgcn_s_setprio(3); far_conv<0,4>(smem, ch, m, kg); __builtin_amdgcn_s_setprio(0); }
    else { __builtin_amdgcn_s_setprio(3); far_conv<4,6>(smem, ch, m, kg); __builtin_amdgcn_s_setprio(0); }
    TSP(5);
  } else if (wave < 8){
    const int ch = wave >> 1;
    const int g = wave & 1;
    const int part = 0;
    const int unit = ch;
    const uint32_t kbase = (uint32_t)(uintptr_t)smem + TOFF + unit*KST + 16*(m - kg + 3);
    const char* ub = smem + ch*UST;
    bf16x8 KA[4], KB[4];
    LDK4(KA, 15);
    f32x4 acc[4];
    {
      bf16x8 Up[2][4];
      {
        const uint32_t sel = (m & 1) ? 0x07060504u : 0x05040302u;
        const char* pp = ub + 272*(m >> 1) + 16*kg + 8*g;
        #pragma unroll
        for (int c=0;c<4;c++){ W6 W = uwin(pp + 64*c); Up[0][c] = usel(W, 0, sel); Up[1][c] = usel(W, 1, sel); }
      }
      f32x4 P0 = {0,0,0,0}, P1 = {0,0,0,0};
#define PH1(dl, CUR, NXT) \
      LDK4(NXT, (dl)-1); WAITK(4, CUR); \
      if ((dl) < 15){ P0 = shrN2(P0); P1 = shrN2(P1); } \
      P0 = MFMA16(CUR[0], Up[0][0], P0); P1 = MFMA16(CUR[0], Up[1][0], P1); \
      P0 = MFMA16(CUR[1], Up[0][1], P0); P1 = MFMA16(CUR[1], Up[1][1], P1); \
      P0 = MFMA16(CUR[2], Up[0][2], P0); P1 = MFMA16(CUR[2], Up[1][2], P1); \
      P0 = MFMA16(CUR[3], Up[0][3], P0); P1 = MFMA16(CUR[3], Up[1][3], P1);
      PH1(15, KA, KB) PH1(14, KB, KA) PH1(13, KA, KB) PH1(12, KB, KA)
      PH1(11, KA, KB) PH1(10, KB, KA) PH1(9, KA, KB) PH1(8, KB, KA)
      const bool ok = m >= 1 && m <= 8;
      const int src = 4*((lane & 48) + ((2*(m-1)) & 15));
      #pragma unroll
      for (int i=0;i<4;i++){
        float a0 = i2f(__builtin_amdgcn_ds_bpermute(src,   f2i(P0[i])));
        float a1 = i2f(__builtin_amdgcn_ds_bpermute(src+4, f2i(P0[i])));
        float a2 = i2f(__builtin_amdgcn_ds_bpermute(src,   f2i(P1[i])));
        float a3 = i2f(__builtin_amdgcn_ds_bpermute(src+4, f2i(P1[i])));
        acc[0][i] = ok ? a0 : 0.f; acc[1][i] = ok ? a1 : 0.f; acc[2][i] = ok ? a2 : 0.f; acc[3][i] = ok ? a3 : 0.f;
      }
    }
    TSP(4);
    bf16x8 U[4][4];
    {
      const char* pp = ub + 272*m + 16*kg + 8*g;
      #pragma unroll
      for (int c=0;c<4;c++){ W6 W = uwin(pp + 64*c); U[0][c] = uodd(W, 0); U[1][c] = uevn(W, 1); U[2][c] = uodd(W, 1); U[3][c] = uevn(W, 2); }
    }
#define PH2(dl, CUR, NXT) \
    if ((dl) > 0){ LDK4(NXT, (dl)-1); WAITK(4, CUR); } else { WAITK(0, CUR); } \
    if ((dl) < 7){ acc[0] = shrN1(acc[0]); acc[1] = shrN1(acc[1]); acc[2] = shrN1(acc[2]); acc[3] = shrN1(acc[3]); } \
    _Pragma("unroll") for (int c=0;c<4;c++){ \
      acc[0] = MFMA16(CUR[c], U[0][c], acc[0]); acc[1] = MFMA16(CUR[c], U[1][c], acc[1]); \
      acc[2] = MFMA16(CUR[c], U[2][c], acc[2]); acc[3] = MFMA16(CUR[c], U[3][c], acc[3]); }
    PH2(7, KA, KB) PH2(6, KB, KA) PH2(5, KA, KB) PH2(4, KB, KA)
    PH2(3, KA, KB) PH2(2, KB, KA) PH2(1, KA, KB) PH2(0, KB, KA)
    TSP(5);
    char* yb = smem + YOFF + (part*4 + ch)*YST + 4*(4*g + 32*kg + 132*m);
    #pragma unroll
    for (int i=0;i<4;i++) *(f32x4*)(yb + 32*i) = f32x4{acc[0][i], acc[1][i], acc[2][i], acc[3][i]};
  }
  __syncthreads();
  TSP(6);
  {
    const char* yb = smem + YOFF + 8*tr + 16*(tr >> 6);
    const char* up = smem + 272*(tr >> 6) + 16 + 4*(tr & 63);
    float o0[4], o1[4];
    if (isA){
      const char* ya = yb + (YAOFF - YOFF);
      const char* yf = smem + YFOFF + (tr >= 257 ? 4*(2*tr - 515) : -16);
      #pragma unroll
      for (int c=0;c<4;c++){
        f32x2 a = *(const f32x2*)(ya + c*YST);
        float b0 = *(const float*)(yf + c*YFST), b1 = *(const float*)(yf + c*YFST + 4);
        uint32_t uu = *(const uint32_t*)(up + c*UST);
        float sd = (c & 1) ? hi16f(dsk[c>>1]) : lo16f(dsk[c>>1]);
        o0[c] = rbf(a.x + b0) + rbf(lo16f(uu)*sd);
        o1[c] = rbf(a.y + b1) + rbf(hi16f(uu)*sd);
      }
    } else {
      u32x4 qq = *(const u32x4*)(smem + QOFF + 16*tr);
      #pragma unroll
      for (int c=0;c<4;c++){
        f32x2 a = *(const f32x2*)(yb + c*YST);
        uint32_t uu = *(const uint32_t*)(up + c*UST);
        float sd = (c & 1) ? hi16f(dsk[c>>1]) : lo16f(dsk[c>>1]);
        float q0 = (c & 1) ? hi16f(qq[c>>1]) : lo16f(qq[c>>1]);
        float q1 = (c & 1) ? hi16f(qq[2+(c>>1)]) : lo16f(qq[2+(c>>1)]);
        o0[c] = q0 * rbf(rbf(a.x) + rbf(lo16f(uu)*sd));
        o1[c] = q1 * rbf(rbf(a.y) + rbf(hi16f(uu)*sd));
      }
    }
    u16* op = out + (isA ? 0 : D_) + c0;
    *(u32x2*)(op + (2*tr)*(2*D_))   = u32x2{pk2(o0[0],o0[1]), pk2(o0[2],o0[3])};
    *(u32x2*)(op + (2*tr+1)*(2*D_)) = u32x2{pk2(o1[0],o1[1]), pk2(o1[2],o1[3])};
  }
}

extern "C" void kernel_launch(void* const* d_in, const int* in_sizes, int n_in,
                              void* d_out, int out_size, void* d_ws, size_t ws_size,
                              hipStream_t stream) {
  mono_kernel<<<256, 1024, LDS_TOTAL, stream>>>((const u16*)d_in[0], (const u16*)d_in[1], (const u16*)d_in[2], (const u16*)d_in[3],
     (const u16*)d_in[4], (const u16*)d_in[5], (const u16*)d_in[6], (const u16*)d_in[7], (const u16*)d_in[8], (const u16*)d_in[9],
     (const u16*)d_in[10], (const u16*)d_in[11], (const u16*)d_in[12], (u16*)d_out
);
}
```

```cpp
#include <hip/hip_runtime.h>
#include <hip/hip_bf16.h>
#include <stdint.h>

typedef unsigned short u16;
typedef short bf16x8 __attribute__((ext_vector_type(8)));
typedef float f32x4 __attribute__((ext_vector_type(4)));
typedef float f32x2 __attribute__((ext_vector_type(2)));
typedef __bf16 bf16x2_t __attribute__((ext_vector_type(2)));

#define L_ 2048
#define D_ 512
#define NS_ 128

__device__ __forceinline__ float bf2f(u16 x){ return __builtin_bit_cast(float, (uint32_t)x << 16); }
__device__ __forceinline__ u16 f2bf(float f){ __hip_bfloat16 h = __float2bfloat16(f); return __builtin_bit_cast(u16, h); }
__device__ __forceinline__ float rbf(float f){ return bf2f(f2bf(f)); }
__device__ __forceinline__ uint32_t pk2(float a, float b){
  f32x2 v = {a,b}; bf16x2_t r = __builtin_convertvector(v, bf16x2_t); return __builtin_bit_cast(uint32_t, r); }
__device__ __forceinline__ float lo16f(uint32_t w){ return __builtin_bit_cast(float, w << 16); }
__device__ __forceinline__ float hi16f(uint32_t w){ return __builtin_bit_cast(float, w & 0xffff0000u); }

struct cpx { float re, im; };
__device__ __forceinline__ float amul(float a, float b){ float r; asm("v_mul_f32 %0, %1, %2" : "=v"(r) : "v"(a), "v"(b)); return r; }
__device__ __forceinline__ float afma(float a, float b, float c){ float r; asm("v_fma_f32 %0, %1, %2, %3" : "=v"(r) : "v"(a), "v"(b), "v"(c)); return r; }
__device__ __forceinline__ float afms(float a, float b, float c){ float r; asm("v_fma_f32 %0, %1, %2, -%3" : "=v"(r) : "v"(a), "v"(b), "v"(c)); return r; }
__device__ __forceinline__ cpx cmul(cpx a, cpx b){
  f32x2 A = {a.re, a.im}, B = {b.re, b.im}, T, R;
  asm("v_pk_mul_f32 %0, %1, %2 op_sel:[1,1] op_sel_hi:[1,0] neg_lo:[1,0]" : "=v"(T) : "v"(A), "v"(B));
  asm("v_pk_fma_f32 %0, %1, %2, %3 op_sel:[0,0,0] op_sel_hi:[0,1,1]" : "=v"(R) : "v"(A), "v"(B), "v"(T));
  cpx r; r.re = R.x; r.im = R.y; return r; }
__device__ __forceinline__ cpx cexp_(float re, float im){ float e = expf(re); float s,c; sincosf(im,&s,&c); cpx r; r.re=e*c; r.im=e*s; return r; }

#define MFMA16(a,b,c) __builtin_amdgcn_mfma_f32_16x16x32_bf16(a,b,c,0,0,0)
typedef uint32_t u32x2 __attribute__((ext_vector_type(2)));
typedef uint32_t u32x4 __attribute__((ext_vector_type(4)));
#define KC_ 656
#define TR 288
#define TLO (57*TR)
#define TSZ (2*TLO)
#define UST 4352
#define KCOFF 17408
#define TOFF 27904
#define KST 4352
#define KS2 1536
#define FTOFF (TOFF + 12288)
#define FCS 320
#define YFOFF (TOFF + 45056 + 16)
#define YFST 6176
#define YAOFF (TOFF + 69888)
#define YOFF (TOFF + 36864)
#define YST 8448
#define QOFF (TOFF + 112128)
#define LDS_TOTAL (TOFF + 4*TSZ)

__device__ __forceinline__ uint32_t resid2(uint32_t h, float a, float b){
  return pk2(a - lo16f(h), b - hi16f(h));
}
__device__ __forceinline__ void put_split(char* p, float a, float b){
  uint32_t h = pk2(a,b);
  uint32_t l = resid2(h, a, b);
  *(uint32_t*)p = h; *(uint32_t*)(p + TLO) = l;
}
__device__ __forceinline__ int f2i(float x){ return __builtin_bit_cast(int, x); }
__device__ __forceinline__ float i2f(int x){ return __builtin_bit_cast(float, x); }
__device__ __forceinline__ f32x4 shrN1(f32x4 x){
  f32x4 r;
  #pragma unroll
  for (int i=0;i<4;i++) r[i] = i2f(__builtin_amdgcn_update_dpp(0, f2i(x[i]), 0x111, 0xf, 0xf, true));
  return r;
}
__device__ __forceinline__ f32x4 shrN2(f32x4 x){
  f32x4 r;
  #pragma unroll
  for (int i=0;i<4;i++) r[i] = i2f(__builtin_amdgcn_update_dpp(0, f2i(x[i]), 0x112, 0xf, 0xf, true));
  return r;
}
struct W6 { uint32_t w[6]; };
__device__ __forceinline__ W6 uwin(const char* p){
  u32x2 a = *(const u32x2*)p, b = *(const u32x2*)(p+8), c = *(const u32x2*)(p+16);
  W6 r; r.w[0]=a.x; r.w[1]=a.y; r.w[2]=b.x; r.w[3]=b.y; r.w[4]=c.x; r.w[5]=c.y; return r;
}
__device__ __forceinline__ bf16x8 usel(const W6& W, int x, uint32_t sel){
  u32x4 r;
  r.x = __builtin_amdgcn_perm(W.w[x+1], W.w[x],   sel); r.y = __builtin_amdgcn_perm(W.w[x+2], W.w[x+1], sel);
  r.z = __builtin_amdgcn_perm(W.w[x+3], W.w[x+2], sel); r.w = __builtin_amdgcn_perm(W.w[x+4], W.w[x+3], sel);
  return __builtin_bit_cast(bf16x8, r);
}
__device__ __forceinline__ bf16x8 uodd(const W6& W, int x){
  u32x4 r;
  r.x = __builtin_amdgcn_alignbit(W.w[x+1], W.w[x],   16); r.y = __builtin_amdgcn_alignbit(W.w[x+2], W.w[x+1], 16);
  r.z = __builtin_amdgcn_alignbit(W.w[x+3], W.w[x+2], 16); r.w = __builtin_amdgcn_alignbit(W.w[x+4], W.w[x+3], 16);
  return __builtin_bit_cast(bf16x8, r);
}
__device__ __forceinline__ bf16x8 uevn(const W6& W, int x){
  u32x4 r = {W.w[x], W.w[x+1], W.w[x+2], W.w[x+3]};
  return __builtin_bit_cast(bf16x8, r);
}
__device__ __forceinline__ void put_u(char* smem, int tr, u32x2 r0, u32x2 r1){
  uint32_t d[4];
  d[0] = __builtin_amdgcn_perm(r1.x, r0.x, 0x05040100); d[1] = __builtin_amdgcn_perm(r1.x, r0.x, 0x07060302);
  d[2] = __builtin_amdgcn_perm(r1.y, r0.y, 0x05040100); d[3] = __builtin_amdgcn_perm(r1.y, r0.y, 0x07060302);
  const int a = tr >> 6, pos = tr & 63;
  char* p = smem + 272*a + 16 + 4*pos;
  #pragma unroll
  for (int c=0;c<4;c++) *(uint32_t*)(p + c*UST) = d[c];
  if (pos >= 60 && a < 15){
    #pragma unroll
    for (int c=0;c<4;c++) *(uint32_t*)(p + c*UST + 16) = d[c];
  }
  if (tr < 16) *(uint32_t*)(smem + (tr>>2)*UST + 4*(tr&3)) = 0u;
}

#define LDK(F, dl, c) asm volatile("ds_read_b128 %0, %1 offset:%2" : "=v"(F) : "v"(kbase), "i"(256*(dl) + 64*(3-(c))))
#define LDK4(S, dl) LDK(S[0], dl, 0); LDK(S[1], dl, 1); LDK(S[2], dl, 2); LDK(S[3], dl, 3)
#define WAITK(n, S) asm volatile("s_waitcnt lgkmcnt(" #n ")" : "+v"(S[0]), "+v"(S[1]), "+v"(S[2]), "+v"(S[3]))

template<int NT, int R0>
__device__ __forceinline__ void near_conv(char* smem, int ch, int m, int kg){
  const uint32_t kbase = (uint32_t)(uintptr_t)smem + TOFF + (2*ch)*KS2 + 16*(m - kg + 3);
#define LDN(F, hs, c) asm volatile("ds_read_b128 %0, %1 offset:%2" : "=v"(F) : "v"(kbase), "i"(((hs)&1)*KS2 + 256*(4-((hs)>>1)) + 64*(3-(c))))
#define LDN4(S, hs) LDN(S[0], hs, 0); LDN(S[1], hs, 1); LDN(S[2], hs, 2); LDN(S[3], hs, 3)
  bf16x8 KA[4], KB[4];
  LDN4(KA, 0);
  bf16x8 U[NT][4];
  {
    const char* pp = smem + ch*UST + 272*m + 16*kg + (R0 ? 8 : 0);
    #pragma unroll
    for (int c=0;c<4;c++){
      W6 W = uwin(pp + 64*c);
      #pragma unroll
      for (int r=0;r<NT;r++){ const int rel = R0 + r + 1 - (R0 ? 4 : 0); U[r][c] = (rel & 1) ? uodd(W, rel >> 1) : uevn(W, rel >> 1); }
    }
  }
  f32x4 acc[NT];
  #pragma unroll
  for (int r=0;r<NT;r++) acc[r] = f32x4{0,0,0,0};
#define NH(hs, CUR, NXT) \
  if ((hs) < 9){ LDN4(NXT, (hs)+1); WAITK(4, CUR); } else { WAITK(0, CUR); } \
  if ((hs) > 0 && !((hs) & 1)){ _Pragma("unroll") for (int r=0;r<NT;r++) acc[r] = shrN1(acc[r]); } \
  _Pragma("unroll") for (int c=0;c<4;c++){ _Pragma("unroll") for (int r=0;r<NT;r++) acc[r] = MFMA16(CUR[c], U[r][c], acc[r]); }
  NH(0, KA, KB) NH(1, KB, KA) NH(2, KA, KB) NH(3, KB, KA) NH(4, KA, KB)
  NH(5, KB, KA) NH(6, KA, KB) NH(7, KB, KA) NH(8, KA, KB) NH(9, KB, KA)
  float* yb = (float*)(smem + YAOFF + ch*YST) + (R0 + 32*kg + 132*m);
  #pragma unroll
  for (int i=0;i<4;i++){
    #pragma unroll
    for (int r=0;r<NT;r++) yb[8*i + r] = acc[r][i];
  }
}
__device__ __forceinline__ bf16x8 band(int sh, int w, int kg){
  const uint32_t by = (((1u << w) - 1u) << sh) >> (8*kg);
  u32x4 r;
  #pragma unroll
  for (int k2=0;k2<4;k2++){ uint32_t x = (by >> (2*k2)) & 3u; x = (x | (x << 15)) & 0x00010001u; r[k2] = x * 0x3f80u; }
  return __builtin_bit_cast(bf16x8, r);
}
__device__ __forceinline__ void split2(const f32x4& a, const f32x4& b, bf16x8& h, bf16x8& l){
  u32x4 hh, ll;
  #pragma unroll
  for (int i=0;i<4;i++){ uint32_t x = pk2(a[i], b[i]); hh[i] = x; ll[i] = resid2(x, a[i], b[i]); }
  h = __builtin_bit_cast(bf16x8, hh); l = __builtin_bit_cast(bf16x8, ll);
}
template<int A0, int A1>
__device__ __forceinline__ void far_conv(char* smem, int ch, int m, int kg){
  const char* ub = smem + ch*UST;
  const f32x4 Z4 = {0,0,0,0};
  const int B0 = 2*m + kg - 2;
  const char* va0 = ub + (B0 < 0 ? 0 : 272*(B0 >> 4) + 16 + 16*(B0 & 15));
  const char* va1 = ub + 272*((B0+32) >> 4) + 16 + 16*((B0+32) & 15);
  const int cp = (3 - m) & 3;
  const char* ft = smem + FTOFF + ch*8192 + cp*FCS + 4*(47 - m + 4*kg - cp);
  char* yf = smem + YFOFF + ch*YFST + 64*m + 16*kg;
  f32x4 acc[6];
  #pragma unroll
  for (int a=0;a<6;a++) acc[a] = Z4;
  {
    bf16x8 M[4];
    M[0] = band(14 + m, 3, kg); M[1] = band(9 + m, 5, kg); M[2] = band(6 + m, 3, kg); M[3] = band(1 + m, 5, kg);
    bf16x8 Ch[3][2], Cl[3][2];
    #pragma unroll
    for (int dl=0;dl<3;dl++){
      #pragma unroll
      for (int h=0;h<2;h++){ Ch[dl][h] = *(const bf16x8*)(ft + h*(8*FCS) - 64*dl); Cl[dl][h] = *(const bf16x8*)(ft + h*(8*FCS) + 4*FCS - 64*dl); }
    }
    #pragma unroll
    for (int b=(A0 > 2 ? A0-2 : 0);b<A1;b++){
      const bf16x8 V = *(const bf16x8*)(b ? va1 + 544*(b-1) : va0);
      f32x4 F[4];
      #pragma unroll
      for (int p=0;p<4;p++) F[p] = MFMA16(V, M[p], Z4);
      #pragma unroll
      for (int h=0;h<2;h++){
        bf16x8 Bh, Bl;
        split2(F[2*h], F[2*h+1], Bh, Bl);
        #pragma unroll
        for (int dl=0;dl<3;dl++) if (b + dl >= A0 && b + dl < A1) acc[b+dl] = MFMA16(Bh, Ch[dl][h], acc[b+dl]);
        #pragma unroll
        for (int dl=0;dl<3;dl++) if (b + dl >= A0 && b + dl < A1) acc[b+dl] = MFMA16(Bh, Cl[dl][h], acc[b+dl]);
        #pragma unroll
        for (int dl=0;dl<3;dl++) if (b + dl >= A0 && b + dl < A1) acc[b+dl] = MFMA16(Bl, Ch[dl][h], acc[b+dl]);
      }
      if (b < 2 && b >= A0) *(f32x4*)(yf + 1024*b) = acc[b];
    }
  }
  {
    const char* vb0 = ub + (B0 < 1 ? 0 : 272*((B0-1) >> 4) + 16 + 16*((B0-1) & 15));
    const char* vb1 = ub + 272*((B0+31) >> 4) + 16 + 16*((B0+31) & 15);
    const char* f8 = smem + FTOFF + ch*8192 + 16*FCS + cp*FCS + 4*(63 - m + 4*kg - cp);
    const bf16x8 Ma = band(8 + m, 7, kg), Mb = band(7 + m, 9, kg), Mc = band(15 + m, 2, kg);
    const float k1024 = ((const float*)(smem + KCOFF))[ch*KC_ + 512];
    bf16x8 Ch[4], Cl[4];
    #pragma unroll
    for (int dl=0;dl<A1-2;dl++){ Ch[dl] = *(const bf16x8*)(f8 - 64*dl); Cl[dl] = *(const bf16x8*)(f8 + 4*FCS - 64*dl); }
    #pragma unroll
    for (int b=0;b<A1-2;b++){
      const bf16x8 V = *(const bf16x8*)(b ? va1 + 544*(b-1) : va0);
      const bf16x8 V2 = *(const bf16x8*)(b ? vb1 + 544*(b-1) : vb0);
      const f32x4 Fa = MFMA16(V, Ma, Z4), Fb = MFMA16(V2, Mb, Z4);
      bf16x8 Bh, Bl;
      split2(Fa, Fb, Bh, Bl);
      if (b + 2 >= A0){
        const f32x4 Fc = MFMA16(Mc, V, Z4);
        #pragma unroll
        for (int i=0;i<4;i++) acc[b+2][i] = __builtin_fmaf(k1024, Fc[i], acc[b+2][i]);
      }
      #pragma unroll
      for (int dl=0;dl<4;dl++) if (b + 2 + dl >= A0 && b + 2 + dl < A1) acc[b+2+dl] = MFMA16(Bh, Ch[dl], acc[b+2+dl]);
      #pragma unroll
      for (int dl=0;dl<4;dl++) if (b + 2 + dl >= A0 && b + 2 + dl < A1) acc[b+2+dl] = MFMA16(Bh, Cl[dl], acc[b+2+dl]);
      #pragma unroll
      for (int dl=0;dl<4;dl++) if (b + 2 + dl >= A0 && b + 2 + dl < A1) acc[b+2+dl] = MFMA16(Bl, Ch[dl], acc[b+2+dl]);
    }
  }
  #pragma unroll
  for (int a=(A0 > 2 ? A0 : 2);a<A1;a++) *(f32x4*)(yf + 1024*a) = acc[a];
}

__global__ __launch_bounds__(1024) void mono_kernel(const u16* __restrict__ u, const u16* __restrict__ logA, const u16* __restrict__ Aim,
    const u16* __restrict__ Cre, const u16* __restrict__ Cim, const u16* __restrict__ logdt, const u16* __restrict__ Dskip,
    const u16* __restrict__ q, const u16* __restrict__ k, const u16* __restrict__ v, const u16* __restrict__ shift_k,
    const u16* __restrict__ ssmk, const u16* __restrict__ ssmD, u16* __restrict__ out
){
  unsigned long long tsv[10], tsc[10];
#define TSP(i) tsv[i] = __builtin_amdgcn_s_memrealtime(); tsc[i] = __builtin_amdgcn_s_memtime()
  TSP(0); TSP(1); TSP(2); TSP(3); TSP(4); TSP(5);
  extern __shared__ __attribute__((aligned(16))) char smem[];
  const int bid = blockIdx.x, t = threadIdx.x;
  const int w5 = bid >> 3;
  const bool isA = w5 < 16;
  const int c0 = (bid & 7)*64 + (w5 & 15)*4;
  const int tr = (t + 64*(w5 & 15)) & 1023;
  const int wave = t >> 6, lane = t & 63, m = lane & 15, kg = lane >> 4;
  const u32x2 dsk = *(const u32x2*)((isA ? Dskip : ssmD) + c0);
  if (isA){
    const int ch = t >> 8, tg = t & 255, wv = (wave + ch) & 3;
    const int d = c0 + ch;
    char* tb = smem + TOFF + ch*TSZ;
    char* eb_ = smem + KCOFF + ch*(4*KC_);
    const u16 ldt = logdt[d];
    const u16 pla = logA[d*NS_+(tg & 127)], pai = Aim[d*NS_+(tg & 127)];
    u16 pc[2][2];
    #pragma unroll
    for (int hp=0;hp<2;hp++){ pc[hp][0] = Cre[d*NS_+64*hp+lane];  pc[hp][1] = Cim[d*NS_+64*hp+lane]; }
    const float dt = rbf(expf(bf2f(ldt)));
    {
      const float zre = -rbf(expf(bf2f(pla))) * dt;
      const float zim = bf2f(pai) * dt;
      const float f = (tg >> 7) ? 128.f : 1.f;
      cpx e = cexp_(zre*f, zim*f);
      *(float2*)(eb_ + 8*tg) = make_float2(e.re, e.im);
    }
    __syncthreads();
    TSP(1);
    __builtin_amdgcn_sched_barrier(0);
    const u32x2 u0 = *(const u32x2*)(u + (2*tr)*D_ + c0);
    const u32x2 u1 = *(const u32x2*)(u + (2*tr+1)*D_ + c0);
    __builtin_amdgcn_sched_barrier(0);
    f32x4 a4 = {0,0,0,0};
    char* col = tb + 4*lane;
    #pragma unroll 1
    for (int hp=0;hp<2;hp++){
      cpx C; C.re = bf2f(hp ? pc[1][0] : pc[0][0])*dt; C.im = bf2f(hp ? pc[1][1] : pc[0][1])*dt;
      cpx e1, e2;
      { float2 a = *(const float2*)(eb_ + 8*(64*hp+lane)); e2.re = a.x; e2.im = a.y;
        float2 c = *(const float2*)(eb_ + 1024 + 8*(64*hp+lane)); e1.re = c.x; e1.im = c.y; }
      if (wv == 0){
        cpx e8 = cmul(e2, e2); e8 = cmul(e8, e8); e8 = cmul(e8, e8);
        cpx xx; xx.re = 1.f; xx.im = 0.f;
        #pragma unroll
        for (int i=0;i<16;i++){ put_split(col + (41+i)*TR, xx.re, -xx.im); if (i < 15) xx = (i == 0) ? e8 : cmul(xx, e8); }
      } else if (wv == 1){
        cpx eb = cmul(e1, e1); eb = cmul(eb, eb); eb = cmul(eb, eb);
        cpx cb = cmul(C, eb);
        #pragma unroll
        for (int i=0;i<9;i++){ put_split(col + (32+i)*TR, cb.re, cb.im); if (i < 8) cb = cmul(cb, e1); }
      } else if (wv == 2){
        cpx S[8];
        S[1] = e2;
        #pragma unroll
        for (int i=2;i<8;i++) S[i] = cmul(S[i-1], S[1]);
        cpx cb = C;
        #pragma unroll
        for (int a=0;a<2;a++){
          put_split(col + (8*a)*TR, cb.re, cb.im);
          #pragma unroll
          for (int p=1;p<8;p++){ cpx g = cmul(cb, S[p]); put_split(col + (8*a+p)*TR, g.re, g.im); }
          if (a == 0) cb = cmul(cb, e1);
        }
      } else {
        cpx S2 = cmul(e2, e2), S4 = cmul(S2, S2), S6 = cmul(S4, S2);
        cpx cb = cmul(C, cmul(e1, e1));
        #pragma unroll
        for (int a=0;a<2;a++){
          put_split(col + (16+4*a)*TR, cb.re, cb.im);
          { cpx g = cmul(cb, S2); put_split(col + (16+4*a+1)*TR, g.re, g.im); }
          { cpx g = cmul(cb, S4); put_split(col + (16+4*a+2)*TR, g.re, g.im); }
          { cpx g = cmul(cb, S6); put_split(col + (16+4*a+3)*TR, g.re, g.im); }
          cb = cmul(cb, e1);
        }
        #pragma unroll
        for (int a=0;a<4;a++){
          put_split(col + (24+2*a)*TR, cb.re, cb.im);
          cpx g = cmul(cb, S4); put_split(col + (24+2*a+1)*TR, g.re, g.im);
          if (a < 3) cb = cmul(cb, e1);
        }
      }
      __syncthreads();
      if (wv < 3){
        const char* ga = tb + (16*wv + m)*TR + 16*kg;
        const char* xb = tb + (41 + m)*TR + 16*kg;
        #pragma unroll
        for (int kc=0;kc<4;kc++){
          bf16x8 ah = *(const bf16x8*)(ga + 64*kc), al = *(const bf16x8*)(ga + 64*kc + TLO);
          bf16x8 bh = *(const bf16x8*)(xb + 64*kc), bl = *(const bf16x8*)(xb + 64*kc + TLO);
          a4 = MFMA16(ah, bh, a4);
          a4 = MFMA16(ah, bl, a4);
          a4 = MFMA16(al, bh, a4);
        }
      }
      if (hp == 0) __syncthreads();
    }
    float* Kc = (float*)(smem + KCOFF) + ch*KC_;
    if (wv < 3){
      #pragma unroll
      for (int i=0;i<4;i++){
        int r = 16*wv + 4*kg + i;
        int idx;
        if (r < 16) idx = 128*(r>>3) + (r&7) + 8*m;
        else if (r < 24) idx = 256 + 64*((r-16)>>2) + (r&3) + 4*m;
        else if (r < 32) idx = 384 + 32*((r-24)>>1) + (r&1) + 2*m;
        else idx = 512 + 16*(r-32) + m;
        if (r < 40 || (r == 40 && m == 0)) Kc[idx] = 2.f*a4[i];
      }
    }
    put_u(smem, tr, u0, u1);
    __syncthreads();
    TSP(2);
    {
      char* ft = smem + FTOFF + ch*8192;
      if (tg < 208){
        int z, idx; bool ok; char* wp;
        if (tg < 128){ z = tg & 63; const int Dd = 47 - z; ok = Dd >= 0 && Dd < 32; idx = 385 + 4*Dd + 2*(tg >> 6); wp = ft + (tg >> 6)*(8*FCS); }
        else { z = tg - 128; const int Dd = 63 - z; ok = Dd >= 0; idx = 513 + 2*Dd; wp = ft + 16*FCS; }
        float a = 0.f, b = 0.f;
        if (ok){ a = Kc[idx]; b = Kc[idx+1]; }
        const uint32_t hh = pk2(a, b), ll = pk2(a - lo16f(hh), b - hi16f(hh));
        #pragma unroll
        for (int c=0;c<4;c++) if (z >= c){ *(uint32_t*)(wp + c*FCS + 4*(z-c)) = hh; *(uint32_t*)(wp + (4+c)*FCS + 4*(z-c)) = ll; }
      }
      if (tg >= 161){
        const int sl = tg - 161, j = sl - 15;
        float kv[8];
        #pragma unroll
        for (int i=0;i<8;i++){
          int l = 8*j + 7 - i;
          int pr = (int)rbf((float)l);
          int p = pr < 256 ? pr : 128 + (pr>>1);
          kv[i] = (l >= 0 && l <= 514) ? Kc[p] : 0.f;
        }
        u32x4 h, lo;
        #pragma unroll
        for (int jj=0;jj<4;jj++){
          uint32_t hh = pk2(kv[2*jj], kv[2*jj+1]);
          h[jj] = hh; lo[jj] = pk2(kv[2*jj] - lo16f(hh), kv[2*jj+1] - hi16f(hh));
        }
        char* kb = smem + TOFF + (2*ch)*KS2;
        *(u32x4*)(kb + 16*sl) = h;
        *(u32x4*)(kb + KS2 + 16*sl) = lo;
      }
      if (tg < 4) *(float*)(smem + YFOFF + ch*YFST - 16 + 4*tg) = 0.f;
    }
    __syncthreads();
    TSP(3);
  } else {
    const u32x2 k0 = *(const u32x2*)(k + (2*tr)*D_ + c0);
    const u32x2 k1 = *(const u32x2*)(k + (2*tr+1)*D_ + c0);
    const u32x4 wa = *(const u32x4*)(shift_k + c0*4), wb = *(const u32x4*)(shift_k + c0*4 + 8);
    char* ks = smem + YOFF;
    __builtin_amdgcn_sched_barrier(0);
    *(u32x4*)(ks + 32 + 16*tr) = u32x4{k0.x, k0.y, k1.x, k1.y};
    __builtin_amdgcn_sched_barrier(0);
    const u32x2 v0 = *(const u32x2*)(v + (2*tr)*D_ + c0);
    const u32x2 v1 = *(const u32x2*)(v + (2*tr+1)*D_ + c0);
    const u32x4 sx = *(const u32x4*)(ssmk + (c0+(t >> 8))*L_ + 8*(t & 255));
    __builtin_amdgcn_sched_barrier(0);
    if (t < 2) *(u32x4*)(ks + 16*t) = u32x4{0,0,0,0};
    __syncthreads();
    TSP(1);
    u32x2 kk[5];
    kk[0] = *(const u32x2*)(ks + 8 + 16*tr);
    { u32x4 x = *(const u32x4*)(ks + 16 + 16*tr); kk[1] = u32x2{x.x, x.y}; kk[2] = u32x2{x.z, x.w}; }
    kk[3] = k0; kk[4] = k1;
    u32x2 o[2];
    #pragma unroll
    for (int j=0;j<2;j++){
      const u32x2 vv = j ? v1 : v0;
      #pragma unroll
      for (int p=0;p<2;p++){
        float r2[2];
        #pragma unroll
        for (int e=0;e<2;e++){
          const u32x4 ww = p ? wb : wa;
          const uint32_t wl = e ? ww.z : ww.x, wh = e ? ww.w : ww.y;
          const float w0 = lo16f(wl), w1 = hi16f(wl), w2 = lo16f(wh), w3 = hi16f(wh);
          auto g = [&](int idx){ uint32_t x = kk[idx][p]; return e? hi16f(x) : lo16f(x); };
          float acc = rbf(g(j)*w3);
          acc = rbf(acc + rbf(g(j+1)*w2));
          acc = rbf(acc + rbf(g(j+2)*w1));
          acc = rbf(acc + rbf(g(j+3)*w0));
          r2[e] = acc * (e? hi16f(vv[p]) : lo16f(vv[p]));
        }
        o[j][p] = pk2(r2[0], r2[1]);
      }
    }
    put_u(smem, tr, o[0], o[1]);
    {
      const int ch = t >> 8, j = t & 255;
      u32x4 r;
      r.x = __builtin_amdgcn_alignbit(sx.w, sx.w, 16); r.y = __builtin_amdgcn_alignbit(sx.z, sx.z, 16);
      r.z = __builtin_amdgcn_alignbit(sx.y, sx.y, 16); r.w = __builtin_amdgcn_alignbit(sx.x, sx.x, 16);
      char* kb = smem + TOFF + ch*KST;
      *(u32x4*)(kb + 16*(j+15)) = r;
      if (j < 15) *(u32x4*)(kb + 16*j) = u32x4{0,0,0,0};
    }
    __syncthreads();
    TSP(2); TSP(3);
    if (wave >= 8){
      const int r4 = (((t - 512) + 32*(w5 & 15)) & 511)*4;
      u32x2 a[4];
      #pragma unroll
      for (int i=0;i<4;i++) a[i] = *(const u32x2*)(q + (r4+i)*D_ + c0);
      *(u32x4*)(smem + QOFF + 8*r4)      = u32x4{a[0].x, a[0].y, a[1].x, a[1].y};
      *(u32x4*)(smem + QOFF + 8*r4 + 16) = u32x4{a[2].x, a[2].y, a[3].x, a[3].y};
    }
  }
  if (isA){
    const int ch = wave >> 2, role = (wave + ch) & 3;
    if (role == 0) near_conv<4,0>(smem, ch, m, kg);
    else if (role == 1) near_conv<4,4>(smem, ch, m, kg);
    else if (role == 2){ __builtin_amdgcn_s_setprio(3); far_conv<0,4>(smem, ch, m, kg); __builtin_amdgcn_s_setprio(0); }
    else { __builtin_amdgcn_s_setprio(3); far_conv<4,6>(smem, ch, m, kg); __builtin_amdgcn_s_setprio(0); }
    TSP(5);
  } else if (wave < 8){
    const int ch = wave >> 1;
    const int g = wave & 1;
    const int part = 0;
    const int unit = ch;
    const uint32_t kbase = (uint32_t)(uintptr_t)smem + TOFF + unit*KST + 16*(m - kg + 3);
    const char* ub = smem + ch*UST;
    bf16x8 KA[4], KB[4];
    LDK4(KA, 15);
    f32x4 acc[4];
    {
      bf16x8 Up[2][4];
      {
        const uint32_t sel = (m & 1) ? 0x07060504u : 0x05040302u;
        const char* pp = ub + 272*(m >> 1) + 16*kg + 8*g;
        #pragma unroll
        for (int c=0;c<4;c++){ W6 W = uwin(pp + 64*c); Up[0][c] = usel(W, 0, sel); Up[1][c] = usel(W, 1, sel); }
      }
      f32x4 P0 = {0,0,0,0}, P1 = {0,0,0,0};
#define PH1(dl, CUR, NXT) \
      LDK4(NXT, (dl)-1); WAITK(4, CUR); \
      if ((dl) < 15){ P0 = shrN2(P0); P1 = shrN2(P1); } \
      P0 = MFMA16(CUR[0], Up[0][0], P0); P1 = MFMA16(CUR[0], Up[1][0], P1); \
      P0 = MFMA16(CUR[1], Up[0][1], P0); P1 = MFMA16(CUR[1], Up[1][1], P1); \
      P0 = MFMA16(CUR[2], Up[0][2], P0); P1 = MFMA16(CUR[2], Up[1][2], P1); \
      P0 = MFMA16(CUR[3], Up[0][3], P0); P1 = MFMA16(CUR[3], Up[1][3], P1);
      PH1(15, KA, KB) PH1(14, KB, KA) PH1(13, KA, KB) PH1(12, KB, KA)
      PH1(11, KA, KB) PH1(10, KB, KA) PH1(9, KA, KB) PH1(8, KB, KA)
      const bool ok = m >= 1 && m <= 8;
      const int src = 4*((lane & 48) + ((2*(m-1)) & 15));
      #pragma unroll
      for (int i=0;i<4;i++){
        float a0 = i2f(__builtin_amdgcn_ds_bpermute(src,   f2i(P0[i])));
        float a1 = i2f(__builtin_amdgcn_ds_bpermute(src+4, f2i(P0[i])));
        float a2 = i2f(__builtin_amdgcn_ds_bpermute(src,   f2i(P1[i])));
        float a3 = i2f(__builtin_amdgcn_ds_bpermute(src+4, f2i(P1[i])));
        acc[0][i] = ok ? a0 : 0.f; acc[1][i] = ok ? a1 : 0.f; acc[2][i] = ok ? a2 : 0.f; acc[3][i] = ok ? a3 : 0.f;
      }
    }
    TSP(4);
    bf16x8 U[4][4];
    {
      const char* pp = ub + 272*m + 16*kg + 8*g;
      #pragma unroll
      for (int c=0;c<4;c++){ W6 W = uwin(pp + 64*c); U[0][c] = uodd(W, 0); U[1][c] = uevn(W, 1); U[2][c] = uodd(W, 1); U[3][c] = uevn(W, 2); }
    }
#define PH2(dl, CUR, NXT) \
    if ((dl) > 0){ LDK4(NXT, (dl)-1); WAITK(4, CUR); } else { WAITK(0, CUR); } \
    if ((dl) < 7){ acc[0] = shrN1(acc[0]); acc[1] = shrN1(acc[1]); acc[2] = shrN1(acc[2]); acc[3] = shrN1(acc[3]); } \
    _Pragma("unroll") for (int c=0;c<4;c++){ \
      acc[0] = MFMA16(CUR[c], U[0][c], acc[0]); acc[1] = MFMA16(CUR[c], U[1][c], acc[1]); \
      acc[2] = MFMA16(CUR[c], U[2][c], acc[2]); acc[3] = MFMA16(CUR[c], U[3][c], acc[3]); }
    PH2(7, KA, KB) PH2(6, KB, KA) PH2(5, KA, KB) PH2(4, KB, KA)
    PH2(3, KA, KB) PH2(2, KB, KA) PH2(1, KA, KB) PH2(0, KB, KA)
    TSP(5);
    char* yb = smem + YOFF + (part*4 + ch)*YST + 4*(4*g + 32*kg + 132*m);
    #pragma unroll
    for (int i=0;i<4;i++) *(f32x4*)(yb + 32*i) = f32x4{acc[0][i], acc[1][i], acc[2][i], acc[3][i]};
  }
  __syncthreads();
  TSP(6);
  {
    const char* yb = smem + YOFF + 8*tr + 16*(tr >> 6);
    const char* up = smem + 272*(tr >> 6) + 16 + 4*(tr & 63);
    float o0[4], o1[4];
    if (isA){
      const char* ya = yb + (YAOFF - YOFF);
      const char* yf = smem + YFOFF + (tr >= 257 ? 4*(2*tr - 515) : -16);
      #pragma unroll
      for (int c=0;c<4;c++){
        f32x2 a = *(const f32x2*)(ya + c*YST);
        float b0 = *(const float*)(yf + c*YFST), b1 = *(const float*)(yf + c*YFST + 4);
        uint32_t uu = *(const uint32_t*)(up + c*UST);
        float sd = (c & 1) ? hi16f(dsk[c>>1]) : lo16f(dsk[c>>1]);
        o0[c] = rbf(a.x + b0) + rbf(lo16f(uu)*sd);
        o1[c] = rbf(a.y + b1) + rbf(hi16f(uu)*sd);
      }
    } else {
      u32x4 qq = *(const u32x4*)(smem + QOFF + 16*tr);
      #pragma unroll
      for (int c=0;c<4;c++){
        f32x2 a = *(const f32x2*)(yb + c*YST);
        uint32_t uu = *(const uint32_t*)(up + c*UST);
        float sd = (c & 1) ? hi16f(dsk[c>>1]) : lo16f(dsk[c>>1]);
        float q0 = (c & 1) ? hi16f(qq[c>>1]) : lo16f(qq[c>>1]);
        float q1 = (c & 1) ? hi16f(qq[2+(c>>1)]) : lo16f(qq[2+(c>>1)]);
        o0[c] = q0 * rbf(rbf(a.x) + rbf(lo16f(uu)*sd));
        o1[c] = q1 * rbf(rbf(a.y) + rbf(hi16f(uu)*sd));
      }
    }
    u16* op = out + (isA ? 0 : D_) + c0;
    *(u32x2*)(op + (2*tr)*(2*D_))   = u32x2{pk2(o0[0],o0[1]), pk2(o0[2],o0[3])};
    *(u32x2*)(op + (2*tr+1)*(2*D_)) = u32x2{pk2(o1[0],o1[1]), pk2(o1[2],o1[3])};
  }
  TSP(7);
  if (!isA){
    unsigned long long dlt = (tsv[7]-tsv[6])*8;
    unsigned long long tgt = tsv[7] + dlt;
    while (__builtin_amdgcn_s_memrealtime() < tgt) __builtin_amdgcn_s_sleep(1);
  }
}

extern "C" void kernel_launch(void* const* d_in, const int* in_sizes, int n_in,
                              void* d_out, int out_size, void* d_ws, size_t ws_size,
                              hipStream_t stream) {
  mono_kernel<<<256, 1024, LDS_TOTAL, stream>>>((const u16*)d_in[0], (const u16*)d_in[1], (const u16*)d_in[2], (const u16*)d_in[3],
     (const u16*)d_in[4], (const u16*)d_in[5], (const u16*)d_in[6], (const u16*)d_in[7], (const u16*)d_in[8], (const u16*)d_in[9],
     (const u16*)d_in[10], (const u16*)d_in[11], (const u16*)d_in[12], (u16*)d_out
);
}
```

```cpp
#include <hip/hip_runtime.h>
#include <hip/hip_bf16.h>
#include <stdint.h>

typedef unsigned short u16;
typedef short bf16x8 __attribute__((ext_vector_type(8)));
typedef float f32x4 __attribute__((ext_vector_type(4)));
typedef float f32x2 __attribute__((ext_vector_type(2)));
typedef __bf16 bf16x2_t __attribute__((ext_vector_type(2)));

#define L_ 2048
#define D_ 512
#define NS_ 128

__device__ __forceinline__ float bf2f(u16 x){ return __builtin_bit_cast(float, (uint32_t)x << 16); }
__device__ __forceinline__ u16 f2bf(float f){ __hip_bfloat16 h = __float2bfloat16(f); return __builtin_bit_cast(u16, h); }
__device__ __forceinline__ float rbf(float f){ return bf2f(f2bf(f)); }
__device__ __forceinline__ uint32_t pk2(float a, float b){
  f32x2 v = {a,b}; bf16x2_t r = __builtin_convertvector(v, bf16x2_t); return __builtin_bit_cast(uint32_t, r); }
__device__ __forceinline__ float lo16f(uint32_t w){ return __builtin_bit_cast(float, w << 16); }
__device__ __forceinline__ float hi16f(uint32_t w){ return __builtin_bit_cast(float, w & 0xffff0000u); }

struct cpx { float re, im; };
__device__ __forceinline__ float amul(float a, float b){ float r; asm("v_mul_f32 %0, %1, %2" : "=v"(r) : "v"(a), "v"(b)); return r; }
__device__ __forceinline__ float afma(float a, float b, float c){ float r; asm("v_fma_f32 %0, %1, %2, %3" : "=v"(r) : "v"(a), "v"(b), "v"(c)); return r; }
__device__ __forceinline__ float afms(float a, float b, float c){ float r; asm("v_fma_f32 %0, %1, %2, -%3" : "=v"(r) : "v"(a), "v"(b), "v"(c)); return r; }
__device__ __forceinline__ cpx cmul(cpx a, cpx b){
  f32x2 A = {a.re, a.im}, B = {b.re, b.im}, T, R;
  asm("v_pk_mul_f32 %0, %1, %2 op_sel:[1,1] op_sel_hi:[1,0] neg_lo:[1,0]" : "=v"(T) : "v"(A), "v"(B));
  asm("v_pk_fma_f32 %0, %1, %2, %3 op_sel:[0,0,0] op_sel_hi:[0,1,1]" : "=v"(R) : "v"(A), "v"(B), "v"(T));
  cpx r; r.re = R.x; r.im = R.y; return r; }
__device__ __forceinline__ cpx cexp_(float re, float im){ float e = expf(re); float s,c; sincosf(im,&s,&c); cpx r; r.re=e*c; r.im=e*s; return r; }

#define MFMA16(a,b,c) __builtin_amdgcn_mfma_f32_16x16x32_bf16(a,b,c,0,0,0)
typedef uint32_t u32x2 __attribute__((ext_vector_type(2)));
typedef uint32_t u32x4 __attribute__((ext_vector_type(4)));
#define KC_ 656
#define TR 288
#define TLO (57*TR)
#define TSZ (2*TLO)
#define UST 4352
#define KCOFF 17408
#define TOFF 27904
#define KST 4352
#define KS2 1536
#define FTOFF (TOFF + 12288)
#define FCS 320
#define YFOFF (TOFF + 45056 + 16)
#define YFST 6176
#define YAOFF (TOFF + 69888)
#define YOFF (TOFF + 36864)
#define YST 8448
#define QOFF (TOFF + 112128)
#define LDS_TOTAL (TOFF + 4*TSZ)

__device__ __forceinline__ uint32_t resid2(uint32_t h, float a, float b){
  return pk2(a - lo16f(h), b - hi16f(h));
}
__device__ __forceinline__ void put_split(char* p, float a, float b){
  uint32_t h = pk2(a,b);
  uint32_t l = resid2(h, a, b);
  *(uint32_t*)p = h; *(uint32_t*)(p + TLO) = l;
}
__device__ __forceinline__ int f2i(float x){ return __builtin_bit_cast(int, x); }
__device__ __forceinline__ float i2f(int x){ return __builtin_bit_cast(float, x); }
__device__ __forceinline__ f32x4 shrN1(f32x4 x){
  f32x4 r;
  #pragma unroll
  for (int i=0;i<4;i++) r[i] = i2f(__builtin_amdgcn_update_dpp(0, f2i(x[i]), 0x111, 0xf, 0xf, true));
  return r;
}
__device__ __forceinline__ f32x4 shrN2(f32x4 x){
  f32x4 r;
  #pragma unroll
  for (int i=0;i<4;i++) r[i] = i2f(__builtin_amdgcn_update_dpp(0, f2i(x[i]), 0x112, 0xf, 0xf, true));
  return r;
}
struct W6 { uint32_t w[6]; };
__device__ __forceinline__ W6 uwin(const char* p){
  u32x2 a = *(const u32x2*)p, b = *(const u32x2*)(p+8), c = *(const u32x2*)(p+16);
  W6 r; r.w[0]=a.x; r.w[1]=a.y; r.w[2]=b.x; r.w[3]=b.y; r.w[4]=c.x; r.w[5]=c.y; return r;
}
__device__ __forceinline__ bf16x8 usel(const W6& W, int x, uint32_t sel){
  u32x4 r;
  r.x = __builtin_amdgcn_perm(W.w[x+1], W.w[x],   sel); r.y = __builtin_amdgcn_perm(W.w[x+2], W.w[x+1], sel);
  r.z = __builtin_amdgcn_perm(W.w[x+3], W.w[x+2], sel); r.w = __builtin_amdgcn_perm(W.w[x+4], W.w[x+3], sel);
  return __builtin_bit_cast(bf16x8, r);
}
__device__ __forceinline__ bf16x8 uodd(const W6& W, int x){
  u32x4 r;
  r.x = __builtin_amdgcn_alignbit(W.w[x+1], W.w[x],   16); r.y = __builtin_amdgcn_alignbit(W.w[x+2], W.w[x+1], 16);
  r.z = __builtin_amdgcn_alignbit(W.w[x+3], W.w[x+2], 16); r.w = __builtin_amdgcn_alignbit(W.w[x+4], W.w[x+3], 16);
  return __builtin_bit_cast(bf16x8, r);
}
__device__ __forceinline__ bf16x8 uevn(const W6& W, int x){
  u32x4 r = {W.w[x], W.w[x+1], W.w[x+2], W.w[x+3]};
  return __builtin_bit_cast(bf16x8, r);
}
__device__ __forceinline__ void put_u(char* smem, int tr, u32x2 r0, u32x2 r1){
  uint32_t d[4];
  d[0] = __builtin_amdgcn_perm(r1.x, r0.x, 0x05040100); d[1] = __builtin_amdgcn_perm(r1.x, r0.x, 0x07060302);
  d[2] = __builtin_amdgcn_perm(r1.y, r0.y, 0x05040100); d[3] = __builtin_amdgcn_perm(r1.y, r0.y, 0x07060302);
  const int a = tr >> 6, pos = tr & 63;
  char* p = smem + 272*a + 16 + 4*pos;
  #pragma unroll
  for (int c=0;c<4;c++) *(uint32_t*)(p + c*UST) = d[c];
  if (pos >= 60 && a < 15){
    #pragma unroll
    for (int c=0;c<4;c++) *(uint32_t*)(p + c*UST + 16) = d[c];
  }
  if (tr < 16) *(uint32_t*)(smem + (tr>>2)*UST + 4*(tr&3)) = 0u;
}

#define LDK(F, dl, c) asm volatile("ds_read_b128 %0, %1 offset:%2" : "=v"(F) : "v"(kbase), "i"(256*(dl) + 64*(3-(c))))
#define LDK4(S, dl) LDK(S[0], dl, 0); LDK(S[1], dl, 1); LDK(S[2], dl, 2); LDK(S[3], dl, 3)
#define WAITK(n, S) asm volatile("s_waitcnt lgkmcnt(" #n ")" : "+v"(S[0]), "+v"(S[1]), "+v"(S[2]), "+v"(S[3]))

template<int NT, int R0>
__device__ __forceinline__ void near_conv(char* smem, int ch, int m, int kg){
  const uint32_t kbase = (uint32_t)(uintptr_t)smem + TOFF + (2*ch)*KS2 + 16*(m - kg + 3);
#define LDN(F, hs, c) asm volatile("ds_read_b128 %0, %1 offset:%2" : "=v"(F) : "v"(kbase), "i"(((hs)&1)*KS2 + 256*(4-((hs)>>1)) + 64*(3-(c))))
#define LDN4(S, hs) LDN(S[0], hs, 0); LDN(S[1], hs, 1); LDN(S[2], hs, 2); LDN(S[3], hs, 3)
  bf16x8 KA[4], KB[4];
  LDN4(KA, 0);
  bf16x8 U[NT][4];
  {
    const char* pp = smem + ch*UST + 272*m + 16*kg + (R0 ? 8 : 0);
    #pragma unroll
    for (int c=0;c<4;c++){
      W6 W = uwin(pp + 64*c);
      #pragma unroll
      for (int r=0;r<NT;r++){ const int rel = R0 + r + 1 - (R0 ? 4 : 0); U[r][c] = (rel & 1) ? uodd(W, rel >> 1) : uevn(W, rel >> 1); }
    }
  }
  f32x4 acc[NT];
  #pragma unroll
  for (int r=0;r<NT;r++) acc[r] = f32x4{0,0,0,0};
#define NH(hs, CUR, NXT) \
  if ((hs) < 9){ LDN4(NXT, (hs)+1); WAITK(4, CUR); } else { WAITK(0, CUR); } \
  if ((hs) > 0 && !((hs) & 1)){ _Pragma("unroll") for (int r=0;r<NT;r++) acc[r] = shrN1(acc[r]); } \
  _Pragma("unroll") for (int c=0;c<4;c++){ _Pragma("unroll") for (int r=0;r<NT;r++) acc[r] = MFMA16(CUR[c], U[r][c], acc[r]); }
  NH(0, KA, KB) NH(1, KB, KA) NH(2, KA, KB) NH(3, KB, KA) NH(4, KA, KB)
  NH(5, KB, KA) NH(6, KA, KB) NH(7, KB, KA) NH(8, KA, KB) NH(9, KB, KA)
  float* yb = (float*)(smem + YAOFF + ch*YST) + (R0 + 32*kg + 132*m);
  #pragma unroll
  for (int i=0;i<4;i++){
    #pragma unroll
    for (int r=0;r<NT;r++) yb[8*i + r] = acc[r][i];
  }
}
__device__ __forceinline__ bf16x8 band(int sh, int w, int kg){
  const uint32_t by = (((1u << w) - 1u) << sh) >> (8*kg);
  u32x4 r;
  #pragma unroll
  for (int k2=0;k2<4;k2++){ uint32_t x = (by >> (2*k2)) & 3u; x = (x | (x << 15)) & 0x00010001u; r[k2] = x * 0x3f80u; }
  return __builtin_bit_cast(bf16x8, r);
}
__device__ __forceinline__ void split2(const f32x4& a, const f32x4& b, bf16x8& h, bf16x8& l){
  u32x4 hh, ll;
  #pragma unroll
  for (int i=0;i<4;i++){ uint32_t x = pk2(a[i], b[i]); hh[i] = x; ll[i] = resid2(x, a[i], b[i]); }
  h = __builtin_bit_cast(bf16x8, hh); l = __builtin_bit_cast(bf16x8, ll);
}
template<int A0, int A1>
__device__ __forceinline__ void far_conv(char* smem, int ch, int m, int kg){
  const char* ub = smem + ch*UST;
  const f32x4 Z4 = {0,0,0,0};
  const int B0 = 2*m + kg - 2;
  const char* va0 = ub + (B0 < 0 ? 0 : 272*(B0 >> 4) + 16 + 16*(B0 & 15));
  const char* va1 = ub + 272*((B0+32) >> 4) + 16 + 16*((B0+32) & 15);
  const int cp = (3 - m) & 3;
  const char* ft = smem + FTOFF + ch*8192 + cp*FCS + 4*(47 - m + 4*kg - cp);
  char* yf = smem + YFOFF + ch*YFST + 64*m + 16*kg;
  f32x4 acc[6];
  #pragma unroll
  for (int a=0;a<6;a++) acc[a] = Z4;
  {
    bf16x8 M[4];
    M[0] = band(14 + m, 3, kg); M[1] = band(9 + m, 5, kg); M[2] = band(6 + m, 3, kg); M[3] = band(1 + m, 5, kg);
    bf16x8 Ch[3][2], Cl[3][2];
    #pragma unroll
    for (int dl=0;dl<3;dl++){
      #pragma unroll
      for (int h=0;h<2;h++){ Ch[dl][h] = *(const bf16x8*)(ft + h*(8*FCS) - 64*dl); Cl[dl][h] = *(const bf16x8*)(ft + h*(8*FCS) + 4*FCS - 64*dl); }
    }
    #pragma unroll
    for (int b=(A0 > 2 ? A0-2 : 0);b<A1;b++){
      const bf16x8 V = *(const bf16x8*)(b ? va1 + 544*(b-1) : va0);
      f32x4 F[4];
      #pragma unroll
      for (int p=0;p<4;p++) F[p] = MFMA16(V, M[p], Z4);
      #pragma unroll
      for (int h=0;h<2;h++){
        bf16x8 Bh, Bl;
        split2(F[2*h], F[2*h+1], Bh, Bl);
        #pragma unroll
        for (int dl=0;dl<3;dl++) if (b + dl >= A0 && b + dl < A1) acc[b+dl] = MFMA16(Bh, Ch[dl][h], acc[b+dl]);
        #pragma unroll
        for (int dl=0;dl<3;dl++) if (b + dl >= A0 && b + dl < A1) acc[b+dl] = MFMA16(Bh, Cl[dl][h], acc[b+dl]);
        #pragma unroll
        for (int dl=0;dl<3;dl++) if (b + dl >= A0 && b + dl < A1) acc[b+dl] = MFMA16(Bl, Ch[dl][h], acc[b+dl]);
      }
      if (b < 2 && b >= A0) *(f32x4*)(yf + 1024*b) = acc[b];
    }
  }
  {
    const char* vb0 = ub + (B0 < 1 ? 0 : 272*((B0-1) >> 4) + 16 + 16*((B0-1) & 15));
    const char* vb1 = ub + 272*((B0+31) >> 4) + 16 + 16*((B0+31) & 15);
    const char* f8 = smem + FTOFF + ch*8192 + 16*FCS + cp*FCS + 4*(63 - m + 4*kg - cp);
    const bf16x8 Ma = band(8 + m, 7, kg), Mb = band(7 + m, 9, kg), Mc = band(15 + m, 2, kg);
    const float k1024 = ((const float*)(smem + KCOFF))[ch*KC_ + 512];
    bf16x8 Ch[4], Cl[4];
    #pragma unroll
    for (int dl=0;dl<A1-2;dl++){ Ch[dl] = *(const bf16x8*)(f8 - 64*dl); Cl[dl] = *(const bf16x8*)(f8 + 4*FCS - 64*dl); }
    #pragma unroll
    for (int b=0;b<A1-2;b++){
      const bf16x8 V = *(const bf16x8*)(b ? va1 + 544*(b-1) : va0);
      const bf16x8 V2 = *(const bf16x8*)(b ? vb1 + 544*(b-1) : vb0);
      const f32x4 Fa = MFMA16(V, Ma, Z4), Fb = MFMA16(V2, Mb, Z4);
      bf16x8 Bh, Bl;
      split2(Fa, Fb, Bh, Bl);
      if (b + 2 >= A0){
        const f32x4 Fc = MFMA16(Mc, V, Z4);
        #pragma unroll
        for (int i=0;i<4;i++) acc[b+2][i] = __builtin_fmaf(k1024, Fc[i], acc[b+2][i]);
      }
      #pragma unroll
      for (int dl=0;dl<4;dl++) if (b + 2 + dl >= A0 && b + 2 + dl < A1) acc[b+2+dl] = MFMA16(Bh, Ch[dl], acc[b+2+dl]);
      #pragma unroll
      for (int dl=0;dl<4;dl++) if (b + 2 + dl >= A0 && b + 2 + dl < A1) acc[b+2+dl] = MFMA16(Bh, Cl[dl], acc[b+2+dl]);
      #pragma unroll
      for (int dl=0;dl<4;dl++) if (b + 2 + dl >= A0 && b + 2 + dl < A1) acc[b+2+dl] = MFMA16(Bl, Ch[dl], acc[b+2+dl]);
    }
  }
  #pragma unroll
  for (int a=(A0 > 2 ? A0 : 2);a<A1;a++) *(f32x4*)(yf + 1024*a) = acc[a];
}

__global__ __launch_bounds__(1024) void mono_kernel(const u16* __restrict__ u, const u16* __restrict__ logA, const u16* __restrict__ Aim,
    const u16* __restrict__ Cre, const u16* __restrict__ Cim, const u16* __restrict__ logdt, const u16* __restrict__ Dskip,
    const u16* __restrict__ q, const u16* __restrict__ k, const u16* __restrict__ v, const u16* __restrict__ shift_k,
    const u16* __restrict__ ssmk, const u16* __restrict__ ssmD, u16* __restrict__ out
){
#define TSP(i)
  TSP(0); TSP(1); TSP(2); TSP(3); TSP(4); TSP(5);
  extern __shared__ __attribute__((aligned(16))) char smem[];
  const int bid = blockIdx.x, t = threadIdx.x;
  const int w5 = bid >> 3;
  const bool isA = w5 < 16;
  const int c0 = (bid & 7)*64 + (w5 & 15)*4;
  const int tr = (t + 64*(w5 & 15)) & 1023;
  const int wave = t >> 6, lane = t & 63, m = lane & 15, kg = lane >> 4;
  asm volatile("" :: "s"(u), "s"(logA), "s"(Aim), "s"(Cre), "s"(Cim), "s"(logdt), "s"(Dskip), "s"(q), "s"(k), "s"(v), "s"(shift_k), "s"(ssmk), "s"(ssmD), "s"(out));
  u32x2 dsk;
  int vz; asm volatile("v_mov_b32 %0, 0" : "=v"(vz));
  if (isA){
    const int ch = t >> 8, tg = t & 255, wv = (wave + ch) & 3;
    const int d = c0 + ch;
    char* tb = smem + TOFF + ch*TSZ;
    char* eb_ = smem + KCOFF + ch*(4*KC_);
    const u16 ldt = logdt[d];
    const u16 pla = logA[d*NS_+(tg & 127)], pai = Aim[d*NS_+(tg & 127)];
    u16 pc[2][2];
    #pragma unroll
    for (int hp=0;hp<2;hp++){ pc[hp][0] = Cre[d*NS_+64*hp+lane];  pc[hp][1] = Cim[d*NS_+64*hp+lane]; }
    const float dt = rbf(expf(bf2f(ldt)));
    {
      const float zre = -rbf(expf(bf2f(pla))) * dt;
      const float zim = bf2f(pai) * dt;
      const float f = (tg >> 7) ? 128.f : 1.f;
      cpx e = cexp_(zre*f, zim*f);
      *(float2*)(eb_ + 8*tg) = make_float2(e.re, e.im);
    }
    __syncthreads();
    TSP(1);
    __builtin_amdgcn_sched_barrier(0);
    const u32x2 u0 = *(const u32x2*)(u + (2*tr)*D_ + c0);
    const u32x2 u1 = *(const u32x2*)(u + (2*tr+1)*D_ + c0);
    dsk = *(const u32x2*)(Dskip + c0 + vz);
    __builtin_amdgcn_sched_barrier(0);
    f32x4 a4 = {0,0,0,0};
    char* col = tb + 4*lane;
    #pragma unroll 1
    for (int hp=0;hp<2;hp++){
      cpx C; C.re = bf2f(hp ? pc[1][0] : pc[0][0])*dt; C.im = bf2f(hp ? pc[1][1] : pc[0][1])*dt;
      cpx e1, e2;
      { float2 a = *(const float2*)(eb_ + 8*(64*hp+lane)); e2.re = a.x; e2.im = a.y;
        float2 c = *(const float2*)(eb_ + 1024 + 8*(64*hp+lane)); e1.re = c.x; e1.im = c.y; }
      if (wv == 0){
        cpx e8 = cmul(e2, e2); e8 = cmul(e8, e8); e8 = cmul(e8, e8);
        cpx xx; xx.re = 1.f; xx.im = 0.f;
        #pragma unroll
        for (int i=0;i<16;i++){ put_split(col + (41+i)*TR, xx.re, -xx.im); if (i < 15) xx = (i == 0) ? e8 : cmul(xx, e8); }
      } else if (wv == 1){
        cpx eb = cmul(e1, e1); eb = cmul(eb, eb); eb = cmul(eb, eb);
        cpx cb = cmul(C, eb);
        #pragma unroll
        for (int i=0;i<9;i++){ put_split(col + (32+i)*TR, cb.re, cb.im); if (i < 8) cb = cmul(cb, e1); }
      } else if (wv == 2){
        cpx S[8];
        S[1] = e2;
        #pragma unroll
        for (int i=2;i<8;i++) S[i] = cmul(S[i-1], S[1]);
        cpx cb = C;
        #pragma unroll
        for (int a=0;a<2;a++){
          put_split(col + (8*a)*TR, cb.re, cb.im);
          #pragma unroll
          for (int p=1;p<8;p++){ cpx g = cmul(cb, S[p]); put_split(col + (8*a+p)*TR, g.re, g.im); }
          if (a == 0) cb = cmul(cb, e1);
        }
      } else {
        cpx S2 = cmul(e2, e2), S4 = cmul(S2, S2), S6 = cmul(S4, S2);
        cpx cb = cmul(C, cmul(e1, e1));
        #pragma unroll
        for (int a=0;a<2;a++){
          put_split(col + (16+4*a)*TR, cb.re, cb.im);
          { cpx g = cmul(cb, S2); put_split(col + (16+4*a+1)*TR, g.re, g.im); }
          { cpx g = cmul(cb, S4); put_split(col + (16+4*a+2)*TR, g.re, g.im); }
          { cpx g = cmul(cb, S6); put_split(col + (16+4*a+3)*TR, g.re, g.im); }
          cb = cmul(cb, e1);
        }
        #pragma unroll
        for (int a=0;a<4;a++){
          put_split(col + (24+2*a)*TR, cb.re, cb.im);
          cpx g = cmul(cb, S4); put_split(col + (24+2*a+1)*TR, g.re, g.im);
          if (a < 3) cb = cmul(cb, e1);
        }
      }
      __syncthreads();
      if (wv < 3){
        const char* ga = tb + (16*wv + m)*TR + 16*kg;
        const char* xb = tb + (41 + m)*TR + 16*kg;
        #pragma unroll
        for (int kc=0;kc<4;kc++){
          bf16x8 ah = *(const bf16x8*)(ga + 64*kc), al = *(const bf16x8*)(ga + 64*kc + TLO);
          bf16x8 bh = *(const bf16x8*)(xb + 64*kc), bl = *(const bf16x8*)(xb + 64*kc + TLO);
          a4 = MFMA16(ah, bh, a4);
          a4 = MFMA16(ah, bl, a4);
          a4 = MFMA16(al, bh, a4);
        }
      }
      if (hp == 0) __syncthreads();
    }
    float* Kc = (float*)(smem + KCOFF) + ch*KC_;
    if (wv < 3){
      #pragma unroll
      for (int i=0;i<4;i++){
        int r = 16*wv + 4*kg + i;
        int idx;
        if (r < 16) idx = 128*(r>>3) + (r&7) + 8*m;
        else if (r < 24) idx = 256 + 64*((r-16)>>2) + (r&3) + 4*m;
        else if (r < 32) idx = 384 + 32*((r-24)>>1) + (r&1) + 2*m;
        else idx = 512 + 16*(r-32) + m;
        if (r < 40 || (r == 40 && m == 0)) Kc[idx] = 2.f*a4[i];
      }
    }
    put_u(smem, tr, u0, u1);
    __syncthreads();
    TSP(2);
    {
      char* ft = smem + FTOFF + ch*8192;
      if (tg < 208){
        int z, idx; bool ok; char* wp;
        if (tg < 128){ z = tg & 63; const int Dd = 47 - z; ok = Dd >= 0 && Dd < 32; idx = 385 + 4*Dd + 2*(tg >> 6); wp = ft + (tg >> 6)*(8*FCS); }
        else { z = tg - 128; const int Dd = 63 - z; ok = Dd >= 0; idx = 513 + 2*Dd; wp = ft + 16*FCS; }
        float a = 0.f, b = 0.f;
        if (ok){ a = Kc[idx]; b = Kc[idx+1]; }
        const uint32_t hh = pk2(a, b), ll = pk2(a - lo16f(hh), b - hi16f(hh));
        #pragma unroll
        for (int c=0;c<4;c++) if (z >= c){ *(uint32_t*)(wp + c*FCS + 4*(z-c)) = hh; *(uint32_t*)(wp + (4+c)*FCS + 4*(z-c)) = ll; }
      }
      if (tg >= 161){
        const int sl = tg - 161, j = sl - 15;
        float kv[8];
        #pragma unroll
        for (int i=0;i<8;i++){
          int l = 8*j + 7 - i;
          int pr = (int)rbf((float)l);
          int p = pr < 256 ? pr : 128 + (pr>>1);
          kv[i] = (l >= 0 && l <= 514) ? Kc[p] : 0.f;
        }
        u32x4 h, lo;
        #pragma unroll
        for (int jj=0;jj<4;jj++){
          uint32_t hh = pk2(kv[2*jj], kv[2*jj+1]);
          h[jj] = hh; lo[jj] = pk2(kv[2*jj] - lo16f(hh), kv[2*jj+1] - hi16f(hh));
        }
        char* kb = smem + TOFF + (2*ch)*KS2;
        *(u32x4*)(kb + 16*sl) = h;
        *(u32x4*)(kb + KS2 + 16*sl) = lo;
      }
      if (tg < 4) *(float*)(smem + YFOFF + ch*YFST - 16 + 4*tg) = 0.f;
    }
    __syncthreads();
    TSP(3);
  } else {
    __builtin_amdgcn_sched_barrier(0);
    const u32x2 k0 = *(const u32x2*)(k + (2*tr)*D_ + c0);
    const u32x2 k1 = *(const u32x2*)(k + (2*tr+1)*D_ + c0);
    const u32x4 wa = *(const u32x4*)(shift_k + c0*4), wb = *(const u32x4*)(shift_k + c0*4 + 8);
    char* ks = smem + YOFF;
    __builtin_amdgcn_sched_barrier(0);
    *(u32x4*)(ks + 32 + 16*tr) = u32x4{k0.x, k0.y, k1.x, k1.y};
    __builtin_amdgcn_sched_barrier(0);
    const u32x2 v0 = *(const u32x2*)(v + (2*tr)*D_ + c0);
    const u32x2 v1 = *(const u32x2*)(v + (2*tr+1)*D_ + c0);
    const u32x4 sx = *(const u32x4*)(ssmk + (c0+(t >> 8))*L_ + 8*(t & 255));
    dsk = *(const u32x2*)(ssmD + c0 + vz);
    __builtin_amdgcn_sched_barrier(0);
    if (t < 2) *(u32x4*)(ks + 16*t) = u32x4{0,0,0,0};
    __syncthreads();
    TSP(1);
    u32x2 kk[5];
    kk[0] = *(const u32x2*)(ks + 8 + 16*tr);
    { u32x4 x = *(const u32x4*)(ks + 16 + 16*tr); kk[1] = u32x2{x.x, x.y}; kk[2] = u32x2{x.z, x.w}; }
    kk[3] = k0; kk[4] = k1;
    u32x2 o[2];
    #pragma unroll
    for (int j=0;j<2;j++){
      const u32x2 vv = j ? v1 : v0;
      #pragma unroll
      for (int p=0;p<2;p++){
        float r2[2];
        #pragma unroll
        for (int e=0;e<2;e++){
          const u32x4 ww = p ? wb : wa;
          const uint32_t wl = e ? ww.z : ww.x, wh = e ? ww.w : ww.y;
          const float w0 = lo16f(wl), w1 = hi16f(wl), w2 = lo16f(wh), w3 = hi16f(wh);
          auto g = [&](int idx){ uint32_t x = kk[idx][p]; return e? hi16f(x) : lo16f(x); };
          float acc = rbf(g(j)*w3);
          acc = rbf(acc + rbf(g(j+1)*w2));
          acc = rbf(acc + rbf(g(j+2)*w1));
          acc = rbf(acc + rbf(g(j+3)*w0));
          r2[e] = acc * (e? hi16f(vv[p]) : lo16f(vv[p]));
        }
        o[j][p] = pk2(r2[0], r2[1]);
      }
    }
    put_u(smem, tr, o[0], o[1]);
    {
      const int ch = t >> 8, j = t & 255;
      u32x4 r;
      r.x = __builtin_amdgcn_alignbit(sx.w, sx.w, 16); r.y = __builtin_amdgcn_alignbit(sx.z, sx.z, 16);
      r.z = __builtin_amdgcn_alignbit(sx.y, sx.y, 16); r.w = __builtin_amdgcn_alignbit(sx.x, sx.x, 16);
      char* kb = smem + TOFF + ch*KST;
      *(u32x4*)(kb + 16*(j+15)) = r;
      if (j < 15) *(u32x4*)(kb + 16*j) = u32x4{0,0,0,0};
    }
    __syncthreads();
    TSP(2); TSP(3);
    if (wave >= 8){
      const int r4 = (((t - 512) + 32*(w5 & 15)) & 511)*4;
      u32x2 a[4];
      #pragma unroll
      for (int i=0;i<4;i++) a[i] = *(const u32x2*)(q + (r4+i)*D_ + c0);
      *(u32x4*)(smem + QOFF + 8*r4)      = u32x4{a[0].x, a[0].y, a[1].x, a[1].y};
      *(u32x4*)(smem + QOFF + 8*r4 + 16) = u32x4{a[2].x, a[2].y, a[3].x, a[3].y};
    }
  }
  if (isA){
    const int ch = wave >> 2, role = (wave + ch) & 3;
    if (role == 0) near_conv<4,0>(smem, ch, m, kg);
    else if (role == 1) near_conv<4,4>(smem, ch, m, kg);
    else if (role == 2){ __builtin_amdgcn_s_setprio(3); far_conv<0,4>(smem, ch, m, kg); __builtin_amdgcn_s_setprio(0); }
    else { __builtin_amdgcn_s_setprio(3); far_conv<4,6>(smem, ch, m, kg); __builtin_amdgcn_s_setprio(0); }
    TSP(5);
  } else if (wave < 8){
    const int ch = wave >> 1;
    const int g = wave & 1;
    const int part = 0;
    const int unit = ch;
    const uint32_t kbase = (uint32_t)(uintptr_t)smem + TOFF + unit*KST + 16*(m - kg + 3);
    const char* ub = smem + ch*UST;
    bf16x8 KA[4], KB[4];
    LDK4(KA, 15);
    f32x4 acc[4];
    {
      bf16x8 Up[2][4];
      {
        const uint32_t sel = (m & 1) ? 0x07060504u : 0x05040302u;
        const char* pp = ub + 272*(m >> 1) + 16*kg + 8*g;
        #pragma unroll
        for (int c=0;c<4;c++){ W6 W = uwin(pp + 64*c); Up[0][c] = usel(W, 0, sel); Up[1][c] = usel(W, 1, sel); }
      }
      f32x4 P0 = {0,0,0,0}, P1 = {0,0,0,0};
#define PH1(dl, CUR, NXT) \
      LDK4(NXT, (dl)-1); WAITK(4, CUR); \
      if ((dl) < 15){ P0 = shrN2(P0); P1 = shrN2(P1); } \
      P0 = MFMA16(CUR[0], Up[0][0], P0); P1 = MFMA16(CUR[0], Up[1][0], P1); \
      P0 = MFMA16(CUR[1], Up[0][1], P0); P1 = MFMA16(CUR[1], Up[1][1], P1); \
      P0 = MFMA16(CUR[2], Up[0][2], P0); P1 = MFMA16(CUR[2], Up[1][2], P1); \
      P0 = MFMA16(CUR[3], Up[0][3], P0); P1 = MFMA16(CUR[3], Up[1][3], P1);
      PH1(15, KA, KB) PH1(14, KB, KA) PH1(13, KA, KB) PH1(12, KB, KA)
      PH1(11, KA, KB) PH1(10, KB, KA) PH1(9, KA, KB) PH1(8, KB, KA)
      const bool ok = m >= 1 && m <= 8;
      const int src = 4*((lane & 48) + ((2*(m-1)) & 15));
      #pragma unroll
      for (int i=0;i<4;i++){
        float a0 = i2f(__builtin_amdgcn_ds_bpermute(src,   f2i(P0[i])));
        float a1 = i2f(__builtin_amdgcn_ds_bpermute(src+4, f2i(P0[i])));
        float a2 = i2f(__builtin_amdgcn_ds_bpermute(src,   f2i(P1[i])));
        float a3 = i2f(__builtin_amdgcn_ds_bpermute(src+4, f2i(P1[i])));
        acc[0][i] = ok ? a0 : 0.f; acc[1][i] = ok ? a1 : 0.f; acc[2][i] = ok ? a2 : 0.f; acc[3][i] = ok ? a3 : 0.f;
      }
    }
    TSP(4);
    bf16x8 U[4][4];
    {
      const char* pp = ub + 272*m + 16*kg + 8*g;
      #pragma unroll
      for (int c=0;c<4;c++){ W6 W = uwin(pp + 64*c); U[0][c] = uodd(W, 0); U[1][c] = uevn(W, 1); U[2][c] = uodd(W, 1); U[3][c] = uevn(W, 2); }
    }
#define PH2(dl, CUR, NXT) \
    if ((dl) > 0){ LDK4(NXT, (dl)-1); WAITK(4, CUR); } else { WAITK(0, CUR); } \
    if ((dl) < 7){ acc[0] = shrN1(acc[0]); acc[1] = shrN1(acc[1]); acc[2] = shrN1(acc[2]); acc[3] = shrN1(acc[3]); } \
    _Pragma("unroll") for (int c=0;c<4;c++){ \
      acc[0] = MFMA16(CUR[c], U[0][c], acc[0]); acc[1] = MFMA16(CUR[c], U[1][c], acc[1]); \
      acc[2] = MFMA16(CUR[c], U[2][c], acc[2]); acc[3] = MFMA16(CUR[c], U[3][c], acc[3]); }
    PH2(7, KA, KB) PH2(6, KB, KA) PH2(5, KA, KB) PH2(4, KB, KA)
    PH2(3, KA, KB) PH2(2, KB, KA) PH2(1, KA, KB) PH2(0, KB, KA)
    TSP(5);
    char* yb = smem + YOFF + (part*4 + ch)*YST + 4*(4*g + 32*kg + 132*m);
    #pragma unroll
    for (int i=0;i<4;i++) *(f32x4*)(yb + 32*i) = f32x4{acc[0][i], acc[1][i], acc[2][i], acc[3][i]};
  }
  __syncthreads();
  TSP(6);
  {
    const char* yb = smem + YOFF + 8*tr + 16*(tr >> 6);
    const char* up = smem + 272*(tr >> 6) + 16 + 4*(tr & 63);
    float o0[4], o1[4];
    if (isA){
      const char* ya = yb + (YAOFF - YOFF);
      const char* yf = smem + YFOFF + (tr >= 257 ? 4*(2*tr - 515) : -16);
      #pragma unroll
      for (int c=0;c<4;c++){
        f32x2 a = *(const f32x2*)(ya + c*YST);
        float b0 = *(const float*)(yf + c*YFST), b1 = *(const float*)(yf + c*YFST + 4);
        uint32_t uu = *(const uint32_t*)(up + c*UST);
        float sd = (c & 1) ? hi16f(dsk[c>>1]) : lo16f(dsk[c>>1]);
        o0[c] = rbf(a.x + b0) + rbf(lo16f(uu)*sd);
        o1[c] = rbf(a.y + b1) + rbf(hi16f(uu)*sd);
      }
    } else {
      u32x4 qq = *(const u32x4*)(smem + QOFF + 16*tr);
      #pragma unroll
      for (int c=0;c<4;c++){
        f32x2 a = *(const f32x2*)(yb + c*YST);
        uint32_t uu = *(const uint32_t*)(up + c*UST);
        float sd = (c & 1) ? hi16f(dsk[c>>1]) : lo16f(dsk[c>>1]);
        float q0 = (c & 1) ? hi16f(qq[c>>1]) : lo16f(qq[c>>1]);
        float q1 = (c & 1) ? hi16f(qq[2+(c>>1)]) : lo16f(qq[2+(c>>1)]);
        o0[c] = q0 * rbf(rbf(a.x) + rbf(lo16f(uu)*sd));
        o1[c] = q1 * rbf(rbf(a.y) + rbf(hi16f(uu)*sd));
      }
    }
    u16* op = out + (isA ? 0 : D_) + c0;
    *(u32x2*)(op + (2*tr)*(2*D_))   = u32x2{pk2(o0[0],o0[1]), pk2(o0[2],o0[3])};
    *(u32x2*)(op + (2*tr+1)*(2*D_)) = u32x2{pk2(o1[0],o1[1]), pk2(o1[2],o1[3])};
  }
}

extern "C" void kernel_launch(void* const* d_in, const int* in_sizes, int n_in,
                              void* d_out, int out_size, void* d_ws, size_t ws_size,
                              hipStream_t stream) {
  mono_kernel<<<256, 1024, LDS_TOTAL, stream>>>((const u16*)d_in[0], (const u16*)d_in[1], (const u16*)d_in[2], (const u16*)d_in[3],
     (const u16*)d_in[4], (const u16*)d_in[5], (const u16*)d_in[6], (const u16*)d_in[7], (const u16*)d_in[8], (const u16*)d_in[9],
     (const u16*)d_in[10], (const u16*)d_in[11], (const u16*)d_in[12], (u16*)d_out
);
}
```

```cpp
#define PF_DATA
#include <hip/hip_runtime.h>
#include <hip/hip_bf16.h>
#include <stdint.h>

typedef unsigned short u16;
typedef short bf16x8 __attribute__((ext_vector_type(8)));
typedef float f32x4 __attribute__((ext_vector_type(4)));
typedef float f32x2 __attribute__((ext_vector_type(2)));
typedef __bf16 bf16x2_t __attribute__((ext_vector_type(2)));

#define L_ 2048
#define D_ 512
#define NS_ 128

__device__ __forceinline__ float bf2f(u16 x){ return __builtin_bit_cast(float, (uint32_t)x << 16); }
__device__ __forceinline__ u16 f2bf(float f){ __hip_bfloat16 h = __float2bfloat16(f); return __builtin_bit_cast(u16, h); }
__device__ __forceinline__ float rbf(float f){ return bf2f(f2bf(f)); }
__device__ __forceinline__ uint32_t pk2(float a, float b){
  f32x2 v = {a,b}; bf16x2_t r = __builtin_convertvector(v, bf16x2_t); return __builtin_bit_cast(uint32_t, r); }
__device__ __forceinline__ float lo16f(uint32_t w){ return __builtin_bit_cast(float, w << 16); }
__device__ __forceinline__ float hi16f(uint32_t w){ return __builtin_bit_cast(float, w & 0xffff0000u); }

struct cpx { float re, im; };
__device__ __forceinline__ float amul(float a, float b){ float r; asm("v_mul_f32 %0, %1, %2" : "=v"(r) : "v"(a), "v"(b)); return r; }
__device__ __forceinline__ float afma(float a, float b, float c){ float r; asm("v_fma_f32 %0, %1, %2, %3" : "=v"(r) : "v"(a), "v"(b), "v"(c)); return r; }
__device__ __forceinline__ float afms(float a, float b, float c){ float r; asm("v_fma_f32 %0, %1, %2, -%3" : "=v"(r) : "v"(a), "v"(b), "v"(c)); return r; }
__device__ __forceinline__ cpx cmul(cpx a, cpx b){
  f32x2 A = {a.re, a.im}, B = {b.re, b.im}, T, R;
  asm("v_pk_mul_f32 %0, %1, %2 op_sel:[1,1] op_sel_hi:[1,0] neg_lo:[1,0]" : "=v"(T) : "v"(A), "v"(B));
  asm("v_pk_fma_f32 %0, %1, %2, %3 op_sel:[0,0,0] op_sel_hi:[0,1,1]" : "=v"(R) : "v"(A), "v"(B), "v"(T));
  cpx r; r.re = R.x; r.im = R.y; return r; }
__device__ __forceinline__ cpx cexp_(float re, float im){ float e = expf(re); float s,c; sincosf(im,&s,&c); cpx r; r.re=e*c; r.im=e*s; return r; }

#define MFMA16(a,b,c) __builtin_amdgcn_mfma_f32_16x16x32_bf16(a,b,c,0,0,0)
typedef uint32_t u32x2 __attribute__((ext_vector_type(2)));
typedef uint32_t u32x4 __attribute__((ext_vector_type(4)));
#define KC_ 656
#define TR 288
#define TLO (57*TR)
#define TSZ (2*TLO)
#define UST 4352
#define KCOFF 17408
#define TOFF 27904
#define KST 4352
#define KS2 1536
#define FTOFF (TOFF + 12288)
#define FCS 320
#define YFOFF (TOFF + 45056 + 16)
#define YFST 6176
#define YAOFF (TOFF + 69888)
#define YOFF (TOFF + 36864)
#define YST 8448
#define QOFF (TOFF + 112128)
#define LDS_TOTAL (TOFF + 4*TSZ)

__device__ __forceinline__ uint32_t resid2(uint32_t h, float a, float b){
  return pk2(a - lo16f(h), b - hi16f(h));
}
__device__ __forceinline__ void put_split(char* p, float a, float b){
  uint32_t h = pk2(a,b);
  uint32_t l = resid2(h, a, b);
  *(uint32_t*)p = h; *(uint32_t*)(p + TLO) = l;
}
__device__ __forceinline__ int f2i(float x){ return __builtin_bit_cast(int, x); }
__device__ __forceinline__ float i2f(int x){ return __builtin_bit_cast(float, x); }
__device__ __forceinline__ f32x4 shrN1(f32x4 x){
  f32x4 r;
  #pragma unroll
  for (int i=0;i<4;i++) r[i] = i2f(__builtin_amdgcn_update_dpp(0, f2i(x[i]), 0x111, 0xf, 0xf, true));
  return r;
}
__device__ __forceinline__ f32x4 shrN2(f32x4 x){
  f32x4 r;
  #pragma unroll
  for (int i=0;i<4;i++) r[i] = i2f(__builtin_amdgcn_update_dpp(0, f2i(x[i]), 0x112, 0xf, 0xf, true));
  return r;
}
struct W6 { uint32_t w[6]; };
__device__ __forceinline__ W6 uwin(const char* p){
  u32x2 a = *(const u32x2*)p, b = *(const u32x2*)(p+8), c = *(const u32x2*)(p+16);
  W6 r; r.w[0]=a.x; r.w[1]=a.y; r.w[2]=b.x; r.w[3]=b.y; r.w[4]=c.x; r.w[5]=c.y; return r;
}
__device__ __forceinline__ bf16x8 usel(const W6& W, int x, uint32_t sel){
  u32x4 r;
  r.x = __builtin_amdgcn_perm(W.w[x+1], W.w[x],   sel); r.y = __builtin_amdgcn_perm(W.w[x+2], W.w[x+1], sel);
  r.z = __builtin_amdgcn_perm(W.w[x+3], W.w[x+2], sel); r.w = __builtin_amdgcn_perm(W.w[x+4], W.w[x+3], sel);
  return __builtin_bit_cast(bf16x8, r);
}
__device__ __forceinline__ bf16x8 uodd(const W6& W, int x){
  u32x4 r;
  r.x = __builtin_amdgcn_alignbit(W.w[x+1], W.w[x],   16); r.y = __builtin_amdgcn_alignbit(W.w[x+2], W.w[x+1], 16);
  r.z = __builtin_amdgcn_alignbit(W.w[x+3], W.w[x+2], 16); r.w = __builtin_amdgcn_alignbit(W.w[x+4], W.w[x+3], 16);
  return __builtin_bit_cast(bf16x8, r);
}
__device__ __forceinline__ bf16x8 uevn(const W6& W, int x){
  u32x4 r = {W.w[x], W.w[x+1], W.w[x+2], W.w[x+3]};
  return __builtin_bit_cast(bf16x8, r);
}
__device__ __forceinline__ void put_u(char* smem, int tr, u32x2 r0, u32x2 r1){
  uint32_t d[4];
  d[0] = __builtin_amdgcn_perm(r1.x, r0.x, 0x05040100); d[1] = __builtin_amdgcn_perm(r1.x, r0.x, 0x07060302);
  d[2] = __builtin_amdgcn_perm(r1.y, r0.y, 0x05040100); d[3] = __builtin_amdgcn_perm(r1.y, r0.y, 0x07060302);
  const int a = tr >> 6, pos = tr & 63;
  char* p = smem + 272*a + 16 + 4*pos;
  #pragma unroll
  for (int c=0;c<4;c++) *(uint32_t*)(p + c*UST) = d[c];
  if (pos >= 60 && a < 15){
    #pragma unroll
    for (int c=0;c<4;c++) *(uint32_t*)(p + c*UST + 16) = d[c];
  }
  if (tr < 16) *(uint32_t*)(smem + (tr>>2)*UST + 4*(tr&3)) = 0u;
}

#define LDK(F, dl, c) asm volatile("ds_read_b128 %0, %1 offset:%2" : "=v"(F) : "v"(kbase), "i"(256*(dl) + 64*(3-(c))))
#define LDK4(S, dl) LDK(S[0], dl, 0); LDK(S[1], dl, 1); LDK(S[2], dl, 2); LDK(S[3], dl, 3)
#define WAITK(n, S) asm volatile("s_waitcnt lgkmcnt(" #n ")" : "+v"(S[0]), "+v"(S[1]), "+v"(S[2]), "+v"(S[3]))

template<int NT, int R0>
__device__ __forceinline__ void near_conv(char* smem, int ch, int m, int kg){
  const uint32_t kbase = (uint32_t)(uintptr_t)smem + TOFF + (2*ch)*KS2 + 16*(m - kg + 3);
#define LDN(F, hs, c) asm volatile("ds_read_b128 %0, %1 offset:%2" : "=v"(F) : "v"(kbase), "i"(((hs)&1)*KS2 + 256*(4-((hs)>>1)) + 64*(3-(c))))
#define LDN4(S, hs) LDN(S[0], hs, 0); LDN(S[1], hs, 1); LDN(S[2], hs, 2); LDN(S[3], hs, 3)
  bf16x8 KA[4], KB[4];
  LDN4(KA, 0);
  bf16x8 U[NT][4];
  {
    const char* pp = smem + ch*UST + 272*m + 16*kg + (R0 ? 8 : 0);
    #pragma unroll
    for (int c=0;c<4;c++){
      W6 W = uwin(pp + 64*c);
      #pragma unroll
      for (int r=0;r<NT;r++){ const int rel = R0 + r + 1 - (R0 ? 4 : 0); U[r][c] = (rel & 1) ? uodd(W, rel >> 1) : uevn(W, rel >> 1); }
    }
  }
  f32x4 acc[NT];
  #pragma unroll
  for (int r=0;r<NT;r++) acc[r] = f32x4{0,0,0,0};
#define NH(hs, CUR, NXT) \
  if ((hs) < 9){ LDN4(NXT, (hs)+1); WAITK(4, CUR); } else { WAITK(0, CUR); } \
  if ((hs) > 0 && !((hs) & 1)){ _Pragma("unroll") for (int r=0;r<NT;r++) acc[r] = shrN1(acc[r]); } \
  _Pragma("unroll") for (int c=0;c<4;c++){ _Pragma("unroll") for (int r=0;r<NT;r++) acc[r] = MFMA16(CUR[c], U[r][c], acc[r]); }
  NH(0, KA, KB) NH(1, KB, KA) NH(2, KA, KB) NH(3, KB, KA) NH(4, KA, KB)
  NH(5, KB, KA) NH(6, KA, KB) NH(7, KB, KA) NH(8, KA, KB) NH(9, KB, KA)
  float* yb = (float*)(smem + YAOFF + ch*YST) + (R0 + 32*kg + 132*m);
  #pragma unroll
  for (int i=0;i<4;i++){
    #pragma unroll
    for (int r=0;r<NT;r++) yb[8*i + r] = acc[r][i];
  }
}
__device__ __forceinline__ bf16x8 band(int sh, int w, int kg){
  const uint32_t by = (((1u << w) - 1u) << sh) >> (8*kg);
  u32x4 r;
  #pragma unroll
  for (int k2=0;k2<4;k2++){ uint32_t x = (by >> (2*k2)) & 3u; x = (x | (x << 15)) & 0x00010001u; r[k2] = x * 0x3f80u; }
  return __builtin_bit_cast(bf16x8, r);
}
__device__ __forceinline__ void split2(const f32x4& a, const f32x4& b, bf16x8& h, bf16x8& l){
  u32x4 hh, ll;
  #pragma unroll
  for (int i=0;i<4;i++){ uint32_t x = pk2(a[i], b[i]); hh[i] = x; ll[i] = resid2(x, a[i], b[i]); }
  h = __builtin_bit_cast(bf16x8, hh); l = __builtin_bit_cast(bf16x8, ll);
}
template<int A0, int A1>
__device__ __forceinline__ void far_conv(char* smem, int ch, int m, int kg){
  const char* ub = smem + ch*UST;
  const f32x4 Z4 = {0,0,0,0};
  const int B0 = 2*m + kg - 2;
  const char* va0 = ub + (B0 < 0 ? 0 : 272*(B0 >> 4) + 16 + 16*(B0 & 15));
  const char* va1 = ub + 272*((B0+32) >> 4) + 16 + 16*((B0+32) & 15);
  const int cp = (3 - m) & 3;
  const char* ft = smem + FTOFF + ch*8192 + cp*FCS + 4*(47 - m + 4*kg - cp);
  char* yf = smem + YFOFF + ch*YFST + 64*m + 16*kg;
  f32x4 acc[6];
  #pragma unroll
  for (int a=0;a<6;a++) acc[a] = Z4;
  {
    bf16x8 M[4];
    M[0] = band(14 + m, 3, kg); M[1] = band(9 + m, 5, kg); M[2] = band(6 + m, 3, kg); M[3] = band(1 + m, 5, kg);
    bf16x8 Ch[3][2], Cl[3][2];
    #pragma unroll
    for (int dl=0;dl<3;dl++){
      #pragma unroll
      for (int h=0;h<2;h++){ Ch[dl][h] = *(const bf16x8*)(ft + h*(8*FCS) - 64*dl); Cl[dl][h] = *(const bf16x8*)(ft + h*(8*FCS) + 4*FCS - 64*dl); }
    }
    #pragma unroll
    for (int b=(A0 > 2 ? A0-2 : 0);b<A1;b++){
      const bf16x8 V = *(const bf16x8*)(b ? va1 + 544*(b-1) : va0);
      f32x4 F[4];
      #pragma unroll
      for (int p=0;p<4;p++) F[p] = MFMA16(V, M[p], Z4);
      #pragma unroll
      for (int h=0;h<2;h++){
        bf16x8 Bh, Bl;
        split2(F[2*h], F[2*h+1], Bh, Bl);
        #pragma unroll
        for (int dl=0;dl<3;dl++) if (b + dl >= A0 && b + dl < A1) acc[b+dl] = MFMA16(Bh, Ch[dl][h], acc[b+dl]);
        #pragma unroll
        for (int dl=0;dl<3;dl++) if (b + dl >= A0 && b + dl < A1) acc[b+dl] = MFMA16(Bh, Cl[dl][h], acc[b+dl]);
        #pragma unroll
        for (int dl=0;dl<3;dl++) if (b + dl >= A0 && b + dl < A1) acc[b+dl] = MFMA16(Bl, Ch[dl][h], acc[b+dl]);
      }
      if (b < 2 && b >= A0) *(f32x4*)(yf + 1024*b) = acc[b];
    }
  }
  {
    const char* vb0 = ub + (B0 < 1 ? 0 : 272*((B0-1) >> 4) + 16 + 16*((B0-1) & 15));
    const char* vb1 = ub + 272*((B0+31) >> 4) + 16 + 16*((B0+31) & 15);
    const char* f8 = smem + FTOFF + ch*8192 + 16*FCS + cp*FCS + 4*(63 - m + 4*kg - cp);
    const bf16x8 Ma = band(8 + m, 7, kg), Mb = band(7 + m, 9, kg), Mc = band(15 + m, 2, kg);
    const float k1024 = ((const float*)(smem + KCOFF))[ch*KC_ + 512];
    bf16x8 Ch[4], Cl[4];
    #pragma unroll
    for (int dl=0;dl<A1-2;dl++){ Ch[dl] = *(const bf16x8*)(f8 - 64*dl); Cl[dl] = *(const bf16x8*)(f8 + 4*FCS - 64*dl); }
    #pragma unroll
    for (int b=0;b<A1-2;b++){
      const bf16x8 V = *(const bf16x8*)(b ? va1 + 544*(b-1) : va0);
      const bf16x8 V2 = *(const bf16x8*)(b ? vb1 + 544*(b-1) : vb0);
      const f32x4 Fa = MFMA16(V, Ma, Z4), Fb = MFMA16(V2, Mb, Z4);
      bf16x8 Bh, Bl;
      split2(Fa, Fb, Bh, Bl);
      if (b + 2 >= A0){
        const f32x4 Fc = MFMA16(Mc, V, Z4);
        #pragma unroll
        for (int i=0;i<4;i++) acc[b+2][i] = __builtin_fmaf(k1024, Fc[i], acc[b+2][i]);
      }
      #pragma unroll
      for (int dl=0;dl<4;dl++) if (b + 2 + dl >= A0 && b + 2 + dl < A1) acc[b+2+dl] = MFMA16(Bh, Ch[dl], acc[b+2+dl]);
      #pragma unroll
      for (int dl=0;dl<4;dl++) if (b + 2 + dl >= A0 && b + 2 + dl < A1) acc[b+2+dl] = MFMA16(Bh, Cl[dl], acc[b+2+dl]);
      #pragma unroll
      for (int dl=0;dl<4;dl++) if (b + 2 + dl >= A0 && b + 2 + dl < A1) acc[b+2+dl] = MFMA16(Bl, Ch[dl], acc[b+2+dl]);
    }
  }
  #pragma unroll
  for (int a=(A0 > 2 ? A0 : 2);a<A1;a++) *(f32x4*)(yf + 1024*a) = acc[a];
}

__global__ __launch_bounds__(1024) void mono_kernel(const u16* __restrict__ u, const u16* __restrict__ logA, const u16* __restrict__ Aim,
    const u16* __restrict__ Cre, const u16* __restrict__ Cim, const u16* __restrict__ logdt, const u16* __restrict__ Dskip,
    const u16* __restrict__ q, const u16* __restrict__ k, const u16* __restrict__ v, const u16* __restrict__ shift_k,
    const u16* __restrict__ ssmk, const u16* __restrict__ ssmD, u16* __restrict__ out
){
  unsigned long long tsv[10];
#define TSP(i) tsv[i] = __builtin_amdgcn_s_memrealtime()
  TSP(0); TSP(1); TSP(2); TSP(3); TSP(4); TSP(5);
  extern __shared__ __attribute__((aligned(16))) char smem[];
  const int bid = blockIdx.x, t = threadIdx.x;
  const int w5 = bid >> 3;
  const bool isA = w5 < 16;
  const int c0 = (bid & 7)*64 + (w5 & 15)*4;
  const int tr = (t + 64*(w5 & 15)) & 1023;
  const int wave = t >> 6, lane = t & 63, m = lane & 15, kg = lane >> 4;
  asm volatile("" :: "s"(u), "s"(logA), "s"(Aim), "s"(Cre), "s"(Cim), "s"(logdt), "s"(Dskip), "s"(q), "s"(k), "s"(v), "s"(shift_k), "s"(ssmk), "s"(ssmD), "s"(out));
  u32x2 dsk;
  int vz; asm volatile("v_mov_b32 %0, 0" : "=v"(vz));
  u32x4 pf0, pf1;
  {
    const int off = (((w5 & 15)*16 + wave)*8 + (lane >> 3))*D_ + (bid & 7)*64 + (lane & 7)*8;
    asm volatile("global_load_dwordx4 %0, %1, off" : "=v"(pf0) : "v"((isA ? u : k) + off));
    asm volatile("global_load_dwordx4 %0, %1, off" : "=v"(pf1) : "v"((isA ? q : v) + off));
  }
  if (isA){
    const int ch = t >> 8, tg = t & 255, wv = (wave + ch) & 3;
    const int d = c0 + ch;
    char* tb = smem + TOFF + ch*TSZ;
    char* eb_ = smem + KCOFF + ch*(4*KC_);
    const u16 ldt = logdt[d];
    const u16 pla = logA[d*NS_+(tg & 127)], pai = Aim[d*NS_+(tg & 127)];
    u16 pc[2][2];
    #pragma unroll
    for (int hp=0;hp<2;hp++){ pc[hp][0] = Cre[d*NS_+64*hp+lane];  pc[hp][1] = Cim[d*NS_+64*hp+lane]; }
    const float dt = rbf(expf(bf2f(ldt)));
    {
      const float zre = -rbf(expf(bf2f(pla))) * dt;
      const float zim = bf2f(pai) * dt;
      const float f = (tg >> 7) ? 128.f : 1.f;
      cpx e = cexp_(zre*f, zim*f);
      *(float2*)(eb_ + 8*tg) = make_float2(e.re, e.im);
    }
    asm volatile("s_waitcnt vmcnt(4)" :: "v"(pf0), "v"(pf1));
    __syncthreads();
    TSP(1);
    __builtin_amdgcn_sched_barrier(0);
    const u32x2 u0 = *(const u32x2*)(u + (2*tr)*D_ + c0);
    const u32x2 u1 = *(const u32x2*)(u + (2*tr+1)*D_ + c0);
    dsk = *(const u32x2*)(Dskip + c0 + vz);
    __builtin_amdgcn_sched_barrier(0);
    f32x4 a4 = {0,0,0,0};
    char* col = tb + 4*lane;
    #pragma unroll 1
    for (int hp=0;hp<2;hp++){
      cpx C; C.re = bf2f(hp ? pc[1][0] : pc[0][0])*dt; C.im = bf2f(hp ? pc[1][1] : pc[0][1])*dt;
      cpx e1, e2;
      { float2 a = *(const float2*)(eb_ + 8*(64*hp+lane)); e2.re = a.x; e2.im = a.y;
        float2 c = *(const float2*)(eb_ + 1024 + 8*(64*hp+lane)); e1.re = c.x; e1.im = c.y; }
      if (wv == 0){
        cpx e8 = cmul(e2, e2); e8 = cmul(e8, e8); e8 = cmul(e8, e8);
        cpx xx; xx.re = 1.f; xx.im = 0.f;
        #pragma unroll
        for (int i=0;i<16;i++){ put_split(col + (41+i)*TR, xx.re, -xx.im); if (i < 15) xx = (i == 0) ? e8 : cmul(xx, e8); }
      } else if (wv == 1){
        cpx eb = cmul(e1, e1); eb = cmul(eb, eb); eb = cmul(eb, eb);
        cpx cb = cmul(C, eb);
        #pragma unroll
        for (int i=0;i<9;i++){ put_split(col + (32+i)*TR, cb.re, cb.im); if (i < 8) cb = cmul(cb, e1); }
      } else if (wv == 2){
        cpx S[8];
        S[1] = e2;
        #pragma unroll
        for (int i=2;i<8;i++) S[i] = cmul(S[i-1], S[1]);
        cpx cb = C;
        #pragma unroll
        for (int a=0;a<2;a++){
          put_split(col + (8*a)*TR, cb.re, cb.im);
          #pragma unroll
          for (int p=1;p<8;p++){ cpx g = cmul(cb, S[p]); put_split(col + (8*a+p)*TR, g.re, g.im); }
          if (a == 0) cb = cmul(cb, e1);
        }
      } else {
        cpx S2 = cmul(e2, e2), S4 = cmul(S2, S2), S6 = cmul(S4, S2);
        cpx cb = cmul(C, cmul(e1, e1));
        #pragma unroll
        for (int a=0;a<2;a++){
          put_split(col + (16+4*a)*TR, cb.re, cb.im);
          { cpx g = cmul(cb, S2); put_split(col + (16+4*a+1)*TR, g.re, g.im); }
          { cpx g = cmul(cb, S4); put_split(col + (16+4*a+2)*TR, g.re, g.im); }
          { cpx g = cmul(cb, S6); put_split(col + (16+4*a+3)*TR, g.re, g.im); }
          cb = cmul(cb, e1);
        }
        #pragma unroll
        for (int a=0;a<4;a++){
          put_split(col + (24+2*a)*TR, cb.re, cb.im);
          cpx g = cmul(cb, S4); put_split(col + (24+2*a+1)*TR, g.re, g.im);
          if (a < 3) cb = cmul(cb, e1);
        }
      }
      __syncthreads();
      if (wv < 3){
        const char* ga = tb + (16*wv + m)*TR + 16*kg;
        const char* xb = tb + (41 + m)*TR + 16*kg;
        #pragma unroll
        for (int kc=0;kc<4;kc++){
          bf16x8 ah = *(const bf16x8*)(ga + 64*kc), al = *(const bf16x8*)(ga + 64*kc + TLO);
          bf16x8 bh = *(const bf16x8*)(xb + 64*kc), bl = *(const bf16x8*)(xb + 64*kc + TLO);
          a4 = MFMA16(ah, bh, a4);
          a4 = MFMA16(ah, bl, a4);
          a4 = MFMA16(al, bh, a4);
        }
      }
      if (hp == 0) __syncthreads();
    }
    float* Kc = (float*)(smem + KCOFF) + ch*KC_;
    if (wv < 3){
      #pragma unroll
      for (int i=0;i<4;i++){
        int r = 16*wv + 4*kg + i;
        int idx;
        if (r < 16) idx = 128*(r>>3) + (r&7) + 8*m;
        else if (r < 24) idx = 256 + 64*((r-16)>>2) + (r&3) + 4*m;
        else if (r < 32) idx = 384 + 32*((r-24)>>1) + (r&1) + 2*m;
        else idx = 512 + 16*(r-32) + m;
        if (r < 40 || (r == 40 && m == 0)) Kc[idx] = 2.f*a4[i];
      }
    }
    put_u(smem, tr, u0, u1);
    __syncthreads();
    TSP(2);
    {
      char* ft = smem + FTOFF + ch*8192;
      if (tg < 208){
        int z, idx; bool ok; char* wp;
        if (tg < 128){ z = tg & 63; const int Dd = 47 - z; ok = Dd >= 0 && Dd < 32; idx = 385 + 4*Dd + 2*(tg >> 6); wp = ft + (tg >> 6)*(8*FCS); }
        else { z = tg - 128; const int Dd = 63 - z; ok = Dd >= 0; idx = 513 + 2*Dd; wp = ft + 16*FCS; }
        float a = 0.f, b = 0.f;
        if (ok){ a = Kc[idx]; b = Kc[idx+1]; }
        const uint32_t hh = pk2(a, b), ll = pk2(a - lo16f(hh), b - hi16f(hh));
        #pragma unroll
        for (int c=0;c<4;c++) if (z >= c){ *(uint32_t*)(wp + c*FCS + 4*(z-c)) = hh; *(uint32_t*)(wp + (4+c)*FCS + 4*(z-c)) = ll; }
      }
      if (tg >= 161){
        const int sl = tg - 161, j = sl - 15;
        float kv[8];
        #pragma unroll
        for (int i=0;i<8;i++){
          int l = 8*j + 7 - i;
          int pr = (int)rbf((float)l);
          int p = pr < 256 ? pr : 128 + (pr>>1);
          kv[i] = (l >= 0 && l <= 514) ? Kc[p] : 0.f;
        }
        u32x4 h, lo;
        #pragma unroll
        for (int jj=0;jj<4;jj++){
          uint32_t hh = pk2(kv[2*jj], kv[2*jj+1]);
          h[jj] = hh; lo[jj] = pk2(kv[2*jj] - lo16f(hh), kv[2*jj+1] - hi16f(hh));
        }
        char* kb = smem + TOFF + (2*ch)*KS2;
        *(u32x4*)(kb + 16*sl) = h;
        *(u32x4*)(kb + KS2 + 16*sl) = lo;
      }
      if (tg < 4) *(float*)(smem + YFOFF + ch*YFST - 16 + 4*tg) = 0.f;
    }
    __syncthreads();
    TSP(3);
  } else {
    asm volatile("s_waitcnt vmcnt(1)" :: "v"(pf0));
    __builtin_amdgcn_sched_barrier(0);
    const u32x2 k0 = *(const u32x2*)(k + (2*tr)*D_ + c0);
    const u32x2 k1 = *(const u32x2*)(k + (2*tr+1)*D_ + c0);
    const u32x4 wa = *(const u32x4*)(shift_k + c0*4), wb = *(const u32x4*)(shift_k + c0*4 + 8);
    char* ks = smem + YOFF;
    __builtin_amdgcn_sched_barrier(0);
    *(u32x4*)(ks + 32 + 16*tr) = u32x4{k0.x, k0.y, k1.x, k1.y};
    asm volatile("" :: "v"(pf1));
    __builtin_amdgcn_sched_barrier(0);
    const u32x2 v0 = *(const u32x2*)(v + (2*tr)*D_ + c0);
    const u32x2 v1 = *(const u32x2*)(v + (2*tr+1)*D_ + c0);
    const u32x4 sx = *(const u32x4*)(ssmk + (c0+(t >> 8))*L_ + 8*(t & 255));
    dsk = *(const u32x2*)(ssmD + c0 + vz);
    __builtin_amdgcn_sched_barrier(0);
    if (t < 2) *(u32x4*)(ks + 16*t) = u32x4{0,0,0,0};
    __syncthreads();
    TSP(1);
    u32x2 kk[5];
    kk[0] = *(const u32x2*)(ks + 8 + 16*tr);
    { u32x4 x = *(const u32x4*)(ks + 16 + 16*tr); kk[1] = u32x2{x.x, x.y}; kk[2] = u32x2{x.z, x.w}; }
    kk[3] = k0; kk[4] = k1;
    u32x2 o[2];
    #pragma unroll
    for (int j=0;j<2;j++){
      const u32x2 vv = j ? v1 : v0;
      #pragma unroll
      for (int p=0;p<2;p++){
        float r2[2];
        #pragma unroll
        for (int e=0;e<2;e++){
          const u32x4 ww = p ? wb : wa;
          const uint32_t wl = e ? ww.z : ww.x, wh = e ? ww.w : ww.y;
          const float w0 = lo16f(wl), w1 = hi16f(wl), w2 = lo16f(wh), w3 = hi16f(wh);
          auto g = [&](int idx){ uint32_t x = kk[idx][p]; return e? hi16f(x) : lo16f(x); };
          float acc = rbf(g(j)*w3);
          acc = rbf(acc + rbf(g(j+1)*w2));
          acc = rbf(acc + rbf(g(j+2)*w1));
          acc = rbf(acc + rbf(g(j+3)*w0));
          r2[e] = acc * (e? hi16f(vv[p]) : lo16f(vv[p]));
        }
        o[j][p] = pk2(r2[0], r2[1]);
      }
    }
    put_u(smem, tr, o[0], o[1]);
    {
      const int ch = t >> 8, j = t & 255;
      u32x4 r;
      r.x = __builtin_amdgcn_alignbit(sx.w, sx.w, 16); r.y = __builtin_amdgcn_alignbit(sx.z, sx.z, 16);
      r.z = __builtin_amdgcn_alignbit(sx.y, sx.y, 16); r.w = __builtin_amdgcn_alignbit(sx.x, sx.x, 16);
      char* kb = smem + TOFF + ch*KST;
      *(u32x4*)(kb + 16*(j+15)) = r;
      if (j < 15) *(u32x4*)(kb + 16*j) = u32x4{0,0,0,0};
    }
    __syncthreads();
    TSP(2); TSP(3);
    if (wave >= 8){
      const int r4 = (((t - 512) + 32*(w5 & 15)) & 511)*4;
      u32x2 a[4];
      #pragma unroll
      for (int i=0;i<4;i++) a[i] = *(const u32x2*)(q + (r4+i)*D_ + c0);
      *(u32x4*)(smem + QOFF + 8*r4)      = u32x4{a[0].x, a[0].y, a[1].x, a[1].y};
      *(u32x4*)(smem + QOFF + 8*r4 + 16) = u32x4{a[2].x, a[2].y, a[3].x, a[3].y};
    }
  }
  if (isA){
    const int ch = wave >> 2, role = (wave + ch) & 3;
    if (role == 0) near_conv<4,0>(smem, ch, m, kg);
    else if (role == 1) near_conv<4,4>(smem, ch, m, kg);
    else if (role == 2){ __builtin_amdgcn_s_setprio(3); far_conv<0,4>(smem, ch, m, kg); __builtin_amdgcn_s_setprio(0); }
    else { __builtin_amdgcn_s_setprio(3); far_conv<4,6>(smem, ch, m, kg); __builtin_amdgcn_s_setprio(0); }
    TSP(5);
  } else if (wave < 8){
    const int ch = wave >> 1;
    const int g = wave & 1;
    const int part = 0;
    const int unit = ch;
    const uint32_t kbase = (uint32_t)(uintptr_t)smem + TOFF + unit*KST + 16*(m - kg + 3);
    const char* ub = smem + ch*UST;
    bf16x8 KA[4], KB[4];
    LDK4(KA, 15);
    f32x4 acc[4];
    {
      bf16x8 Up[2][4];
      {
        const uint32_t sel = (m & 1) ? 0x07060504u : 0x05040302u;
        const char* pp = ub + 272*(m >> 1) + 16*kg + 8*g;
        #pragma unroll
        for (int c=0;c<4;c++){ W6 W = uwin(pp + 64*c); Up[0][c] = usel(W, 0, sel); Up[1][c] = usel(W, 1, sel); }
      }
      f32x4 P0 = {0,0,0,0}, P1 = {0,0,0,0};
#define PH1(dl, CUR, NXT) \
      LDK4(NXT, (dl)-1); WAITK(4, CUR); \
      if ((dl) < 15){ P0 = shrN2(P0); P1 = shrN2(P1); } \
      P0 = MFMA16(CUR[0], Up[0][0], P0); P1 = MFMA16(CUR[0], Up[1][0], P1); \
      P0 = MFMA16(CUR[1], Up[0][1], P0); P1 = MFMA16(CUR[1], Up[1][1], P1); \
      P0 = MFMA16(CUR[2], Up[0][2], P0); P1 = MFMA16(CUR[2], Up[1][2], P1); \
      P0 = MFMA16(CUR[3], Up[0][3], P0); P1 = MFMA16(CUR[3], Up[1][3], P1);
      PH1(15, KA, KB) PH1(14, KB, KA) PH1(13, KA, KB) PH1(12, KB, KA)
      PH1(11, KA, KB) PH1(10, KB, KA) PH1(9, KA, KB) PH1(8, KB, KA)
      const bool ok = m >= 1 && m <= 8;
      const int src = 4*((lane & 48) + ((2*(m-1)) & 15));
      #pragma unroll
      for (int i=0;i<4;i++){
        float a0 = i2f(__builtin_amdgcn_ds_bpermute(src,   f2i(P0[i])));
        float a1 = i2f(__builtin_amdgcn_ds_bpermute(src+4, f2i(P0[i])));
        float a2 = i2f(__builtin_amdgcn_ds_bpermute(src,   f2i(P1[i])));
        float a3 = i2f(__builtin_amdgcn_ds_bpermute(src+4, f2i(P1[i])));
        acc[0][i] = ok ? a0 : 0.f; acc[1][i] = ok ? a1 : 0.f; acc[2][i] = ok ? a2 : 0.f; acc[3][i] = ok ? a3 : 0.f;
      }
    }
    TSP(4);
    bf16x8 U[4][4];
    {
      const char* pp = ub + 272*m + 16*kg + 8*g;
      #pragma unroll
      for (int c=0;c<4;c++){ W6 W = uwin(pp + 64*c); U[0][c] = uodd(W, 0); U[1][c] = uevn(W, 1); U[2][c] = uodd(W, 1); U[3][c] = uevn(W, 2); }
    }
#define PH2(dl, CUR, NXT) \
    if ((dl) > 0){ LDK4(NXT, (dl)-1); WAITK(4, CUR); } else { WAITK(0, CUR); } \
    if ((dl) < 7){ acc[0] = shrN1(acc[0]); acc[1] = shrN1(acc[1]); acc[2] = shrN1(acc[2]); acc[3] = shrN1(acc[3]); } \
    _Pragma("unroll") for (int c=0;c<4;c++){ \
      acc[0] = MFMA16(CUR[c], U[0][c], acc[0]); acc[1] = MFMA16(CUR[c], U[1][c], acc[1]); \
      acc[2] = MFMA16(CUR[c], U[2][c], acc[2]); acc[3] = MFMA16(CUR[c], U[3][c], acc[3]); }
    PH2(7, KA, KB) PH2(6, KB, KA) PH2(5, KA, KB) PH2(4, KB, KA)
    PH2(3, KA, KB) PH2(2, KB, KA) PH2(1, KA, KB) PH2(0, KB, KA)
    TSP(5);
    char* yb = smem + YOFF + (part*4 + ch)*YST + 4*(4*g + 32*kg + 132*m);
    #pragma unroll
    for (int i=0;i<4;i++) *(f32x4*)(yb + 32*i) = f32x4{acc[0][i], acc[1][i], acc[2][i], acc[3][i]};
  }
  __syncthreads();
  TSP(6);
  {
    const char* yb = smem + YOFF + 8*tr + 16*(tr >> 6);
    const char* up = smem + 272*(tr >> 6) + 16 + 4*(tr & 63);
    float o0[4], o1[4];
    if (isA){
      const char* ya = yb + (YAOFF - YOFF);
      const char* yf = smem + YFOFF + (tr >= 257 ? 4*(2*tr - 515) : -16);
      #pragma unroll
      for (int c=0;c<4;c++){
        f32x2 a = *(const f32x2*)(ya + c*YST);
        float b0 = *(const float*)(yf + c*YFST), b1 = *(const float*)(yf + c*YFST + 4);
        uint32_t uu = *(const uint32_t*)(up + c*UST);
        float sd = (c & 1) ? hi16f(dsk[c>>1]) : lo16f(dsk[c>>1]);
        o0[c] = rbf(a.x + b0) + rbf(lo16f(uu)*sd);
        o1[c] = rbf(a.y + b1) + rbf(hi16f(uu)*sd);
      }
    } else {
      u32x4 qq = *(const u32x4*)(smem + QOFF + 16*tr);
      #pragma unroll
      for (int c=0;c<4;c++){
        f32x2 a = *(const f32x2*)(yb + c*YST);
        uint32_t uu = *(const uint32_t*)(up + c*UST);
        float sd = (c & 1) ? hi16f(dsk[c>>1]) : lo16f(dsk[c>>1]);
        float q0 = (c & 1) ? hi16f(qq[c>>1]) : lo16f(qq[c>>1]);
        float q1 = (c & 1) ? hi16f(qq[2+(c>>1)]) : lo16f(qq[2+(c>>1)]);
        o0[c] = q0 * rbf(rbf(a.x) + rbf(lo16f(uu)*sd));
        o1[c] = q1 * rbf(rbf(a.y) + rbf(hi16f(uu)*sd));
      }
    }
    u16* op = out + (isA ? 0 : D_) + c0;
    *(u32x2*)(op + (2*tr)*(2*D_))   = u32x2{pk2(o0[0],o0[1]), pk2(o0[2],o0[3])};
    *(u32x2*)(op + (2*tr+1)*(2*D_)) = u32x2{pk2(o1[0],o1[1]), pk2(o1[2],o1[3])};
  }
  TSP(7);
  if (!isA){
    unsigned long long dlt = (tsv[2]-tsv[1])*8;
    unsigned long long tgt = tsv[7] + dlt;
    while (__builtin_amdgcn_s_memrealtime() < tgt) __builtin_amdgcn_s_sleep(1);
  }
}

extern "C" void kernel_launch(void* const* d_in, const int* in_sizes, int n_in,
                              void* d_out, int out_size, void* d_ws, size_t ws_size,
                              hipStream_t stream) {
  mono_kernel<<<256, 1024, LDS_TOTAL, stream>>>((const u16*)d_in[0], (const u16*)d_in[1], (const u16*)d_in[2], (const u16*)d_in[3],
     (const u16*)d_in[4], (const u16*)d_in[5], (const u16*)d_in[6], (const u16*)d_in[7], (const u16*)d_in[8], (const u16*)d_in[9],
     (const u16*)d_in[10], (const u16*)d_in[11], (const u16*)d_in[12], (u16*)d_out
);
}
```

```cpp
#include <hip/hip_runtime.h>
#include <hip/hip_bf16.h>
#include <stdint.h>

typedef unsigned short u16;
typedef short bf16x8 __attribute__((ext_vector_type(8)));
typedef float f32x4 __attribute__((ext_vector_type(4)));
typedef float f32x2 __attribute__((ext_vector_type(2)));
typedef __bf16 bf16x2_t __attribute__((ext_vector_type(2)));

#define L_ 2048
#define D_ 512
#define NS_ 128

__device__ __forceinline__ float bf2f(u16 x){ return __builtin_bit_cast(float, (uint32_t)x << 16); }
__device__ __forceinline__ u16 f2bf(float f){ __hip_bfloat16 h = __float2bfloat16(f); return __builtin_bit_cast(u16, h); }
__device__ __forceinline__ float rbf(float f){ return bf2f(f2bf(f)); }
__device__ __forceinline__ uint32_t pk2(float a, float b){
  f32x2 v = {a,b}; bf16x2_t r = __builtin_convertvector(v, bf16x2_t); return __builtin_bit_cast(uint32_t, r); }
__device__ __forceinline__ float lo16f(uint32_t w){ return __builtin_bit_cast(float, w << 16); }
__device__ __forceinline__ float hi16f(uint32_t w){ return __builtin_bit_cast(float, w & 0xffff0000u); }

struct cpx { float re, im; };
__device__ __forceinline__ float amul(float a, float b){ float r; asm("v_mul_f32 %0, %1, %2" : "=v"(r) : "v"(a), "v"(b)); return r; }
__device__ __forceinline__ float afma(float a, float b, float c){ float r; asm("v_fma_f32 %0, %1, %2, %3" : "=v"(r) : "v"(a), "v"(b), "v"(c)); return r; }
__device__ __forceinline__ float afms(float a, float b, float c){ float r; asm("v_fma_f32 %0, %1, %2, -%3" : "=v"(r) : "v"(a), "v"(b), "v"(c)); return r; }
__device__ __forceinline__ cpx cmul(cpx a, cpx b){
  f32x2 A = {a.re, a.im}, B = {b.re, b.im}, T, R;
  asm("v_pk_mul_f32 %0, %1, %2 op_sel:[1,1] op_sel_hi:[1,0] neg_lo:[1,0]" : "=v"(T) : "v"(A), "v"(B));
  asm("v_pk_fma_f32 %0, %1, %2, %3 op_sel:[0,0,0] op_sel_hi:[0,1,1]" : "=v"(R) : "v"(A), "v"(B), "v"(T));
  cpx r; r.re = R.x; r.im = R.y; return r; }
__device__ __forceinline__ cpx cexp_(float re, float im){ float e = expf(re); float s,c; sincosf(im,&s,&c); cpx r; r.re=e*c; r.im=e*s; return r; }

#define MFMA16(a,b,c) __builtin_amdgcn_mfma_f32_16x16x32_bf16(a,b,c,0,0,0)
typedef uint32_t u32x2 __attribute__((ext_vector_type(2)));
typedef uint32_t u32x4 __attribute__((ext_vector_type(4)));
#define KC_ 656
#define TR 288
#define TLO (57*TR)
#define TSZ (2*TLO)
#define UST 4352
#define KCOFF 17408
#define TOFF 27904
#define KST 4352
#define KS2 1536
#define FTOFF (TOFF + 12288)
#define FCS 320
#define YFOFF (TOFF + 45056 + 16)
#define YFST 6176
#define YAOFF (TOFF + 69888)
#define YOFF (TOFF + 36864)
#define YST 8448
#define QOFF (TOFF + 112128)
#define CNTOFF (TOFF + 4*TSZ)
#define LDS_TOTAL (CNTOFF + 64)

__device__ __forceinline__ uint32_t resid2(uint32_t h, float a, float b){
  return pk2(a - lo16f(h), b - hi16f(h));
}
__device__ __forceinline__ void put_split(char* p, float a, float b){
  uint32_t h = pk2(a,b);
  uint32_t l = resid2(h, a, b);
  *(uint32_t*)p = h; *(uint32_t*)(p + TLO) = l;
}
__device__ __forceinline__ int f2i(float x){ return __builtin_bit_cast(int, x); }
__device__ __forceinline__ float i2f(int x){ return __builtin_bit_cast(float, x); }
__device__ __forceinline__ f32x4 shrN1(f32x4 x){
  f32x4 r;
  #pragma unroll
  for (int i=0;i<4;i++) r[i] = i2f(__builtin_amdgcn_update_dpp(0, f2i(x[i]), 0x111, 0xf, 0xf, true));
  return r;
}
__device__ __forceinline__ f32x4 shrN2(f32x4 x){
  f32x4 r;
  #pragma unroll
  for (int i=0;i<4;i++) r[i] = i2f(__builtin_amdgcn_update_dpp(0, f2i(x[i]), 0x112, 0xf, 0xf, true));
  return r;
}
struct W6 { uint32_t w[6]; };
__device__ __forceinline__ W6 uwin(const char* p){
  u32x2 a = *(const u32x2*)p, b = *(const u32x2*)(p+8), c = *(const u32x2*)(p+16);
  W6 r; r.w[0]=a.x; r.w[1]=a.y; r.w[2]=b.x; r.w[3]=b.y; r.w[4]=c.x; r.w[5]=c.y; return r;
}
__device__ __forceinline__ bf16x8 usel(const W6& W, int x, uint32_t sel){
  u32x4 r;
  r.x = __builtin_amdgcn_perm(W.w[x+1], W.w[x],   sel); r.y = __builtin_amdgcn_perm(W.w[x+2], W.w[x+1], sel);
  r.z = __builtin_amdgcn_perm(W.w[x+3], W.w[x+2], sel); r.w = __builtin_amdgcn_perm(W.w[x+4], W.w[x+3], sel);
  return __builtin_bit_cast(bf16x8, r);
}
__device__ __forceinline__ bf16x8 uodd(const W6& W, int x){
  u32x4 r;
  r.x = __builtin_amdgcn_alignbit(W.w[x+1], W.w[x],   16); r.y = __builtin_amdgcn_alignbit(W.w[x+2], W.w[x+1], 16);
  r.z = __builtin_amdgcn_alignbit(W.w[x+3], W.w[x+2], 16); r.w = __builtin_amdgcn_alignbit(W.w[x+4], W.w[x+3], 16);
  return __builtin_bit_cast(bf16x8, r);
}
__device__ __forceinline__ bf16x8 uevn(const W6& W, int x){
  u32x4 r = {W.w[x], W.w[x+1], W.w[x+2], W.w[x+3]};
  return __builtin_bit_cast(bf16x8, r);
}
__device__ __forceinline__ void put_u(char* smem, int tr, u32x2 r0, u32x2 r1){
  uint32_t d[4];
  d[0] = __builtin_amdgcn_perm(r1.x, r0.x, 0x05040100); d[1] = __builtin_amdgcn_perm(r1.x, r0.x, 0x07060302);
  d[2] = __builtin_amdgcn_perm(r1.y, r0.y, 0x05040100); d[3] = __builtin_amdgcn_perm(r1.y, r0.y, 0x07060302);
  const int a = tr >> 6, pos = tr & 63;
  char* p = smem + 272*a + 16 + 4*pos;
  #pragma unroll
  for (int c=0;c<4;c++) *(uint32_t*)(p + c*UST) = d[c];
  if (pos >= 60 && a < 15){
    #pragma unroll
    for (int c=0;c<4;c++) *(uint32_t*)(p + c*UST + 16) = d[c];
  }
  if (tr < 16) *(uint32_t*)(smem + (tr>>2)*UST + 4*(tr&3)) = 0u;
}

#define LDK(F, dl, c) asm volatile("ds_read_b128 %0, %1 offset:%2" : "=v"(F) : "v"(kbase), "i"(256*(dl) + 64*(3-(c))))
#define LDK4(S, dl) LDK(S[0], dl, 0); LDK(S[1], dl, 1); LDK(S[2], dl, 2); LDK(S[3], dl, 3)
#define WAITK(n, S) asm volatile("s_waitcnt lgkmcnt(" #n ")" : "+v"(S[0]), "+v"(S[1]), "+v"(S[2]), "+v"(S[3]))

template<int NT, int R0>
__device__ __forceinline__ void near_conv(char* smem, int ch, int m, int kg){
  const uint32_t kbase = (uint32_t)(uintptr_t)smem + TOFF + (2*ch)*KS2 + 16*(m - kg + 3);
#define LDN(F, hs, c) asm volatile("ds_read_b128 %0, %1 offset:%2" : "=v"(F) : "v"(kbase), "i"(256*(4-(hs)) + 64*(3-(c))))
#define LDN4(S, hs) LDN(S[0], hs, 0); LDN(S[1], hs, 1); LDN(S[2], hs, 2); LDN(S[3], hs, 3)
  bf16x8 KA[4], KB[4];
  LDN4(KA, 0);
  bf16x8 U[NT][4];
  {
    const char* pp = smem + ch*UST + 272*m + 16*kg + (R0 ? 8 : 0);
    #pragma unroll
    for (int c=0;c<4;c++){
      W6 W = uwin(pp + 64*c);
      #pragma unroll
      for (int r=0;r<NT;r++){ const int rel = R0 + r + 1 - (R0 ? 4 : 0); U[r][c] = (rel & 1) ? uodd(W, rel >> 1) : uevn(W, rel >> 1); }
    }
  }
  f32x4 acc[NT];
  #pragma unroll
  for (int r=0;r<NT;r++) acc[r] = f32x4{0,0,0,0};
#define NH(hs, CUR, NXT) \
  if ((hs) < 4){ LDN4(NXT, (hs)+1); WAITK(4, CUR); } else { WAITK(0, CUR); } \
  if ((hs) > 0){ _Pragma("unroll") for (int r=0;r<NT;r++) acc[r] = shrN1(acc[r]); } \
  _Pragma("unroll") for (int c=0;c<4;c++){ _Pragma("unroll") for (int r=0;r<NT;r++) acc[r] = MFMA16(CUR[c], U[r][c], acc[r]); }
  NH(0, KA, KB) NH(1, KB, KA) NH(2, KA, KB) NH(3, KB, KA) NH(4, KA, KB)
  float* yb = (float*)(smem + YAOFF + ch*YST) + (R0 + 32*kg + 132*m);
  #pragma unroll
  for (int i=0;i<4;i++){
    #pragma unroll
    for (int r=0;r<NT;r++) yb[8*i + r] = acc[r][i];
  }
}
__device__ __forceinline__ bf16x8 band(int sh, int w, int kg){
  const uint32_t by = (((1u << w) - 1u) << sh) >> (8*kg);
  u32x4 r;
  #pragma unroll
  for (int k2=0;k2<4;k2++){ uint32_t x = (by >> (2*k2)) & 3u; x = (x | (x << 15)) & 0x00010001u; r[k2] = x * 0x3f80u; }
  return __builtin_bit_cast(bf16x8, r);
}
__device__ __forceinline__ void split2(const f32x4& a, const f32x4& b, bf16x8& h, bf16x8& l){
  u32x4 hh, ll;
  #pragma unroll
  for (int i=0;i<4;i++){ uint32_t x = pk2(a[i], b[i]); hh[i] = x; ll[i] = resid2(x, a[i], b[i]); }
  h = __builtin_bit_cast(bf16x8, hh); l = __builtin_bit_cast(bf16x8, ll);
}
__device__ __forceinline__ bf16x8 pack2(const f32x4& a, const f32x4& b){
  u32x4 hh;
  #pragma unroll
  for (int i=0;i<4;i++) hh[i] = pk2(a[i], b[i]);
  return __builtin_bit_cast(bf16x8, hh);
}
template<int A0, int A1>
__device__ __forceinline__ void far_conv(char* smem, int ch, int m, int kg){
  const char* ub = smem + ch*UST;
  const f32x4 Z4 = {0,0,0,0};
  const int B0 = 2*m + kg - 2;
  const char* va0 = ub + (B0 < 0 ? 0 : 272*(B0 >> 4) + 16 + 16*(B0 & 15));
  const char* va1 = ub + 272*((B0+32) >> 4) + 16 + 16*((B0+32) & 15);
  const int cp = (3 - m) & 3;
  const char* ft = smem + FTOFF + ch*8192 + cp*FCS + 4*(47 - m + 4*kg - cp);
  char* yf = smem + YFOFF + ch*YFST + 64*m + 16*kg;
  f32x4 acc[6];
  #pragma unroll
  for (int a=0;a<6;a++) acc[a] = Z4;
  {
    bf16x8 M[4];
    M[0] = band(14 + m, 3, kg); M[1] = band(9 + m, 5, kg); M[2] = band(6 + m, 3, kg); M[3] = band(1 + m, 5, kg);
    bf16x8 Ch[3][2];
    #pragma unroll
    for (int dl=0;dl<3;dl++){
      #pragma unroll
      for (int h=0;h<2;h++) Ch[dl][h] = *(const bf16x8*)(ft + h*(8*FCS) - 64*dl);
    }
    #pragma unroll
    for (int b=(A0 > 2 ? A0-2 : 0);b<A1;b++){
      const bf16x8 V = *(const bf16x8*)(b ? va1 + 544*(b-1) : va0);
      f32x4 F[4];
      #pragma unroll
      for (int p=0;p<4;p++) F[p] = MFMA16(V, M[p], Z4);
      #pragma unroll
      for (int h=0;h<2;h++){
        const bf16x8 Bh = pack2(F[2*h], F[2*h+1]);
        #pragma unroll
        for (int dl=0;dl<3;dl++) if (b + dl >= A0 && b + dl < A1) acc[b+dl] = MFMA16(Bh, Ch[dl][h], acc[b+dl]);
      }
      if (b < 2 && b >= A0) *(f32x4*)(yf + 1024*b) = acc[b];
    }
  }
  {
    const char* vb0 = ub + (B0 < 1 ? 0 : 272*((B0-1) >> 4) + 16 + 16*((B0-1) & 15));
    const char* vb1 = ub + 272*((B0+31) >> 4) + 16 + 16*((B0+31) & 15);
    const char* f8 = smem + FTOFF + ch*8192 + 16*FCS + cp*FCS + 4*(63 - m + 4*kg - cp);
    const bf16x8 Ma = band(8 + m, 7, kg), Mb = band(7 + m, 9, kg), Mc = band(15 + m, 2, kg);
    const float k1024 = ((const float*)(smem + KCOFF))[ch*KC_ + 512];
    bf16x8 Ch[4];
    #pragma unroll
    for (int dl=0;dl<A1-2;dl++) Ch[dl] = *(const bf16x8*)(f8 - 64*dl);
    #pragma unroll
    for (int b=0;b<A1-2;b++){
      const bf16x8 V = *(const bf16x8*)(b ? va1 + 544*(b-1) : va0);
      const bf16x8 V2 = *(const bf16x8*)(b ? vb1 + 544*(b-1) : vb0);
      const f32x4 Fa = MFMA16(V, Ma, Z4), Fb = MFMA16(V2, Mb, Z4);
      const bf16x8 Bh = pack2(Fa, Fb);
      if (b + 2 >= A0){
        const f32x4 Fc = MFMA16(Mc, V, Z4);
        #pragma unroll
        for (int i=0;i<4;i++) acc[b+2][i] = __builtin_fmaf(k1024, Fc[i], acc[b+2][i]);
      }
      #pragma unroll
      for (int dl=0;dl<4;dl++) if (b + 2 + dl >= A0 && b + 2 + dl < A1) acc[b+2+dl] = MFMA16(Bh, Ch[dl], acc[b+2+dl]);
    }
  }
  #pragma unroll
  for (int a=(A0 > 2 ? A0 : 2);a<A1;a++) *(f32x4*)(yf + 1024*a) = acc[a];
}

__global__ __launch_bounds__(1024) void mono_kernel(const u16* __restrict__ u, const u16* __restrict__ logA, const u16* __restrict__ Aim,
    const u16* __restrict__ Cre, const u16* __restrict__ Cim, const u16* __restrict__ logdt, const u16* __restrict__ Dskip,
    const u16* __restrict__ q, const u16* __restrict__ k, const u16* __restrict__ v, const u16* __restrict__ shift_k,
    const u16* __restrict__ ssmk, const u16* __restrict__ ssmD, u16* __restrict__ out
){
#define TSP(i)
  TSP(0); TSP(1); TSP(2); TSP(3); TSP(4); TSP(5);
  extern __shared__ __attribute__((aligned(16))) char smem[];
  const int bid = blockIdx.x, t = threadIdx.x;
  const int w5 = bid >> 3;
  const bool isA = w5 < 16;
  const int c0 = (bid & 7)*64 + (w5 & 15)*4;
  const int tr = (t + 64*(w5 & 15)) & 1023;
  const int wave = t >> 6, lane = t & 63, m = lane & 15, kg = lane >> 4;
  asm volatile("" :: "s"(u), "s"(logA), "s"(Aim), "s"(Cre), "s"(Cim), "s"(logdt), "s"(Dskip), "s"(q), "s"(k), "s"(v), "s"(shift_k), "s"(ssmk), "s"(ssmD), "s"(out));
  u32x2 dsk;
  if (t == 0) *(u32x2*)(smem + CNTOFF) = u32x2{0u, 0u};
  int vz; asm volatile("v_mov_b32 %0, 0" : "=v"(vz));
  if (isA){
    const int ch = t >> 8, tg = t & 255, wv = (wave + ch) & 3;
    const int d = c0 + ch;
    char* tb = smem + TOFF + ch*TSZ;
    char* eb_ = smem + KCOFF + ch*(4*KC_);
    const u16 ldt = logdt[d];
    const u16 pla = logA[d*NS_+(tg & 127)], pai = Aim[d*NS_+(tg & 127)];
    u16 pc[2][2];
    #pragma unroll
    for (int hp=0;hp<2;hp++){ pc[hp][0] = Cre[d*NS_+64*hp+lane];  pc[hp][1] = Cim[d*NS_+64*hp+lane]; }
    const float dt = rbf(expf(bf2f(ldt)));
    {
      const float zre = -rbf(expf(bf2f(pla))) * dt;
      const float zim = bf2f(pai) * dt;
      const float f = (tg >> 7) ? 128.f : 1.f;
      cpx e = cexp_(zre*f, zim*f);
      *(float2*)(eb_ + 8*tg) = make_float2(e.re, e.im);
    }
    __syncthreads();
    TSP(1);
    __builtin_amdgcn_sched_barrier(0);
    const u32x2 u0 = *(const u32x2*)(u + (2*tr)*D_ + c0);
    const u32x2 u1 = *(const u32x2*)(u + (2*tr+1)*D_ + c0);
    dsk = *(const u32x2*)(Dskip + c0 + vz);
    __builtin_amdgcn_sched_barrier(0);
    f32x4 a4 = {0,0,0,0};
    char* col = tb + 4*lane;
    #pragma unroll 1
    for (int hp=0;hp<2;hp++){
      cpx C; C.re = bf2f(hp ? pc[1][0] : pc[0][0])*dt; C.im = bf2f(hp ? pc[1][1] : pc[0][1])*dt;
      cpx e1, e2;
      { float2 a = *(const float2*)(eb_ + 8*(64*hp+lane)); e2.re = a.x; e2.im = a.y;
        float2 c = *(const float2*)(eb_ + 1024 + 8*(64*hp+lane)); e1.re = c.x; e1.im = c.y; }
      if (wv == 0){
        cpx e8 = cmul(e2, e2); e8 = cmul(e8, e8); e8 = cmul(e8, e8);
        cpx xx; xx.re = 1.f; xx.im = 0.f;
        #pragma unroll
        for (int i=0;i<16;i++){ put_split(col + (41+i)*TR, xx.re, -xx.im); if (i < 15) xx = (i == 0) ? e8 : cmul(xx, e8); }
      } else if (wv == 1){
        cpx eb = cmul(e1, e1); eb = cmul(eb, eb); eb = cmul(eb, eb);
        cpx cb = cmul(C, eb);
        #pragma unroll
        for (int i=0;i<9;i++){ put_split(col + (32+i)*TR, cb.re, cb.im); if (i < 8) cb = cmul(cb, e1); }
      } else if (wv == 2){
        cpx S[8];
        S[1] = e2;
        #pragma unroll
        for (int i=2;i<8;i++) S[i] = cmul(S[i-1], S[1]);
        cpx cb = C;
        #pragma unroll
        for (int a=0;a<2;a++){
          put_split(col + (8*a)*TR, cb.re, cb.im);
          #pragma unroll
          for (int p=1;p<8;p++){ cpx g = cmul(cb, S[p]); put_split(col + (8*a+p)*TR, g.re, g.im); }
          if (a == 0) cb = cmul(cb, e1);
        }
      } else {
        cpx S2 = cmul(e2, e2), S4 = cmul(S2, S2), S6 = cmul(S4, S2);
        cpx cb = cmul(C, cmul(e1, e1));
        #pragma unroll
        for (int a=0;a<2;a++){
          put_split(col + (16+4*a)*TR, cb.re, cb.im);
          { cpx g = cmul(cb, S2); put_split(col + (16+4*a+1)*TR, g.re, g.im); }
          { cpx g = cmul(cb, S4); put_split(col + (16+4*a+2)*TR, g.re, g.im); }
          { cpx g = cmul(cb, S6); put_split(col + (16+4*a+3)*TR, g.re, g.im); }
          cb = cmul(cb, e1);
        }
        #pragma unroll
        for (int a=0;a<4;a++){
          put_split(col + (24+2*a)*TR, cb.re, cb.im);
          cpx g = cmul(cb, S4); put_split(col + (24+2*a+1)*TR, g.re, g.im);
          if (a < 3) cb = cmul(cb, e1);
        }
      }
      __syncthreads();
      if (wv < 3){
        const char* ga = tb + (16*wv + m)*TR + 16*kg;
        const char* xb = tb + (41 + m)*TR + 16*kg;
        #pragma unroll
        for (int kc=0;kc<4;kc++){
          bf16x8 ah = *(const bf16x8*)(ga + 64*kc), al = *(const bf16x8*)(ga + 64*kc + TLO);
          bf16x8 bh = *(const bf16x8*)(xb + 64*kc), bl = *(const bf16x8*)(xb + 64*kc + TLO);
          a4 = MFMA16(ah, bh, a4);
          a4 = MFMA16(ah, bl, a4);
          a4 = MFMA16(al, bh, a4);
        }
      }
      if (hp == 0) __syncthreads();
    }
    float* Kc = (float*)(smem + KCOFF) + ch*KC_;
    if (wv < 3){
      #pragma unroll
      for (int i=0;i<4;i++){
        int r = 16*wv + 4*kg + i;
        int idx;
        if (r < 16) idx = 128*(r>>3) + (r&7) + 8*m;
        else if (r < 24) idx = 256 + 64*((r-16)>>2) + (r&3) + 4*m;
        else if (r < 32) idx = 384 + 32*((r-24)>>1) + (r&1) + 2*m;
        else idx = 512 + 16*(r-32) + m;
        if (r < 40 || (r == 40 && m == 0)) Kc[idx] = 2.f*a4[i];
      }
    }
    put_u(smem, tr, u0, u1);
    __syncthreads();
    TSP(2);
    {
      char* ft = smem + FTOFF + ch*8192;
      if (tg < 208){
        int z, idx; bool ok; char* wp;
        if (tg < 128){ z = tg & 63; const int Dd = 47 - z; ok = Dd >= 0 && Dd < 32; idx = 385 + 4*Dd + 2*(tg >> 6); wp = ft + (tg >> 6)*(8*FCS); }
        else { z = tg - 128; const int Dd = 63 - z; ok = Dd >= 0; idx = 513 + 2*Dd; wp = ft + 16*FCS; }
        float a = 0.f, b = 0.f;
        if (ok){ a = Kc[idx]; b = Kc[idx+1]; }
        const uint32_t hh = pk2(a, b);
        #pragma unroll
        for (int c=0;c<4;c++) if (z >= c) *(uint32_t*)(wp + c*FCS + 4*(z-c)) = hh;
      }
      if (tg >= 161){
        const int sl = tg - 161, j = sl - 15;
        float kv[8];
        #pragma unroll
        for (int i=0;i<8;i++){
          int l = 8*j + 7 - i;
          int pr = (int)rbf((float)l);
          int p = pr < 256 ? pr : 128 + (pr>>1);
          kv[i] = (l >= 0 && l <= 514) ? Kc[p] : 0.f;
        }
        u32x4 h;
        #pragma unroll
        for (int jj=0;jj<4;jj++) h[jj] = pk2(kv[2*jj], kv[2*jj+1]);
        char* kb = smem + TOFF + (2*ch)*KS2;
        *(u32x4*)(kb + 16*sl) = h;
      }
      if (tg < 4) *(float*)(smem + YFOFF + ch*YFST - 16 + 4*tg) = 0.f;
    }
    __syncthreads();
    TSP(3);
  } else {
    __builtin_amdgcn_sched_barrier(0);
    const u32x2 k0 = *(const u32x2*)(k + (2*tr)*D_ + c0);
    const u32x2 k1 = *(const u32x2*)(k + (2*tr+1)*D_ + c0);
    const u32x4 wa = *(const u32x4*)(shift_k + c0*4), wb = *(const u32x4*)(shift_k + c0*4 + 8);
    char* ks = smem + YOFF;
    __builtin_amdgcn_sched_barrier(0);
    *(u32x4*)(ks + 32 + 16*tr) = u32x4{k0.x, k0.y, k1.x, k1.y};
    __builtin_amdgcn_sched_barrier(0);
    const u32x2 v0 = *(const u32x2*)(v + (2*tr)*D_ + c0);
    const u32x2 v1 = *(const u32x2*)(v + (2*tr+1)*D_ + c0);
    const u32x4 sx = *(const u32x4*)(ssmk + (c0+(t >> 8))*L_ + 8*(t & 255));
    dsk = *(const u32x2*)(ssmD + c0 + vz);
    __builtin_amdgcn_sched_barrier(0);
    if (t < 2) *(u32x4*)(ks + 16*t) = u32x4{0,0,0,0};
    __syncthreads();
    TSP(1);
    u32x2 kk[5];
    kk[0] = *(const u32x2*)(ks + 8 + 16*tr);
    { u32x4 x = *(const u32x4*)(ks + 16 + 16*tr); kk[1] = u32x2{x.x, x.y}; kk[2] = u32x2{x.z, x.w}; }
    kk[3] = k0; kk[4] = k1;
    u32x2 o[2];
    #pragma unroll
    for (int j=0;j<2;j++){
      const u32x2 vv = j ? v1 : v0;
      #pragma unroll
      for (int p=0;p<2;p++){
        float r2[2];
        #pragma unroll
        for (int e=0;e<2;e++){
          const u32x4 ww = p ? wb : wa;
          const uint32_t wl = e ? ww.z : ww.x, wh = e ? ww.w : ww.y;
          const float w0 = lo16f(wl), w1 = hi16f(wl), w2 = lo16f(wh), w3 = hi16f(wh);
          auto g = [&](int idx){ uint32_t x = kk[idx][p]; return e? hi16f(x) : lo16f(x); };
          float acc = rbf(g(j)*w3);
          acc = rbf(acc + rbf(g(j+1)*w2));
          acc = rbf(acc + rbf(g(j+2)*w1));
          acc = rbf(acc + rbf(g(j+3)*w0));
          r2[e] = acc * (e? hi16f(vv[p]) : lo16f(vv[p]));
        }
        o[j][p] = pk2(r2[0], r2[1]);
      }
    }
    put_u(smem, tr, o[0], o[1]);
    {
      const int ch = t >> 8, j = t & 255;
      u32x4 r;
      r.x = __builtin_amdgcn_alignbit(sx.w, sx.w, 16); r.y = __builtin_amdgcn_alignbit(sx.z, sx.z, 16);
      r.z = __builtin_amdgcn_alignbit(sx.y, sx.y, 16); r.w = __builtin_amdgcn_alignbit(sx.x, sx.x, 16);
      char* kb = smem + TOFF + ch*KST;
      *(u32x4*)(kb + 16*(j+15)) = r;
      if (j < 15) *(u32x4*)(kb + 16*j) = u32x4{0,0,0,0};
    }
    __syncthreads();
    TSP(2); TSP(3);
  }
  int sig = -1, fi = wave - 8;
  if (isA){
    const int ch = wave >> 2, role = (wave + ch) & 3;
    if (role == 0){ __builtin_amdgcn_s_setprio(2); near_conv<4,0>(smem, ch, m, kg); }
    else if (role == 1) near_conv<4,4>(smem, ch, m, kg);
    else if (role == 2){ __builtin_amdgcn_s_setprio(3); far_conv<0,4>(smem, ch, m, kg); }
    else { __builtin_amdgcn_s_setprio(3); far_conv<4,6>(smem, ch, m, kg); }
    TSP(5);
    sig = role == 1;
    fi = role >= 2 ? 2*ch + role - 2 : -1;
  } else if (wave < 8){
    const int ch = wave >> 1;
    const int g = (wave ^ (wave >> 2)) & 1;
    if (g == 0) __builtin_amdgcn_s_setprio(2);
    sig = g;
    const int part = 0;
    const int unit = ch;
    const uint32_t kbase = (uint32_t)(uintptr_t)smem + TOFF + unit*KST + 16*(m - kg + 3);
    const char* ub = smem + ch*UST;
    bf16x8 KA[4], KB[4];
    LDK4(KA, 15);
    f32x4 acc[4];
    {
      bf16x8 Up[2][4];
      {
        const uint32_t sel = (m & 1) ? 0x07060504u : 0x05040302u;
        const char* pp = ub + 272*(m >> 1) + 16*kg + 8*g;
        #pragma unroll
        for (int c=0;c<4;c++){ W6 W = uwin(pp + 64*c); Up[0][c] = usel(W, 0, sel); Up[1][c] = usel(W, 1, sel); }
      }
      f32x4 P0 = {0,0,0,0}, P1 = {0,0,0,0};
#define PH1(dl, CUR, NXT) \
      LDK4(NXT, (dl)-1); WAITK(4, CUR); \
      if ((dl) < 15){ P0 = shrN2(P0); P1 = shrN2(P1); } \
      P0 = MFMA16(CUR[0], Up[0][0], P0); P1 = MFMA16(CUR[0], Up[1][0], P1); \
      P0 = MFMA16(CUR[1], Up[0][1], P0); P1 = MFMA16(CUR[1], Up[1][1], P1); \
      P0 = MFMA16(CUR[2], Up[0][2], P0); P1 = MFMA16(CUR[2], Up[1][2], P1); \
      P0 = MFMA16(CUR[3], Up[0][3], P0); P1 = MFMA16(CUR[3], Up[1][3], P1);
      PH1(15, KA, KB) PH1(14, KB, KA) PH1(13, KA, KB) PH1(12, KB, KA)
      PH1(11, KA, KB) PH1(10, KB, KA) PH1(9, KA, KB) PH1(8, KB, KA)
      const bool ok = m >= 1 && m <= 8;
      const int src = 4*((lane & 48) + ((2*(m-1)) & 15));
      #pragma unroll
      for (int i=0;i<4;i++){
        float a0 = i2f(__builtin_amdgcn_ds_bpermute(src,   f2i(P0[i])));
        float a1 = i2f(__builtin_amdgcn_ds_bpermute(src+4, f2i(P0[i])));
        float a2 = i2f(__builtin_amdgcn_ds_bpermute(src,   f2i(P1[i])));
        float a3 = i2f(__builtin_amdgcn_ds_bpermute(src+4, f2i(P1[i])));
        acc[0][i] = ok ? a0 : 0.f; acc[1][i] = ok ? a1 : 0.f; acc[2][i] = ok ? a2 : 0.f; acc[3][i] = ok ? a3 : 0.f;
      }
    }
    TSP(4);
    bf16x8 U[4][4];
    {
      const char* pp = ub + 272*m + 16*kg + 8*g;
      #pragma unroll
      for (int c=0;c<4;c++){ W6 W = uwin(pp + 64*c); U[0][c] = uodd(W, 0); U[1][c] = uevn(W, 1); U[2][c] = uodd(W, 1); U[3][c] = uevn(W, 2); }
    }
#define PH2(dl, CUR, NXT) \
    if ((dl) > 0){ LDK4(NXT, (dl)-1); WAITK(4, CUR); } else { WAITK(0, CUR); } \
    if ((dl) < 7){ acc[0] = shrN1(acc[0]); acc[1] = shrN1(acc[1]); acc[2] = shrN1(acc[2]); acc[3] = shrN1(acc[3]); } \
    _Pragma("unroll") for (int c=0;c<4;c++){ \
      acc[0] = MFMA16(CUR[c], U[0][c], acc[0]); acc[1] = MFMA16(CUR[c], U[1][c], acc[1]); \
      acc[2] = MFMA16(CUR[c], U[2][c], acc[2]); acc[3] = MFMA16(CUR[c], U[3][c], acc[3]); }
    PH2(7, KA, KB) PH2(6, KB, KA) PH2(5, KA, KB) PH2(4, KB, KA)
    PH2(3, KA, KB) PH2(2, KB, KA) PH2(1, KA, KB) PH2(0, KB, KA)
    TSP(5);
    char* yb = smem + YOFF + (part*4 + ch)*YST + 4*(4*g + 32*kg + 132*m);
    #pragma unroll
    for (int i=0;i<4;i++) *(f32x4*)(yb + 32*i) = f32x4{acc[0][i], acc[1][i], acc[2][i], acc[3][i]};
  }
  TSP(6);
  if (sig >= 0){
    __builtin_amdgcn_fence(__ATOMIC_RELEASE, "workgroup");
    if (lane == 0) __hip_atomic_fetch_add((unsigned*)(smem + CNTOFF) + sig, 1u, __ATOMIC_RELAXED, __HIP_MEMORY_SCOPE_WORKGROUP);
  }
  if (fi >= 0){
    const int p = (fi*64 + lane + 32*(w5 & 15)) & 511;
    u32x2 qr[4];
    if (!isA){
      const u16* qp = q + (8*(p >> 1) + 2*(p & 1))*D_ + c0;
      qr[0] = *(const u32x2*)(qp); qr[1] = *(const u32x2*)(qp + D_);
      qr[2] = *(const u32x2*)(qp + 4*D_); qr[3] = *(const u32x2*)(qp + 5*D_);
    }
    __builtin_amdgcn_sched_barrier(0);
    u16* op = out + (isA ? 0 : D_) + c0;
    const unsigned need0 = isA ? 12u : 4u;
    #pragma unroll
    for (int st=0; st<2; st++){
      const int tq = 4*(p >> 1) + (p & 1) + 2*st;
      const char* yb = smem + (isA ? YAOFF : YOFF) + 8*tq + 16*(tq >> 6);
      const char* up = smem + 272*(tq >> 6) + 16 + 4*(tq & 63);
      float s0[4], s1[4];
      #pragma unroll
      for (int c=0;c<4;c++){
        uint32_t uu = *(const uint32_t*)(up + c*UST);
        float sd = (c & 1) ? hi16f(dsk[c>>1]) : lo16f(dsk[c>>1]);
        s0[c] = rbf(lo16f(uu)*sd); s1[c] = rbf(hi16f(uu)*sd);
      }
      while (__hip_atomic_load((const unsigned*)(smem + CNTOFF) + st, __ATOMIC_RELAXED, __HIP_MEMORY_SCOPE_WORKGROUP) < (st ? 4u : need0)) __builtin_amdgcn_s_sleep(1);
      __builtin_amdgcn_fence(__ATOMIC_ACQUIRE, "workgroup");
      float o0[4], o1[4];
      if (isA){
        const char* yf = smem + YFOFF + (tq >= 257 ? 4*(2*tq - 515) : -16);
        #pragma unroll
        for (int c=0;c<4;c++){
          f32x2 a = *(const f32x2*)(yb + c*YST);
          float b0 = *(const float*)(yf + c*YFST), b1 = *(const float*)(yf + c*YFST + 4);
          o0[c] = rbf(a.x + b0) + s0[c];
          o1[c] = rbf(a.y + b1) + s1[c];
        }
      } else {
        #pragma unroll
        for (int c=0;c<4;c++){
          f32x2 a = *(const f32x2*)(yb + c*YST);
          float q0 = (c & 1) ? hi16f(qr[2*st][c>>1]) : lo16f(qr[2*st][c>>1]);
          float q1 = (c & 1) ? hi16f(qr[2*st+1][c>>1]) : lo16f(qr[2*st+1][c>>1]);
          o0[c] = q0 * rbf(rbf(a.x) + s0[c]);
          o1[c] = q1 * rbf(rbf(a.y) + s1[c]);
        }
      }
      *(u32x2*)(op + (2*tq)*(2*D_))   = u32x2{pk2(o0[0],o0[1]), pk2(o0[2],o0[3])};
      *(u32x2*)(op + (2*tq+1)*(2*D_)) = u32x2{pk2(o1[0],o1[1]), pk2(o1[2],o1[3])};
    }
  }
}

extern "C" void kernel_launch(void* const* d_in, const int* in_sizes, int n_in,
                              void* d_out, int out_size, void* d_ws, size_t ws_size,
                              hipStream_t stream) {
  mono_kernel<<<256, 1024, LDS_TOTAL, stream>>>((const u16*)d_in[0], (const u16*)d_in[1], (const u16*)d_in[2], (const u16*)d_in[3],
     (const u16*)d_in[4], (const u16*)d_in[5], (const u16*)d_in[6], (const u16*)d_in[7], (const u16*)d_in[8], (const u16*)d_in[9],
     (const u16*)d_in[10], (const u16*)d_in[11], (const u16*)d_in[12], (u16*)d_out
);
}
```

```cpp
#include <hip/hip_runtime.h>
#include <hip/hip_bf16.h>
#include <stdint.h>

typedef unsigned short u16;
typedef short bf16x8 __attribute__((ext_vector_type(8)));
typedef float f32x4 __attribute__((ext_vector_type(4)));
typedef float f32x2 __attribute__((ext_vector_type(2)));
typedef __bf16 bf16x2_t __attribute__((ext_vector_type(2)));

#define L_ 2048
#define D_ 512
#define NS_ 128

__device__ __forceinline__ float bf2f(u16 x){ return __builtin_bit_cast(float, (uint32_t)x << 16); }
__device__ __forceinline__ u16 f2bf(float f){ __hip_bfloat16 h = __float2bfloat16(f); return __builtin_bit_cast(u16, h); }
__device__ __forceinline__ float rbf(float f){ return bf2f(f2bf(f)); }
__device__ __forceinline__ uint32_t pk2(float a, float b){
  f32x2 v = {a,b}; bf16x2_t r = __builtin_convertvector(v, bf16x2_t); return __builtin_bit_cast(uint32_t, r); }
__device__ __forceinline__ float lo16f(uint32_t w){ return __builtin_bit_cast(float, w << 16); }
__device__ __forceinline__ float hi16f(uint32_t w){ return __builtin_bit_cast(float, w & 0xffff0000u); }

struct cpx { float re, im; };
__device__ __forceinline__ float amul(float a, float b){ float r; asm("v_mul_f32 %0, %1, %2" : "=v"(r) : "v"(a), "v"(b)); return r; }
__device__ __forceinline__ float afma(float a, float b, float c){ float r; asm("v_fma_f32 %0, %1, %2, %3" : "=v"(r) : "v"(a), "v"(b), "v"(c)); return r; }
__device__ __forceinline__ float afms(float a, float b, float c){ float r; asm("v_fma_f32 %0, %1, %2, -%3" : "=v"(r) : "v"(a), "v"(b), "v"(c)); return r; }
__device__ __forceinline__ cpx cmul(cpx a, cpx b){
  f32x2 A = {a.re, a.im}, B = {b.re, b.im}, T, R;
  asm("v_pk_mul_f32 %0, %1, %2 op_sel:[1,1] op_sel_hi:[1,0] neg_lo:[1,0]" : "=v"(T) : "v"(A), "v"(B));
  asm("v_pk_fma_f32 %0, %1, %2, %3 op_sel:[0,0,0] op_sel_hi:[0,1,1]" : "=v"(R) : "v"(A), "v"(B), "v"(T));
  cpx r; r.re = R.x; r.im = R.y; return r; }
__device__ __forceinline__ cpx cexp_(float re, float im){ float e = expf(re); float s,c; sincosf(im,&s,&c); cpx r; r.re=e*c; r.im=e*s; return r; }

#define MFMA16(a,b,c) __builtin_amdgcn_mfma_f32_16x16x32_bf16(a,b,c,0,0,0)
typedef uint32_t u32x2 __attribute__((ext_vector_type(2)));
typedef uint32_t u32x4 __attribute__((ext_vector_type(4)));
#define KC_ 656
#define TR 288
#define TLO (57*TR)
#define TSZ (2*TLO)
#define UST 4352
#define KCOFF 17408
#define TOFF 27904
#define KST 4352
#define KS2 1536
#define FTOFF (TOFF + 12288)
#define FCS 320
#define YFOFF (TOFF + 45056 + 16)
#define YFST 6176
#define YAOFF (TOFF + 69888)
#define YOFF (TOFF + 36864)
#define YST 8448
#define QOFF (TOFF + 112128)
#define CNTOFF (TOFF + 4*TSZ)
#define LDS_TOTAL (CNTOFF + 64)

__device__ __forceinline__ uint32_t resid2(uint32_t h, float a, float b){
  return pk2(a - lo16f(h), b - hi16f(h));
}
__device__ __forceinline__ void put_split(char* p, float a, float b){
  uint32_t h = pk2(a,b);
  uint32_t l = resid2(h, a, b);
  *(uint32_t*)p = h; *(uint32_t*)(p + TLO) = l;
}
__device__ __forceinline__ int f2i(float x){ return __builtin_bit_cast(int, x); }
__device__ __forceinline__ float i2f(int x){ return __builtin_bit_cast(float, x); }
__device__ __forceinline__ f32x4 shrN1(f32x4 x){
  f32x4 r;
  #pragma unroll
  for (int i=0;i<4;i++) r[i] = i2f(__builtin_amdgcn_update_dpp(0, f2i(x[i]), 0x111, 0xf, 0xf, true));
  return r;
}
__device__ __forceinline__ f32x4 shrN2(f32x4 x){
  f32x4 r;
  #pragma unroll
  for (int i=0;i<4;i++) r[i] = i2f(__builtin_amdgcn_update_dpp(0, f2i(x[i]), 0x112, 0xf, 0xf, true));
  return r;
}
struct W6 { uint32_t w[6]; };
__device__ __forceinline__ W6 uwin(const char* p){
  u32x2 a = *(const u32x2*)p, b = *(const u32x2*)(p+8), c = *(const u32x2*)(p+16);
  W6 r; r.w[0]=a.x; r.w[1]=a.y; r.w[2]=b.x; r.w[3]=b.y; r.w[4]=c.x; r.w[5]=c.y; return r;
}
__device__ __forceinline__ bf16x8 usel(const W6& W, int x, uint32_t sel){
  u32x4 r;
  r.x = __builtin_amdgcn_perm(W.w[x+1], W.w[x],   sel); r.y = __builtin_amdgcn_perm(W.w[x+2], W.w[x+1], sel);
  r.z = __builtin_amdgcn_perm(W.w[x+3], W.w[x+2], sel); r.w = __builtin_amdgcn_perm(W.w[x+4], W.w[x+3], sel);
  return __builtin_bit_cast(bf16x8, r);
}
__device__ __forceinline__ bf16x8 uodd(const W6& W, int x){
  u32x4 r;
  r.x = __builtin_amdgcn_alignbit(W.w[x+1], W.w[x],   16); r.y = __builtin_amdgcn_alignbit(W.w[x+2], W.w[x+1], 16);
  r.z = __builtin_amdgcn_alignbit(W.w[x+3], W.w[x+2], 16); r.w = __builtin_amdgcn_alignbit(W.w[x+4], W.w[x+3], 16);
  return __builtin_bit_cast(bf16x8, r);
}
__device__ __forceinline__ bf16x8 uevn(const W6& W, int x){
  u32x4 r = {W.w[x], W.w[x+1], W.w[x+2], W.w[x+3]};
  return __builtin_bit_cast(bf16x8, r);
}
__device__ __forceinline__ void put_u(char* smem, int tr, u32x2 r0, u32x2 r1){
  uint32_t d[4];
  d[0] = __builtin_amdgcn_perm(r1.x, r0.x, 0x05040100); d[1] = __builtin_amdgcn_perm(r1.x, r0.x, 0x07060302);
  d[2] = __builtin_amdgcn_perm(r1.y, r0.y, 0x05040100); d[3] = __builtin_amdgcn_perm(r1.y, r0.y, 0x07060302);
  const int a = tr >> 6, pos = tr & 63;
  char* p = smem + 272*a + 16 + 4*pos;
  #pragma unroll
  for (int c=0;c<4;c++) *(uint32_t*)(p + c*UST) = d[c];
  if (pos >= 60 && a < 15){
    #pragma unroll
    for (int c=0;c<4;c++) *(uint32_t*)(p + c*UST + 16) = d[c];
  }
  if (tr < 16) *(uint32_t*)(smem + (tr>>2)*UST + 4*(tr&3)) = 0u;
}

#define LDK(F, dl, c) asm volatile("ds_read_b128 %0, %1 offset:%2" : "=v"(F) : "v"(kbase), "i"(256*(dl) + 64*(3-(c))))
#define LDK4(S, dl) LDK(S[0], dl, 0); LDK(S[1], dl, 1); LDK(S[2], dl, 2); LDK(S[3], dl, 3)
#define WAITK(n, S) asm volatile("s_waitcnt lgkmcnt(" #n ")" : "+v"(S[0]), "+v"(S[1]), "+v"(S[2]), "+v"(S[3]))

template<int NT, int R0>
__device__ __forceinline__ void near_conv(char* smem, int ch, int m, int kg){
  const uint32_t kbase = (uint32_t)(uintptr_t)smem + TOFF + (2*ch)*KS2 + 16*(m - kg + 3);
#define LDN(F, hs, c) asm volatile("ds_read_b128 %0, %1 offset:%2" : "=v"(F) : "v"(kbase), "i"(256*(4-(hs)) + 64*(3-(c))))
#define LDN4(S, hs) LDN(S[0], hs, 0); LDN(S[1], hs, 1); LDN(S[2], hs, 2); LDN(S[3], hs, 3)
  bf16x8 KA[4], KB[4];
  LDN4(KA, 0);
  bf16x8 U[NT][4];
  {
    const char* pp = smem + ch*UST + 272*m + 16*kg + (R0 ? 8 : 0);
    #pragma unroll
    for (int c=0;c<4;c++){
      W6 W = uwin(pp + 64*c);
      #pragma unroll
      for (int r=0;r<NT;r++){ const int rel = R0 + r + 1 - (R0 ? 4 : 0); U[r][c] = (rel & 1) ? uodd(W, rel >> 1) : uevn(W, rel >> 1); }
    }
  }
  f32x4 acc[NT];
  #pragma unroll
  for (int r=0;r<NT;r++) acc[r] = f32x4{0,0,0,0};
#define NH(hs, CUR, NXT) \
  if ((hs) < 4){ LDN4(NXT, (hs)+1); WAITK(4, CUR); } else { WAITK(0, CUR); } \
  if ((hs) > 0){ _Pragma("unroll") for (int r=0;r<NT;r++) acc[r] = shrN1(acc[r]); } \
  _Pragma("unroll") for (int c=0;c<4;c++){ _Pragma("unroll") for (int r=0;r<NT;r++) acc[r] = MFMA16(CUR[c], U[r][c], acc[r]); }
  NH(0, KA, KB) NH(1, KB, KA) NH(2, KA, KB) NH(3, KB, KA) NH(4, KA, KB)
  float* yb = (float*)(smem + YAOFF + ch*YST) + (R0 + 32*kg + 132*m);
  #pragma unroll
  for (int i=0;i<4;i++){
    #pragma unroll
    for (int r=0;r<NT;r++) yb[8*i + r] = acc[r][i];
  }
}
__device__ __forceinline__ bf16x8 band(int sh, int w, int kg){
  const uint32_t by = (((1u << w) - 1u) << sh) >> (8*kg);
  u32x4 r;
  #pragma unroll
  for (int k2=0;k2<4;k2++){ uint32_t x = (by >> (2*k2)) & 3u; x = (x | (x << 15)) & 0x00010001u; r[k2] = x * 0x3f80u; }
  return __builtin_bit_cast(bf16x8, r);
}
__device__ __forceinline__ void split2(const f32x4& a, const f32x4& b, bf16x8& h, bf16x8& l){
  u32x4 hh, ll;
  #pragma unroll
  for (int i=0;i<4;i++){ uint32_t x = pk2(a[i], b[i]); hh[i] = x; ll[i] = resid2(x, a[i], b[i]); }
  h = __builtin_bit_cast(bf16x8, hh); l = __builtin_bit_cast(bf16x8, ll);
}
__device__ __forceinline__ bf16x8 pack2(const f32x4& a, const f32x4& b){
  u32x4 hh;
  #pragma unroll
  for (int i=0;i<4;i++) hh[i] = pk2(a[i], b[i]);
  return __builtin_bit_cast(bf16x8, hh);
}
template<int A0, int A1>
__device__ __forceinline__ void far_conv(char* smem, int ch, int m, int kg){
  const char* ub = smem + ch*UST;
  const f32x4 Z4 = {0,0,0,0};
  const int B0 = 2*m + kg - 2;
  const char* va0 = ub + (B0 < 0 ? 0 : 272*(B0 >> 4) + 16 + 16*(B0 & 15));
  const char* va1 = ub + 272*((B0+32) >> 4) + 16 + 16*((B0+32) & 15);
  const int cp = (3 - m) & 3;
  const char* ft = smem + FTOFF + ch*8192 + cp*FCS + 4*(47 - m + 4*kg - cp);
  char* yf = smem + YFOFF + ch*YFST + 64*m + 16*kg;
  f32x4 acc[6];
  #pragma unroll
  for (int a=0;a<6;a++) acc[a] = Z4;
  {
    bf16x8 M[4];
    M[0] = band(14 + m, 3, kg); M[1] = band(9 + m, 5, kg); M[2] = band(6 + m, 3, kg); M[3] = band(1 + m, 5, kg);
    bf16x8 Ch[3][2];
    #pragma unroll
    for (int dl=0;dl<3;dl++){
      #pragma unroll
      for (int h=0;h<2;h++) Ch[dl][h] = *(const bf16x8*)(ft + h*(8*FCS) - 64*dl);
    }
    #pragma unroll
    for (int b=(A0 > 2 ? A0-2 : 0);b<A1;b++){
      const bf16x8 V = *(const bf16x8*)(b ? va1 + 544*(b-1) : va0);
      f32x4 F[4];
      #pragma unroll
      for (int p=0;p<4;p++) F[p] = MFMA16(V, M[p], Z4);
      #pragma unroll
      for (int h=0;h<2;h++){
        const bf16x8 Bh = pack2(F[2*h], F[2*h+1]);
        #pragma unroll
        for (int dl=0;dl<3;dl++) if (b + dl >= A0 && b + dl < A1) acc[b+dl] = MFMA16(Bh, Ch[dl][h], acc[b+dl]);
      }
      if (b < 2 && b >= A0) *(f32x4*)(yf + 1024*b) = acc[b];
    }
  }
  {
    const char* vb0 = ub + (B0 < 1 ? 0 : 272*((B0-1) >> 4) + 16 + 16*((B0-1) & 15));
    const char* vb1 = ub + 272*((B0+31) >> 4) + 16 + 16*((B0+31) & 15);
    const char* f8 = smem + FTOFF + ch*8192 + 16*FCS + cp*FCS + 4*(63 - m + 4*kg - cp);
    const bf16x8 Ma = band(8 + m, 7, kg), Mb = band(7 + m, 9, kg), Mc = band(15 + m, 2, kg);
    const float k1024 = ((const float*)(smem + KCOFF))[ch*KC_ + 512];
    bf16x8 Ch[4];
    #pragma unroll
    for (int dl=0;dl<A1-2;dl++) Ch[dl] = *(const bf16x8*)(f8 - 64*dl);
    #pragma unroll
    for (int b=0;b<A1-2;b++){
      const bf16x8 V = *(const bf16x8*)(b ? va1 + 544*(b-1) : va0);
      const bf16x8 V2 = *(const bf16x8*)(b ? vb1 + 544*(b-1) : vb0);
      const f32x4 Fa = MFMA16(V, Ma, Z4), Fb = MFMA16(V2, Mb, Z4);
      const bf16x8 Bh = pack2(Fa, Fb);
      if (b + 2 >= A0){
        const f32x4 Fc = MFMA16(Mc, V, Z4);
        #pragma unroll
        for (int i=0;i<4;i++) acc[b+2][i] = __builtin_fmaf(k1024, Fc[i], acc[b+2][i]);
      }
      #pragma unroll
      for (int dl=0;dl<4;dl++) if (b + 2 + dl >= A0 && b + 2 + dl < A1) acc[b+2+dl] = MFMA16(Bh, Ch[dl], acc[b+2+dl]);
    }
  }
  #pragma unroll
  for (int a=(A0 > 2 ? A0 : 2);a<A1;a++) *(f32x4*)(yf + 1024*a) = acc[a];
}

__global__ __launch_bounds__(1024) void mono_kernel(const u16* __restrict__ u, const u16* __restrict__ logA, const u16* __restrict__ Aim,
    const u16* __restrict__ Cre, const u16* __restrict__ Cim, const u16* __restrict__ logdt, const u16* __restrict__ Dskip,
    const u16* __restrict__ q, const u16* __restrict__ k, const u16* __restrict__ v, const u16* __restrict__ shift_k,
    const u16* __restrict__ ssmk, const u16* __restrict__ ssmD, u16* __restrict__ out
){
  unsigned long long tsv[10];
#define TSP(i) tsv[i] = __builtin_amdgcn_s_memrealtime()
  TSP(0); TSP(1); TSP(2); TSP(3); TSP(4); TSP(5);
  extern __shared__ __attribute__((aligned(16))) char smem[];
  const int bid = blockIdx.x, t = threadIdx.x;
  const int w5 = bid >> 3;
  const bool isA = w5 < 16;
  const int c0 = (bid & 7)*64 + (w5 & 15)*4;
  const int tr = (t + 64*(w5 & 15)) & 1023;
  const int wave = t >> 6, lane = t & 63, m = lane & 15, kg = lane >> 4;
  asm volatile("" :: "s"(u), "s"(logA), "s"(Aim), "s"(Cre), "s"(Cim), "s"(logdt), "s"(Dskip), "s"(q), "s"(k), "s"(v), "s"(shift_k), "s"(ssmk), "s"(ssmD), "s"(out));
  u32x2 dsk;
  if (t == 0) *(u32x2*)(smem + CNTOFF) = u32x2{0u, 0u};
  int vz; asm volatile("v_mov_b32 %0, 0" : "=v"(vz));
  if (isA){
    const int ch = t >> 8, tg = t & 255, wv = (wave + ch) & 3;
    const int d = c0 + ch;
    char* tb = smem + TOFF + ch*TSZ;
    char* eb_ = smem + KCOFF + ch*(4*KC_);
    const u16 ldt = logdt[d];
    const u16 pla = logA[d*NS_+(tg & 127)], pai = Aim[d*NS_+(tg & 127)];
    u16 pc[2][2];
    #pragma unroll
    for (int hp=0;hp<2;hp++){ pc[hp][0] = Cre[d*NS_+64*hp+lane];  pc[hp][1] = Cim[d*NS_+64*hp+lane]; }
    const float dt = rbf(expf(bf2f(ldt)));
    {
      const float zre = -rbf(expf(bf2f(pla))) * dt;
      const float zim = bf2f(pai) * dt;
      const float f = (tg >> 7) ? 128.f : 1.f;
      cpx e = cexp_(zre*f, zim*f);
      *(float2*)(eb_ + 8*tg) = make_float2(e.re, e.im);
    }
    __syncthreads();
    TSP(1);
    __builtin_amdgcn_sched_barrier(0);
    const u32x2 u0 = *(const u32x2*)(u + (2*tr)*D_ + c0);
    const u32x2 u1 = *(const u32x2*)(u + (2*tr+1)*D_ + c0);
    dsk = *(const u32x2*)(Dskip + c0 + vz);
    __builtin_amdgcn_sched_barrier(0);
    f32x4 a4 = {0,0,0,0};
    char* col = tb + 4*lane;
    #pragma unroll 1
    for (int hp=0;hp<2;hp++){
      cpx C; C.re = bf2f(hp ? pc[1][0] : pc[0][0])*dt; C.im = bf2f(hp ? pc[1][1] : pc[0][1])*dt;
      cpx e1, e2;
      { float2 a = *(const float2*)(eb_ + 8*(64*hp+lane)); e2.re = a.x; e2.im = a.y;
        float2 c = *(const float2*)(eb_ + 1024 + 8*(64*hp+lane)); e1.re = c.x; e1.im = c.y; }
      if (wv == 0){
        cpx e8 = cmul(e2, e2); e8 = cmul(e8, e8); e8 = cmul(e8, e8);
        cpx xx; xx.re = 1.f; xx.im = 0.f;
        #pragma unroll
        for (int i=0;i<16;i++){ put_split(col + (41+i)*TR, xx.re, -xx.im); if (i < 15) xx = (i == 0) ? e8 : cmul(xx, e8); }
      } else if (wv == 1){
        cpx eb = cmul(e1, e1); eb = cmul(eb, eb); eb = cmul(eb, eb);
        cpx cb = cmul(C, eb);
        #pragma unroll
        for (int i=0;i<9;i++){ put_split(col + (32+i)*TR, cb.re, cb.im); if (i < 8) cb = cmul(cb, e1); }
      } else if (wv == 2){
        cpx S[8];
        S[1] = e2;
        #pragma unroll
        for (int i=2;i<8;i++) S[i] = cmul(S[i-1], S[1]);
        cpx cb = C;
        #pragma unroll
        for (int a=0;a<2;a++){
          put_split(col + (8*a)*TR, cb.re, cb.im);
          #pragma unroll
          for (int p=1;p<8;p++){ cpx g = cmul(cb, S[p]); put_split(col + (8*a+p)*TR, g.re, g.im); }
          if (a == 0) cb = cmul(cb, e1);
        }
      } else {
        cpx S2 = cmul(e2, e2), S4 = cmul(S2, S2), S6 = cmul(S4, S2);
        cpx cb = cmul(C, cmul(e1, e1));
        #pragma unroll
        for (int a=0;a<2;a++){
          put_split(col + (16+4*a)*TR, cb.re, cb.im);
          { cpx g = cmul(cb, S2); put_split(col + (16+4*a+1)*TR, g.re, g.im); }
          { cpx g = cmul(cb, S4); put_split(col + (16+4*a+2)*TR, g.re, g.im); }
          { cpx g = cmul(cb, S6); put_split(col + (16+4*a+3)*TR, g.re, g.im); }
          cb = cmul(cb, e1);
        }
        #pragma unroll
        for (int a=0;a<4;a++){
          put_split(col + (24+2*a)*TR, cb.re, cb.im);
          cpx g = cmul(cb, S4); put_split(col + (24+2*a+1)*TR, g.re, g.im);
          if (a < 3) cb = cmul(cb, e1);
        }
      }
      __syncthreads();
      if (wv < 3){
        const char* ga = tb + (16*wv + m)*TR + 16*kg;
        const char* xb = tb + (41 + m)*TR + 16*kg;
        #pragma unroll
        for (int kc=0;kc<4;kc++){
          bf16x8 ah = *(const bf16x8*)(ga + 64*kc), al = *(const bf16x8*)(ga + 64*kc + TLO);
          bf16x8 bh = *(const bf16x8*)(xb + 64*kc), bl = *(const bf16x8*)(xb + 64*kc + TLO);
          a4 = MFMA16(ah, bh, a4);
          a4 = MFMA16(ah, bl, a4);
          a4 = MFMA16(al, bh, a4);
        }
      }
      if (hp == 0) __syncthreads();
    }
    float* Kc = (float*)(smem + KCOFF) + ch*KC_;
    if (wv < 3){
      #pragma unroll
      for (int i=0;i<4;i++){
        int r = 16*wv + 4*kg + i;
        int idx;
        if (r < 16) idx = 128*(r>>3) + (r&7) + 8*m;
        else if (r < 24) idx = 256 + 64*((r-16)>>2) + (r&3) + 4*m;
        else if (r < 32) idx = 384 + 32*((r-24)>>1) + (r&1) + 2*m;
        else idx = 512 + 16*(r-32) + m;
        if (r < 40 || (r == 40 && m == 0)) Kc[idx] = 2.f*a4[i];
      }
    }
    put_u(smem, tr, u0, u1);
    __syncthreads();
    TSP(2);
    {
      char* ft = smem + FTOFF + ch*8192;
      if (tg < 208){
        int z, idx; bool ok; char* wp;
        if (tg < 128){ z = tg & 63; const int Dd = 47 - z; ok = Dd >= 0 && Dd < 32; idx = 385 + 4*Dd + 2*(tg >> 6); wp = ft + (tg >> 6)*(8*FCS); }
        else { z = tg - 128; const int Dd = 63 - z; ok = Dd >= 0; idx = 513 + 2*Dd; wp = ft + 16*FCS; }
        float a = 0.f, b = 0.f;
        if (ok){ a = Kc[idx]; b = Kc[idx+1]; }
        const uint32_t hh = pk2(a, b);
        #pragma unroll
        for (int c=0;c<4;c++) if (z >= c) *(uint32_t*)(wp + c*FCS + 4*(z-c)) = hh;
      }
      if (tg >= 161){
        const int sl = tg - 161, j = sl - 15;
        float kv[8];
        #pragma unroll
        for (int i=0;i<8;i++){
          int l = 8*j + 7 - i;
          int pr = (int)rbf((float)l);
          int p = pr < 256 ? pr : 128 + (pr>>1);
          kv[i] = (l >= 0 && l <= 514) ? Kc[p] : 0.f;
        }
        u32x4 h;
        #pragma unroll
        for (int jj=0;jj<4;jj++) h[jj] = pk2(kv[2*jj], kv[2*jj+1]);
        char* kb = smem + TOFF + (2*ch)*KS2;
        *(u32x4*)(kb + 16*sl) = h;
      }
      if (tg < 4) *(float*)(smem + YFOFF + ch*YFST - 16 + 4*tg) = 0.f;
    }
    __syncthreads();
    TSP(3);
  } else {
    __builtin_amdgcn_sched_barrier(0);
    const u32x2 k0 = *(const u32x2*)(k + (2*tr)*D_ + c0);
    const u32x2 k1 = *(const u32x2*)(k + (2*tr+1)*D_ + c0);
    const u32x4 wa = *(const u32x4*)(shift_k + c0*4), wb = *(const u32x4*)(shift_k + c0*4 + 8);
    char* ks = smem + YOFF;
    __builtin_amdgcn_sched_barrier(0);
    *(u32x4*)(ks + 32 + 16*tr) = u32x4{k0.x, k0.y, k1.x, k1.y};
    __builtin_amdgcn_sched_barrier(0);
    const u32x2 v0 = *(const u32x2*)(v + (2*tr)*D_ + c0);
    const u32x2 v1 = *(const u32x2*)(v + (2*tr+1)*D_ + c0);
    const u32x4 sx = *(const u32x4*)(ssmk + (c0+(t >> 8))*L_ + 8*(t & 255));
    dsk = *(const u32x2*)(ssmD + c0 + vz);
    __builtin_amdgcn_sched_barrier(0);
    if (t < 2) *(u32x4*)(ks + 16*t) = u32x4{0,0,0,0};
    __syncthreads();
    TSP(1);
    u32x2 kk[5];
    kk[0] = *(const u32x2*)(ks + 8 + 16*tr);
    { u32x4 x = *(const u32x4*)(ks + 16 + 16*tr); kk[1] = u32x2{x.x, x.y}; kk[2] = u32x2{x.z, x.w}; }
    kk[3] = k0; kk[4] = k1;
    u32x2 o[2];
    #pragma unroll
    for (int j=0;j<2;j++){
      const u32x2 vv = j ? v1 : v0;
      #pragma unroll
      for (int p=0;p<2;p++){
        float r2[2];
        #pragma unroll
        for (int e=0;e<2;e++){
          const u32x4 ww = p ? wb : wa;
          const uint32_t wl = e ? ww.z : ww.x, wh = e ? ww.w : ww.y;
          const float w0 = lo16f(wl), w1 = hi16f(wl), w2 = lo16f(wh), w3 = hi16f(wh);
          auto g = [&](int idx){ uint32_t x = kk[idx][p]; return e? hi16f(x) : lo16f(x); };
          float acc = rbf(g(j)*w3);
          acc = rbf(acc + rbf(g(j+1)*w2));
          acc = rbf(acc + rbf(g(j+2)*w1));
          acc = rbf(acc + rbf(g(j+3)*w0));
          r2[e] = acc * (e? hi16f(vv[p]) : lo16f(vv[p]));
        }
        o[j][p] = pk2(r2[0], r2[1]);
      }
    }
    put_u(smem, tr, o[0], o[1]);
    {
      const int ch = t >> 8, j = t & 255;
      u32x4 r;
      r.x = __builtin_amdgcn_alignbit(sx.w, sx.w, 16); r.y = __builtin_amdgcn_alignbit(sx.z, sx.z, 16);
      r.z = __builtin_amdgcn_alignbit(sx.y, sx.y, 16); r.w = __builtin_amdgcn_alignbit(sx.x, sx.x, 16);
      char* kb = smem + TOFF + ch*KST;
      *(u32x4*)(kb + 16*(j+15)) = r;
      if (j < 15) *(u32x4*)(kb + 16*j) = u32x4{0,0,0,0};
    }
    __syncthreads();
    TSP(2); TSP(3);
  }
  int sig = -1, fi = wave - 8;
  if (isA){
    const int ch = wave >> 2, role = (wave + ch) & 3;
    if (role == 0){ __builtin_amdgcn_s_setprio(2); near_conv<4,0>(smem, ch, m, kg); }
    else if (role == 1) near_conv<4,4>(smem, ch, m, kg);
    else if (role == 2){ __builtin_amdgcn_s_setprio(3); far_conv<0,4>(smem, ch, m, kg); }
    else { __builtin_amdgcn_s_setprio(3); far_conv<4,6>(smem, ch, m, kg); }
    TSP(5);
    sig = role == 1;
    fi = role >= 2 ? 2*ch + role - 2 : -1;
  } else if (wave < 8){
    const int ch = wave >> 1;
    const int g = (wave ^ (wave >> 2)) & 1;
    if (g == 0) __builtin_amdgcn_s_setprio(2);
    sig = g;
    const int part = 0;
    const int unit = ch;
    const uint32_t kbase = (uint32_t)(uintptr_t)smem + TOFF + unit*KST + 16*(m - kg + 3);
    const char* ub = smem + ch*UST;
    bf16x8 KA[4], KB[4];
    LDK4(KA, 15);
    f32x4 acc[4];
    {
      bf16x8 Up[2][4];
      {
        const uint32_t sel = (m & 1) ? 0x07060504u : 0x05040302u;
        const char* pp = ub + 272*(m >> 1) + 16*kg + 8*g;
        #pragma unroll
        for (int c=0;c<4;c++){ W6 W = uwin(pp + 64*c); Up[0][c] = usel(W, 0, sel); Up[1][c] = usel(W, 1, sel); }
      }
      f32x4 P0 = {0,0,0,0}, P1 = {0,0,0,0};
#define PH1(dl, CUR, NXT) \
      LDK4(NXT, (dl)-1); WAITK(4, CUR); \
      if ((dl) < 15){ P0 = shrN2(P0); P1 = shrN2(P1); } \
      P0 = MFMA16(CUR[0], Up[0][0], P0); P1 = MFMA16(CUR[0], Up[1][0], P1); \
      P0 = MFMA16(CUR[1], Up[0][1], P0); P1 = MFMA16(CUR[1], Up[1][1], P1); \
      P0 = MFMA16(CUR[2], Up[0][2], P0); P1 = MFMA16(CUR[2], Up[1][2], P1); \
      P0 = MFMA16(CUR[3], Up[0][3], P0); P1 = MFMA16(CUR[3], Up[1][3], P1);
      PH1(15, KA, KB) PH1(14, KB, KA) PH1(13, KA, KB) PH1(12, KB, KA)
      PH1(11, KA, KB) PH1(10, KB, KA) PH1(9, KA, KB) PH1(8, KB, KA)
      const bool ok = m >= 1 && m <= 8;
      const int src = 4*((lane & 48) + ((2*(m-1)) & 15));
      #pragma unroll
      for (int i=0;i<4;i++){
        float a0 = i2f(__builtin_amdgcn_ds_bpermute(src,   f2i(P0[i])));
        float a1 = i2f(__builtin_amdgcn_ds_bpermute(src+4, f2i(P0[i])));
        float a2 = i2f(__builtin_amdgcn_ds_bpermute(src,   f2i(P1[i])));
        float a3 = i2f(__builtin_amdgcn_ds_bpermute(src+4, f2i(P1[i])));
        acc[0][i] = ok ? a0 : 0.f; acc[1][i] = ok ? a1 : 0.f; acc[2][i] = ok ? a2 : 0.f; acc[3][i] = ok ? a3 : 0.f;
      }
    }
    TSP(4);
    bf16x8 U[4][4];
    {
      const char* pp = ub + 272*m + 16*kg + 8*g;
      #pragma unroll
      for (int c=0;c<4;c++){ W6 W = uwin(pp + 64*c); U[0][c] = uodd(W, 0); U[1][c] = uevn(W, 1); U[2][c] = uodd(W, 1); U[3][c] = uevn(W, 2); }
    }
#define PH2(dl, CUR, NXT) \
    if ((dl) > 0){ LDK4(NXT, (dl)-1); WAITK(4, CUR); } else { WAITK(0, CUR); } \
    if ((dl) < 7){ acc[0] = shrN1(acc[0]); acc[1] = shrN1(acc[1]); acc[2] = shrN1(acc[2]); acc[3] = shrN1(acc[3]); } \
    _Pragma("unroll") for (int c=0;c<4;c++){ \
      acc[0] = MFMA16(CUR[c], U[0][c], acc[0]); acc[1] = MFMA16(CUR[c], U[1][c], acc[1]); \
      acc[2] = MFMA16(CUR[c], U[2][c], acc[2]); acc[3] = MFMA16(CUR[c], U[3][c], acc[3]); }
    PH2(7, KA, KB) PH2(6, KB, KA) PH2(5, KA, KB) PH2(4, KB, KA)
    PH2(3, KA, KB) PH2(2, KB, KA) PH2(1, KA, KB) PH2(0, KB, KA)
    TSP(5);
    char* yb = smem + YOFF + (part*4 + ch)*YST + 4*(4*g + 32*kg + 132*m);
    #pragma unroll
    for (int i=0;i<4;i++) *(f32x4*)(yb + 32*i) = f32x4{acc[0][i], acc[1][i], acc[2][i], acc[3][i]};
  }
  TSP(6);
  if (sig >= 0){
    __builtin_amdgcn_fence(__ATOMIC_RELEASE, "workgroup");
    if (lane == 0) __hip_atomic_fetch_add((unsigned*)(smem + CNTOFF) + sig, 1u, __ATOMIC_RELAXED, __HIP_MEMORY_SCOPE_WORKGROUP);
  }
  if (fi >= 0){
    const int p = (fi*64 + lane + 32*(w5 & 15)) & 511;
    u32x2 qr[4];
    if (!isA){
      const u16* qp = q + (8*(p >> 1) + 2*(p & 1))*D_ + c0;
      qr[0] = *(const u32x2*)(qp); qr[1] = *(const u32x2*)(qp + D_);
      qr[2] = *(const u32x2*)(qp + 4*D_); qr[3] = *(const u32x2*)(qp + 5*D_);
    }
    __builtin_amdgcn_sched_barrier(0);
    u16* op = out + (isA ? 0 : D_) + c0;
    const unsigned need0 = isA ? 12u : 4u;
    #pragma unroll
    for (int st=0; st<2; st++){
      const int tq = 4*(p >> 1) + (p & 1) + 2*st;
      const char* yb = smem + (isA ? YAOFF : YOFF) + 8*tq + 16*(tq >> 6);
      const char* up = smem + 272*(tq >> 6) + 16 + 4*(tq & 63);
      float s0[4], s1[4];
      #pragma unroll
      for (int c=0;c<4;c++){
        uint32_t uu = *(const uint32_t*)(up + c*UST);
        float sd = (c & 1) ? hi16f(dsk[c>>1]) : lo16f(dsk[c>>1]);
        s0[c] = rbf(lo16f(uu)*sd); s1[c] = rbf(hi16f(uu)*sd);
      }
      while (__hip_atomic_load((const unsigned*)(smem + CNTOFF) + st, __ATOMIC_RELAXED, __HIP_MEMORY_SCOPE_WORKGROUP) < (st ? 4u : need0)) __builtin_amdgcn_s_sleep(1);
      __builtin_amdgcn_fence(__ATOMIC_ACQUIRE, "workgroup");
      float o0[4], o1[4];
      if (isA){
        const char* yf = smem + YFOFF + (tq >= 257 ? 4*(2*tq - 515) : -16);
        #pragma unroll
        for (int c=0;c<4;c++){
          f32x2 a = *(const f32x2*)(yb + c*YST);
          float b0 = *(const float*)(yf + c*YFST), b1 = *(const float*)(yf + c*YFST + 4);
          o0[c] = rbf(a.x + b0) + s0[c];
          o1[c] = rbf(a.y + b1) + s1[c];
        }
      } else {
        #pragma unroll
        for (int c=0;c<4;c++){
          f32x2 a = *(const f32x2*)(yb + c*YST);
          float q0 = (c & 1) ? hi16f(qr[2*st][c>>1]) : lo16f(qr[2*st][c>>1]);
          float q1 = (c & 1) ? hi16f(qr[2*st+1][c>>1]) : lo16f(qr[2*st+1][c>>1]);
          o0[c] = q0 * rbf(rbf(a.x) + s0[c]);
          o1[c] = q1 * rbf(rbf(a.y) + s1[c]);
        }
      }
      *(u32x2*)(op + (2*tq)*(2*D_))   = u32x2{pk2(o0[0],o0[1]), pk2(o0[2],o0[3])};
      *(u32x2*)(op + (2*tq+1)*(2*D_)) = u32x2{pk2(o1[0],o1[1]), pk2(o1[2],o1[3])};
    }
  }
  TSP(7);
  if (!isA){
    unsigned long long dlt = (tsv[1]-tsv[0])*8;
    unsigned long long tgt = tsv[7] + dlt;
    while (__builtin_amdgcn_s_memrealtime() < tgt) __builtin_amdgcn_s_sleep(1);
  }
}

extern "C" void kernel_launch(void* const* d_in, const int* in_sizes, int n_in,
                              void* d_out, int out_size, void* d_ws, size_t ws_size,
                              hipStream_t stream) {
  mono_kernel<<<256, 1024, LDS_TOTAL, stream>>>((const u16*)d_in[0], (const u16*)d_in[1], (const u16*)d_in[2], (const u16*)d_in[3],
     (const u16*)d_in[4], (const u16*)d_in[5], (const u16*)d_in[6], (const u16*)d_in[7], (const u16*)d_in[8], (const u16*)d_in[9],
     (const u16*)d_in[10], (const u16*)d_in[11], (const u16*)d_in[12], (u16*)d_out
);
}
```

```cpp
#include <hip/hip_runtime.h>
#include <hip/hip_bf16.h>
#include <stdint.h>

typedef unsigned short u16;
typedef short bf16x8 __attribute__((ext_vector_type(8)));
typedef float f32x4 __attribute__((ext_vector_type(4)));
typedef float f32x2 __attribute__((ext_vector_type(2)));
typedef __bf16 bf16x2_t __attribute__((ext_vector_type(2)));

#define L_ 2048
#define D_ 512
#define NS_ 128

__device__ __forceinline__ float bf2f(u16 x){ return __builtin_bit_cast(float, (uint32_t)x << 16); }
__device__ __forceinline__ u16 f2bf(float f){ __hip_bfloat16 h = __float2bfloat16(f); return __builtin_bit_cast(u16, h); }
__device__ __forceinline__ float rbf(float f){ return bf2f(f2bf(f)); }
__device__ __forceinline__ uint32_t pk2(float a, float b){
  f32x2 v = {a,b}; bf16x2_t r = __builtin_convertvector(v, bf16x2_t); return __builtin_bit_cast(uint32_t, r); }
__device__ __forceinline__ float lo16f(uint32_t w){ return __builtin_bit_cast(float, w << 16); }
__device__ __forceinline__ float hi16f(uint32_t w){ return __builtin_bit_cast(float, w & 0xffff0000u); }

struct cpx { float re, im; };
__device__ __forceinline__ float amul(float a, float b){ float r; asm("v_mul_f32 %0, %1, %2" : "=v"(r) : "v"(a), "v"(b)); return r; }
__device__ __forceinline__ float afma(float a, float b, float c){ float r; asm("v_fma_f32 %0, %1, %2, %3" : "=v"(r) : "v"(a), "v"(b), "v"(c)); return r; }
__device__ __forceinline__ float afms(float a, float b, float c){ float r; asm("v_fma_f32 %0, %1, %2, -%3" : "=v"(r) : "v"(a), "v"(b), "v"(c)); return r; }
__device__ __forceinline__ cpx cmul(cpx a, cpx b){
  f32x2 A = {a.re, a.im}, B = {b.re, b.im}, T, R;
  asm("v_pk_mul_f32 %0, %1, %2 op_sel:[1,1] op_sel_hi:[1,0] neg_lo:[1,0]" : "=v"(T) : "v"(A), "v"(B));
  asm("v_pk_fma_f32 %0, %1, %2, %3 op_sel:[0,0,0] op_sel_hi:[0,1,1]" : "=v"(R) : "v"(A), "v"(B), "v"(T));
  cpx r; r.re = R.x; r.im = R.y; return r; }
__device__ __forceinline__ cpx cexp_(float re, float im){ float e = expf(re); float s,c; sincosf(im,&s,&c); cpx r; r.re=e*c; r.im=e*s; return r; }

#define MFMA16(a,b,c) __builtin_amdgcn_mfma_f32_16x16x32_bf16(a,b,c,0,0,0)
typedef uint32_t u32x2 __attribute__((ext_vector_type(2)));
typedef uint32_t u32x4 __attribute__((ext_vector_type(4)));
#define TR 544
#define TSZ (57*TR)
#define UST 4352
#define TOFF 17408
#define KST 4352
#define KS2 1536
#define XOFF (TOFF + 4*TSZ)
#define NOFF XOFF
#define FCS 320
#define FTS (12*FCS)
#define FTOFF (XOFF + 4*KS2)
#define K1OFF (FTOFF + 4*FTS)
#define YFOFF (TOFF + 45056 + 16)
#define YFST 6176
#define YAOFF (TOFF + 69888)
#define YOFF (TOFF + 36864)
#define YST 8448
#define CNTOFF (K1OFF + 16)
#define LDS_TOTAL (CNTOFF + 64)
typedef _Float16 f16x8 __attribute__((ext_vector_type(8)));
typedef _Float16 f16x2 __attribute__((ext_vector_type(2)));
#define MFMAH(a,b,c) __builtin_amdgcn_mfma_f32_16x16x32_f16(a,b,c,0,0,0)
__device__ __forceinline__ uint32_t pkh(float a, float b){
  f32x2 v = {a,b}; f16x2 r = __builtin_convertvector(v, f16x2); return __builtin_bit_cast(uint32_t, r); }
struct cp2 { cpx a, b; };
__device__ __forceinline__ cp2 cmul2(const cp2& x, const cp2& y){ cp2 r; r.a = cmul(x.a, y.a); r.b = cmul(x.b, y.b); return r; }
__device__ __forceinline__ void putg(char* p, const cp2& x){ *(u32x2*)p = u32x2{pkh(x.a.re, x.a.im), pkh(x.b.re, x.b.im)}; }
__device__ __forceinline__ void putx(char* p, const cp2& x){ *(u32x2*)p = u32x2{pkh(x.a.re, -x.a.im), pkh(x.b.re, -x.b.im)}; }

__device__ __forceinline__ uint32_t resid2(uint32_t h, float a, float b){
  return pk2(a - lo16f(h), b - hi16f(h));
}
__device__ __forceinline__ int f2i(float x){ return __builtin_bit_cast(int, x); }
__device__ __forceinline__ float i2f(int x){ return __builtin_bit_cast(float, x); }
__device__ __forceinline__ f32x4 shrN1(f32x4 x){
  f32x4 r;
  #pragma unroll
  for (int i=0;i<4;i++) r[i] = i2f(__builtin_amdgcn_update_dpp(0, f2i(x[i]), 0x111, 0xf, 0xf, true));
  return r;
}
__device__ __forceinline__ f32x4 shrN2(f32x4 x){
  f32x4 r;
  #pragma unroll
  for (int i=0;i<4;i++) r[i] = i2f(__builtin_amdgcn_update_dpp(0, f2i(x[i]), 0x112, 0xf, 0xf, true));
  return r;
}
struct W6 { uint32_t w[6]; };
__device__ __forceinline__ W6 uwin(const char* p){
  u32x2 a = *(const u32x2*)p, b = *(const u32x2*)(p+8), c = *(const u32x2*)(p+16);
  W6 r; r.w[0]=a.x; r.w[1]=a.y; r.w[2]=b.x; r.w[3]=b.y; r.w[4]=c.x; r.w[5]=c.y; return r;
}
__device__ __forceinline__ bf16x8 usel(const W6& W, int x, uint32_t sel){
  u32x4 r;
  r.x = __builtin_amdgcn_perm(W.w[x+1], W.w[x],   sel); r.y = __builtin_amdgcn_perm(W.w[x+2], W.w[x+1], sel);
  r.z = __builtin_amdgcn_perm(W.w[x+3], W.w[x+2], sel); r.w = __builtin_amdgcn_perm(W.w[x+4], W.w[x+3], sel);
  return __builtin_bit_cast(bf16x8, r);
}
__device__ __forceinline__ bf16x8 uodd(const W6& W, int x){
  u32x4 r;
  r.x = __builtin_amdgcn_alignbit(W.w[x+1], W.w[x],   16); r.y = __builtin_amdgcn_alignbit(W.w[x+2], W.w[x+1], 16);
  r.z = __builtin_amdgcn_alignbit(W.w[x+3], W.w[x+2], 16); r.w = __builtin_amdgcn_alignbit(W.w[x+4], W.w[x+3], 16);
  return __builtin_bit_cast(bf16x8, r);
}
__device__ __forceinline__ bf16x8 uevn(const W6& W, int x){
  u32x4 r = {W.w[x], W.w[x+1], W.w[x+2], W.w[x+3]};
  return __builtin_bit_cast(bf16x8, r);
}
__device__ __forceinline__ void put_u(char* smem, int tr, u32x2 r0, u32x2 r1){
  uint32_t d[4];
  d[0] = __builtin_amdgcn_perm(r1.x, r0.x, 0x05040100); d[1] = __builtin_amdgcn_perm(r1.x, r0.x, 0x07060302);
  d[2] = __builtin_amdgcn_perm(r1.y, r0.y, 0x05040100); d[3] = __builtin_amdgcn_perm(r1.y, r0.y, 0x07060302);
  const int a = tr >> 6, pos = tr & 63;
  char* p = smem + 272*a + 16 + 4*pos;
  #pragma unroll
  for (int c=0;c<4;c++) *(uint32_t*)(p + c*UST) = d[c];
  if (pos >= 60 && a < 15){
    #pragma unroll
    for (int c=0;c<4;c++) *(uint32_t*)(p + c*UST + 16) = d[c];
  }
  if (tr < 16) *(uint32_t*)(smem + (tr>>2)*UST + 4*(tr&3)) = 0u;
}

#define LDK(F, dl, c) asm volatile("ds_read_b128 %0, %1 offset:%2" : "=v"(F) : "v"(kbase), "i"(256*(dl) + 64*(3-(c))))
#define LDK4(S, dl) LDK(S[0], dl, 0); LDK(S[1], dl, 1); LDK(S[2], dl, 2); LDK(S[3], dl, 3)
#define WAITK(n, S) asm volatile("s_waitcnt lgkmcnt(" #n ")" : "+v"(S[0]), "+v"(S[1]), "+v"(S[2]), "+v"(S[3]))

template<int NT, int R0>
__device__ __forceinline__ void near_conv(char* smem, int ch, int m, int kg){
  const uint32_t kbase = (uint32_t)(uintptr_t)smem + NOFF + ch*KS2 + 16*(m - kg + 3);
#define LDN(F, hs, c) asm volatile("ds_read_b128 %0, %1 offset:%2" : "=v"(F) : "v"(kbase), "i"(256*(4-(hs)) + 64*(3-(c))))
#define LDN4(S, hs) LDN(S[0], hs, 0); LDN(S[1], hs, 1); LDN(S[2], hs, 2); LDN(S[3], hs, 3)
  bf16x8 KA[4], KB[4];
  LDN4(KA, 0);
  bf16x8 U[NT][4];
  {
    const char* pp = smem + ch*UST + 272*m + 16*kg + (R0 ? 8 : 0);
    #pragma unroll
    for (int c=0;c<4;c++){
      W6 W = uwin(pp + 64*c);
      #pragma unroll
      for (int r=0;r<NT;r++){ const int rel = R0 + r + 1 - (R0 ? 4 : 0); U[r][c] = (rel & 1) ? uodd(W, rel >> 1) : uevn(W, rel >> 1); }
    }
  }
  f32x4 acc[NT];
  #pragma unroll
  for (int r=0;r<NT;r++) acc[r] = f32x4{0,0,0,0};
#define NH(hs, CUR, NXT) \
  if ((hs) < 4){ LDN4(NXT, (hs)+1); WAITK(4, CUR); } else { WAITK(0, CUR); } \
  if ((hs) > 0){ _Pragma("unroll") for (int r=0;r<NT;r++) acc[r] = shrN1(acc[r]); } \
  _Pragma("unroll") for (int c=0;c<4;c++){ _Pragma("unroll") for (int r=0;r<NT;r++) acc[r] = MFMA16(CUR[c], U[r][c], acc[r]); }
  NH(0, KA, KB) NH(1, KB, KA) NH(2, KA, KB) NH(3, KB, KA) NH(4, KA, KB)
  float* yb = (float*)(smem + YAOFF + ch*YST) + (R0 + 32*kg + 132*m);
  #pragma unroll
  for (int i=0;i<4;i++){
    #pragma unroll
    for (int r=0;r<NT;r++) yb[8*i + r] = acc[r][i];
  }
}
__device__ __forceinline__ bf16x8 band(int sh, int w, int kg){
  const uint32_t by = (((1u << w) - 1u) << sh) >> (8*kg);
  u32x4 r;
  #pragma unroll
  for (int k2=0;k2<4;k2++){ uint32_t x = (by >> (2*k2)) & 3u; x = (x | (x << 15)) & 0x00010001u; r[k2] = x * 0x3f80u; }
  return __builtin_bit_cast(bf16x8, r);
}
__device__ __forceinline__ void split2(const f32x4& a, const f32x4& b, bf16x8& h, bf16x8& l){
  u32x4 hh, ll;
  #pragma unroll
  for (int i=0;i<4;i++){ uint32_t x = pk2(a[i], b[i]); hh[i] = x; ll[i] = resid2(x, a[i], b[i]); }
  h = __builtin_bit_cast(bf16x8, hh); l = __builtin_bit_cast(bf16x8, ll);
}
__device__ __forceinline__ bf16x8 pack2(const f32x4& a, const f32x4& b){
  u32x4 hh;
  #pragma unroll
  for (int i=0;i<4;i++) hh[i] = pk2(a[i], b[i]);
  return __builtin_bit_cast(bf16x8, hh);
}
template<int A0, int A1>
__device__ __forceinline__ void far_conv(char* smem, int ch, int m, int kg){
  const char* ub = smem + ch*UST;
  const f32x4 Z4 = {0,0,0,0};
  const int B0 = 2*m + kg - 2;
  const char* va0 = ub + (B0 < 0 ? 0 : 272*(B0 >> 4) + 16 + 16*(B0 & 15));
  const char* va1 = ub + 272*((B0+32) >> 4) + 16 + 16*((B0+32) & 15);
  const int cp = (3 - m) & 3;
  const char* ft = smem + FTOFF + ch*FTS + cp*FCS + 4*(47 - m + 4*kg - cp);
  char* yf = smem + YFOFF + ch*YFST + 64*m + 16*kg;
  if (A0 == 0 && kg == 0 && m < 4) *(float*)(smem + YFOFF + ch*YFST - 16 + 4*m) = 0.f;
  f32x4 acc[6];
  #pragma unroll
  for (int a=0;a<6;a++) acc[a] = Z4;
  {
    bf16x8 M[4];
    M[0] = band(14 + m, 3, kg); M[1] = band(9 + m, 5, kg); M[2] = band(6 + m, 3, kg); M[3] = band(1 + m, 5, kg);
    bf16x8 Ch[3][2];
    #pragma unroll
    for (int dl=0;dl<3;dl++){
      #pragma unroll
      for (int h=0;h<2;h++) Ch[dl][h] = *(const bf16x8*)(ft + h*(4*FCS) - 64*dl);
    }
    #pragma unroll
    for (int b=(A0 > 2 ? A0-2 : 0);b<A1;b++){
      const bf16x8 V = *(const bf16x8*)(b ? va1 + 544*(b-1) : va0);
      f32x4 F[4];
      #pragma unroll
      for (int p=0;p<4;p++) F[p] = MFMA16(V, M[p], Z4);
      #pragma unroll
      for (int h=0;h<2;h++){
        const bf16x8 Bh = pack2(F[2*h], F[2*h+1]);
        #pragma unroll
        for (int dl=0;dl<3;dl++) if (b + dl >= A0 && b + dl < A1) acc[b+dl] = MFMA16(Bh, Ch[dl][h], acc[b+dl]);
      }
      if (b < 2 && b >= A0) *(f32x4*)(yf + 1024*b) = acc[b];
    }
  }
  {
    const char* vb0 = ub + (B0 < 1 ? 0 : 272*((B0-1) >> 4) + 16 + 16*((B0-1) & 15));
    const char* vb1 = ub + 272*((B0+31) >> 4) + 16 + 16*((B0+31) & 15);
    const char* f8 = smem + FTOFF + ch*FTS + 8*FCS + cp*FCS + 4*(63 - m + 4*kg - cp);
    const bf16x8 Ma = band(8 + m, 7, kg), Mb = band(7 + m, 9, kg), Mc = band(15 + m, 2, kg);
    const float k1024 = ((const float*)(smem + K1OFF))[ch];
    bf16x8 Ch[4];
    #pragma unroll
    for (int dl=0;dl<A1-2;dl++) Ch[dl] = *(const bf16x8*)(f8 - 64*dl);
    #pragma unroll
    for (int b=0;b<A1-2;b++){
      const bf16x8 V = *(const bf16x8*)(b ? va1 + 544*(b-1) : va0);
      const bf16x8 V2 = *(const bf16x8*)(b ? vb1 + 544*(b-1) : vb0);
      const f32x4 Fa = MFMA16(V, Ma, Z4), Fb = MFMA16(V2, Mb, Z4);
      const bf16x8 Bh = pack2(Fa, Fb);
      if (b + 2 >= A0){
        const f32x4 Fc = MFMA16(Mc, V, Z4);
        #pragma unroll
        for (int i=0;i<4;i++) acc[b+2][i] = __builtin_fmaf(k1024, Fc[i], acc[b+2][i]);
      }
      #pragma unroll
      for (int dl=0;dl<4;dl++) if (b + 2 + dl >= A0 && b + 2 + dl < A1) acc[b+2+dl] = MFMA16(Bh, Ch[dl], acc[b+2+dl]);
    }
  }
  #pragma unroll
  for (int a=(A0 > 2 ? A0 : 2);a<A1;a++) *(f32x4*)(yf + 1024*a) = acc[a];
}

__global__ __launch_bounds__(1024) void mono_kernel(const u16* __restrict__ u, const u16* __restrict__ logA, const u16* __restrict__ Aim,
    const u16* __restrict__ Cre, const u16* __restrict__ Cim, const u16* __restrict__ logdt, const u16* __restrict__ Dskip,
    const u16* __restrict__ q, const u16* __restrict__ k, const u16* __restrict__ v, const u16* __restrict__ shift_k,
    const u16* __restrict__ ssmk, const u16* __restrict__ ssmD, u16* __restrict__ out
){
#define TSP(i)
  TSP(0); TSP(1); TSP(2); TSP(3); TSP(4); TSP(5);
  extern __shared__ __attribute__((aligned(16))) char smem[];
  const int bid = blockIdx.x, t = threadIdx.x;
  const int w5 = bid >> 3;
  const bool isA = w5 < 16;
  const int c0 = (bid & 7)*64 + (w5 & 15)*4;
  const int tr = (t + 64*(w5 & 15)) & 1023;
  const int wave = t >> 6, lane = t & 63, m = lane & 15, kg = lane >> 4;
  asm volatile("" :: "s"(u), "s"(logA), "s"(Aim), "s"(Cre), "s"(Cim), "s"(logdt), "s"(Dskip), "s"(q), "s"(k), "s"(v), "s"(shift_k), "s"(ssmk), "s"(ssmD), "s"(out));
  u32x2 dsk;
  if (t == 0) *(u32x2*)(smem + CNTOFF) = u32x2{0u, 0u};
  int vz; asm volatile("v_mov_b32 %0, 0" : "=v"(vz));
  asm volatile("global_load_dwordx2 %0, %1, %2" : "=v"(dsk) : "v"(2*c0 + vz), "s"(isA ? Dskip : ssmD));
  if (isA){
    const int ch = wave & 3, rj = wave >> 2;
    const int d = c0 + ch;
    char* tb = smem + TOFF + ch*TSZ;
    unsigned* cnt = (unsigned*)(smem + CNTOFF);
    uint32_t ldt, pcr, pci, lde, pla, pai;
    asm volatile("global_load_ushort %0, %1, %2" : "=v"(lde) : "v"(2*(c0 + ((t >> 7) & 3))), "s"(logdt));
    asm volatile("global_load_ushort %0, %1, %2" : "=v"(pla) : "v"(2*(c0*NS_ + (t & 511))), "s"(logA));
    asm volatile("global_load_ushort %0, %1, %2" : "=v"(pai) : "v"(2*(c0*NS_ + (t & 511))), "s"(Aim));
    asm volatile("global_load_ushort %0, %1, %2" : "=v"(ldt) : "v"(2*d), "s"(logdt));
    asm volatile("global_load_dword %0, %1, %2" : "=v"(pcr) : "v"(2*d*NS_ + 4*lane), "s"(Cre));
    asm volatile("global_load_dword %0, %1, %2" : "=v"(pci) : "v"(2*d*NS_ + 4*lane), "s"(Cim));
#define PWAIT(n) asm volatile("s_waitcnt vmcnt(" #n ")" : "+v"(lde), "+v"(pla), "+v"(pai), "+v"(ldt), "+v"(pcr), "+v"(pci), "+v"(dsk))
    if (t < 8) cnt[2 + t] = 0u;
    __builtin_amdgcn_sched_barrier(0);
    __builtin_amdgcn_s_barrier();
    __builtin_amdgcn_sched_barrier(0);
    u32x2 uu[4];
    const int tl = (t + 64*(w5 & 15)) & 511;
    cp2 C; float ksc;
    auto prep = [&](){
      uint32_t e0 = pcr & 0x7f807f80u, e1 = pci & 0x7f807f80u;
      uint32_t x = max(max(e0 & 0xffffu, e0 >> 16), max(e1 & 0xffffu, e1 >> 16));
      x = max(x, (uint32_t)__builtin_amdgcn_update_dpp(0, (int)x, 0xb1, 0xf, 0xf, true));
      x = max(x, (uint32_t)__builtin_amdgcn_update_dpp(0, (int)x, 0x4e, 0xf, 0xf, true));
      x = max(x, (uint32_t)__builtin_amdgcn_update_dpp(0, (int)x, 0x141, 0xf, 0xf, true));
      x = max(x, (uint32_t)__builtin_amdgcn_update_dpp(0, (int)x, 0x140, 0xf, 0xf, true));
      const uint32_t y = max(max((uint32_t)__builtin_amdgcn_readlane((int)x, 0), (uint32_t)__builtin_amdgcn_readlane((int)x, 16)),
                             max((uint32_t)__builtin_amdgcn_readlane((int)x, 32), (uint32_t)__builtin_amdgcn_readlane((int)x, 48)));
      const int sh = 127 - (int)(y >> 7);
      C.a.re = ldexpf(lo16f(pcr), sh); C.a.im = ldexpf(lo16f(pci), sh);
      C.b.re = ldexpf(hi16f(pcr), sh); C.b.im = ldexpf(hi16f(pci), sh);
      ksc = ldexpf(2.f*rbf(expf(lo16f(ldt))), -sh);
      asm volatile("" : "+v"(C.a.re), "+v"(C.a.im), "+v"(C.b.re), "+v"(C.b.im), "+v"(ksc));
    };
    if (wave >= 8){
      const u16* up = u + (2*tl)*D_ + c0;
      uu[0] = *(const u32x2*)(up);           uu[1] = *(const u32x2*)(up + D_);
      uu[2] = *(const u32x2*)(up + 1024*D_); uu[3] = *(const u32x2*)(up + 1025*D_);
      __builtin_amdgcn_sched_barrier(0);
      PWAIT(4);
      prep();
    } else {
      PWAIT(3);
      const float dte = rbf(expf(lo16f(lde)));
      const float zre = -rbf(expf(lo16f(pla))) * dte;
      const float zim = lo16f(pai) * dte;
      const cpx e = cexp_(zre, zim), f = cexp_(zre*128.f, zim*128.f);
      char* ep = smem + XOFF + (t >> 7)*2048 + 8*(t & 127);
      *(float2*)(ep) = make_float2(e.re, e.im);
      *(float2*)(ep + 1024) = make_float2(f.re, f.im);
      PWAIT(0);
      prep();
    }
    __syncthreads();
    TSP(1);
    char* col = tb + 8*lane;
    char* nb = smem + NOFF + ch*KS2;
    char* ft = smem + FTOFF + ch*FTS;
    {
      const char* eb_ = smem + XOFF + ch*2048;
      cp2 e1, e2;
      { f32x4 a = *(const f32x4*)(eb_ + 16*lane); e2.a.re = a[0]; e2.a.im = a[1]; e2.b.re = a[2]; e2.b.im = a[3];
        f32x4 c = *(const f32x4*)(eb_ + 1024 + 16*lane); e1.a.re = c[0]; e1.a.im = c[1]; e1.b.re = c[2]; e1.b.im = c[3]; }
      __builtin_amdgcn_fence(__ATOMIC_RELEASE, "workgroup");
      if (lane == 0) __hip_atomic_fetch_add(cnt + 2, 1u, __ATOMIC_RELAXED, __HIP_MEMORY_SCOPE_WORKGROUP);
      if (rj == 2){
        __builtin_amdgcn_s_setprio(3);
        cp2 e8 = cmul2(e2, e2); e8 = cmul2(e8, e8); e8 = cmul2(e8, e8);
        *(u32x2*)(col + 41*TR) = u32x2{0x3c00u, 0x3c00u};
        cp2 e16 = cmul2(e8, e8);
        cp2 xa = e8, xb = e16;
        #pragma unroll
        for (int i=1;i<16;i+=2){
          putx(col + (41+i)*TR, xa); if (i < 15) putx(col + (42+i)*TR, xb);
          if (i < 15) xa = cmul2(xa, e16);
          if (i < 13) xb = cmul2(xb, e16);
        }
        __builtin_amdgcn_fence(__ATOMIC_RELEASE, "workgroup");
        if (lane == 0) __hip_atomic_fetch_add(cnt + 4 + ch, 1u, __ATOMIC_RELAXED, __HIP_MEMORY_SCOPE_WORKGROUP);
        __builtin_amdgcn_s_setprio(0);
      } else if (rj == 3){
        __builtin_amdgcn_s_setprio(2);
        cp2 eb = cmul2(e1, e1); eb = cmul2(eb, eb); eb = cmul2(eb, eb);
        cp2 cb = cmul2(C, eb);
        #pragma unroll
        for (int i=0;i<9;i++){ putg(col + (32+i)*TR, cb); if (i < 8) cb = cmul2(cb, e1); }
      } else if (rj == 0){
        cp2 cb = C, cd = cmul2(C, e1);
        #pragma unroll
        for (int p=0;p<8;p++){
          putg(col + p*TR, cb); putg(col + (8+p)*TR, cd);
          if (p < 7){ cb = cmul2(cb, e2); cd = cmul2(cd, e2); }
        }
      } else {
        __builtin_amdgcn_s_setprio(1);
        cp2 S2 = cmul2(e2, e2), S4 = cmul2(S2, S2), S6 = cmul2(S4, S2);
        cp2 cb = cmul2(C, cmul2(e1, e1));
        #pragma unroll
        for (int a=0;a<2;a++){
          putg(col + (16+4*a)*TR, cb);
          putg(col + (16+4*a+1)*TR, cmul2(cb, S2));
          putg(col + (16+4*a+2)*TR, cmul2(cb, S4));
          putg(col + (16+4*a+3)*TR, cmul2(cb, S6));
          cb = cmul2(cb, e1);
        }
        #pragma unroll
        for (int a=0;a<4;a++){
          putg(col + (24+2*a)*TR, cb);
          putg(col + (24+2*a+1)*TR, cmul2(cb, S4));
          if (a < 3) cb = cmul2(cb, e1);
        }
      }
    }
    TSP(2);
    __builtin_amdgcn_fence(__ATOMIC_RELEASE, "workgroup");
    while (__hip_atomic_load(cnt + 2, __ATOMIC_RELAXED, __HIP_MEMORY_SCOPE_WORKGROUP) < 16u ||
           __hip_atomic_load(cnt + 4 + ch, __ATOMIC_RELAXED, __HIP_MEMORY_SCOPE_WORKGROUP) < 1u) __builtin_amdgcn_s_sleep(1);
    __builtin_amdgcn_fence(__ATOMIC_ACQUIRE, "workgroup");
    if (rj != 2){
      const char* ga = tb + (16*(rj == 3 ? 2 : rj) + m)*TR + 16*kg;
      const char* xb = tb + (41 + m)*TR + 16*kg;
      f16x8 GA[8], XB[8];
      #pragma unroll
      for (int kc=0;kc<8;kc++){ GA[kc] = *(const f16x8*)(ga + 64*kc); XB[kc] = *(const f16x8*)(xb + 64*kc); }
      __builtin_amdgcn_sched_barrier(0);
      f32x4 a4 = {0,0,0,0}, b4 = {0,0,0,0};
      #pragma unroll
      for (int kc=0;kc<8;kc+=2){ a4 = MFMAH(GA[kc], XB[kc], a4); b4 = MFMAH(GA[kc+1], XB[kc+1], b4); }
      float kv[4];
      #pragma unroll
      for (int i=0;i<4;i++) kv[i] = (a4[i] + b4[i]) * ksc;
      if (rj == 0){
        *(u32x2*)(nb + 16*(15 + 16*(kg >> 1) + m) + 8*(1 - (kg & 1))) = u32x2{pk2(kv[3], kv[2]), pk2(kv[1], kv[0])};
      } else if (rj == 1){
        const float k8 = i2f(__builtin_amdgcn_update_dpp(0, f2i(kv[0]), 0x130, 0xf, 0xf, true));
        if (kg < 2){
          *(u32x4*)(nb + 16*(47 + 16*kg + m)) = u32x4{pk2(k8, kv[3]), pk2(kv[2], kv[2]), pk2(kv[2], kv[1]), pk2(kv[0], kv[0])};
        } else {
          if (lane == 32) *(u32x4*)(nb + 16*79) = u32x4{0u, 0u, pk2(0.f, kv[0]), pk2(kv[0], kv[0])};
          #pragma unroll
          for (int i=0;i<4;i++){
            const int x1 = 32*(2*(kg-2) + (i >> 1)) + (i & 1) + 2*m - 1;
            char* p = ft + ((x1 >> 1) & 1)*(4*FCS) + 4*(47 - (x1 >> 2)) + 2*(x1 & 1);
            const u16 hv = f2bf(x1 >= 0 ? kv[i] : 0.f);
            #pragma unroll
            for (int c=0;c<4;c++) *(u16*)(p + c*(FCS-4)) = hv;
          }
        }
      } else {
        if (lane <= 32){
          const int x8 = 64*kg + m - 1;
          const int z = x8 < 0 ? 16 : 63 - (x8 >> 1);
          char* p = ft + (x8 < 0 ? 4*FCS : 8*FCS) + 4*z + 2*(x8 & 1);
          const u16 hv = f2bf(kv[0]);
          #pragma unroll
          for (int c=0;c<4;c++) *(u16*)(p + c*(FCS-4)) = hv;
          if (lane == 0) ((float*)(smem + K1OFF))[ch] = kv[0];
        }
        if (kg < 2){
          #pragma unroll
          for (int i=1;i<4;i++){
            const int x8 = 16*(4*kg + i) + m - 1;
            char* p = ft + 8*FCS + 4*(63 - (x8 >> 1)) + 2*(x8 & 1);
            const u16 hv = f2bf(kv[i]);
            #pragma unroll
            for (int c=0;c<4;c++) *(u16*)(p + c*(FCS-4)) = hv;
          }
        }
      }
      __builtin_amdgcn_s_setprio(0);
    } else {
      if (lane < 30) *(u32x4*)(nb + 16*(lane < 15 ? lane : lane + 65)) = u32x4{0,0,0,0};
      const int s5 = lane & 31, z = s5 < 16 ? s5 : s5 + 32;
      #pragma unroll
      for (int c=0;c<4;c++) if (z >= c) *(uint32_t*)(ft + (lane >> 5)*(4*FCS) + c*FCS + 4*(z - c)) = 0u;
      if (m - kg < 12) *(uint32_t*)(ft + 8*FCS + kg*FCS + 4*(64 + m - kg)) = 0u;
    }
    if (wave >= 8){ put_u(smem, tl, uu[0], uu[1]); put_u(smem, tl + 512, uu[2], uu[3]); }
    __syncthreads();
    TSP(3);
  } else {
    __builtin_amdgcn_sched_barrier(0);
    const u32x2 k0 = *(const u32x2*)(k + (2*tr)*D_ + c0);
    const u32x2 k1 = *(const u32x2*)(k + (2*tr+1)*D_ + c0);
    const u32x4 wa = *(const u32x4*)(shift_k + c0*4), wb = *(const u32x4*)(shift_k + c0*4 + 8);
    char* ks = smem + YOFF;
    __builtin_amdgcn_sched_barrier(0);
    *(u32x4*)(ks + 32 + 16*tr) = u32x4{k0.x, k0.y, k1.x, k1.y};
    __builtin_amdgcn_sched_barrier(0);
    const u32x2 v0 = *(const u32x2*)(v + (2*tr)*D_ + c0);
    const u32x2 v1 = *(const u32x2*)(v + (2*tr+1)*D_ + c0);
    const u32x4 sx = *(const u32x4*)(ssmk + (c0+(t >> 8))*L_ + 8*(t & 255));
    __builtin_amdgcn_sched_barrier(0);
    if (t < 2) *(u32x4*)(ks + 16*t) = u32x4{0,0,0,0};
    __syncthreads();
    TSP(1);
    u32x2 kk[5];
    kk[0] = *(const u32x2*)(ks + 8 + 16*tr);
    { u32x4 x = *(const u32x4*)(ks + 16 + 16*tr); kk[1] = u32x2{x.x, x.y}; kk[2] = u32x2{x.z, x.w}; }
    kk[3] = k0; kk[4] = k1;
    u32x2 o[2];
    #pragma unroll
    for (int j=0;j<2;j++){
      const u32x2 vv = j ? v1 : v0;
      #pragma unroll
      for (int p=0;p<2;p++){
        float r2[2];
        #pragma unroll
        for (int e=0;e<2;e++){
          const u32x4 ww = p ? wb : wa;
          const uint32_t wl = e ? ww.z : ww.x, wh = e ? ww.w : ww.y;
          const float w0 = lo16f(wl), w1 = hi16f(wl), w2 = lo16f(wh), w3 = hi16f(wh);
          auto g = [&](int idx){ uint32_t x = kk[idx][p]; return e? hi16f(x) : lo16f(x); };
          float acc = rbf(g(j)*w3);
          acc = rbf(acc + rbf(g(j+1)*w2));
          acc = rbf(acc + rbf(g(j+2)*w1));
          acc = rbf(acc + rbf(g(j+3)*w0));
          r2[e] = acc * (e? hi16f(vv[p]) : lo16f(vv[p]));
        }
        o[j][p] = pk2(r2[0], r2[1]);
      }
    }
    put_u(smem, tr, o[0], o[1]);
    {
      const int ch = t >> 8, j = t & 255;
      u32x4 r;
      r.x = __builtin_amdgcn_alignbit(sx.w, sx.w, 16); r.y = __builtin_amdgcn_alignbit(sx.z, sx.z, 16);
      r.z = __builtin_amdgcn_alignbit(sx.y, sx.y, 16); r.w = __builtin_amdgcn_alignbit(sx.x, sx.x, 16);
      char* kb = smem + TOFF + ch*KST;
      *(u32x4*)(kb + 16*(j+15)) = r;
      if (j < 15) *(u32x4*)(kb + 16*j) = u32x4{0,0,0,0};
    }
    __syncthreads();
    TSP(2); TSP(3);
  }
  int sig = -1, fi = wave - 8;
  if (isA){
    const int ch = wave >> 2, role = (wave + ch) & 3;
    if (role == 0){ __builtin_amdgcn_s_setprio(2); near_conv<4,0>(smem, ch, m, kg); }
    else if (role == 1) near_conv<4,4>(smem, ch, m, kg);
    else if (role == 2){ __builtin_amdgcn_s_setprio(3); far_conv<0,4>(smem, ch, m, kg); }
    else { __builtin_amdgcn_s_setprio(3); far_conv<4,6>(smem, ch, m, kg); }
    TSP(5);
    sig = role == 1;
    fi = role >= 2 ? 2*ch + role - 2 : -1;
  } else if (wave < 8){
    const int ch = wave >> 1;
    const int g = (wave ^ (wave >> 2)) & 1;
    if (g == 0) __builtin_amdgcn_s_setprio(2);
    sig = g;
    const int part = 0;
    const int unit = ch;
    const uint32_t kbase = (uint32_t)(uintptr_t)smem + TOFF + unit*KST + 16*(m - kg + 3);
    const char* ub = smem + ch*UST;
    bf16x8 KA[4], KB[4];
    LDK4(KA, 15);
    f32x4 acc[4];
    {
      bf16x8 Up[2][4];
      {
        const uint32_t sel = (m & 1) ? 0x07060504u : 0x05040302u;
        const char* pp = ub + 272*(m >> 1) + 16*kg + 8*g;
        #pragma unroll
        for (int c=0;c<4;c++){ W6 W = uwin(pp + 64*c); Up[0][c] = usel(W, 0, sel); Up[1][c] = usel(W, 1, sel); }
      }
      f32x4 P0 = {0,0,0,0}, P1 = {0,0,0,0};
#define PH1(dl, CUR, NXT) \
      LDK4(NXT, (dl)-1); WAITK(4, CUR); \
      if ((dl) < 15){ P0 = shrN2(P0); P1 = shrN2(P1); } \
      P0 = MFMA16(CUR[0], Up[0][0], P0); P1 = MFMA16(CUR[0], Up[1][0], P1); \
      P0 = MFMA16(CUR[1], Up[0][1], P0); P1 = MFMA16(CUR[1], Up[1][1], P1); \
      P0 = MFMA16(CUR[2], Up[0][2], P0); P1 = MFMA16(CUR[2], Up[1][2], P1); \
      P0 = MFMA16(CUR[3], Up[0][3], P0); P1 = MFMA16(CUR[3], Up[1][3], P1);
      PH1(15, KA, KB) PH1(14, KB, KA) PH1(13, KA, KB) PH1(12, KB, KA)
      PH1(11, KA, KB) PH1(10, KB, KA) PH1(9, KA, KB) PH1(8, KB, KA)
      const bool ok = m >= 1 && m <= 8;
      const int src = 4*((lane & 48) + ((2*(m-1)) & 15));
      #pragma unroll
      for (int i=0;i<4;i++){
        float a0 = i2f(__builtin_amdgcn_ds_bpermute(src,   f2i(P0[i])));
        float a1 = i2f(__builtin_amdgcn_ds_bpermute(src+4, f2i(P0[i])));
        float a2 = i2f(__builtin_amdgcn_ds_bpermute(src,   f2i(P1[i])));
        float a3 = i2f(__builtin_amdgcn_ds_bpermute(src+4, f2i(P1[i])));
        acc[0][i] = ok ? a0 : 0.f; acc[1][i] = ok ? a1 : 0.f; acc[2][i] = ok ? a2 : 0.f; acc[3][i] = ok ? a3 : 0.f;
      }
    }
    TSP(4);
    bf16x8 U[4][4];
    {
      const char* pp = ub + 272*m + 16*kg + 8*g;
      #pragma unroll
      for (int c=0;c<4;c++){ W6 W = uwin(pp + 64*c); U[0][c] = uodd(W, 0); U[1][c] = uevn(W, 1); U[2][c] = uodd(W, 1); U[3][c] = uevn(W, 2); }
    }
#define PH2(dl, CUR, NXT) \
    if ((dl) > 0){ LDK4(NXT, (dl)-1); WAITK(4, CUR); } else { WAITK(0, CUR); } \
    if ((dl) < 7){ acc[0] = shrN1(acc[0]); acc[1] = shrN1(acc[1]); acc[2] = shrN1(acc[2]); acc[3] = shrN1(acc[3]); } \
    _Pragma("unroll") for (int c=0;c<4;c++){ \
      acc[0] = MFMA16(CUR[c], U[0][c], acc[0]); acc[1] = MFMA16(CUR[c], U[1][c], acc[1]); \
      acc[2] = MFMA16(CUR[c], U[2][c], acc[2]); acc[3] = MFMA16(CUR[c], U[3][c], acc[3]); }
    PH2(7, KA, KB) PH2(6, KB, KA) PH2(5, KA, KB) PH2(4, KB, KA)
    PH2(3, KA, KB) PH2(2, KB, KA) PH2(1, KA, KB) PH2(0, KB, KA)
    TSP(5);
    char* yb = smem + YOFF + (part*4 + ch)*YST + 4*(4*g + 32*kg + 132*m);
    #pragma unroll
    for (int i=0;i<4;i++) *(f32x4*)(yb + 32*i) = f32x4{acc[0][i], acc[1][i], acc[2][i], acc[3][i]};
  }
  TSP(6);
  if (sig >= 0){
    __builtin_amdgcn_fence(__ATOMIC_RELEASE, "workgroup");
    if (lane == 0) __hip_atomic_fetch_add((unsigned*)(smem + CNTOFF) + sig, 1u, __ATOMIC_RELAXED, __HIP_MEMORY_SCOPE_WORKGROUP);
  }
  if (fi >= 0){
    const int p = (fi*64 + lane + 32*(w5 & 15)) & 511;
    u32x2 qr[4];
    if (!isA){
      const u16* qp = q + (8*(p >> 1) + 2*(p & 1))*D_ + c0;
      qr[0] = *(const u32x2*)(qp); qr[1] = *(const u32x2*)(qp + D_);
      qr[2] = *(const u32x2*)(qp + 4*D_); qr[3] = *(const u32x2*)(qp + 5*D_);
    }
    __builtin_amdgcn_sched_barrier(0);
    u16* op = out + (isA ? 0 : D_) + c0;
    const unsigned need0 = isA ? 12u : 4u;
    #pragma unroll
    for (int st=0; st<2; st++){
      const int tq = 4*(p >> 1) + (p & 1) + 2*st;
      const char* yb = smem + (isA ? YAOFF : YOFF) + 8*tq + 16*(tq >> 6);
      const char* up = smem + 272*(tq >> 6) + 16 + 4*(tq & 63);
      float s0[4], s1[4];
      #pragma unroll
      for (int c=0;c<4;c++){
        uint32_t uu = *(const uint32_t*)(up + c*UST);
        float sd = (c & 1) ? hi16f(dsk[c>>1]) : lo16f(dsk[c>>1]);
        s0[c] = rbf(lo16f(uu)*sd); s1[c] = rbf(hi16f(uu)*sd);
      }
      while (__hip_atomic_load((const unsigned*)(smem + CNTOFF) + st, __ATOMIC_RELAXED, __HIP_MEMORY_SCOPE_WORKGROUP) < (st ? 4u : need0)) __builtin_amdgcn_s_sleep(1);
      __builtin_amdgcn_fence(__ATOMIC_ACQUIRE, "workgroup");
      float o0[4], o1[4];
      if (isA){
        const char* yf = smem + YFOFF + (tq >= 257 ? 4*(2*tq - 515) : -16);
        #pragma unroll
        for (int c=0;c<4;c++){
          f32x2 a = *(const f32x2*)(yb + c*YST);
          float b0 = *(const float*)(yf + c*YFST), b1 = *(const float*)(yf + c*YFST + 4);
          o0[c] = rbf(a.x + b0) + s0[c];
          o1[c] = rbf(a.y + b1) + s1[c];
        }
      } else {
        #pragma unroll
        for (int c=0;c<4;c++){
          f32x2 a = *(const f32x2*)(yb + c*YST);
          float q0 = (c & 1) ? hi16f(qr[2*st][c>>1]) : lo16f(qr[2*st][c>>1]);
          float q1 = (c & 1) ? hi16f(qr[2*st+1][c>>1]) : lo16f(qr[2*st+1][c>>1]);
          o0[c] = q0 * rbf(rbf(a.x) + s0[c]);
          o1[c] = q1 * rbf(rbf(a.y) + s1[c]);
        }
      }
      *(u32x2*)(op + (2*tq)*(2*D_))   = u32x2{pk2(o0[0],o0[1]), pk2(o0[2],o0[3])};
      *(u32x2*)(op + (2*tq+1)*(2*D_)) = u32x2{pk2(o1[0],o1[1]), pk2(o1[2],o1[3])};
    }
  }
}

extern "C" void kernel_launch(void* const* d_in, const int* in_sizes, int n_in,
                              void* d_out, int out_size, void* d_ws, size_t ws_size,
                              hipStream_t stream) {
  mono_kernel<<<256, 1024, LDS_TOTAL, stream>>>((const u16*)d_in[0], (const u16*)d_in[1], (const u16*)d_in[2], (const u16*)d_in[3],
     (const u16*)d_in[4], (const u16*)d_in[5], (const u16*)d_in[6], (const u16*)d_in[7], (const u16*)d_in[8], (const u16*)d_in[9],
     (const u16*)d_in[10], (const u16*)d_in[11], (const u16*)d_in[12], (u16*)d_out
);
}
```

```cpp
#include <hip/hip_runtime.h>
#include <hip/hip_bf16.h>
#include <stdint.h>

typedef unsigned short u16;
typedef short bf16x8 __attribute__((ext_vector_type(8)));
typedef float f32x4 __attribute__((ext_vector_type(4)));
typedef float f32x2 __attribute__((ext_vector_type(2)));
typedef __bf16 bf16x2_t __attribute__((ext_vector_type(2)));

#define L_ 2048
#define D_ 512
#define NS_ 128

__device__ __forceinline__ float bf2f(u16 x){ return __builtin_bit_cast(float, (uint32_t)x << 16); }
__device__ __forceinline__ u16 f2bf(float f){ __hip_bfloat16 h = __float2bfloat16(f); return __builtin_bit_cast(u16, h); }
__device__ __forceinline__ float rbf(float f){ return bf2f(f2bf(f)); }
__device__ __forceinline__ uint32_t pk2(float a, float b){
  f32x2 v = {a,b}; bf16x2_t r = __builtin_convertvector(v, bf16x2_t); return __builtin_bit_cast(uint32_t, r); }
__device__ __forceinline__ float lo16f(uint32_t w){ return __builtin_bit_cast(float, w << 16); }
__device__ __forceinline__ float hi16f(uint32_t w){ return __builtin_bit_cast(float, w & 0xffff0000u); }

struct cpx { float re, im; };
__device__ __forceinline__ float amul(float a, float b){ float r; asm("v_mul_f32 %0, %1, %2" : "=v"(r) : "v"(a), "v"(b)); return r; }
__device__ __forceinline__ float afma(float a, float b, float c){ float r; asm("v_fma_f32 %0, %1, %2, %3" : "=v"(r) : "v"(a), "v"(b), "v"(c)); return r; }
__device__ __forceinline__ float afms(float a, float b, float c){ float r; asm("v_fma_f32 %0, %1, %2, -%3" : "=v"(r) : "v"(a), "v"(b), "v"(c)); return r; }
__device__ __forceinline__ cpx cmul(cpx a, cpx b){
  f32x2 A = {a.re, a.im}, B = {b.re, b.im}, T, R;
  asm("v_pk_mul_f32 %0, %1, %2 op_sel:[1,1] op_sel_hi:[1,0] neg_lo:[1,0]" : "=v"(T) : "v"(A), "v"(B));
  asm("v_pk_fma_f32 %0, %1, %2, %3 op_sel:[0,0,0] op_sel_hi:[0,1,1]" : "=v"(R) : "v"(A), "v"(B), "v"(T));
  cpx r; r.re = R.x; r.im = R.y; return r; }
__device__ __forceinline__ cpx cexp_(float re, float im){ float e = expf(re); float s,c; sincosf(im,&s,&c); cpx r; r.re=e*c; r.im=e*s; return r; }

#define MFMA16(a,b,c) __builtin_amdgcn_mfma_f32_16x16x32_bf16(a,b,c,0,0,0)
typedef uint32_t u32x2 __attribute__((ext_vector_type(2)));
typedef uint32_t u32x4 __attribute__((ext_vector_type(4)));
#define TR 544
#define TSZ (57*TR)
#define UST 4352
#define TOFF 17408
#define KST 4352
#define KS2 1536
#define XOFF (TOFF + 4*TSZ)
#define NOFF XOFF
#define FCS 320
#define FTS (12*FCS)
#define FTOFF (XOFF + 4*KS2)
#define K1OFF (FTOFF + 4*FTS)
#define YFOFF (TOFF + 45056 + 16)
#define YFST 6176
#define YAOFF (TOFF + 69888)
#define YOFF (TOFF + 36864)
#define YST 8448
#define YBOFF (YOFF + 4*YST)
#define YB2 4352
#define CNTOFF (K1OFF + 16)
#define LDS_TOTAL (CNTOFF + 80)
typedef uint32_t u32x6 __attribute__((ext_vector_type(6)));
typedef _Float16 f16x8 __attribute__((ext_vector_type(8)));
typedef _Float16 f16x2 __attribute__((ext_vector_type(2)));
#define MFMAH(a,b,c) __builtin_amdgcn_mfma_f32_16x16x32_f16(a,b,c,0,0,0)
__device__ __forceinline__ uint32_t pkh(float a, float b){
  f32x2 v = {a,b}; f16x2 r = __builtin_convertvector(v, f16x2); return __builtin_bit_cast(uint32_t, r); }
struct cp2 { cpx a, b; };
__device__ __forceinline__ cp2 cmul2(const cp2& x, const cp2& y){ cp2 r; r.a = cmul(x.a, y.a); r.b = cmul(x.b, y.b); return r; }
__device__ __forceinline__ void putg(char* p, const cp2& x){ *(u32x2*)p = u32x2{pkh(x.a.re, x.a.im), pkh(x.b.re, x.b.im)}; }
__device__ __forceinline__ void putx(char* p, const cp2& x){ *(u32x2*)p = u32x2{pkh(x.a.re, -x.a.im), pkh(x.b.re, -x.b.im)}; }

__device__ __forceinline__ uint32_t resid2(uint32_t h, float a, float b){
  return pk2(a - lo16f(h), b - hi16f(h));
}
__device__ __forceinline__ int f2i(float x){ return __builtin_bit_cast(int, x); }
__device__ __forceinline__ float i2f(int x){ return __builtin_bit_cast(float, x); }
__device__ __forceinline__ f32x4 shrN1(f32x4 x){
  f32x4 r;
  #pragma unroll
  for (int i=0;i<4;i++) r[i] = i2f(__builtin_amdgcn_update_dpp(0, f2i(x[i]), 0x111, 0xf, 0xf, true));
  return r;
}
__device__ __forceinline__ f32x4 shrN2(f32x4 x){
  f32x4 r;
  #pragma unroll
  for (int i=0;i<4;i++) r[i] = i2f(__builtin_amdgcn_update_dpp(0, f2i(x[i]), 0x112, 0xf, 0xf, true));
  return r;
}
__device__ __forceinline__ f32x4 shlN1(f32x4 x){
  f32x4 r;
  #pragma unroll
  for (int i=0;i<4;i++) r[i] = i2f(__builtin_amdgcn_update_dpp(0, f2i(x[i]), 0x101, 0xf, 0xf, true));
  return r;
}
__device__ __forceinline__ f32x4 shlN2(f32x4 x){
  f32x4 r;
  #pragma unroll
  for (int i=0;i<4;i++) r[i] = i2f(__builtin_amdgcn_update_dpp(0, f2i(x[i]), 0x102, 0xf, 0xf, true));
  return r;
}
__device__ __forceinline__ f32x4 shlN8(f32x4 x){
  f32x4 r;
  #pragma unroll
  for (int i=0;i<4;i++) r[i] = i2f(__builtin_amdgcn_update_dpp(0, f2i(x[i]), 0x108, 0xf, 0xf, true));
  return r;
}
__device__ __forceinline__ u32x2 wshr1(u32x2 old, u32x2 x){
  u32x2 r;
  r.x = (uint32_t)__builtin_amdgcn_update_dpp((int)old.x, (int)x.x, 0x138, 0xf, 0xf, false);
  r.y = (uint32_t)__builtin_amdgcn_update_dpp((int)old.y, (int)x.y, 0x138, 0xf, 0xf, false);
  return r;
}
struct W6 { uint32_t w[6]; };
__device__ __forceinline__ W6 uwin(const char* p){
  u32x2 a = *(const u32x2*)p, b = *(const u32x2*)(p+8), c = *(const u32x2*)(p+16);
  W6 r; r.w[0]=a.x; r.w[1]=a.y; r.w[2]=b.x; r.w[3]=b.y; r.w[4]=c.x; r.w[5]=c.y; return r;
}
__device__ __forceinline__ bf16x8 usel(const W6& W, int x, uint32_t sel){
  u32x4 r;
  r.x = __builtin_amdgcn_perm(W.w[x+1], W.w[x],   sel); r.y = __builtin_amdgcn_perm(W.w[x+2], W.w[x+1], sel);
  r.z = __builtin_amdgcn_perm(W.w[x+3], W.w[x+2], sel); r.w = __builtin_amdgcn_perm(W.w[x+4], W.w[x+3], sel);
  return __builtin_bit_cast(bf16x8, r);
}
__device__ __forceinline__ bf16x8 uodd(const W6& W, int x){
  u32x4 r;
  r.x = __builtin_amdgcn_alignbit(W.w[x+1], W.w[x],   16); r.y = __builtin_amdgcn_alignbit(W.w[x+2], W.w[x+1], 16);
  r.z = __builtin_amdgcn_alignbit(W.w[x+3], W.w[x+2], 16); r.w = __builtin_amdgcn_alignbit(W.w[x+4], W.w[x+3], 16);
  return __builtin_bit_cast(bf16x8, r);
}
__device__ __forceinline__ bf16x8 uevn(const W6& W, int x){
  u32x4 r = {W.w[x], W.w[x+1], W.w[x+2], W.w[x+3]};
  return __builtin_bit_cast(bf16x8, r);
}
__device__ __forceinline__ void put_u(char* smem, int tr, u32x2 r0, u32x2 r1){
  uint32_t d[4];
  d[0] = __builtin_amdgcn_perm(r1.x, r0.x, 0x05040100); d[1] = __builtin_amdgcn_perm(r1.x, r0.x, 0x07060302);
  d[2] = __builtin_amdgcn_perm(r1.y, r0.y, 0x05040100); d[3] = __builtin_amdgcn_perm(r1.y, r0.y, 0x07060302);
  const int a = tr >> 6, pos = tr & 63;
  char* p = smem + 272*a + 16 + 4*pos;
  #pragma unroll
  for (int c=0;c<4;c++) *(uint32_t*)(p + c*UST) = d[c];
  if (pos >= 60 && a < 15){
    #pragma unroll
    for (int c=0;c<4;c++) *(uint32_t*)(p + c*UST + 16) = d[c];
  }
  if (tr < 16) *(uint32_t*)(smem + (tr>>2)*UST + 4*(tr&3)) = 0u;
}

#define LDK(F, dl, c) asm volatile("ds_read_b128 %0, %1 offset:%2" : "=v"(F) : "v"(kbase), "i"(256*(dl) + 64*(3-(c))))
#define LDK4(S, dl) LDK(S[0], dl, 0); LDK(S[1], dl, 1); LDK(S[2], dl, 2); LDK(S[3], dl, 3)
#define WAITK(n, S) asm volatile("s_waitcnt lgkmcnt(" #n ")" : "+v"(S[0]), "+v"(S[1]), "+v"(S[2]), "+v"(S[3]))

template<int NT, int R0>
__device__ __forceinline__ void near_conv(char* smem, int ch, int m, int kg){
  const uint32_t kbase = (uint32_t)(uintptr_t)smem + NOFF + ch*KS2 + 16*(m - kg + 3);
#define LDN(F, hs, c) asm volatile("ds_read_b128 %0, %1 offset:%2" : "=v"(F) : "v"(kbase), "i"(256*(4-(hs)) + 64*(3-(c))))
#define LDN4(S, hs) LDN(S[0], hs, 0); LDN(S[1], hs, 1); LDN(S[2], hs, 2); LDN(S[3], hs, 3)
  bf16x8 KA[4], KB[4];
  LDN4(KA, 0);
  bf16x8 U[NT][4];
  {
    const char* pp = smem + ch*UST + 272*m + 16*kg + (R0 ? 8 : 0);
    #pragma unroll
    for (int c=0;c<4;c++){
      W6 W = uwin(pp + 64*c);
      #pragma unroll
      for (int r=0;r<NT;r++){ const int rel = R0 + r + 1 - (R0 ? 4 : 0); U[r][c] = (rel & 1) ? uodd(W, rel >> 1) : uevn(W, rel >> 1); }
    }
  }
  f32x4 acc[NT];
  #pragma unroll
  for (int r=0;r<NT;r++) acc[r] = f32x4{0,0,0,0};
#define NH(hs, CUR, NXT) \
  if ((hs) < 4){ LDN4(NXT, (hs)+1); WAITK(4, CUR); } else { WAITK(0, CUR); } \
  if ((hs) > 0){ _Pragma("unroll") for (int r=0;r<NT;r++) acc[r] = shrN1(acc[r]); } \
  _Pragma("unroll") for (int c=0;c<4;c++){ _Pragma("unroll") for (int r=0;r<NT;r++) acc[r] = MFMA16(CUR[c], U[r][c], acc[r]); }
  NH(0, KA, KB) NH(1, KB, KA) NH(2, KA, KB) NH(3, KB, KA) NH(4, KA, KB)
  float* yb = (float*)(smem + YAOFF + ch*YST) + (R0 + 32*kg + 132*m);
  #pragma unroll
  for (int i=0;i<4;i++){
    #pragma unroll
    for (int r=0;r<NT;r++) yb[8*i + r] = acc[r][i];
  }
}
__device__ __forceinline__ bf16x8 band(int sh, int w, int kg){
  const uint32_t by = (((1u << w) - 1u) << sh) >> (8*kg);
  u32x4 r;
  #pragma unroll
  for (int k2=0;k2<4;k2++){ uint32_t x = (by >> (2*k2)) & 3u; x = (x | (x << 15)) & 0x00010001u; r[k2] = x * 0x3f80u; }
  return __builtin_bit_cast(bf16x8, r);
}
__device__ __forceinline__ void split2(const f32x4& a, const f32x4& b, bf16x8& h, bf16x8& l){
  u32x4 hh, ll;
  #pragma unroll
  for (int i=0;i<4;i++){ uint32_t x = pk2(a[i], b[i]); hh[i] = x; ll[i] = resid2(x, a[i], b[i]); }
  h = __builtin_bit_cast(bf16x8, hh); l = __builtin_bit_cast(bf16x8, ll);
}
__device__ __forceinline__ bf16x8 pack2(const f32x4& a, const f32x4& b){
  u32x4 hh;
  #pragma unroll
  for (int i=0;i<4;i++) hh[i] = pk2(a[i], b[i]);
  return __builtin_bit_cast(bf16x8, hh);
}
template<int A0, int A1>
__device__ __forceinline__ void far_conv(char* smem, int ch, int m, int kg){
  const char* ub = smem + ch*UST;
  const f32x4 Z4 = {0,0,0,0};
  const int B0 = 2*m + kg - 2;
  const char* va0 = ub + (B0 < 0 ? 0 : 272*(B0 >> 4) + 16 + 16*(B0 & 15));
  const char* va1 = ub + 272*((B0+32) >> 4) + 16 + 16*((B0+32) & 15);
  const int cp = (3 - m) & 3;
  const char* ft = smem + FTOFF + ch*FTS + cp*FCS + 4*(47 - m + 4*kg - cp);
  char* yf = smem + YFOFF + ch*YFST + 64*m + 16*kg;
  if (A0 == 0 && kg == 0 && m < 4) *(float*)(smem + YFOFF + ch*YFST - 16 + 4*m) = 0.f;
  f32x4 acc[6];
  #pragma unroll
  for (int a=0;a<6;a++) acc[a] = Z4;
  {
    bf16x8 M[4];
    M[0] = band(14 + m, 3, kg); M[1] = band(9 + m, 5, kg); M[2] = band(6 + m, 3, kg); M[3] = band(1 + m, 5, kg);
    bf16x8 Ch[3][2];
    #pragma unroll
    for (int dl=0;dl<3;dl++){
      #pragma unroll
      for (int h=0;h<2;h++) Ch[dl][h] = *(const bf16x8*)(ft + h*(4*FCS) - 64*dl);
    }
    #pragma unroll
    for (int b=(A0 > 2 ? A0-2 : 0);b<A1;b++){
      const bf16x8 V = *(const bf16x8*)(b ? va1 + 544*(b-1) : va0);
      f32x4 F[4];
      #pragma unroll
      for (int p=0;p<4;p++) F[p] = MFMA16(V, M[p], Z4);
      #pragma unroll
      for (int h=0;h<2;h++){
        const bf16x8 Bh = pack2(F[2*h], F[2*h+1]);
        #pragma unroll
        for (int dl=0;dl<3;dl++) if (b + dl >= A0 && b + dl < A1) acc[b+dl] = MFMA16(Bh, Ch[dl][h], acc[b+dl]);
      }
      if (b < 2 && b >= A0) *(f32x4*)(yf + 1024*b) = acc[b];
    }
  }
  {
    const char* vb0 = ub + (B0 < 1 ? 0 : 272*((B0-1) >> 4) + 16 + 16*((B0-1) & 15));
    const char* vb1 = ub + 272*((B0+31) >> 4) + 16 + 16*((B0+31) & 15);
    const char* f8 = smem + FTOFF + ch*FTS + 8*FCS + cp*FCS + 4*(63 - m + 4*kg - cp);
    const bf16x8 Ma = band(8 + m, 7, kg), Mb = band(7 + m, 9, kg), Mc = band(15 + m, 2, kg);
    const float k1024 = ((const float*)(smem + K1OFF))[ch];
    bf16x8 Ch[4];
    #pragma unroll
    for (int dl=0;dl<A1-2;dl++) Ch[dl] = *(const bf16x8*)(f8 - 64*dl);
    #pragma unroll
    for (int b=0;b<A1-2;b++){
      const bf16x8 V = *(const bf16x8*)(b ? va1 + 544*(b-1) : va0);
      const bf16x8 V2 = *(const bf16x8*)(b ? vb1 + 544*(b-1) : vb0);
      const f32x4 Fa = MFMA16(V, Ma, Z4), Fb = MFMA16(V2, Mb, Z4);
      const bf16x8 Bh = pack2(Fa, Fb);
      if (b + 2 >= A0){
        const f32x4 Fc = MFMA16(Mc, V, Z4);
        #pragma unroll
        for (int i=0;i<4;i++) acc[b+2][i] = __builtin_fmaf(k1024, Fc[i], acc[b+2][i]);
      }
      #pragma unroll
      for (int dl=0;dl<4;dl++) if (b + 2 + dl >= A0 && b + 2 + dl < A1) acc[b+2+dl] = MFMA16(Bh, Ch[dl], acc[b+2+dl]);
    }
  }
  #pragma unroll
  for (int a=(A0 > 2 ? A0 : 2);a<A1;a++) *(f32x4*)(yf + 1024*a) = acc[a];
}

__global__ __launch_bounds__(1024) void mono_kernel(const u16* __restrict__ u, const u16* __restrict__ logA, const u16* __restrict__ Aim,
    const u16* __restrict__ Cre, const u16* __restrict__ Cim, const u16* __restrict__ logdt, const u16* __restrict__ Dskip,
    const u16* __restrict__ q, const u16* __restrict__ k, const u16* __restrict__ v, const u16* __restrict__ shift_k,
    const u16* __restrict__ ssmk, const u16* __restrict__ ssmD, u16* __restrict__ out
){
#define TSP(i)
  TSP(0); TSP(1); TSP(2); TSP(3); TSP(4); TSP(5);
  extern __shared__ __attribute__((aligned(16))) char smem[];
  const int bid = blockIdx.x, t = threadIdx.x;
  const int w5 = bid >> 3;
  const bool isA = w5 < 16;
  const int c0 = (bid & 7)*64 + (w5 & 15)*4;
  const int wave = t >> 6, lane = t & 63, m = lane & 15, kg = lane >> 4;
  asm volatile("" :: "s"(u), "s"(logA), "s"(Aim), "s"(Cre), "s"(Cim), "s"(logdt), "s"(Dskip), "s"(q), "s"(k), "s"(v), "s"(shift_k), "s"(ssmk), "s"(ssmD), "s"(out));
  u32x2 dsk;
  if (t < 20) ((unsigned*)(smem + CNTOFF))[t] = 0u;
  int vz; asm volatile("v_mov_b32 %0, 0" : "=v"(vz));
  asm volatile("global_load_dwordx2 %0, %1, %2" : "=v"(dsk) : "v"(2*c0 + vz), "s"(isA ? Dskip : ssmD));
  if (isA){
    const int ch = wave & 3, rj = wave >> 2;
    const int d = c0 + ch;
    char* tb = smem + TOFF + ch*TSZ;
    unsigned* cnt = (unsigned*)(smem + CNTOFF);
    uint32_t ldt, pcr, pci, lde, pla, pai;
    asm volatile("global_load_ushort %0, %1, %2" : "=v"(lde) : "v"(2*(c0 + ((t >> 7) & 3))), "s"(logdt));
    asm volatile("global_load_ushort %0, %1, %2" : "=v"(pla) : "v"(2*(c0*NS_ + (t & 511))), "s"(logA));
    asm volatile("global_load_ushort %0, %1, %2" : "=v"(pai) : "v"(2*(c0*NS_ + (t & 511))), "s"(Aim));
    asm volatile("global_load_ushort %0, %1, %2" : "=v"(ldt) : "v"(2*d), "s"(logdt));
    asm volatile("global_load_dword %0, %1, %2" : "=v"(pcr) : "v"(2*d*NS_ + 4*lane), "s"(Cre));
    asm volatile("global_load_dword %0, %1, %2" : "=v"(pci) : "v"(2*d*NS_ + 4*lane), "s"(Cim));
#define PWAIT(n) asm volatile("s_waitcnt vmcnt(" #n ")" : "+v"(lde), "+v"(pla), "+v"(pai), "+v"(ldt), "+v"(pcr), "+v"(pci), "+v"(dsk))
    __builtin_amdgcn_sched_barrier(0);
    __builtin_amdgcn_s_barrier();
    __builtin_amdgcn_sched_barrier(0);
    u32x2 uu[4];
    const int tl = (t + 64*(w5 & 15)) & 511;
    cp2 C; float ksc;
    auto prep = [&](){
      uint32_t e0 = pcr & 0x7f807f80u, e1 = pci & 0x7f807f80u;
      uint32_t x = max(max(e0 & 0xffffu, e0 >> 16), max(e1 & 0xffffu, e1 >> 16));
      x = max(x, (uint32_t)__builtin_amdgcn_update_dpp(0, (int)x, 0xb1, 0xf, 0xf, true));
      x = max(x, (uint32_t)__builtin_amdgcn_update_dpp(0, (int)x, 0x4e, 0xf, 0xf, true));
      x = max(x, (uint32_t)__builtin_amdgcn_update_dpp(0, (int)x, 0x141, 0xf, 0xf, true));
      x = max(x, (uint32_t)__builtin_amdgcn_update_dpp(0, (int)x, 0x140, 0xf, 0xf, true));
      const uint32_t y = max(max((uint32_t)__builtin_amdgcn_readlane((int)x, 0), (uint32_t)__builtin_amdgcn_readlane((int)x, 16)),
                             max((uint32_t)__builtin_amdgcn_readlane((int)x, 32), (uint32_t)__builtin_amdgcn_readlane((int)x, 48)));
      const int sh = 127 - (int)(y >> 7);
      C.a.re = ldexpf(lo16f(pcr), sh); C.a.im = ldexpf(lo16f(pci), sh);
      C.b.re = ldexpf(hi16f(pcr), sh); C.b.im = ldexpf(hi16f(pci), sh);
      ksc = ldexpf(2.f*rbf(expf(lo16f(ldt))), -sh);
      asm volatile("" : "+v"(C.a.re), "+v"(C.a.im), "+v"(C.b.re), "+v"(C.b.im), "+v"(ksc));
    };
    if (wave >= 8){
      const u16* up = u + (2*tl)*D_ + c0;
      uu[0] = *(const u32x2*)(up);           uu[1] = *(const u32x2*)(up + D_);
      uu[2] = *(const u32x2*)(up + 1024*D_); uu[3] = *(const u32x2*)(up + 1025*D_);
      __builtin_amdgcn_sched_barrier(0);
      PWAIT(4);
      prep();
    } else {
      PWAIT(3);
      const float dte = rbf(expf(lo16f(lde)));
      const float zre = -rbf(expf(lo16f(pla))) * dte;
      const float zim = lo16f(pai) * dte;
      const cpx e = cexp_(zre, zim), f = cexp_(zre*128.f, zim*128.f);
      char* ep = smem + XOFF + (t >> 7)*2048 + 8*(t & 127);
      *(float2*)(ep) = make_float2(e.re, e.im);
      *(float2*)(ep + 1024) = make_float2(f.re, f.im);
      PWAIT(0);
      prep();
    }
    __syncthreads();
    TSP(1);
    char* col = tb + 8*lane;
    char* nb = smem + NOFF + ch*KS2;
    char* ft = smem + FTOFF + ch*FTS;
    {
      const char* eb_ = smem + XOFF + ch*2048;
      cp2 e1, e2;
      { f32x4 a = *(const f32x4*)(eb_ + 16*lane); e2.a.re = a[0]; e2.a.im = a[1]; e2.b.re = a[2]; e2.b.im = a[3];
        f32x4 c = *(const f32x4*)(eb_ + 1024 + 16*lane); e1.a.re = c[0]; e1.a.im = c[1]; e1.b.re = c[2]; e1.b.im = c[3]; }
      __builtin_amdgcn_fence(__ATOMIC_RELEASE, "workgroup");
      if (lane == 0) __hip_atomic_fetch_add(cnt + 2, 1u, __ATOMIC_RELAXED, __HIP_MEMORY_SCOPE_WORKGROUP);
      if (rj == 2){
        __builtin_amdgcn_s_setprio(3);
        cp2 e8 = cmul2(e2, e2); e8 = cmul2(e8, e8); e8 = cmul2(e8, e8);
        *(u32x2*)(col + 41*TR) = u32x2{0x3c00u, 0x3c00u};
        cp2 e16 = cmul2(e8, e8);
        cp2 xa = e8, xb = e16;
        #pragma unroll
        for (int i=1;i<16;i+=2){
          putx(col + (41+i)*TR, xa); if (i < 15) putx(col + (42+i)*TR, xb);
          if (i < 15) xa = cmul2(xa, e16);
          if (i < 13) xb = cmul2(xb, e16);
        }
        __builtin_amdgcn_fence(__ATOMIC_RELEASE, "workgroup");
        if (lane == 0) __hip_atomic_fetch_add(cnt + 4 + ch, 1u, __ATOMIC_RELAXED, __HIP_MEMORY_SCOPE_WORKGROUP);
        __builtin_amdgcn_s_setprio(0);
      } else if (rj == 3){
        __builtin_amdgcn_s_setprio(2);
        cp2 eb = cmul2(e1, e1); eb = cmul2(eb, eb); eb = cmul2(eb, eb);
        cp2 cb = cmul2(C, eb);
        #pragma unroll
        for (int i=0;i<9;i++){ putg(col + (32+i)*TR, cb); if (i < 8) cb = cmul2(cb, e1); }
      } else if (rj == 0){
        cp2 cb = C, cd = cmul2(C, e1);
        #pragma unroll
        for (int p=0;p<8;p++){
          putg(col + p*TR, cb); putg(col + (8+p)*TR, cd);
          if (p < 7){ cb = cmul2(cb, e2); cd = cmul2(cd, e2); }
        }
      } else {
        __builtin_amdgcn_s_setprio(1);
        cp2 S2 = cmul2(e2, e2), S4 = cmul2(S2, S2), S6 = cmul2(S4, S2);
        cp2 cb = cmul2(C, cmul2(e1, e1));
        #pragma unroll
        for (int a=0;a<2;a++){
          putg(col + (16+4*a)*TR, cb);
          putg(col + (16+4*a+1)*TR, cmul2(cb, S2));
          putg(col + (16+4*a+2)*TR, cmul2(cb, S4));
          putg(col + (16+4*a+3)*TR, cmul2(cb, S6));
          cb = cmul2(cb, e1);
        }
        #pragma unroll
        for (int a=0;a<4;a++){
          putg(col + (24+2*a)*TR, cb);
          putg(col + (24+2*a+1)*TR, cmul2(cb, S4));
          if (a < 3) cb = cmul2(cb, e1);
        }
      }
    }
    TSP(2);
    __builtin_amdgcn_fence(__ATOMIC_RELEASE, "workgroup");
    while (__hip_atomic_load(cnt + 2, __ATOMIC_RELAXED, __HIP_MEMORY_SCOPE_WORKGROUP) < 16u ||
           __hip_atomic_load(cnt + 4 + ch, __ATOMIC_RELAXED, __HIP_MEMORY_SCOPE_WORKGROUP) < 1u) __builtin_amdgcn_s_sleep(1);
    __builtin_amdgcn_fence(__ATOMIC_ACQUIRE, "workgroup");
    if (rj != 2){
      const char* ga = tb + (16*(rj == 3 ? 2 : rj) + m)*TR + 16*kg;
      const char* xb = tb + (41 + m)*TR + 16*kg;
      f16x8 GA[8], XB[8];
      #pragma unroll
      for (int kc=0;kc<8;kc++){ GA[kc] = *(const f16x8*)(ga + 64*kc); XB[kc] = *(const f16x8*)(xb + 64*kc); }
      __builtin_amdgcn_sched_barrier(0);
      f32x4 a4 = {0,0,0,0}, b4 = {0,0,0,0};
      #pragma unroll
      for (int kc=0;kc<8;kc+=2){ a4 = MFMAH(GA[kc], XB[kc], a4); b4 = MFMAH(GA[kc+1], XB[kc+1], b4); }
      float kv[4];
      #pragma unroll
      for (int i=0;i<4;i++) kv[i] = (a4[i] + b4[i]) * ksc;
      if (rj == 0){
        *(u32x2*)(nb + 16*(15 + 16*(kg >> 1) + m) + 8*(1 - (kg & 1))) = u32x2{pk2(kv[3], kv[2]), pk2(kv[1], kv[0])};
      } else if (rj == 1){
        const float k8 = i2f(__builtin_amdgcn_update_dpp(0, f2i(kv[0]), 0x130, 0xf, 0xf, true));
        if (kg < 2){
          *(u32x4*)(nb + 16*(47 + 16*kg + m)) = u32x4{pk2(k8, kv[3]), pk2(kv[2], kv[2]), pk2(kv[2], kv[1]), pk2(kv[0], kv[0])};
        } else {
          if (lane == 32) *(u32x4*)(nb + 16*79) = u32x4{0u, 0u, pk2(0.f, kv[0]), pk2(kv[0], kv[0])};
          #pragma unroll
          for (int i=0;i<4;i++){
            const int x1 = 32*(2*(kg-2) + (i >> 1)) + (i & 1) + 2*m - 1;
            char* p = ft + ((x1 >> 1) & 1)*(4*FCS) + 4*(47 - (x1 >> 2)) + 2*(x1 & 1);
            const u16 hv = f2bf(x1 >= 0 ? kv[i] : 0.f);
            #pragma unroll
            for (int c=0;c<4;c++) *(u16*)(p + c*(FCS-4)) = hv;
          }
        }
      } else {
        if (lane <= 32){
          const int x8 = 64*kg + m - 1;
          const int z = x8 < 0 ? 16 : 63 - (x8 >> 1);
          char* p = ft + (x8 < 0 ? 4*FCS : 8*FCS) + 4*z + 2*(x8 & 1);
          const u16 hv = f2bf(kv[0]);
          #pragma unroll
          for (int c=0;c<4;c++) *(u16*)(p + c*(FCS-4)) = hv;
          if (lane == 0) ((float*)(smem + K1OFF))[ch] = kv[0];
        }
        if (kg < 2){
          #pragma unroll
          for (int i=1;i<4;i++){
            const int x8 = 16*(4*kg + i) + m - 1;
            char* p = ft + 8*FCS + 4*(63 - (x8 >> 1)) + 2*(x8 & 1);
            const u16 hv = f2bf(kv[i]);
            #pragma unroll
            for (int c=0;c<4;c++) *(u16*)(p + c*(FCS-4)) = hv;
          }
        }
      }
      __builtin_amdgcn_s_setprio(0);
    } else {
      if (lane < 30) *(u32x4*)(nb + 16*(lane < 15 ? lane : lane + 65)) = u32x4{0,0,0,0};
      const int s5 = lane & 31, z = s5 < 16 ? s5 : s5 + 32;
      #pragma unroll
      for (int c=0;c<4;c++) if (z >= c) *(uint32_t*)(ft + (lane >> 5)*(4*FCS) + c*FCS + 4*(z - c)) = 0u;
      if (m - kg < 12) *(uint32_t*)(ft + 8*FCS + kg*FCS + 4*(64 + m - kg)) = 0u;
    }
    if (wave >= 8){ put_u(smem, tl, uu[0], uu[1]); put_u(smem, tl + 512, uu[2], uu[3]); }
    __syncthreads();
    TSP(3);
  } else {
    __builtin_amdgcn_sched_barrier(0);
    const u32x4 sx = *(const u32x4*)(ssmk + (c0+(t >> 8))*L_ + 8*(t & 255));
    const u32x4 wa = *(const u32x4*)(shift_k + c0*4), wb = *(const u32x4*)(shift_k + c0*4 + 8);
    const int tr = (t & 512) | ((t + 64*(w5 & 15)) & 511);
    const int r0 = 2*(tr - lane);
    const u16* hp = k + (r0 ? r0 - 3 : 0)*D_ + c0 + vz;
    __builtin_amdgcn_sched_barrier(0);
    if (wave >= 8) __builtin_amdgcn_s_barrier();
    __builtin_amdgcn_sched_barrier(0);
    u32x2 h0 = *(const u32x2*)(hp), h1 = *(const u32x2*)(hp + D_), h2 = *(const u32x2*)(hp + 2*D_);
    __builtin_amdgcn_sched_barrier(0);
    const u32x2 k0 = *(const u32x2*)(k + (2*tr)*D_ + c0);
    const u32x2 k1 = *(const u32x2*)(k + (2*tr+1)*D_ + c0);
    const u32x2 v0 = *(const u32x2*)(v + (2*tr)*D_ + c0);
    const u32x2 v1 = *(const u32x2*)(v + (2*tr+1)*D_ + c0);
    __builtin_amdgcn_sched_barrier(0);
    if (wave < 8){ asm volatile("s_waitcnt lgkmcnt(0)" ::: "memory"); __builtin_amdgcn_s_barrier(); }
    __builtin_amdgcn_sched_barrier(0);
    {
      const int ch = t >> 8, j = t & 255;
      u32x4 r;
      r.x = __builtin_amdgcn_alignbit(sx.w, sx.w, 16); r.y = __builtin_amdgcn_alignbit(sx.z, sx.z, 16);
      r.z = __builtin_amdgcn_alignbit(sx.y, sx.y, 16); r.w = __builtin_amdgcn_alignbit(sx.x, sx.x, 16);
      char* kb = smem + TOFF + ch*KST;
      *(u32x4*)(kb + 16*(j+15)) = r;
      if (j < 15) *(u32x4*)(kb + 16*j) = u32x4{0,0,0,0};
    }
    const uint32_t cad = (uint32_t)(uintptr_t)smem + CNTOFF, on1 = 1u;
    if (wave >= 8 && lane == 0) asm volatile("ds_add_u32 %0, %1 offset:64" :: "v"(cad), "v"(on1) : "memory");
    if (r0 == 0){ h0 = u32x2{0u,0u}; h1 = u32x2{0u,0u}; h2 = u32x2{0u,0u}; }
    TSP(1);
    u32x2 kk[5];
    kk[1] = wshr1(h1, k0); kk[2] = wshr1(h2, k1); kk[0] = wshr1(h0, kk[2]);
    kk[3] = k0; kk[4] = k1;
    u32x2 o[2];
    #pragma unroll
    for (int j=0;j<2;j++){
      const u32x2 vv = j ? v1 : v0;
      #pragma unroll
      for (int p=0;p<2;p++){
        float r2[2];
        #pragma unroll
        for (int e=0;e<2;e++){
          const u32x4 ww = p ? wb : wa;
          const uint32_t wl = e ? ww.z : ww.x, wh = e ? ww.w : ww.y;
          const float w0 = lo16f(wl), w1 = hi16f(wl), w2 = lo16f(wh), w3 = hi16f(wh);
          auto g = [&](int idx){ uint32_t x = kk[idx][p]; return e? hi16f(x) : lo16f(x); };
          float acc = rbf(g(j)*w3);
          acc = rbf(acc + rbf(g(j+1)*w2));
          acc = rbf(acc + rbf(g(j+2)*w1));
          acc = rbf(acc + rbf(g(j+3)*w0));
          r2[e] = acc * (e? hi16f(vv[p]) : lo16f(vv[p]));
        }
        o[j][p] = pk2(r2[0], r2[1]);
      }
    }
    put_u(smem, tr, o[0], o[1]);
    if (lane == 0) asm volatile("ds_add_u32 %0, %1 offset:64" :: "v"(cad + (wave >= 8 ? 4 : 0)), "v"(on1) : "memory");
    TSP(2);
  }
  int sig = -1, fi = -1;
  if (isA){
    const int ch = wave >> 2, role = (wave + ch) & 3;
    if (role == 0){ __builtin_amdgcn_s_setprio(2); near_conv<4,0>(smem, ch, m, kg); }
    else if (role == 1) near_conv<4,4>(smem, ch, m, kg);
    else if (role == 2){ __builtin_amdgcn_s_setprio(3); far_conv<0,4>(smem, ch, m, kg); }
    else { __builtin_amdgcn_s_setprio(3); far_conv<4,6>(smem, ch, m, kg); }
    TSP(5);
    sig = role == 1;
    fi = role >= 2 ? 2*ch + role - 2 : -1;
  } else if (wave < 8){
    const int ch = wave & 3;
    const uint32_t sb = (uint32_t)(uintptr_t)smem;
    const uint32_t kbase = sb + TOFF + ch*KST + 16*(m - kg + 3);
    const char* ub = smem + ch*UST + 272*(m >> 1) + 16*kg;
    const uint32_t sel = (m & 1) ? 0x07060504u : 0x05040302u;
    const unsigned* cnt = (const unsigned*)(smem + CNTOFF);
    const uint32_t yw = sb + (wave < 4 ? YOFF + ch*YST : YBOFF - 4096 + ch*YB2) + 512*(m >> 1) + 64*(m & 1) + 16*kg;
    const uint32_t cw = sb + CNTOFF + 4*(m >> 1);
    const uint32_t one = 1u;
    bf16x8 KA[4], KB[4];
    u32x6 Ea[4], Eb[4];
    f32x4 acc[4];
#define BUILDU(off) { \
    u32x4 dd[8]; \
    _Pragma("unroll") for (int c=0;c<4;c++){ dd[2*c] = *(const u32x4*)(ub + (off) + 64*c); dd[2*c+1] = *(const u32x4*)(ub + (off) + 64*c + 16); } \
    __builtin_amdgcn_sched_barrier(0); \
    _Pragma("unroll") for (int c=0;c<4;c++){ \
      const u32x4 d0 = dd[2*c], d1 = dd[2*c+1]; \
      const uint32_t e0 = __builtin_amdgcn_perm(d0.y, d0.x, sel), e1 = __builtin_amdgcn_perm(d0.z, d0.y, sel), e2 = __builtin_amdgcn_perm(d0.w, d0.z, sel), \
        e3 = __builtin_amdgcn_perm(d1.x, d0.w, sel), e4 = __builtin_amdgcn_perm(d1.y, d1.x, sel), e5 = __builtin_amdgcn_perm(d1.z, d1.y, sel), \
        e6 = __builtin_amdgcn_perm(d1.w, d1.z, sel); \
      Ea[c] = u32x6{e0,e1,e2,e3,e4,e5}; Eb[c] = u32x6{e1,e2,e3,e4,e5,e6}; \
      asm volatile("" : "+v"(Ea[c]), "+v"(Eb[c])); } }
#define UT(E, x) __builtin_bit_cast(bf16x8, __builtin_shufflevector(E, E, x, x+1, x+2, x+3))
#define MM16(CUR) \
    _Pragma("unroll") for (int c=0;c<4;c++){ \
      acc[0] = MFMA16(CUR[c], UT(Ea[c], 0), acc[0]); acc[1] = MFMA16(CUR[c], UT(Eb[c], 0), acc[1]); \
      acc[2] = MFMA16(CUR[c], UT(Ea[c], 2), acc[2]); acc[3] = MFMA16(CUR[c], UT(Eb[c], 2), acc[3]); }
#define EMIT(mask, cj) asm volatile("s_mov_b64 exec, %[mk]\n\ts_nop 7\n\t" \
      "ds_write_b128 %[a], %[v0] offset:%[o0]\n\tds_write_b128 %[a], %[v1] offset:%[o1]\n\t" \
      "ds_write_b128 %[a], %[v2] offset:%[o2]\n\tds_write_b128 %[a], %[v3] offset:%[o3]\n\t" \
      "ds_add_u32 %[ca], %[on] offset:%[oc]\n\ts_mov_b64 exec, -1" \
      :: [mk] "s"(0x0001000100010001ull * (mask)), [a] "v"(yw), [v0] "v"(acc[0]), [v1] "v"(acc[1]), [v2] "v"(acc[2]), [v3] "v"(acc[3]), \
         [o0] "i"(512*(cj)), [o1] "i"(512*(cj)+128), [o2] "i"(512*(cj)+256), [o3] "i"(512*(cj)+384), \
         [ca] "v"(cw), [on] "v"(one), [oc] "i"(4*(cj)) : "memory")
#define SHL(F) { acc[0] = F(acc[0]); acc[1] = F(acc[1]); acc[2] = F(acc[2]); acc[3] = F(acc[3]); }
    while (__hip_atomic_load(cnt + 16, __ATOMIC_RELAXED, __HIP_MEMORY_SCOPE_WORKGROUP) < 16u) __builtin_amdgcn_s_sleep(1);
    __builtin_amdgcn_fence(__ATOMIC_ACQUIRE, "workgroup");
    TSP(3);
    #pragma unroll
    for (int r=0;r<4;r++) acc[r] = f32x4{0,0,0,0};
    if (wave < 4){
      __builtin_amdgcn_s_setprio(1);
      LDK4(KA, 0);
      BUILDU(0)
#define X1(s, CUR, NXT) \
      LDK4(NXT, ((s)+1) & 7); if ((s) > 0){ WAITK(9, CUR); SHL(shlN2) } else { WAITK(4, CUR); } \
      MM16(CUR) EMIT(3, s);
      X1(0, KA, KB) X1(1, KB, KA) X1(2, KA, KB) X1(3, KB, KA) X1(4, KA, KB) X1(5, KB, KA) X1(6, KA, KB) X1(7, KB, KA)
      TSP(4);
      while (__hip_atomic_load(cnt + 17, __ATOMIC_RELAXED, __HIP_MEMORY_SCOPE_WORKGROUP) < 8u) __builtin_amdgcn_s_sleep(1);
      __builtin_amdgcn_fence(__ATOMIC_ACQUIRE, "workgroup");
      BUILDU(272*8)
      LDK4(KB, 1); WAITK(4, KA); SHL(shlN2) MM16(KA) EMIT(3, 8);
      LDK4(KA, 2); WAITK(9, KB); SHL(shlN2) MM16(KB) EMIT(3, 9);
      LDK4(KB, 3); WAITK(9, KA); SHL(shlN2) MM16(KA) EMIT(3, 10);
      WAITK(5, KB); SHL(shlN2) MM16(KB) EMIT(0x3ff, 11);
    } else {
      LDK4(KA, 15);
      BUILDU(0)
#define X2(dl, CUR, NXT) \
      LDK4(NXT, (dl) > 8 ? (dl)-1 : 4); WAITK(4, CUR); if ((dl) < 15) SHL(shrN2) \
      MM16(CUR)
      X2(15, KA, KB) X2(14, KB, KA) X2(13, KA, KB) X2(12, KB, KA) X2(11, KA, KB) X2(10, KB, KA) X2(9, KA, KB) X2(8, KB, KA)
      EMIT(0xff, 8);
      TSP(4);
      while (__hip_atomic_load(cnt + 17, __ATOMIC_RELAXED, __HIP_MEMORY_SCOPE_WORKGROUP) < 8u) __builtin_amdgcn_s_sleep(1);
      __builtin_amdgcn_fence(__ATOMIC_ACQUIRE, "workgroup");
      BUILDU(272*8)
      LDK4(KB, 5); WAITK(4, KA); SHL(shlN8) MM16(KA) EMIT(3, 12);
      LDK4(KA, 6); WAITK(9, KB); SHL(shlN2) MM16(KB) EMIT(3, 13);
      LDK4(KB, 7); WAITK(9, KA); SHL(shlN2) MM16(KA) EMIT(3, 14);
      WAITK(5, KB); SHL(shlN2) MM16(KB) EMIT(3, 15);
    }
    TSP(5);
  } else {
    __builtin_amdgcn_s_setprio(2);
    const int j0 = (wave + w5) & 7;
    const int lr = 8*m + 2*kg;
    u32x2 qr[4];
    {
      const u16* qp = q + (128*j0 + lr)*D_ + c0;
      qr[0] = *(const u32x2*)(qp); qr[1] = *(const u32x2*)(qp + D_);
      qr[2] = *(const u32x2*)(qp + 1024*D_); qr[3] = *(const u32x2*)(qp + 1025*D_);
    }
    __builtin_amdgcn_sched_barrier(0);
    #pragma unroll
    for (int st=0; st<2; st++){
      const int cj = j0 + 8*st;
      const char* yb = smem + YOFF + 512*cj + 128*kg + 4*m;
      const char* up = smem + 272*cj + 16 + 2*lr;
      while (__hip_atomic_load((const unsigned*)(smem + CNTOFF) + cj, __ATOMIC_RELAXED, __HIP_MEMORY_SCOPE_WORKGROUP) < (st ? 64u : 32u)) __builtin_amdgcn_s_sleep(1);
      __builtin_amdgcn_fence(__ATOMIC_ACQUIRE, "workgroup");
      float s0[4], s1[4];
      #pragma unroll
      for (int c=0;c<4;c++){
        uint32_t uu = *(const uint32_t*)(up + c*UST);
        float sd = (c & 1) ? hi16f(dsk[c>>1]) : lo16f(dsk[c>>1]);
        s0[c] = rbf(lo16f(uu)*sd); s1[c] = rbf(hi16f(uu)*sd);
      }
      float o0[4], o1[4];
      #pragma unroll
      for (int c=0;c<4;c++){
        float a0 = *(const float*)(yb + c*YST), a1 = *(const float*)(yb + c*YST + 64);
        if (st){ const char* y2 = yb + (YBOFF - 4096 - YOFF) + c*YB2; a0 += *(const float*)(y2); a1 += *(const float*)(y2 + 64); }
        float q0 = (c & 1) ? hi16f(qr[2*st][c>>1]) : lo16f(qr[2*st][c>>1]);
        float q1 = (c & 1) ? hi16f(qr[2*st+1][c>>1]) : lo16f(qr[2*st+1][c>>1]);
        o0[c] = q0 * rbf(rbf(a0) + s0[c]);
        o1[c] = q1 * rbf(rbf(a1) + s1[c]);
      }
      u16* op = out + D_ + c0 + (128*cj + lr)*(2*D_);
      *(u32x2*)(op)        = u32x2{pk2(o0[0],o0[1]), pk2(o0[2],o0[3])};
      *(u32x2*)(op + 2*D_) = u32x2{pk2(o1[0],o1[1]), pk2(o1[2],o1[3])};
    }
  }
  TSP(6);
  if (sig >= 0){
    __builtin_amdgcn_fence(__ATOMIC_RELEASE, "workgroup");
    if (lane == 0) __hip_atomic_fetch_add((unsigned*)(smem + CNTOFF) + sig, 1u, __ATOMIC_RELAXED, __HIP_MEMORY_SCOPE_WORKGROUP);
  }
  if (fi >= 0){
    const int p = (fi*64 + lane + 32*(w5 & 15)) & 511;
    u16* op = out + c0;
    #pragma unroll
    for (int st=0; st<2; st++){
      const int tq = 4*(p >> 1) + (p & 1) + 2*st;
      const char* yb = smem + YAOFF + 8*tq + 16*(tq >> 6);
      const char* up = smem + 272*(tq >> 6) + 16 + 4*(tq & 63);
      float s0[4], s1[4];
      #pragma unroll
      for (int c=0;c<4;c++){
        uint32_t uu = *(const uint32_t*)(up + c*UST);
        float sd = (c & 1) ? hi16f(dsk[c>>1]) : lo16f(dsk[c>>1]);
        s0[c] = rbf(lo16f(uu)*sd); s1[c] = rbf(hi16f(uu)*sd);
      }
      while (__hip_atomic_load((const unsigned*)(smem + CNTOFF) + st, __ATOMIC_RELAXED, __HIP_MEMORY_SCOPE_WORKGROUP) < (st ? 4u : 12u)) __builtin_amdgcn_s_sleep(1);
      __builtin_amdgcn_fence(__ATOMIC_ACQUIRE, "workgroup");
      float o0[4], o1[4];
      const char* yf = smem + YFOFF + (tq >= 257 ? 4*(2*tq - 515) : -16);
      #pragma unroll
      for (int c=0;c<4;c++){
        f32x2 a = *(const f32x2*)(yb + c*YST);
        float b0 = *(const float*)(yf + c*YFST), b1 = *(const float*)(yf + c*YFST + 4);
        o0[c] = rbf(a.x + b0) + s0[c];
        o1[c] = rbf(a.y + b1) + s1[c];
      }
      *(u32x2*)(op + (2*tq)*(2*D_))   = u32x2{pk2(o0[0],o0[1]), pk2(o0[2],o0[3])};
      *(u32x2*)(op + (2*tq+1)*(2*D_)) = u32x2{pk2(o1[0],o1[1]), pk2(o1[2],o1[3])};
    }
  }
}

extern "C" void kernel_launch(void* const* d_in, const int* in_sizes, int n_in,
                              void* d_out, int out_size, void* d_ws, size_t ws_size,
                              hipStream_t stream) {
  mono_kernel<<<256, 1024, LDS_TOTAL, stream>>>((const u16*)d_in[0], (const u16*)d_in[1], (const u16*)d_in[2], (const u16*)d_in[3],
     (const u16*)d_in[4], (const u16*)d_in[5], (const u16*)d_in[6], (const u16*)d_in[7], (const u16*)d_in[8], (const u16*)d_in[9],
     (const u16*)d_in[10], (const u16*)d_in[11], (const u16*)d_in[12], (u16*)d_out
);
}
```

```cpp
#include <hip/hip_runtime.h>
#include <hip/hip_bf16.h>
#include <stdint.h>

typedef unsigned short u16;
typedef short bf16x8 __attribute__((ext_vector_type(8)));
typedef float f32x4 __attribute__((ext_vector_type(4)));
typedef float f32x2 __attribute__((ext_vector_type(2)));
typedef __bf16 bf16x2_t __attribute__((ext_vector_type(2)));

#define L_ 2048
#define D_ 512
#define NS_ 128

__device__ __forceinline__ float bf2f(u16 x){ return __builtin_bit_cast(float, (uint32_t)x << 16); }
__device__ __forceinline__ u16 f2bf(float f){ __hip_bfloat16 h = __float2bfloat16(f); return __builtin_bit_cast(u16, h); }
__device__ __forceinline__ float rbf(float f){ return bf2f(f2bf(f)); }
__device__ __forceinline__ uint32_t pk2(float a, float b){
  f32x2 v = {a,b}; bf16x2_t r = __builtin_convertvector(v, bf16x2_t); return __builtin_bit_cast(uint32_t, r); }
__device__ __forceinline__ float lo16f(uint32_t w){ return __builtin_bit_cast(float, w << 16); }
__device__ __forceinline__ float hi16f(uint32_t w){ return __builtin_bit_cast(float, w & 0xffff0000u); }

struct cpx { float re, im; };
__device__ __forceinline__ float amul(float a, float b){ float r; asm("v_mul_f32 %0, %1, %2" : "=v"(r) : "v"(a), "v"(b)); return r; }
__device__ __forceinline__ float afma(float a, float b, float c){ float r; asm("v_fma_f32 %0, %1, %2, %3" : "=v"(r) : "v"(a), "v"(b), "v"(c)); return r; }
__device__ __forceinline__ float afms(float a, float b, float c){ float r; asm("v_fma_f32 %0, %1, %2, -%3" : "=v"(r) : "v"(a), "v"(b), "v"(c)); return r; }
__device__ __forceinline__ cpx cmul(cpx a, cpx b){
  f32x2 A = {a.re, a.im}, B = {b.re, b.im}, T, R;
  asm("v_pk_mul_f32 %0, %1, %2 op_sel:[1,1] op_sel_hi:[1,0] neg_lo:[1,0]" : "=v"(T) : "v"(A), "v"(B));
  asm("v_pk_fma_f32 %0, %1, %2, %3 op_sel:[0,0,0] op_sel_hi:[0,1,1]" : "=v"(R) : "v"(A), "v"(B), "v"(T));
  cpx r; r.re = R.x; r.im = R.y; return r; }
__device__ __forceinline__ cpx cexp_(float re, float im){ float e = expf(re); float s,c; sincosf(im,&s,&c); cpx r; r.re=e*c; r.im=e*s; return r; }

#define MFMA16(a,b,c) __builtin_amdgcn_mfma_f32_16x16x32_bf16(a,b,c,0,0,0)
typedef uint32_t u32x2 __attribute__((ext_vector_type(2)));
typedef uint32_t u32x4 __attribute__((ext_vector_type(4)));
#define TR 544
#define TSZ (57*TR)
#define UST 4352
#define TOFF 17408
#define KST 4352
#define KS2 1536
#define XOFF (TOFF + 4*TSZ)
#define NOFF XOFF
#define FCS 320
#define FTS (12*FCS)
#define FTOFF (XOFF + 4*KS2)
#define K1OFF (FTOFF + 4*FTS)
#define YFOFF (TOFF + 45056 + 16)
#define YFST 6176
#define YAOFF (TOFF + 69888)
#define YOFF (TOFF + 36864)
#define YST 8448
#define YBOFF (YOFF + 4*YST)
#define YB2 4352
#define CNTOFF (K1OFF + 16)
#define LDS_TOTAL (CNTOFF + 80)
typedef uint32_t u32x6 __attribute__((ext_vector_type(6)));
typedef _Float16 f16x8 __attribute__((ext_vector_type(8)));
typedef _Float16 f16x2 __attribute__((ext_vector_type(2)));
#define MFMAH(a,b,c) __builtin_amdgcn_mfma_f32_16x16x32_f16(a,b,c,0,0,0)
__device__ __forceinline__ uint32_t pkh(float a, float b){
  f32x2 v = {a,b}; f16x2 r = __builtin_convertvector(v, f16x2); return __builtin_bit_cast(uint32_t, r); }
struct cp2 { cpx a, b; };
__device__ __forceinline__ cp2 cmul2(const cp2& x, const cp2& y){ cp2 r; r.a = cmul(x.a, y.a); r.b = cmul(x.b, y.b); return r; }
__device__ __forceinline__ void putg(char* p, const cp2& x){ *(u32x2*)p = u32x2{pkh(x.a.re, x.a.im), pkh(x.b.re, x.b.im)}; }
__device__ __forceinline__ void putx(char* p, const cp2& x){ *(u32x2*)p = u32x2{pkh(x.a.re, -x.a.im), pkh(x.b.re, -x.b.im)}; }

__device__ __forceinline__ uint32_t resid2(uint32_t h, float a, float b){
  return pk2(a - lo16f(h), b - hi16f(h));
}
__device__ __forceinline__ int f2i(float x){ return __builtin_bit_cast(int, x); }
__device__ __forceinline__ float i2f(int x){ return __builtin_bit_cast(float, x); }
__device__ __forceinline__ f32x4 shrN1(f32x4 x){
  f32x4 r;
  #pragma unroll
  for (int i=0;i<4;i++) r[i] = i2f(__builtin_amdgcn_update_dpp(0, f2i(x[i]), 0x111, 0xf, 0xf, true));
  return r;
}
__device__ __forceinline__ f32x4 shrN2(f32x4 x){
  f32x4 r;
  #pragma unroll
  for (int i=0;i<4;i++) r[i] = i2f(__builtin_amdgcn_update_dpp(0, f2i(x[i]), 0x112, 0xf, 0xf, true));
  return r;
}
__device__ __forceinline__ f32x4 shlN1(f32x4 x){
  f32x4 r;
  #pragma unroll
  for (int i=0;i<4;i++) r[i] = i2f(__builtin_amdgcn_update_dpp(0, f2i(x[i]), 0x101, 0xf, 0xf, true));
  return r;
}
__device__ __forceinline__ f32x4 shlN2(f32x4 x){
  f32x4 r;
  #pragma unroll
  for (int i=0;i<4;i++) r[i] = i2f(__builtin_amdgcn_update_dpp(0, f2i(x[i]), 0x102, 0xf, 0xf, true));
  return r;
}
__device__ __forceinline__ f32x4 shlN8(f32x4 x){
  f32x4 r;
  #pragma unroll
  for (int i=0;i<4;i++) r[i] = i2f(__builtin_amdgcn_update_dpp(0, f2i(x[i]), 0x108, 0xf, 0xf, true));
  return r;
}
__device__ __forceinline__ u32x2 wshr1(u32x2 old, u32x2 x){
  u32x2 r;
  r.x = (uint32_t)__builtin_amdgcn_update_dpp((int)old.x, (int)x.x, 0x138, 0xf, 0xf, false);
  r.y = (uint32_t)__builtin_amdgcn_update_dpp((int)old.y, (int)x.y, 0x138, 0xf, 0xf, false);
  return r;
}
struct W6 { uint32_t w[6]; };
__device__ __forceinline__ W6 uwin(const char* p){
  u32x2 a = *(const u32x2*)p, b = *(const u32x2*)(p+8), c = *(const u32x2*)(p+16);
  W6 r; r.w[0]=a.x; r.w[1]=a.y; r.w[2]=b.x; r.w[3]=b.y; r.w[4]=c.x; r.w[5]=c.y; return r;
}
__device__ __forceinline__ bf16x8 usel(const W6& W, int x, uint32_t sel){
  u32x4 r;
  r.x = __builtin_amdgcn_perm(W.w[x+1], W.w[x],   sel); r.y = __builtin_amdgcn_perm(W.w[x+2], W.w[x+1], sel);
  r.z = __builtin_amdgcn_perm(W.w[x+3], W.w[x+2], sel); r.w = __builtin_amdgcn_perm(W.w[x+4], W.w[x+3], sel);
  return __builtin_bit_cast(bf16x8, r);
}
__device__ __forceinline__ bf16x8 uodd(const W6& W, int x){
  u32x4 r;
  r.x = __builtin_amdgcn_alignbit(W.w[x+1], W.w[x],   16); r.y = __builtin_amdgcn_alignbit(W.w[x+2], W.w[x+1], 16);
  r.z = __builtin_amdgcn_alignbit(W.w[x+3], W.w[x+2], 16); r.w = __builtin_amdgcn_alignbit(W.w[x+4], W.w[x+3], 16);
  return __builtin_bit_cast(bf16x8, r);
}
__device__ __forceinline__ bf16x8 uevn(const W6& W, int x){
  u32x4 r = {W.w[x], W.w[x+1], W.w[x+2], W.w[x+3]};
  return __builtin_bit_cast(bf16x8, r);
}
__device__ __forceinline__ void put_u(char* smem, int tr, u32x2 r0, u32x2 r1){
  uint32_t d[4];
  d[0] = __builtin_amdgcn_perm(r1.x, r0.x, 0x05040100); d[1] = __builtin_amdgcn_perm(r1.x, r0.x, 0x07060302);
  d[2] = __builtin_amdgcn_perm(r1.y, r0.y, 0x05040100); d[3] = __builtin_amdgcn_perm(r1.y, r0.y, 0x07060302);
  const int a = tr >> 6, pos = tr & 63;
  char* p = smem + 272*a + 16 + 4*pos;
  #pragma unroll
  for (int c=0;c<4;c++) *(uint32_t*)(p + c*UST) = d[c];
  if (pos >= 60 && a < 15){
    #pragma unroll
    for (int c=0;c<4;c++) *(uint32_t*)(p + c*UST + 16) = d[c];
  }
  if (tr < 16) *(uint32_t*)(smem + (tr>>2)*UST + 4*(tr&3)) = 0u;
}

#define LDK(F, dl, c) asm volatile("ds_read_b128 %0, %1 offset:%2" : "=v"(F) : "v"(kbase), "i"(256*(dl) + 64*(3-(c))))
#define LDK4(S, dl) LDK(S[0], dl, 0); LDK(S[1], dl, 1); LDK(S[2], dl, 2); LDK(S[3], dl, 3)
#define WAITK(n, S) asm volatile("s_waitcnt lgkmcnt(" #n ")" : "+v"(S[0]), "+v"(S[1]), "+v"(S[2]), "+v"(S[3]))

template<int NT, int R0>
__device__ __forceinline__ void near_conv(char* smem, int ch, int m, int kg){
  const uint32_t kbase = (uint32_t)(uintptr_t)smem + NOFF + ch*KS2 + 16*(m - kg + 3);
#define LDN(F, hs, c) asm volatile("ds_read_b128 %0, %1 offset:%2" : "=v"(F) : "v"(kbase), "i"(256*(4-(hs)) + 64*(3-(c))))
#define LDN4(S, hs) LDN(S[0], hs, 0); LDN(S[1], hs, 1); LDN(S[2], hs, 2); LDN(S[3], hs, 3)
  bf16x8 KA[4], KB[4];
  LDN4(KA, 0);
  bf16x8 U[NT][4];
  {
    const char* pp = smem + ch*UST + 272*m + 16*kg + (R0 ? 8 : 0);
    #pragma unroll
    for (int c=0;c<4;c++){
      W6 W = uwin(pp + 64*c);
      #pragma unroll
      for (int r=0;r<NT;r++){ const int rel = R0 + r + 1 - (R0 ? 4 : 0); U[r][c] = (rel & 1) ? uodd(W, rel >> 1) : uevn(W, rel >> 1); }
    }
  }
  f32x4 acc[NT];
  #pragma unroll
  for (int r=0;r<NT;r++) acc[r] = f32x4{0,0,0,0};
#define NH(hs, CUR, NXT) \
  if ((hs) < 4){ LDN4(NXT, (hs)+1); WAITK(4, CUR); } else { WAITK(0, CUR); } \
  if ((hs) > 0){ _Pragma("unroll") for (int r=0;r<NT;r++) acc[r] = shrN1(acc[r]); } \
  _Pragma("unroll") for (int c=0;c<4;c++){ _Pragma("unroll") for (int r=0;r<NT;r++) acc[r] = MFMA16(CUR[c], U[r][c], acc[r]); }
  NH(0, KA, KB) NH(1, KB, KA) NH(2, KA, KB) NH(3, KB, KA) NH(4, KA, KB)
  float* yb = (float*)(smem + YAOFF + ch*YST) + (R0 + 32*kg + 132*m);
  #pragma unroll
  for (int i=0;i<4;i++){
    #pragma unroll
    for (int r=0;r<NT;r++) yb[8*i + r] = acc[r][i];
  }
}
__device__ __forceinline__ bf16x8 band(int sh, int w, int kg){
  const uint32_t by = (((1u << w) - 1u) << sh) >> (8*kg);
  u32x4 r;
  #pragma unroll
  for (int k2=0;k2<4;k2++){ uint32_t x = (by >> (2*k2)) & 3u; x = (x | (x << 15)) & 0x00010001u; r[k2] = x * 0x3f80u; }
  return __builtin_bit_cast(bf16x8, r);
}
__device__ __forceinline__ void split2(const f32x4& a, const f32x4& b, bf16x8& h, bf16x8& l){
  u32x4 hh, ll;
  #pragma unroll
  for (int i=0;i<4;i++){ uint32_t x = pk2(a[i], b[i]); hh[i] = x; ll[i] = resid2(x, a[i], b[i]); }
  h = __builtin_bit_cast(bf16x8, hh); l = __builtin_bit_cast(bf16x8, ll);
}
__device__ __forceinline__ bf16x8 pack2(const f32x4& a, const f32x4& b){
  u32x4 hh;
  #pragma unroll
  for (int i=0;i<4;i++) hh[i] = pk2(a[i], b[i]);
  return __builtin_bit_cast(bf16x8, hh);
}
template<int A0, int A1>
__device__ __forceinline__ void far_conv(char* smem, int ch, int m, int kg){
  const char* ub = smem + ch*UST;
  const f32x4 Z4 = {0,0,0,0};
  const int B0 = 2*m + kg - 2;
  const char* va0 = ub + (B0 < 0 ? 0 : 272*(B0 >> 4) + 16 + 16*(B0 & 15));
  const char* va1 = ub + 272*((B0+32) >> 4) + 16 + 16*((B0+32) & 15);
  const int cp = (3 - m) & 3;
  const char* ft = smem + FTOFF + ch*FTS + cp*FCS + 4*(47 - m + 4*kg - cp);
  char* yf = smem + YFOFF + ch*YFST + 64*m + 16*kg;
  if (A0 == 0 && kg == 0 && m < 4) *(float*)(smem + YFOFF + ch*YFST - 16 + 4*m) = 0.f;
  f32x4 acc[6];
  #pragma unroll
  for (int a=0;a<6;a++) acc[a] = Z4;
  {
    bf16x8 M[4];
    M[0] = band(14 + m, 3, kg); M[1] = band(9 + m, 5, kg); M[2] = band(6 + m, 3, kg); M[3] = band(1 + m, 5, kg);
    bf16x8 Ch[3][2];
    #pragma unroll
    for (int dl=0;dl<3;dl++){
      #pragma unroll
      for (int h=0;h<2;h++) Ch[dl][h] = *(const bf16x8*)(ft + h*(4*FCS) - 64*dl);
    }
    #pragma unroll
    for (int b=(A0 > 2 ? A0-2 : 0);b<A1;b++){
      const bf16x8 V = *(const bf16x8*)(b ? va1 + 544*(b-1) : va0);
      f32x4 F[4];
      #pragma unroll
      for (int p=0;p<4;p++) F[p] = MFMA16(V, M[p], Z4);
      #pragma unroll
      for (int h=0;h<2;h++){
        const bf16x8 Bh = pack2(F[2*h], F[2*h+1]);
        #pragma unroll
        for (int dl=0;dl<3;dl++) if (b + dl >= A0 && b + dl < A1) acc[b+dl] = MFMA16(Bh, Ch[dl][h], acc[b+dl]);
      }
      if (b < 2 && b >= A0) *(f32x4*)(yf + 1024*b) = acc[b];
    }
  }
  {
    const char* vb0 = ub + (B0 < 1 ? 0 : 272*((B0-1) >> 4) + 16 + 16*((B0-1) & 15));
    const char* vb1 = ub + 272*((B0+31) >> 4) + 16 + 16*((B0+31) & 15);
    const char* f8 = smem + FTOFF + ch*FTS + 8*FCS + cp*FCS + 4*(63 - m + 4*kg - cp);
    const bf16x8 Ma = band(8 + m, 7, kg), Mb = band(7 + m, 9, kg), Mc = band(15 + m, 2, kg);
    const float k1024 = ((const float*)(smem + K1OFF))[ch];
    bf16x8 Ch[4];
    #pragma unroll
    for (int dl=0;dl<A1-2;dl++) Ch[dl] = *(const bf16x8*)(f8 - 64*dl);
    #pragma unroll
    for (int b=0;b<A1-2;b++){
      const bf16x8 V = *(const bf16x8*)(b ? va1 + 544*(b-1) : va0);
      const bf16x8 V2 = *(const bf16x8*)(b ? vb1 + 544*(b-1) : vb0);
      const f32x4 Fa = MFMA16(V, Ma, Z4), Fb = MFMA16(V2, Mb, Z4);
      const bf16x8 Bh = pack2(Fa, Fb);
      if (b + 2 >= A0){
        const f32x4 Fc = MFMA16(Mc, V, Z4);
        #pragma unroll
        for (int i=0;i<4;i++) acc[b+2][i] = __builtin_fmaf(k1024, Fc[i], acc[b+2][i]);
      }
      #pragma unroll
      for (int dl=0;dl<4;dl++) if (b + 2 + dl >= A0 && b + 2 + dl < A1) acc[b+2+dl] = MFMA16(Bh, Ch[dl], acc[b+2+dl]);
    }
  }
  #pragma unroll
  for (int a=(A0 > 2 ? A0 : 2);a<A1;a++) *(f32x4*)(yf + 1024*a) = acc[a];
}

__global__ __launch_bounds__(1024) void mono_kernel(const u16* __restrict__ u, const u16* __restrict__ logA, const u16* __restrict__ Aim,
    const u16* __restrict__ Cre, const u16* __restrict__ Cim, const u16* __restrict__ logdt, const u16* __restrict__ Dskip,
    const u16* __restrict__ q, const u16* __restrict__ k, const u16* __restrict__ v, const u16* __restrict__ shift_k,
    const u16* __restrict__ ssmk, const u16* __restrict__ ssmD, u16* __restrict__ out
){
  unsigned long long tsv[10];
#define TSP(i) tsv[i] = __builtin_amdgcn_s_memrealtime()
  TSP(0); TSP(1); TSP(2); TSP(3); TSP(4); TSP(5);
  extern __shared__ __attribute__((aligned(16))) char smem[];
  const int bid = blockIdx.x, t = threadIdx.x;
  const int w5 = bid >> 3;
  const bool isA = w5 < 16;
  const int c0 = (bid & 7)*64 + (w5 & 15)*4;
  const int wave = t >> 6, lane = t & 63, m = lane & 15, kg = lane >> 4;
  asm volatile("" :: "s"(u), "s"(logA), "s"(Aim), "s"(Cre), "s"(Cim), "s"(logdt), "s"(Dskip), "s"(q), "s"(k), "s"(v), "s"(shift_k), "s"(ssmk), "s"(ssmD), "s"(out));
  u32x2 dsk;
  if (t < 20) ((unsigned*)(smem + CNTOFF))[t] = 0u;
  int vz; asm volatile("v_mov_b32 %0, 0" : "=v"(vz));
  asm volatile("global_load_dwordx2 %0, %1, %2" : "=v"(dsk) : "v"(2*c0 + vz), "s"(isA ? Dskip : ssmD));
  if (isA){
    const int ch = wave & 3, rj = wave >> 2;
    const int d = c0 + ch;
    char* tb = smem + TOFF + ch*TSZ;
    unsigned* cnt = (unsigned*)(smem + CNTOFF);
    uint32_t ldt, pcr, pci, lde, pla, pai;
    asm volatile("global_load_ushort %0, %1, %2" : "=v"(lde) : "v"(2*(c0 + ((t >> 7) & 3))), "s"(logdt));
    asm volatile("global_load_ushort %0, %1, %2" : "=v"(pla) : "v"(2*(c0*NS_ + (t & 511))), "s"(logA));
    asm volatile("global_load_ushort %0, %1, %2" : "=v"(pai) : "v"(2*(c0*NS_ + (t & 511))), "s"(Aim));
    asm volatile("global_load_ushort %0, %1, %2" : "=v"(ldt) : "v"(2*d), "s"(logdt));
    asm volatile("global_load_dword %0, %1, %2" : "=v"(pcr) : "v"(2*d*NS_ + 4*lane), "s"(Cre));
    asm volatile("global_load_dword %0, %1, %2" : "=v"(pci) : "v"(2*d*NS_ + 4*lane), "s"(Cim));
#define PWAIT(n) asm volatile("s_waitcnt vmcnt(" #n ")" : "+v"(lde), "+v"(pla), "+v"(pai), "+v"(ldt), "+v"(pcr), "+v"(pci), "+v"(dsk))
    __builtin_amdgcn_sched_barrier(0);
    __builtin_amdgcn_s_barrier();
    __builtin_amdgcn_sched_barrier(0);
    u32x2 uu[4];
    const int tl = (t + 64*(w5 & 15)) & 511;
    cp2 C; float ksc;
    auto prep = [&](){
      uint32_t e0 = pcr & 0x7f807f80u, e1 = pci & 0x7f807f80u;
      uint32_t x = max(max(e0 & 0xffffu, e0 >> 16), max(e1 & 0xffffu, e1 >> 16));
      x = max(x, (uint32_t)__builtin_amdgcn_update_dpp(0, (int)x, 0xb1, 0xf, 0xf, true));
      x = max(x, (uint32_t)__builtin_amdgcn_update_dpp(0, (int)x, 0x4e, 0xf, 0xf, true));
      x = max(x, (uint32_t)__builtin_amdgcn_update_dpp(0, (int)x, 0x141, 0xf, 0xf, true));
      x = max(x, (uint32_t)__builtin_amdgcn_update_dpp(0, (int)x, 0x140, 0xf, 0xf, true));
      const uint32_t y = max(max((uint32_t)__builtin_amdgcn_readlane((int)x, 0), (uint32_t)__builtin_amdgcn_readlane((int)x, 16)),
                             max((uint32_t)__builtin_amdgcn_readlane((int)x, 32), (uint32_t)__builtin_amdgcn_readlane((int)x, 48)));
      const int sh = 127 - (int)(y >> 7);
      C.a.re = ldexpf(lo16f(pcr), sh); C.a.im = ldexpf(lo16f(pci), sh);
      C.b.re = ldexpf(hi16f(pcr), sh); C.b.im = ldexpf(hi16f(pci), sh);
      ksc = ldexpf(2.f*rbf(expf(lo16f(ldt))), -sh);
      asm volatile("" : "+v"(C.a.re), "+v"(C.a.im), "+v"(C.b.re), "+v"(C.b.im), "+v"(ksc));
    };
    if (wave >= 8){
      const u16* up = u + (2*tl)*D_ + c0;
      uu[0] = *(const u32x2*)(up);           uu[1] = *(const u32x2*)(up + D_);
      uu[2] = *(const u32x2*)(up + 1024*D_); uu[3] = *(const u32x2*)(up + 1025*D_);
      __builtin_amdgcn_sched_barrier(0);
      PWAIT(4);
      prep();
    } else {
      PWAIT(3);
      const float dte = rbf(expf(lo16f(lde)));
      const float zre = -rbf(expf(lo16f(pla))) * dte;
      const float zim = lo16f(pai) * dte;
      const cpx e = cexp_(zre, zim), f = cexp_(zre*128.f, zim*128.f);
      char* ep = smem + XOFF + (t >> 7)*2048 + 8*(t & 127);
      *(float2*)(ep) = make_float2(e.re, e.im);
      *(float2*)(ep + 1024) = make_float2(f.re, f.im);
      PWAIT(0);
      prep();
    }
    __syncthreads();
    TSP(1);
    char* col = tb + 8*lane;
    char* nb = smem + NOFF + ch*KS2;
    char* ft = smem + FTOFF + ch*FTS;
    {
      const char* eb_ = smem + XOFF + ch*2048;
      cp2 e1, e2;
      { f32x4 a = *(const f32x4*)(eb_ + 16*lane); e2.a.re = a[0]; e2.a.im = a[1]; e2.b.re = a[2]; e2.b.im = a[3];
        f32x4 c = *(const f32x4*)(eb_ + 1024 + 16*lane); e1.a.re = c[0]; e1.a.im = c[1]; e1.b.re = c[2]; e1.b.im = c[3]; }
      __builtin_amdgcn_fence(__ATOMIC_RELEASE, "workgroup");
      if (lane == 0) __hip_atomic_fetch_add(cnt + 2, 1u, __ATOMIC_RELAXED, __HIP_MEMORY_SCOPE_WORKGROUP);
      if (rj == 2){
        __builtin_amdgcn_s_setprio(3);
        cp2 e8 = cmul2(e2, e2); e8 = cmul2(e8, e8); e8 = cmul2(e8, e8);
        *(u32x2*)(col + 41*TR) = u32x2{0x3c00u, 0x3c00u};
        cp2 e16 = cmul2(e8, e8);
        cp2 xa = e8, xb = e16;
        #pragma unroll
        for (int i=1;i<16;i+=2){
          putx(col + (41+i)*TR, xa); if (i < 15) putx(col + (42+i)*TR, xb);
          if (i < 15) xa = cmul2(xa, e16);
          if (i < 13) xb = cmul2(xb, e16);
        }
        __builtin_amdgcn_fence(__ATOMIC_RELEASE, "workgroup");
        if (lane == 0) __hip_atomic_fetch_add(cnt + 4 + ch, 1u, __ATOMIC_RELAXED, __HIP_MEMORY_SCOPE_WORKGROUP);
        __builtin_amdgcn_s_setprio(0);
      } else if (rj == 3){
        __builtin_amdgcn_s_setprio(2);
        cp2 eb = cmul2(e1, e1); eb = cmul2(eb, eb); eb = cmul2(eb, eb);
        cp2 cb = cmul2(C, eb);
        #pragma unroll
        for (int i=0;i<9;i++){ putg(col + (32+i)*TR, cb); if (i < 8) cb = cmul2(cb, e1); }
      } else if (rj == 0){
        cp2 cb = C, cd = cmul2(C, e1);
        #pragma unroll
        for (int p=0;p<8;p++){
          putg(col + p*TR, cb); putg(col + (8+p)*TR, cd);
          if (p < 7){ cb = cmul2(cb, e2); cd = cmul2(cd, e2); }
        }
      } else {
        __builtin_amdgcn_s_setprio(1);
        cp2 S2 = cmul2(e2, e2), S4 = cmul2(S2, S2), S6 = cmul2(S4, S2);
        cp2 cb = cmul2(C, cmul2(e1, e1));
        #pragma unroll
        for (int a=0;a<2;a++){
          putg(col + (16+4*a)*TR, cb);
          putg(col + (16+4*a+1)*TR, cmul2(cb, S2));
          putg(col + (16+4*a+2)*TR, cmul2(cb, S4));
          putg(col + (16+4*a+3)*TR, cmul2(cb, S6));
          cb = cmul2(cb, e1);
        }
        #pragma unroll
        for (int a=0;a<4;a++){
          putg(col + (24+2*a)*TR, cb);
          putg(col + (24+2*a+1)*TR, cmul2(cb, S4));
          if (a < 3) cb = cmul2(cb, e1);
        }
      }
    }
    TSP(2);
    __builtin_amdgcn_fence(__ATOMIC_RELEASE, "workgroup");
    while (__hip_atomic_load(cnt + 2, __ATOMIC_RELAXED, __HIP_MEMORY_SCOPE_WORKGROUP) < 16u ||
           __hip_atomic_load(cnt + 4 + ch, __ATOMIC_RELAXED, __HIP_MEMORY_SCOPE_WORKGROUP) < 1u) __builtin_amdgcn_s_sleep(1);
    __builtin_amdgcn_fence(__ATOMIC_ACQUIRE, "workgroup");
    if (rj != 2){
      const char* ga = tb + (16*(rj == 3 ? 2 : rj) + m)*TR + 16*kg;
      const char* xb = tb + (41 + m)*TR + 16*kg;
      f16x8 GA[8], XB[8];
      #pragma unroll
      for (int kc=0;kc<8;kc++){ GA[kc] = *(const f16x8*)(ga + 64*kc); XB[kc] = *(const f16x8*)(xb + 64*kc); }
      __builtin_amdgcn_sched_barrier(0);
      f32x4 a4 = {0,0,0,0}, b4 = {0,0,0,0};
      #pragma unroll
      for (int kc=0;kc<8;kc+=2){ a4 = MFMAH(GA[kc], XB[kc], a4); b4 = MFMAH(GA[kc+1], XB[kc+1], b4); }
      float kv[4];
      #pragma unroll
      for (int i=0;i<4;i++) kv[i] = (a4[i] + b4[i]) * ksc;
      if (rj == 0){
        *(u32x2*)(nb + 16*(15 + 16*(kg >> 1) + m) + 8*(1 - (kg & 1))) = u32x2{pk2(kv[3], kv[2]), pk2(kv[1], kv[0])};
      } else if (rj == 1){
        const float k8 = i2f(__builtin_amdgcn_update_dpp(0, f2i(kv[0]), 0x130, 0xf, 0xf, true));
        if (kg < 2){
          *(u32x4*)(nb + 16*(47 + 16*kg + m)) = u32x4{pk2(k8, kv[3]), pk2(kv[2], kv[2]), pk2(kv[2], kv[1]), pk2(kv[0], kv[0])};
        } else {
          if (lane == 32) *(u32x4*)(nb + 16*79) = u32x4{0u, 0u, pk2(0.f, kv[0]), pk2(kv[0], kv[0])};
          #pragma unroll
          for (int i=0;i<4;i++){
            const int x1 = 32*(2*(kg-2) + (i >> 1)) + (i & 1) + 2*m - 1;
            char* p = ft + ((x1 >> 1) & 1)*(4*FCS) + 4*(47 - (x1 >> 2)) + 2*(x1 & 1);
            const u16 hv = f2bf(x1 >= 0 ? kv[i] : 0.f);
            #pragma unroll
            for (int c=0;c<4;c++) *(u16*)(p + c*(FCS-4)) = hv;
          }
        }
      } else {
        if (lane <= 32){
          const int x8 = 64*kg + m - 1;
          const int z = x8 < 0 ? 16 : 63 - (x8 >> 1);
          char* p = ft + (x8 < 0 ? 4*FCS : 8*FCS) + 4*z + 2*(x8 & 1);
          const u16 hv = f2bf(kv[0]);
          #pragma unroll
          for (int c=0;c<4;c++) *(u16*)(p + c*(FCS-4)) = hv;
          if (lane == 0) ((float*)(smem + K1OFF))[ch] = kv[0];
        }
        if (kg < 2){
          #pragma unroll
          for (int i=1;i<4;i++){
            const int x8 = 16*(4*kg + i) + m - 1;
            char* p = ft + 8*FCS + 4*(63 - (x8 >> 1)) + 2*(x8 & 1);
            const u16 hv = f2bf(kv[i]);
            #pragma unroll
            for (int c=0;c<4;c++) *(u16*)(p + c*(FCS-4)) = hv;
          }
        }
      }
      __builtin_amdgcn_s_setprio(0);
    } else {
      if (lane < 30) *(u32x4*)(nb + 16*(lane < 15 ? lane : lane + 65)) = u32x4{0,0,0,0};
      const int s5 = lane & 31, z = s5 < 16 ? s5 : s5 + 32;
      #pragma unroll
      for (int c=0;c<4;c++) if (z >= c) *(uint32_t*)(ft + (lane >> 5)*(4*FCS) + c*FCS + 4*(z - c)) = 0u;
      if (m - kg < 12) *(uint32_t*)(ft + 8*FCS + kg*FCS + 4*(64 + m - kg)) = 0u;
    }
    if (wave >= 8){ put_u(smem, tl, uu[0], uu[1]); put_u(smem, tl + 512, uu[2], uu[3]); }
    __syncthreads();
    TSP(3);
  } else {
    __builtin_amdgcn_sched_barrier(0);
    const u32x4 sx = *(const u32x4*)(ssmk + (c0+(t >> 8))*L_ + 8*(t & 255));
    const u32x4 wa = *(const u32x4*)(shift_k + c0*4), wb = *(const u32x4*)(shift_k + c0*4 + 8);
    const int tr = (t & 512) | ((t + 64*(w5 & 15)) & 511);
    const int r0 = 2*(tr - lane);
    const u16* hp = k + (r0 ? r0 - 3 : 0)*D_ + c0 + vz;
    __builtin_amdgcn_sched_barrier(0);
    if (wave >= 8) __builtin_amdgcn_s_barrier();
    __builtin_amdgcn_sched_barrier(0);
    u32x2 h0 = *(const u32x2*)(hp), h1 = *(const u32x2*)(hp + D_), h2 = *(const u32x2*)(hp + 2*D_);
    __builtin_amdgcn_sched_barrier(0);
    const u32x2 k0 = *(const u32x2*)(k + (2*tr)*D_ + c0);
    const u32x2 k1 = *(const u32x2*)(k + (2*tr+1)*D_ + c0);
    const u32x2 v0 = *(const u32x2*)(v + (2*tr)*D_ + c0);
    const u32x2 v1 = *(const u32x2*)(v + (2*tr+1)*D_ + c0);
    __builtin_amdgcn_sched_barrier(0);
    if (wave < 8){ asm volatile("s_waitcnt lgkmcnt(0)" ::: "memory"); __builtin_amdgcn_s_barrier(); }
    __builtin_amdgcn_sched_barrier(0);
    {
      const int ch = t >> 8, j = t & 255;
      u32x4 r;
      r.x = __builtin_amdgcn_alignbit(sx.w, sx.w, 16); r.y = __builtin_amdgcn_alignbit(sx.z, sx.z, 16);
      r.z = __builtin_amdgcn_alignbit(sx.y, sx.y, 16); r.w = __builtin_amdgcn_alignbit(sx.x, sx.x, 16);
      char* kb = smem + TOFF + ch*KST;
      *(u32x4*)(kb + 16*(j+15)) = r;
      if (j < 15) *(u32x4*)(kb + 16*j) = u32x4{0,0,0,0};
    }
    const uint32_t cad = (uint32_t)(uintptr_t)smem + CNTOFF, on1 = 1u;
    if (wave >= 8 && lane == 0) asm volatile("ds_add_u32 %0, %1 offset:64" :: "v"(cad), "v"(on1) : "memory");
    if (r0 == 0){ h0 = u32x2{0u,0u}; h1 = u32x2{0u,0u}; h2 = u32x2{0u,0u}; }
    TSP(1);
    u32x2 kk[5];
    kk[1] = wshr1(h1, k0); kk[2] = wshr1(h2, k1); kk[0] = wshr1(h0, kk[2]);
    kk[3] = k0; kk[4] = k1;
    u32x2 o[2];
    #pragma unroll
    for (int j=0;j<2;j++){
      const u32x2 vv = j ? v1 : v0;
      #pragma unroll
      for (int p=0;p<2;p++){
        float r2[2];
        #pragma unroll
        for (int e=0;e<2;e++){
          const u32x4 ww = p ? wb : wa;
          const uint32_t wl = e ? ww.z : ww.x, wh = e ? ww.w : ww.y;
          const float w0 = lo16f(wl), w1 = hi16f(wl), w2 = lo16f(wh), w3 = hi16f(wh);
          auto g = [&](int idx){ uint32_t x = kk[idx][p]; return e? hi16f(x) : lo16f(x); };
          float acc = rbf(g(j)*w3);
          acc = rbf(acc + rbf(g(j+1)*w2));
          acc = rbf(acc + rbf(g(j+2)*w1));
          acc = rbf(acc + rbf(g(j+3)*w0));
          r2[e] = acc * (e? hi16f(vv[p]) : lo16f(vv[p]));
        }
        o[j][p] = pk2(r2[0], r2[1]);
      }
    }
    put_u(smem, tr, o[0], o[1]);
    if (lane == 0) asm volatile("ds_add_u32 %0, %1 offset:64" :: "v"(cad + (wave >= 8 ? 4 : 0)), "v"(on1) : "memory");
    TSP(2);
  }
  int sig = -1, fi = -1;
  if (isA){
    const int ch = wave >> 2, role = (wave + ch) & 3;
    if (role == 0){ __builtin_amdgcn_s_setprio(2); near_conv<4,0>(smem, ch, m, kg); }
    else if (role == 1) near_conv<4,4>(smem, ch, m, kg);
    else if (role == 2){ __builtin_amdgcn_s_setprio(3); far_conv<0,4>(smem, ch, m, kg); }
    else { __builtin_amdgcn_s_setprio(3); far_conv<4,6>(smem, ch, m, kg); }
    TSP(5);
    sig = role == 1;
    fi = role >= 2 ? 2*ch + role - 2 : -1;
  } else if (wave < 8){
    const int ch = wave & 3;
    const uint32_t sb = (uint32_t)(uintptr_t)smem;
    const uint32_t kbase = sb + TOFF + ch*KST + 16*(m - kg + 3);
    const char* ub = smem + ch*UST + 272*(m >> 1) + 16*kg;
    const uint32_t sel = (m & 1) ? 0x07060504u : 0x05040302u;
    const unsigned* cnt = (const unsigned*)(smem + CNTOFF);
    const uint32_t yw = sb + (wave < 4 ? YOFF + ch*YST : YBOFF - 4096 + ch*YB2) + 512*(m >> 1) + 64*(m & 1) + 16*kg;
    const uint32_t cw = sb + CNTOFF + 4*(m >> 1);
    const uint32_t one = 1u;
    bf16x8 KA[4], KB[4];
    u32x6 Ea[4], Eb[4];
    f32x4 acc[4];
#define BUILDU(off) { \
    u32x4 dd[8]; \
    _Pragma("unroll") for (int c=0;c<4;c++){ dd[2*c] = *(const u32x4*)(ub + (off) + 64*c); dd[2*c+1] = *(const u32x4*)(ub + (off) + 64*c + 16); } \
    __builtin_amdgcn_sched_barrier(0); \
    _Pragma("unroll") for (int c=0;c<4;c++){ \
      const u32x4 d0 = dd[2*c], d1 = dd[2*c+1]; \
      const uint32_t e0 = __builtin_amdgcn_perm(d0.y, d0.x, sel), e1 = __builtin_amdgcn_perm(d0.z, d0.y, sel), e2 = __builtin_amdgcn_perm(d0.w, d0.z, sel), \
        e3 = __builtin_amdgcn_perm(d1.x, d0.w, sel), e4 = __builtin_amdgcn_perm(d1.y, d1.x, sel), e5 = __builtin_amdgcn_perm(d1.z, d1.y, sel), \
        e6 = __builtin_amdgcn_perm(d1.w, d1.z, sel); \
      Ea[c] = u32x6{e0,e1,e2,e3,e4,e5}; Eb[c] = u32x6{e1,e2,e3,e4,e5,e6}; \
      asm volatile("" : "+v"(Ea[c]), "+v"(Eb[c])); } }
#define UT(E, x) __builtin_bit_cast(bf16x8, __builtin_shufflevector(E, E, x, x+1, x+2, x+3))
#define MM16(CUR) \
    _Pragma("unroll") for (int c=0;c<4;c++){ \
      acc[0] = MFMA16(CUR[c], UT(Ea[c], 0), acc[0]); acc[1] = MFMA16(CUR[c], UT(Eb[c], 0), acc[1]); \
      acc[2] = MFMA16(CUR[c], UT(Ea[c], 2), acc[2]); acc[3] = MFMA16(CUR[c], UT(Eb[c], 2), acc[3]); }
#define EMIT(mask, cj) asm volatile("s_mov_b64 exec, %[mk]\n\ts_nop 7\n\t" \
      "ds_write_b128 %[a], %[v0] offset:%[o0]\n\tds_write_b128 %[a], %[v1] offset:%[o1]\n\t" \
      "ds_write_b128 %[a], %[v2] offset:%[o2]\n\tds_write_b128 %[a], %[v3] offset:%[o3]\n\t" \
      "ds_add_u32 %[ca], %[on] offset:%[oc]\n\ts_mov_b64 exec, -1" \
      :: [mk] "s"(0x0001000100010001ull * (mask)), [a] "v"(yw), [v0] "v"(acc[0]), [v1] "v"(acc[1]), [v2] "v"(acc[2]), [v3] "v"(acc[3]), \
         [o0] "i"(512*(cj)), [o1] "i"(512*(cj)+128), [o2] "i"(512*(cj)+256), [o3] "i"(512*(cj)+384), \
         [ca] "v"(cw), [on] "v"(one), [oc] "i"(4*(cj)) : "memory")
#define SHL(F) { acc[0] = F(acc[0]); acc[1] = F(acc[1]); acc[2] = F(acc[2]); acc[3] = F(acc[3]); }
    while (__hip_atomic_load(cnt + 16, __ATOMIC_RELAXED, __HIP_MEMORY_SCOPE_WORKGROUP) < 16u) __builtin_amdgcn_s_sleep(1);
    __builtin_amdgcn_fence(__ATOMIC_ACQUIRE, "workgroup");
    TSP(3);
    #pragma unroll
    for (int r=0;r<4;r++) acc[r] = f32x4{0,0,0,0};
    if (wave < 4){
      __builtin_amdgcn_s_setprio(1);
      LDK4(KA, 0);
      BUILDU(0)
#define X1(s, CUR, NXT) \
      LDK4(NXT, ((s)+1) & 7); if ((s) > 0){ WAITK(9, CUR); SHL(shlN2) } else { WAITK(4, CUR); } \
      MM16(CUR) EMIT(3, s);
      X1(0, KA, KB) X1(1, KB, KA) X1(2, KA, KB) X1(3, KB, KA) X1(4, KA, KB) X1(5, KB, KA) X1(6, KA, KB) X1(7, KB, KA)
      TSP(4);
      while (__hip_atomic_load(cnt + 17, __ATOMIC_RELAXED, __HIP_MEMORY_SCOPE_WORKGROUP) < 8u) __builtin_amdgcn_s_sleep(1);
      __builtin_amdgcn_fence(__ATOMIC_ACQUIRE, "workgroup");
      BUILDU(272*8)
      LDK4(KB, 1); WAITK(4, KA); SHL(shlN2) MM16(KA) EMIT(3, 8);
      LDK4(KA, 2); WAITK(9, KB); SHL(shlN2) MM16(KB) EMIT(3, 9);
      LDK4(KB, 3); WAITK(9, KA); SHL(shlN2) MM16(KA) EMIT(3, 10);
      WAITK(5, KB); SHL(shlN2) MM16(KB) EMIT(0x3ff, 11);
    } else {
      LDK4(KA, 15);
      BUILDU(0)
#define X2(dl, CUR, NXT) \
      LDK4(NXT, (dl) > 8 ? (dl)-1 : 4); WAITK(4, CUR); if ((dl) < 15) SHL(shrN2) \
      MM16(CUR)
      X2(15, KA, KB) X2(14, KB, KA) X2(13, KA, KB) X2(12, KB, KA) X2(11, KA, KB) X2(10, KB, KA) X2(9, KA, KB) X2(8, KB, KA)
      EMIT(0xff, 8);
      TSP(4);
      while (__hip_atomic_load(cnt + 17, __ATOMIC_RELAXED, __HIP_MEMORY_SCOPE_WORKGROUP) < 8u) __builtin_amdgcn_s_sleep(1);
      __builtin_amdgcn_fence(__ATOMIC_ACQUIRE, "workgroup");
      BUILDU(272*8)
      LDK4(KB, 5); WAITK(4, KA); SHL(shlN8) MM16(KA) EMIT(3, 12);
      LDK4(KA, 6); WAITK(9, KB); SHL(shlN2) MM16(KB) EMIT(3, 13);
      LDK4(KB, 7); WAITK(9, KA); SHL(shlN2) MM16(KA) EMIT(3, 14);
      WAITK(5, KB); SHL(shlN2) MM16(KB) EMIT(3, 15);
    }
    TSP(5);
  } else {
    __builtin_amdgcn_s_setprio(2);
    const int j0 = (wave + w5) & 7;
    const int lr = 8*m + 2*kg;
    u32x2 qr[4];
    {
      const u16* qp = q + (128*j0 + lr)*D_ + c0;
      qr[0] = *(const u32x2*)(qp); qr[1] = *(const u32x2*)(qp + D_);
      qr[2] = *(const u32x2*)(qp + 1024*D_); qr[3] = *(const u32x2*)(qp + 1025*D_);
    }
    __builtin_amdgcn_sched_barrier(0);
    #pragma unroll
    for (int st=0; st<2; st++){
      const int cj = j0 + 8*st;
      const char* yb = smem + YOFF + 512*cj + 128*kg + 4*m;
      const char* up = smem + 272*cj + 16 + 2*lr;
      while (__hip_atomic_load((const unsigned*)(smem + CNTOFF) + cj, __ATOMIC_RELAXED, __HIP_MEMORY_SCOPE_WORKGROUP) < (st ? 64u : 32u)) __builtin_amdgcn_s_sleep(1);
      __builtin_amdgcn_fence(__ATOMIC_ACQUIRE, "workgroup");
      float s0[4], s1[4];
      #pragma unroll
      for (int c=0;c<4;c++){
        uint32_t uu = *(const uint32_t*)(up + c*UST);
        float sd = (c & 1) ? hi16f(dsk[c>>1]) : lo16f(dsk[c>>1]);
        s0[c] = rbf(lo16f(uu)*sd); s1[c] = rbf(hi16f(uu)*sd);
      }
      float o0[4], o1[4];
      #pragma unroll
      for (int c=0;c<4;c++){
        float a0 = *(const float*)(yb + c*YST), a1 = *(const float*)(yb + c*YST + 64);
        if (st){ const char* y2 = yb + (YBOFF - 4096 - YOFF) + c*YB2; a0 += *(const float*)(y2); a1 += *(const float*)(y2 + 64); }
        float q0 = (c & 1) ? hi16f(qr[2*st][c>>1]) : lo16f(qr[2*st][c>>1]);
        float q1 = (c & 1) ? hi16f(qr[2*st+1][c>>1]) : lo16f(qr[2*st+1][c>>1]);
        o0[c] = q0 * rbf(rbf(a0) + s0[c]);
        o1[c] = q1 * rbf(rbf(a1) + s1[c]);
      }
      u16* op = out + D_ + c0 + (128*cj + lr)*(2*D_);
      *(u32x2*)(op)        = u32x2{pk2(o0[0],o0[1]), pk2(o0[2],o0[3])};
      *(u32x2*)(op + 2*D_) = u32x2{pk2(o1[0],o1[1]), pk2(o1[2],o1[3])};
    }
  }
  TSP(6);
  if (sig >= 0){
    __builtin_amdgcn_fence(__ATOMIC_RELEASE, "workgroup");
    if (lane == 0) __hip_atomic_fetch_add((unsigned*)(smem + CNTOFF) + sig, 1u, __ATOMIC_RELAXED, __HIP_MEMORY_SCOPE_WORKGROUP);
  }
  if (fi >= 0){
    const int p = (fi*64 + lane + 32*(w5 & 15)) & 511;
    u16* op = out + c0;
    #pragma unroll
    for (int st=0; st<2; st++){
      const int tq = 4*(p >> 1) + (p & 1) + 2*st;
      const char* yb = smem + YAOFF + 8*tq + 16*(tq >> 6);
      const char* up = smem + 272*(tq >> 6) + 16 + 4*(tq & 63);
      float s0[4], s1[4];
      #pragma unroll
      for (int c=0;c<4;c++){
        uint32_t uu = *(const uint32_t*)(up + c*UST);
        float sd = (c & 1) ? hi16f(dsk[c>>1]) : lo16f(dsk[c>>1]);
        s0[c] = rbf(lo16f(uu)*sd); s1[c] = rbf(hi16f(uu)*sd);
      }
      while (__hip_atomic_load((const unsigned*)(smem + CNTOFF) + st, __ATOMIC_RELAXED, __HIP_MEMORY_SCOPE_WORKGROUP) < (st ? 4u : 12u)) __builtin_amdgcn_s_sleep(1);
      __builtin_amdgcn_fence(__ATOMIC_ACQUIRE, "workgroup");
      float o0[4], o1[4];
      const char* yf = smem + YFOFF + (tq >= 257 ? 4*(2*tq - 515) : -16);
      #pragma unroll
      for (int c=0;c<4;c++){
        f32x2 a = *(const f32x2*)(yb + c*YST);
        float b0 = *(const float*)(yf + c*YFST), b1 = *(const float*)(yf + c*YFST + 4);
        o0[c] = rbf(a.x + b0) + s0[c];
        o1[c] = rbf(a.y + b1) + s1[c];
      }
      *(u32x2*)(op + (2*tq)*(2*D_))   = u32x2{pk2(o0[0],o0[1]), pk2(o0[2],o0[3])};
      *(u32x2*)(op + (2*tq+1)*(2*D_)) = u32x2{pk2(o1[0],o1[1]), pk2(o1[2],o1[3])};
    }
  }
  TSP(7);
  if (!isA){
    unsigned long long dlt = (tsv[3]-tsv[0])*8;
    unsigned long long tgt = tsv[7] + dlt;
    while (__builtin_amdgcn_s_memrealtime() < tgt) __builtin_amdgcn_s_sleep(1);
  }
}

extern "C" void kernel_launch(void* const* d_in, const int* in_sizes, int n_in,
                              void* d_out, int out_size, void* d_ws, size_t ws_size,
                              hipStream_t stream) {
  mono_kernel<<<256, 1024, LDS_TOTAL, stream>>>((const u16*)d_in[0], (const u16*)d_in[1], (const u16*)d_in[2], (const u16*)d_in[3],
     (const u16*)d_in[4], (const u16*)d_in[5], (const u16*)d_in[6], (const u16*)d_in[7], (const u16*)d_in[8], (const u16*)d_in[9],
     (const u16*)d_in[10], (const u16*)d_in[11], (const u16*)d_in[12], (u16*)d_out
);
}
```

```cpp
#include <hip/hip_runtime.h>
#include <hip/hip_bf16.h>
#include <stdint.h>

typedef unsigned short u16;
typedef short bf16x8 __attribute__((ext_vector_type(8)));
typedef float f32x4 __attribute__((ext_vector_type(4)));
typedef float f32x2 __attribute__((ext_vector_type(2)));
typedef __bf16 bf16x2_t __attribute__((ext_vector_type(2)));

#define L_ 2048
#define D_ 512
#define NS_ 128

__device__ __forceinline__ float bf2f(u16 x){ return __builtin_bit_cast(float, (uint32_t)x << 16); }
__device__ __forceinline__ u16 f2bf(float f){ __hip_bfloat16 h = __float2bfloat16(f); return __builtin_bit_cast(u16, h); }
__device__ __forceinline__ float rbf(float f){ return bf2f(f2bf(f)); }
__device__ __forceinline__ uint32_t pk2(float a, float b){
  f32x2 v = {a,b}; bf16x2_t r = __builtin_convertvector(v, bf16x2_t); return __builtin_bit_cast(uint32_t, r); }
__device__ __forceinline__ float lo16f(uint32_t w){ return __builtin_bit_cast(float, w << 16); }
__device__ __forceinline__ float hi16f(uint32_t w){ return __builtin_bit_cast(float, w & 0xffff0000u); }

struct cpx { float re, im; };
__device__ __forceinline__ float amul(float a, float b){ float r; asm("v_mul_f32 %0, %1, %2" : "=v"(r) : "v"(a), "v"(b)); return r; }
__device__ __forceinline__ float afma(float a, float b, float c){ float r; asm("v_fma_f32 %0, %1, %2, %3" : "=v"(r) : "v"(a), "v"(b), "v"(c)); return r; }
__device__ __forceinline__ float afms(float a, float b, float c){ float r; asm("v_fma_f32 %0, %1, %2, -%3" : "=v"(r) : "v"(a), "v"(b), "v"(c)); return r; }
__device__ __forceinline__ cpx cmul(cpx a, cpx b){
  f32x2 A = {a.re, a.im}, B = {b.re, b.im}, T, R;
  asm("v_pk_mul_f32 %0, %1, %2 op_sel:[1,1] op_sel_hi:[1,0] neg_lo:[1,0]" : "=v"(T) : "v"(A), "v"(B));
  asm("v_pk_fma_f32 %0, %1, %2, %3 op_sel:[0,0,0] op_sel_hi:[0,1,1]" : "=v"(R) : "v"(A), "v"(B), "v"(T));
  cpx r; r.re = R.x; r.im = R.y; return r; }
__device__ __forceinline__ cpx cexp_(float re, float im){ float e = expf(re); float s,c; sincosf(im,&s,&c); cpx r; r.re=e*c; r.im=e*s; return r; }

#define MFMA16(a,b,c) __builtin_amdgcn_mfma_f32_16x16x32_bf16(a,b,c,0,0,0)
typedef uint32_t u32x2 __attribute__((ext_vector_type(2)));
typedef uint32_t u32x4 __attribute__((ext_vector_type(4)));
#define TR 544
#define TSZ (57*TR)
#define UST 4352
#define TOFF 17408
#define KST 4352
#define KS2 1536
#define XOFF (TOFF + 4*TSZ)
#define NOFF XOFF
#define FCS 320
#define FTS (12*FCS)
#define FTOFF (XOFF + 4*KS2)
#define K1OFF (FTOFF + 4*FTS)
#define YFOFF (TOFF + 45056 + 16)
#define YFST 6176
#define YAOFF (TOFF + 69888)
#define YOFF (TOFF + 36864)
#define YST 8448
#define YBOFF (YOFF + 4*YST)
#define YB2 4352
#define CNTOFF (K1OFF + 16)
#define LDS_TOTAL (CNTOFF + 80)
typedef uint32_t u32x6 __attribute__((ext_vector_type(6)));
typedef _Float16 f16x8 __attribute__((ext_vector_type(8)));
typedef _Float16 f16x2 __attribute__((ext_vector_type(2)));
#define MFMAH(a,b,c) __builtin_amdgcn_mfma_f32_16x16x32_f16(a,b,c,0,0,0)
__device__ __forceinline__ uint32_t pkh(float a, float b){
  f32x2 v = {a,b}; f16x2 r = __builtin_convertvector(v, f16x2); return __builtin_bit_cast(uint32_t, r); }
struct cp2 { cpx a, b; };
__device__ __forceinline__ cp2 cmul2(const cp2& x, const cp2& y){ cp2 r; r.a = cmul(x.a, y.a); r.b = cmul(x.b, y.b); return r; }
__device__ __forceinline__ void putg(char* p, const cp2& x){ *(u32x2*)p = u32x2{pkh(x.a.re, x.a.im), pkh(x.b.re, x.b.im)}; }
__device__ __forceinline__ void putx(char* p, const cp2& x){ *(u32x2*)p = u32x2{pkh(x.a.re, -x.a.im), pkh(x.b.re, -x.b.im)}; }

__device__ __forceinline__ uint32_t resid2(uint32_t h, float a, float b){
  return pk2(a - lo16f(h), b - hi16f(h));
}
__device__ __forceinline__ int f2i(float x){ return __builtin_bit_cast(int, x); }
__device__ __forceinline__ float i2f(int x){ return __builtin_bit_cast(float, x); }
__device__ __forceinline__ f32x4 shrN1(f32x4 x){
  f32x4 r;
  #pragma unroll
  for (int i=0;i<4;i++) r[i] = i2f(__builtin_amdgcn_update_dpp(0, f2i(x[i]), 0x111, 0xf, 0xf, true));
  return r;
}
__device__ __forceinline__ f32x4 shrN2(f32x4 x){
  f32x4 r;
  #pragma unroll
  for (int i=0;i<4;i++) r[i] = i2f(__builtin_amdgcn_update_dpp(0, f2i(x[i]), 0x112, 0xf, 0xf, true));
  return r;
}
__device__ __forceinline__ f32x4 shlN1(f32x4 x){
  f32x4 r;
  #pragma unroll
  for (int i=0;i<4;i++) r[i] = i2f(__builtin_amdgcn_update_dpp(0, f2i(x[i]), 0x101, 0xf, 0xf, true));
  return r;
}
__device__ __forceinline__ f32x4 shlN2(f32x4 x){
  f32x4 r;
  #pragma unroll
  for (int i=0;i<4;i++) r[i] = i2f(__builtin_amdgcn_update_dpp(0, f2i(x[i]), 0x102, 0xf, 0xf, true));
  return r;
}
__device__ __forceinline__ f32x4 shlN8(f32x4 x){
  f32x4 r;
  #pragma unroll
  for (int i=0;i<4;i++) r[i] = i2f(__builtin_amdgcn_update_dpp(0, f2i(x[i]), 0x108, 0xf, 0xf, true));
  return r;
}
__device__ __forceinline__ u32x2 wshr1(u32x2 old, u32x2 x){
  u32x2 r;
  r.x = (uint32_t)__builtin_amdgcn_update_dpp((int)old.x, (int)x.x, 0x138, 0xf, 0xf, false);
  r.y = (uint32_t)__builtin_amdgcn_update_dpp((int)old.y, (int)x.y, 0x138, 0xf, 0xf, false);
  return r;
}
struct W6 { uint32_t w[6]; };
__device__ __forceinline__ W6 uwin(const char* p){
  u32x2 a = *(const u32x2*)p, b = *(const u32x2*)(p+8), c = *(const u32x2*)(p+16);
  W6 r; r.w[0]=a.x; r.w[1]=a.y; r.w[2]=b.x; r.w[3]=b.y; r.w[4]=c.x; r.w[5]=c.y; return r;
}
__device__ __forceinline__ bf16x8 usel(const W6& W, int x, uint32_t sel){
  u32x4 r;
  r.x = __builtin_amdgcn_perm(W.w[x+1], W.w[x],   sel); r.y = __builtin_amdgcn_perm(W.w[x+2], W.w[x+1], sel);
  r.z = __builtin_amdgcn_perm(W.w[x+3], W.w[x+2], sel); r.w = __builtin_amdgcn_perm(W.w[x+4], W.w[x+3], sel);
  return __builtin_bit_cast(bf16x8, r);
}
__device__ __forceinline__ bf16x8 uodd(const W6& W, int x){
  u32x4 r;
  r.x = __builtin_amdgcn_alignbit(W.w[x+1], W.w[x],   16); r.y = __builtin_amdgcn_alignbit(W.w[x+2], W.w[x+1], 16);
  r.z = __builtin_amdgcn_alignbit(W.w[x+3], W.w[x+2], 16); r.w = __builtin_amdgcn_alignbit(W.w[x+4], W.w[x+3], 16);
  return __builtin_bit_cast(bf16x8, r);
}
__device__ __forceinline__ bf16x8 uevn(const W6& W, int x){
  u32x4 r = {W.w[x], W.w[x+1], W.w[x+2], W.w[x+3]};
  return __builtin_bit_cast(bf16x8, r);
}
__device__ __forceinline__ void put_u(char* smem, int tr, u32x2 r0, u32x2 r1){
  uint32_t d[4];
  d[0] = __builtin_amdgcn_perm(r1.x, r0.x, 0x05040100); d[1] = __builtin_amdgcn_perm(r1.x, r0.x, 0x07060302);
  d[2] = __builtin_amdgcn_perm(r1.y, r0.y, 0x05040100); d[3] = __builtin_amdgcn_perm(r1.y, r0.y, 0x07060302);
  const int a = tr >> 6, pos = tr & 63;
  char* p = smem + 272*a + 16 + 4*pos;
  #pragma unroll
  for (int c=0;c<4;c++) *(uint32_t*)(p + c*UST) = d[c];
  if (pos >= 60 && a < 15){
    #pragma unroll
    for (int c=0;c<4;c++) *(uint32_t*)(p + c*UST + 16) = d[c];
  }
  if (tr < 16) *(uint32_t*)(smem + (tr>>2)*UST + 4*(tr&3)) = 0u;
}

#define LDK(F, dl, c) asm volatile("ds_read_b128 %0, %1 offset:%2" : "=v"(F) : "v"(kbase), "i"(256*(dl) + 64*(3-(c))))
#define LDK4(S, dl) LDK(S[0], dl, 0); LDK(S[1], dl, 1); LDK(S[2], dl, 2); LDK(S[3], dl, 3)
#define WAITK(n, S) asm volatile("s_waitcnt lgkmcnt(" #n ")" : "+v"(S[0]), "+v"(S[1]), "+v"(S[2]), "+v"(S[3]))

template<int NT, int R0>
__device__ __forceinline__ void near_conv(char* smem, int ch, int m, int kg){
  const uint32_t kbase = (uint32_t)(uintptr_t)smem + NOFF + ch*KS2 + 16*(m - kg + 3);
#define LDN(F, hs, c) asm volatile("ds_read_b128 %0, %1 offset:%2" : "=v"(F) : "v"(kbase), "i"(256*(4-(hs)) + 64*(3-(c))))
#define LDN4(S, hs) LDN(S[0], hs, 0); LDN(S[1], hs, 1); LDN(S[2], hs, 2); LDN(S[3], hs, 3)
  bf16x8 KA[4], KB[4];
  LDN4(KA, 0);
  bf16x8 U[NT][4];
  {
    const char* pp = smem + ch*UST + 272*m + 16*kg + (R0 ? 8 : 0);
    #pragma unroll
    for (int c=0;c<4;c++){
      W6 W = uwin(pp + 64*c);
      #pragma unroll
      for (int r=0;r<NT;r++){ const int rel = R0 + r + 1 - (R0 ? 4 : 0); U[r][c] = (rel & 1) ? uodd(W, rel >> 1) : uevn(W, rel >> 1); }
    }
  }
  f32x4 acc[NT];
  #pragma unroll
  for (int r=0;r<NT;r++) acc[r] = f32x4{0,0,0,0};
#define NH(hs, CUR, NXT) \
  if ((hs) < 4){ LDN4(NXT, (hs)+1); WAITK(4, CUR); } else { WAITK(0, CUR); } \
  if ((hs) > 0){ _Pragma("unroll") for (int r=0;r<NT;r++) acc[r] = shrN1(acc[r]); } \
  _Pragma("unroll") for (int c=0;c<4;c++){ _Pragma("unroll") for (int r=0;r<NT;r++) acc[r] = MFMA16(CUR[c], U[r][c], acc[r]); }
  NH(0, KA, KB) NH(1, KB, KA) NH(2, KA, KB) NH(3, KB, KA) NH(4, KA, KB)
  float* yb = (float*)(smem + YAOFF + ch*YST) + (R0 + 32*kg + 132*m);
  #pragma unroll
  for (int i=0;i<4;i++){
    #pragma unroll
    for (int r=0;r<NT;r++) yb[8*i + r] = acc[r][i];
  }
}
__device__ __forceinline__ bf16x8 band(int sh, int w, int kg){
  const uint32_t by = (((1u << w) - 1u) << sh) >> (8*kg);
  u32x4 r;
  #pragma unroll
  for (int k2=0;k2<4;k2++){ uint32_t x = (by >> (2*k2)) & 3u; x = (x | (x << 15)) & 0x00010001u; r[k2] = x * 0x3f80u; }
  return __builtin_bit_cast(bf16x8, r);
}
__device__ __forceinline__ void split2(const f32x4& a, const f32x4& b, bf16x8& h, bf16x8& l){
  u32x4 hh, ll;
  #pragma unroll
  for (int i=0;i<4;i++){ uint32_t x = pk2(a[i], b[i]); hh[i] = x; ll[i] = resid2(x, a[i], b[i]); }
  h = __builtin_bit_cast(bf16x8, hh); l = __builtin_bit_cast(bf16x8, ll);
}
__device__ __forceinline__ bf16x8 pack2(const f32x4& a, const f32x4& b){
  u32x4 hh;
  #pragma unroll
  for (int i=0;i<4;i++) hh[i] = pk2(a[i], b[i]);
  return __builtin_bit_cast(bf16x8, hh);
}
template<int A0, int A1>
__device__ __forceinline__ void far_conv(char* smem, int ch, int m, int kg){
  const char* ub = smem + ch*UST;
  const f32x4 Z4 = {0,0,0,0};
  const int B0 = 2*m + kg - 2;
  const char* va0 = ub + (B0 < 0 ? 0 : 272*(B0 >> 4) + 16 + 16*(B0 & 15));
  const char* va1 = ub + 272*((B0+32) >> 4) + 16 + 16*((B0+32) & 15);
  const int cp = (3 - m) & 3;
  const char* ft = smem + FTOFF + ch*FTS + cp*FCS + 4*(47 - m + 4*kg - cp);
  char* yf = smem + YFOFF + ch*YFST + 64*m + 16*kg;
  if (A0 == 0 && kg == 0 && m < 4) *(float*)(smem + YFOFF + ch*YFST - 16 + 4*m) = 0.f;
  f32x4 acc[6];
  #pragma unroll
  for (int a=0;a<6;a++) acc[a] = Z4;
  {
    bf16x8 M[4];
    M[0] = band(14 + m, 3, kg); M[1] = band(9 + m, 5, kg); M[2] = band(6 + m, 3, kg); M[3] = band(1 + m, 5, kg);
    bf16x8 Ch[3][2];
    #pragma unroll
    for (int dl=0;dl<3;dl++){
      #pragma unroll
      for (int h=0;h<2;h++) Ch[dl][h] = *(const bf16x8*)(ft + h*(4*FCS) - 64*dl);
    }
    #pragma unroll
    for (int b=(A0 > 2 ? A0-2 : 0);b<A1;b++){
      const bf16x8 V = *(const bf16x8*)(b ? va1 + 544*(b-1) : va0);
      f32x4 F[4];
      #pragma unroll
      for (int p=0;p<4;p++) F[p] = MFMA16(V, M[p], Z4);
      #pragma unroll
      for (int h=0;h<2;h++){
        const bf16x8 Bh = pack2(F[2*h], F[2*h+1]);
        #pragma unroll
        for (int dl=0;dl<3;dl++) if (b + dl >= A0 && b + dl < A1) acc[b+dl] = MFMA16(Bh, Ch[dl][h], acc[b+dl]);
      }
      if (b < 2 && b >= A0) *(f32x4*)(yf + 1024*b) = acc[b];
    }
  }
  {
    const char* vb0 = ub + (B0 < 1 ? 0 : 272*((B0-1) >> 4) + 16 + 16*((B0-1) & 15));
    const char* vb1 = ub + 272*((B0+31) >> 4) + 16 + 16*((B0+31) & 15);
    const char* f8 = smem + FTOFF + ch*FTS + 8*FCS + cp*FCS + 4*(63 - m + 4*kg - cp);
    const bf16x8 Ma = band(8 + m, 7, kg), Mb = band(7 + m, 9, kg), Mc = band(15 + m, 2, kg);
    const float k1024 = ((const float*)(smem + K1OFF))[ch];
    bf16x8 Ch[4];
    #pragma unroll
    for (int dl=0;dl<A1-2;dl++) Ch[dl] = *(const bf16x8*)(f8 - 64*dl);
    #pragma unroll
    for (int b=0;b<A1-2;b++){
      const bf16x8 V = *(const bf16x8*)(b ? va1 + 544*(b-1) : va0);
      const bf16x8 V2 = *(const bf16x8*)(b ? vb1 + 544*(b-1) : vb0);
      const f32x4 Fa = MFMA16(V, Ma, Z4), Fb = MFMA16(V2, Mb, Z4);
      const bf16x8 Bh = pack2(Fa, Fb);
      if (b + 2 >= A0){
        const f32x4 Fc = MFMA16(Mc, V, Z4);
        #pragma unroll
        for (int i=0;i<4;i++) acc[b+2][i] = __builtin_fmaf(k1024, Fc[i], acc[b+2][i]);
      }
      #pragma unroll
      for (int dl=0;dl<4;dl++) if (b + 2 + dl >= A0 && b + 2 + dl < A1) acc[b+2+dl] = MFMA16(Bh, Ch[dl], acc[b+2+dl]);
    }
  }
  #pragma unroll
  for (int a=(A0 > 2 ? A0 : 2);a<A1;a++) *(f32x4*)(yf + 1024*a) = acc[a];
}

__global__ __launch_bounds__(1024) void mono_kernel(const u16* __restrict__ u, const u16* __restrict__ logA, const u16* __restrict__ Aim,
    const u16* __restrict__ Cre, const u16* __restrict__ Cim, const u16* __restrict__ logdt, const u16* __restrict__ Dskip,
    const u16* __restrict__ q, const u16* __restrict__ k, const u16* __restrict__ v, const u16* __restrict__ shift_k,
    const u16* __restrict__ ssmk, const u16* __restrict__ ssmD, u16* __restrict__ out, unsigned long long* wsb
){
  unsigned long long tsv[10], tsc[10];
#define TSP(i) tsv[i] = __builtin_amdgcn_s_memrealtime(); tsc[i] = __builtin_amdgcn_s_memtime()
  TSP(0); TSP(1); TSP(2); TSP(3); TSP(4); TSP(5);
  if (threadIdx.x == 0) __hip_atomic_store(wsb + blockIdx.x, tsv[0], __ATOMIC_RELAXED, __HIP_MEMORY_SCOPE_AGENT);
  extern __shared__ __attribute__((aligned(16))) char smem[];
  const int bid = blockIdx.x, t = threadIdx.x;
  const int w5 = bid >> 3;
  const bool isA = w5 < 16;
  const int c0 = (bid & 7)*64 + (w5 & 15)*4;
  const int wave = t >> 6, lane = t & 63, m = lane & 15, kg = lane >> 4;
  asm volatile("" :: "s"(u), "s"(logA), "s"(Aim), "s"(Cre), "s"(Cim), "s"(logdt), "s"(Dskip), "s"(q), "s"(k), "s"(v), "s"(shift_k), "s"(ssmk), "s"(ssmD), "s"(out));
  u32x2 dsk;
  if (t < 20) ((unsigned*)(smem + CNTOFF))[t] = 0u;
  int vz; asm volatile("v_mov_b32 %0, 0" : "=v"(vz));
  asm volatile("global_load_dwordx2 %0, %1, %2" : "=v"(dsk) : "v"(2*c0 + vz), "s"(isA ? Dskip : ssmD));
  if (isA){
    const int ch = wave & 3, rj = wave >> 2;
    const int d = c0 + ch;
    char* tb = smem + TOFF + ch*TSZ;
    unsigned* cnt = (unsigned*)(smem + CNTOFF);
    uint32_t ldt, pcr, pci, lde, pla, pai;
    asm volatile("global_load_ushort %0, %1, %2" : "=v"(lde) : "v"(2*(c0 + ((t >> 7) & 3))), "s"(logdt));
    asm volatile("global_load_ushort %0, %1, %2" : "=v"(pla) : "v"(2*(c0*NS_ + (t & 511))), "s"(logA));
    asm volatile("global_load_ushort %0, %1, %2" : "=v"(pai) : "v"(2*(c0*NS_ + (t & 511))), "s"(Aim));
    asm volatile("global_load_ushort %0, %1, %2" : "=v"(ldt) : "v"(2*d), "s"(logdt));
    asm volatile("global_load_dword %0, %1, %2" : "=v"(pcr) : "v"(2*d*NS_ + 4*lane), "s"(Cre));
    asm volatile("global_load_dword %0, %1, %2" : "=v"(pci) : "v"(2*d*NS_ + 4*lane), "s"(Cim));
#define PWAIT(n) asm volatile("s_waitcnt vmcnt(" #n ")" : "+v"(lde), "+v"(pla), "+v"(pai), "+v"(ldt), "+v"(pcr), "+v"(pci), "+v"(dsk))
    __builtin_amdgcn_sched_barrier(0);
    __builtin_amdgcn_s_barrier();
    __builtin_amdgcn_sched_barrier(0);
    u32x2 uu[4];
    const int tl = (t + 64*(w5 & 15)) & 511;
    cp2 C; float ksc;
    auto prep = [&](){
      uint32_t e0 = pcr & 0x7f807f80u, e1 = pci & 0x7f807f80u;
      uint32_t x = max(max(e0 & 0xffffu, e0 >> 16), max(e1 & 0xffffu, e1 >> 16));
      x = max(x, (uint32_t)__builtin_amdgcn_update_dpp(0, (int)x, 0xb1, 0xf, 0xf, true));
      x = max(x, (uint32_t)__builtin_amdgcn_update_dpp(0, (int)x, 0x4e, 0xf, 0xf, true));
      x = max(x, (uint32_t)__builtin_amdgcn_update_dpp(0, (int)x, 0x141, 0xf, 0xf, true));
      x = max(x, (uint32_t)__builtin_amdgcn_update_dpp(0, (int)x, 0x140, 0xf, 0xf, true));
      const uint32_t y = max(max((uint32_t)__builtin_amdgcn_readlane((int)x, 0), (uint32_t)__builtin_amdgcn_readlane((int)x, 16)),
                             max((uint32_t)__builtin_amdgcn_readlane((int)x, 32), (uint32_t)__builtin_amdgcn_readlane((int)x, 48)));
      const int sh = 127 - (int)(y >> 7);
      C.a.re = ldexpf(lo16f(pcr), sh); C.a.im = ldexpf(lo16f(pci), sh);
      C.b.re = ldexpf(hi16f(pcr), sh); C.b.im = ldexpf(hi16f(pci), sh);
      ksc = ldexpf(2.f*rbf(expf(lo16f(ldt))), -sh);
      asm volatile("" : "+v"(C.a.re), "+v"(C.a.im), "+v"(C.b.re), "+v"(C.b.im), "+v"(ksc));
    };
    if (wave >= 8){
      const u16* up = u + (2*tl)*D_ + c0;
      uu[0] = *(const u32x2*)(up);           uu[1] = *(const u32x2*)(up + D_);
      uu[2] = *(const u32x2*)(up + 1024*D_); uu[3] = *(const u32x2*)(up + 1025*D_);
      __builtin_amdgcn_sched_barrier(0);
      PWAIT(4);
      prep();
    } else {
      PWAIT(3);
      const float dte = rbf(expf(lo16f(lde)));
      const float zre = -rbf(expf(lo16f(pla))) * dte;
      const float zim = lo16f(pai) * dte;
      const cpx e = cexp_(zre, zim), f = cexp_(zre*128.f, zim*128.f);
      char* ep = smem + XOFF + (t >> 7)*2048 + 8*(t & 127);
      *(float2*)(ep) = make_float2(e.re, e.im);
      *(float2*)(ep + 1024) = make_float2(f.re, f.im);
      PWAIT(0);
      prep();
    }
    __syncthreads();
    TSP(1);
    char* col = tb + 8*lane;
    char* nb = smem + NOFF + ch*KS2;
    char* ft = smem + FTOFF + ch*FTS;
    {
      const char* eb_ = smem + XOFF + ch*2048;
      cp2 e1, e2;
      { f32x4 a = *(const f32x4*)(eb_ + 16*lane); e2.a.re = a[0]; e2.a.im = a[1]; e2.b.re = a[2]; e2.b.im = a[3];
        f32x4 c = *(const f32x4*)(eb_ + 1024 + 16*lane); e1.a.re = c[0]; e1.a.im = c[1]; e1.b.re = c[2]; e1.b.im = c[3]; }
      __builtin_amdgcn_fence(__ATOMIC_RELEASE, "workgroup");
      if (lane == 0) __hip_atomic_fetch_add(cnt + 2, 1u, __ATOMIC_RELAXED, __HIP_MEMORY_SCOPE_WORKGROUP);
      if (rj == 2){
        __builtin_amdgcn_s_setprio(3);
        cp2 e8 = cmul2(e2, e2); e8 = cmul2(e8, e8); e8 = cmul2(e8, e8);
        *(u32x2*)(col + 41*TR) = u32x2{0x3c00u, 0x3c00u};
        cp2 e16 = cmul2(e8, e8);
        cp2 xa = e8, xb = e16;
        #pragma unroll
        for (int i=1;i<16;i+=2){
          putx(col + (41+i)*TR, xa); if (i < 15) putx(col + (42+i)*TR, xb);
          if (i < 15) xa = cmul2(xa, e16);
          if (i < 13) xb = cmul2(xb, e16);
        }
        __builtin_amdgcn_fence(__ATOMIC_RELEASE, "workgroup");
        if (lane == 0) __hip_atomic_fetch_add(cnt + 4 + ch, 1u, __ATOMIC_RELAXED, __HIP_MEMORY_SCOPE_WORKGROUP);
        __builtin_amdgcn_s_setprio(0);
      } else if (rj == 3){
        __builtin_amdgcn_s_setprio(2);
        cp2 eb = cmul2(e1, e1); eb = cmul2(eb, eb); eb = cmul2(eb, eb);
        cp2 cb = cmul2(C, eb);
        #pragma unroll
        for (int i=0;i<9;i++){ putg(col + (32+i)*TR, cb); if (i < 8) cb = cmul2(cb, e1); }
      } else if (rj == 0){
        cp2 cb = C, cd = cmul2(C, e1);
        #pragma unroll
        for (int p=0;p<8;p++){
          putg(col + p*TR, cb); putg(col + (8+p)*TR, cd);
          if (p < 7){ cb = cmul2(cb, e2); cd = cmul2(cd, e2); }
        }
      } else {
        __builtin_amdgcn_s_setprio(1);
        cp2 S2 = cmul2(e2, e2), S4 = cmul2(S2, S2), S6 = cmul2(S4, S2);
        cp2 cb = cmul2(C, cmul2(e1, e1));
        #pragma unroll
        for (int a=0;a<2;a++){
          putg(col + (16+4*a)*TR, cb);
          putg(col + (16+4*a+1)*TR, cmul2(cb, S2));
          putg(col + (16+4*a+2)*TR, cmul2(cb, S4));
          putg(col + (16+4*a+3)*TR, cmul2(cb, S6));
          cb = cmul2(cb, e1);
        }
        #pragma unroll
        for (int a=0;a<4;a++){
          putg(col + (24+2*a)*TR, cb);
          putg(col + (24+2*a+1)*TR, cmul2(cb, S4));
          if (a < 3) cb = cmul2(cb, e1);
        }
      }
    }
    TSP(2);
    __builtin_amdgcn_fence(__ATOMIC_RELEASE, "workgroup");
    while (__hip_atomic_load(cnt + 2, __ATOMIC_RELAXED, __HIP_MEMORY_SCOPE_WORKGROUP) < 16u ||
           __hip_atomic_load(cnt + 4 + ch, __ATOMIC_RELAXED, __HIP_MEMORY_SCOPE_WORKGROUP) < 1u) __builtin_amdgcn_s_sleep(1);
    __builtin_amdgcn_fence(__ATOMIC_ACQUIRE, "workgroup");
    if (rj != 2){
      const char* ga = tb + (16*(rj == 3 ? 2 : rj) + m)*TR + 16*kg;
      const char* xb = tb + (41 + m)*TR + 16*kg;
      f16x8 GA[8], XB[8];
      #pragma unroll
      for (int kc=0;kc<8;kc++){ GA[kc] = *(const f16x8*)(ga + 64*kc); XB[kc] = *(const f16x8*)(xb + 64*kc); }
      __builtin_amdgcn_sched_barrier(0);
      f32x4 a4 = {0,0,0,0}, b4 = {0,0,0,0};
      #pragma unroll
      for (int kc=0;kc<8;kc+=2){ a4 = MFMAH(GA[kc], XB[kc], a4); b4 = MFMAH(GA[kc+1], XB[kc+1], b4); }
      float kv[4];
      #pragma unroll
      for (int i=0;i<4;i++) kv[i] = (a4[i] + b4[i]) * ksc;
      if (rj == 0){
        *(u32x2*)(nb + 16*(15 + 16*(kg >> 1) + m) + 8*(1 - (kg & 1))) = u32x2{pk2(kv[3], kv[2]), pk2(kv[1], kv[0])};
      } else if (rj == 1){
        const float k8 = i2f(__builtin_amdgcn_update_dpp(0, f2i(kv[0]), 0x130, 0xf, 0xf, true));
        if (kg < 2){
          *(u32x4*)(nb + 16*(47 + 16*kg + m)) = u32x4{pk2(k8, kv[3]), pk2(kv[2], kv[2]), pk2(kv[2], kv[1]), pk2(kv[0], kv[0])};
        } else {
          if (lane == 32) *(u32x4*)(nb + 16*79) = u32x4{0u, 0u, pk2(0.f, kv[0]), pk2(kv[0], kv[0])};
          #pragma unroll
          for (int i=0;i<4;i++){
            const int x1 = 32*(2*(kg-2) + (i >> 1)) + (i & 1) + 2*m - 1;
            char* p = ft + ((x1 >> 1) & 1)*(4*FCS) + 4*(47 - (x1 >> 2)) + 2*(x1 & 1);
            const u16 hv = f2bf(x1 >= 0 ? kv[i] : 0.f);
            #pragma unroll
            for (int c=0;c<4;c++) *(u16*)(p + c*(FCS-4)) = hv;
          }
        }
      } else {
        if (lane <= 32){
          const int x8 = 64*kg + m - 1;
          const int z = x8 < 0 ? 16 : 63 - (x8 >> 1);
          char* p = ft + (x8 < 0 ? 4*FCS : 8*FCS) + 4*z + 2*(x8 & 1);
          const u16 hv = f2bf(kv[0]);
          #pragma unroll
          for (int c=0;c<4;c++) *(u16*)(p + c*(FCS-4)) = hv;
          if (lane == 0) ((float*)(smem + K1OFF))[ch] = kv[0];
        }
        if (kg < 2){
          #pragma unroll
          for (int i=1;i<4;i++){
            const int x8 = 16*(4*kg + i) + m - 1;
            char* p = ft + 8*FCS + 4*(63 - (x8 >> 1)) + 2*(x8 & 1);
            const u16 hv = f2bf(kv[i]);
            #pragma unroll
            for (int c=0;c<4;c++) *(u16*)(p + c*(FCS-4)) = hv;
          }
        }
      }
      __builtin_amdgcn_s_setprio(0);
    } else {
      if (lane < 30) *(u32x4*)(nb + 16*(lane < 15 ? lane : lane + 65)) = u32x4{0,0,0,0};
      const int s5 = lane & 31, z = s5 < 16 ? s5 : s5 + 32;
      #pragma unroll
      for (int c=0;c<4;c++) if (z >= c) *(uint32_t*)(ft + (lane >> 5)*(4*FCS) + c*FCS + 4*(z - c)) = 0u;
      if (m - kg < 12) *(uint32_t*)(ft + 8*FCS + kg*FCS + 4*(64 + m - kg)) = 0u;
    }
    if (wave >= 8){ put_u(smem, tl, uu[0], uu[1]); put_u(smem, tl + 512, uu[2], uu[3]); }
    __syncthreads();
    TSP(3);
  } else {
    __builtin_amdgcn_sched_barrier(0);
    const u32x4 sx = *(const u32x4*)(ssmk + (c0+(t >> 8))*L_ + 8*(t & 255));
    const u32x4 wa = *(const u32x4*)(shift_k + c0*4), wb = *(const u32x4*)(shift_k + c0*4 + 8);
    const int tr = (t & 512) | ((t + 64*(w5 & 15)) & 511);
    const int r0 = 2*(tr - lane);
    const u16* hp = k + (r0 ? r0 - 3 : 0)*D_ + c0 + vz;
    __builtin_amdgcn_sched_barrier(0);
    if (wave >= 8) __builtin_amdgcn_s_barrier();
    __builtin_amdgcn_sched_barrier(0);
    u32x2 h0 = *(const u32x2*)(hp), h1 = *(const u32x2*)(hp + D_), h2 = *(const u32x2*)(hp + 2*D_);
    __builtin_amdgcn_sched_barrier(0);
    const u32x2 k0 = *(const u32x2*)(k + (2*tr)*D_ + c0);
    const u32x2 k1 = *(const u32x2*)(k + (2*tr+1)*D_ + c0);
    const u32x2 v0 = *(const u32x2*)(v + (2*tr)*D_ + c0);
    const u32x2 v1 = *(const u32x2*)(v + (2*tr+1)*D_ + c0);
    __builtin_amdgcn_sched_barrier(0);
    if (wave < 8){ asm volatile("s_waitcnt lgkmcnt(0)" ::: "memory"); __builtin_amdgcn_s_barrier(); }
    __builtin_amdgcn_sched_barrier(0);
    {
      const int ch = t >> 8, j = t & 255;
      u32x4 r;
      r.x = __builtin_amdgcn_alignbit(sx.w, sx.w, 16); r.y = __builtin_amdgcn_alignbit(sx.z, sx.z, 16);
      r.z = __builtin_amdgcn_alignbit(sx.y, sx.y, 16); r.w = __builtin_amdgcn_alignbit(sx.x, sx.x, 16);
      char* kb = smem + TOFF + ch*KST;
      *(u32x4*)(kb + 16*(j+15)) = r;
      if (j < 15) *(u32x4*)(kb + 16*j) = u32x4{0,0,0,0};
    }
    const uint32_t cad = (uint32_t)(uintptr_t)smem + CNTOFF, on1 = 1u;
    if (wave >= 8 && lane == 0) asm volatile("ds_add_u32 %0, %1 offset:64" :: "v"(cad), "v"(on1) : "memory");
    if (r0 == 0){ h0 = u32x2{0u,0u}; h1 = u32x2{0u,0u}; h2 = u32x2{0u,0u}; }
    TSP(1);
    u32x2 kk[5];
    kk[1] = wshr1(h1, k0); kk[2] = wshr1(h2, k1); kk[0] = wshr1(h0, kk[2]);
    kk[3] = k0; kk[4] = k1;
    u32x2 o[2];
    #pragma unroll
    for (int j=0;j<2;j++){
      const u32x2 vv = j ? v1 : v0;
      #pragma unroll
      for (int p=0;p<2;p++){
        float r2[2];
        #pragma unroll
        for (int e=0;e<2;e++){
          const u32x4 ww = p ? wb : wa;
          const uint32_t wl = e ? ww.z : ww.x, wh = e ? ww.w : ww.y;
          const float w0 = lo16f(wl), w1 = hi16f(wl), w2 = lo16f(wh), w3 = hi16f(wh);
          auto g = [&](int idx){ uint32_t x = kk[idx][p]; return e? hi16f(x) : lo16f(x); };
          float acc = rbf(g(j)*w3);
          acc = rbf(acc + rbf(g(j+1)*w2));
          acc = rbf(acc + rbf(g(j+2)*w1));
          acc = rbf(acc + rbf(g(j+3)*w0));
          r2[e] = acc * (e? hi16f(vv[p]) : lo16f(vv[p]));
        }
        o[j][p] = pk2(r2[0], r2[1]);
      }
    }
    put_u(smem, tr, o[0], o[1]);
    if (lane == 0) asm volatile("ds_add_u32 %0, %1 offset:64" :: "v"(cad + (wave >= 8 ? 4 : 0)), "v"(on1) : "memory");
    TSP(2);
  }
  int sig = -1, fi = -1;
  if (isA){
    const int ch = wave >> 2, role = (wave + ch) & 3;
    if (role == 0){ __builtin_amdgcn_s_setprio(2); near_conv<4,0>(smem, ch, m, kg); }
    else if (role == 1) near_conv<4,4>(smem, ch, m, kg);
    else if (role == 2){ __builtin_amdgcn_s_setprio(3); far_conv<0,4>(smem, ch, m, kg); }
    else { __builtin_amdgcn_s_setprio(3); far_conv<4,6>(smem, ch, m, kg); }
    TSP(5);
    sig = role == 1;
    fi = role >= 2 ? 2*ch + role - 2 : -1;
  } else if (wave < 8){
    const int ch = wave & 3;
    const uint32_t sb = (uint32_t)(uintptr_t)smem;
    const uint32_t kbase = sb + TOFF + ch*KST + 16*(m - kg + 3);
    const char* ub = smem + ch*UST + 272*(m >> 1) + 16*kg;
    const uint32_t sel = (m & 1) ? 0x07060504u : 0x05040302u;
    const unsigned* cnt = (const unsigned*)(smem + CNTOFF);
    const uint32_t yw = sb + (wave < 4 ? YOFF + ch*YST : YBOFF - 4096 + ch*YB2) + 512*(m >> 1) + 64*(m & 1) + 16*kg;
    const uint32_t cw = sb + CNTOFF + 4*(m >> 1);
    const uint32_t one = 1u;
    bf16x8 KA[4], KB[4];
    u32x6 Ea[4], Eb[4];
    f32x4 acc[4];
#define BUILDU(off) { \
    u32x4 dd[8]; \
    _Pragma("unroll") for (int c=0;c<4;c++){ dd[2*c] = *(const u32x4*)(ub + (off) + 64*c); dd[2*c+1] = *(const u32x4*)(ub + (off) + 64*c + 16); } \
    __builtin_amdgcn_sched_barrier(0); \
    _Pragma("unroll") for (int c=0;c<4;c++){ \
      const u32x4 d0 = dd[2*c], d1 = dd[2*c+1]; \
      const uint32_t e0 = __builtin_amdgcn_perm(d0.y, d0.x, sel), e1 = __builtin_amdgcn_perm(d0.z, d0.y, sel), e2 = __builtin_amdgcn_perm(d0.w, d0.z, sel), \
        e3 = __builtin_amdgcn_perm(d1.x, d0.w, sel), e4 = __builtin_amdgcn_perm(d1.y, d1.x, sel), e5 = __builtin_amdgcn_perm(d1.z, d1.y, sel), \
        e6 = __builtin_amdgcn_perm(d1.w, d1.z, sel); \
      Ea[c] = u32x6{e0,e1,e2,e3,e4,e5}; Eb[c] = u32x6{e1,e2,e3,e4,e5,e6}; \
      asm volatile("" : "+v"(Ea[c]), "+v"(Eb[c])); } }
#define UT(E, x) __builtin_bit_cast(bf16x8, __builtin_shufflevector(E, E, x, x+1, x+2, x+3))
#define MM16(CUR) \
    _Pragma("unroll") for (int c=0;c<4;c++){ \
      acc[0] = MFMA16(CUR[c], UT(Ea[c], 0), acc[0]); acc[1] = MFMA16(CUR[c], UT(Eb[c], 0), acc[1]); \
      acc[2] = MFMA16(CUR[c], UT(Ea[c], 2), acc[2]); acc[3] = MFMA16(CUR[c], UT(Eb[c], 2), acc[3]); }
#define EMIT(mask, cj) asm volatile("s_mov_b64 exec, %[mk]\n\ts_nop 7\n\t" \
      "ds_write_b128 %[a], %[v0] offset:%[o0]\n\tds_write_b128 %[a], %[v1] offset:%[o1]\n\t" \
      "ds_write_b128 %[a], %[v2] offset:%[o2]\n\tds_write_b128 %[a], %[v3] offset:%[o3]\n\t" \
      "ds_add_u32 %[ca], %[on] offset:%[oc]\n\ts_mov_b64 exec, -1" \
      :: [mk] "s"(0x0001000100010001ull * (mask)), [a] "v"(yw), [v0] "v"(acc[0]), [v1] "v"(acc[1]), [v2] "v"(acc[2]), [v3] "v"(acc[3]), \
         [o0] "i"(512*(cj)), [o1] "i"(512*(cj)+128), [o2] "i"(512*(cj)+256), [o3] "i"(512*(cj)+384), \
         [ca] "v"(cw), [on] "v"(one), [oc] "i"(4*(cj)) : "memory")
#define SHL(F) { acc[0] = F(acc[0]); acc[1] = F(acc[1]); acc[2] = F(acc[2]); acc[3] = F(acc[3]); }
    while (__hip_atomic_load(cnt + 16, __ATOMIC_RELAXED, __HIP_MEMORY_SCOPE_WORKGROUP) < 16u) __builtin_amdgcn_s_sleep(1);
    __builtin_amdgcn_fence(__ATOMIC_ACQUIRE, "workgroup");
    TSP(3);
    #pragma unroll
    for (int r=0;r<4;r++) acc[r] = f32x4{0,0,0,0};
    if (wave < 4){
      __builtin_amdgcn_s_setprio(1);
      LDK4(KA, 0);
      BUILDU(0)
#define X1(s, CUR, NXT) \
      LDK4(NXT, ((s)+1) & 7); if ((s) > 0){ WAITK(9, CUR); SHL(shlN2) } else { WAITK(4, CUR); } \
      MM16(CUR) EMIT(3, s);
      X1(0, KA, KB) X1(1, KB, KA) X1(2, KA, KB) X1(3, KB, KA) X1(4, KA, KB) X1(5, KB, KA) X1(6, KA, KB) X1(7, KB, KA)
      TSP(4);
      while (__hip_atomic_load(cnt + 17, __ATOMIC_RELAXED, __HIP_MEMORY_SCOPE_WORKGROUP) < 8u) __builtin_amdgcn_s_sleep(1);
      __builtin_amdgcn_fence(__ATOMIC_ACQUIRE, "workgroup");
      BUILDU(272*8)
      LDK4(KB, 1); WAITK(4, KA); SHL(shlN2) MM16(KA) EMIT(3, 8);
      LDK4(KA, 2); WAITK(9, KB); SHL(shlN2) MM16(KB) EMIT(3, 9);
      LDK4(KB, 3); WAITK(9, KA); SHL(shlN2) MM16(KA) EMIT(3, 10);
      WAITK(5, KB); SHL(shlN2) MM16(KB) EMIT(0x3ff, 11);
    } else {
      LDK4(KA, 15);
      BUILDU(0)
#define X2(dl, CUR, NXT) \
      LDK4(NXT, (dl) > 8 ? (dl)-1 : 4); WAITK(4, CUR); if ((dl) < 15) SHL(shrN2) \
      MM16(CUR)
      X2(15, KA, KB) X2(14, KB, KA) X2(13, KA, KB) X2(12, KB, KA) X2(11, KA, KB) X2(10, KB, KA) X2(9, KA, KB) X2(8, KB, KA)
      EMIT(0xff, 8);
      TSP(4);
      while (__hip_atomic_load(cnt + 17, __ATOMIC_RELAXED, __HIP_MEMORY_SCOPE_WORKGROUP) < 8u) __builtin_amdgcn_s_sleep(1);
      __builtin_amdgcn_fence(__ATOMIC_ACQUIRE, "workgroup");
      BUILDU(272*8)
      LDK4(KB, 5); WAITK(4, KA); SHL(shlN8) MM16(KA) EMIT(3, 12);
      LDK4(KA, 6); WAITK(9, KB); SHL(shlN2) MM16(KB) EMIT(3, 13);
      LDK4(KB, 7); WAITK(9, KA); SHL(shlN2) MM16(KA) EMIT(3, 14);
      WAITK(5, KB); SHL(shlN2) MM16(KB) EMIT(3, 15);
    }
    TSP(5);
  } else {
    __builtin_amdgcn_s_setprio(2);
    const int j0 = (wave + w5) & 7;
    const int lr = 8*m + 2*kg;
    u32x2 qr[4];
    {
      const u16* qp = q + (128*j0 + lr)*D_ + c0;
      qr[0] = *(const u32x2*)(qp); qr[1] = *(const u32x2*)(qp + D_);
      qr[2] = *(const u32x2*)(qp + 1024*D_); qr[3] = *(const u32x2*)(qp + 1025*D_);
    }
    __builtin_amdgcn_sched_barrier(0);
    #pragma unroll
    for (int st=0; st<2; st++){
      const int cj = j0 + 8*st;
      const char* yb = smem + YOFF + 512*cj + 128*kg + 4*m;
      const char* up = smem + 272*cj + 16 + 2*lr;
      while (__hip_atomic_load((const unsigned*)(smem + CNTOFF) + cj, __ATOMIC_RELAXED, __HIP_MEMORY_SCOPE_WORKGROUP) < (st ? 64u : 32u)) __builtin_amdgcn_s_sleep(1);
      __builtin_amdgcn_fence(__ATOMIC_ACQUIRE, "workgroup");
      float s0[4], s1[4];
      #pragma unroll
      for (int c=0;c<4;c++){
        uint32_t uu = *(const uint32_t*)(up + c*UST);
        float sd = (c & 1) ? hi16f(dsk[c>>1]) : lo16f(dsk[c>>1]);
        s0[c] = rbf(lo16f(uu)*sd); s1[c] = rbf(hi16f(uu)*sd);
      }
      float o0[4], o1[4];
      #pragma unroll
      for (int c=0;c<4;c++){
        float a0 = *(const float*)(yb + c*YST), a1 = *(const float*)(yb + c*YST + 64);
        if (st){ const char* y2 = yb + (YBOFF - 4096 - YOFF) + c*YB2; a0 += *(const float*)(y2); a1 += *(const float*)(y2 + 64); }
        float q0 = (c & 1) ? hi16f(qr[2*st][c>>1]) : lo16f(qr[2*st][c>>1]);
        float q1 = (c & 1) ? hi16f(qr[2*st+1][c>>1]) : lo16f(qr[2*st+1][c>>1]);
        o0[c] = q0 * rbf(rbf(a0) + s0[c]);
        o1[c] = q1 * rbf(rbf(a1) + s1[c]);
      }
      u16* op = out + D_ + c0 + (128*cj + lr)*(2*D_);
      *(u32x2*)(op)        = u32x2{pk2(o0[0],o0[1]), pk2(o0[2],o0[3])};
      *(u32x2*)(op + 2*D_) = u32x2{pk2(o1[0],o1[1]), pk2(o1[2],o1[3])};
    }
  }
  TSP(6);
  if (sig >= 0){
    __builtin_amdgcn_fence(__ATOMIC_RELEASE, "workgroup");
    if (lane == 0) __hip_atomic_fetch_add((unsigned*)(smem + CNTOFF) + sig, 1u, __ATOMIC_RELAXED, __HIP_MEMORY_SCOPE_WORKGROUP);
  }
  if (fi >= 0){
    const int p = (fi*64 + lane + 32*(w5 & 15)) & 511;
    u16* op = out + c0;
    #pragma unroll
    for (int st=0; st<2; st++){
      const int tq = 4*(p >> 1) + (p & 1) + 2*st;
      const char* yb = smem + YAOFF + 8*tq + 16*(tq >> 6);
      const char* up = smem + 272*(tq >> 6) + 16 + 4*(tq & 63);
      float s0[4], s1[4];
      #pragma unroll
      for (int c=0;c<4;c++){
        uint32_t uu = *(const uint32_t*)(up + c*UST);
        float sd = (c & 1) ? hi16f(dsk[c>>1]) : lo16f(dsk[c>>1]);
        s0[c] = rbf(lo16f(uu)*sd); s1[c] = rbf(hi16f(uu)*sd);
      }
      while (__hip_atomic_load((const unsigned*)(smem + CNTOFF) + st, __ATOMIC_RELAXED, __HIP_MEMORY_SCOPE_WORKGROUP) < (st ? 4u : 12u)) __builtin_amdgcn_s_sleep(1);
      __builtin_amdgcn_fence(__ATOMIC_ACQUIRE, "workgroup");
      float o0[4], o1[4];
      const char* yf = smem + YFOFF + (tq >= 257 ? 4*(2*tq - 515) : -16);
      #pragma unroll
      for (int c=0;c<4;c++){
        f32x2 a = *(const f32x2*)(yb + c*YST);
        float b0 = *(const float*)(yf + c*YFST), b1 = *(const float*)(yf + c*YFST + 4);
        o0[c] = rbf(a.x + b0) + s0[c];
        o1[c] = rbf(a.y + b1) + s1[c];
      }
      *(u32x2*)(op + (2*tq)*(2*D_))   = u32x2{pk2(o0[0],o0[1]), pk2(o0[2],o0[3])};
      *(u32x2*)(op + (2*tq+1)*(2*D_)) = u32x2{pk2(o1[0],o1[1]), pk2(o1[2],o1[3])};
    }
  }
  TSP(7);
  if (wave == 0){
    unsigned long long mn = tsv[0];
    for (int i=0;i<4;i++){
      unsigned long long x = __hip_atomic_load(wsb + 64*i + lane, __ATOMIC_RELAXED, __HIP_MEMORY_SCOPE_AGENT);
      if (x + 1500 > tsv[0] && x < mn) mn = x;
    }
    unsigned d32 = (unsigned)(tsv[0] - mn);
    for (int o=32;o>=1;o>>=1){ unsigned y = __shfl_xor(d32, o); d32 = y > d32 ? y : d32; }
    d32 = __builtin_amdgcn_readfirstlane(d32);
    if (d32 > 1500) d32 = 1500;
    unsigned long long dlt = (unsigned long long)d32*8;
    unsigned long long tgt = tsv[7] + dlt;
    while (__builtin_amdgcn_s_memrealtime() < tgt) __builtin_amdgcn_s_sleep(1);
  }
}

extern "C" void kernel_launch(void* const* d_in, const int* in_sizes, int n_in,
                              void* d_out, int out_size, void* d_ws, size_t ws_size,
                              hipStream_t stream) {
  mono_kernel<<<256, 1024, LDS_TOTAL, stream>>>((const u16*)d_in[0], (const u16*)d_in[1], (const u16*)d_in[2], (const u16*)d_in[3],
     (const u16*)d_in[4], (const u16*)d_in[5], (const u16*)d_in[6], (const u16*)d_in[7], (const u16*)d_in[8], (const u16*)d_in[9],
     (const u16*)d_in[10], (const u16*)d_in[11], (const u16*)d_in[12], (u16*)d_out, (unsigned long long*)d_ws
);
}
```

```cpp
#include <hip/hip_runtime.h>
#include <hip/hip_bf16.h>
#include <stdint.h>

typedef unsigned short u16;
typedef short bf16x8 __attribute__((ext_vector_type(8)));
typedef float f32x4 __attribute__((ext_vector_type(4)));
typedef float f32x2 __attribute__((ext_vector_type(2)));
typedef __bf16 bf16x2_t __attribute__((ext_vector_type(2)));

#define ULATE 2
#define HPRIO 2
#define L_ 2048
#define D_ 512
#define NS_ 128

__device__ __forceinline__ float bf2f(u16 x){ return __builtin_bit_cast(float, (uint32_t)x << 16); }
__device__ __forceinline__ u16 f2bf(float f){ __hip_bfloat16 h = __float2bfloat16(f); return __builtin_bit_cast(u16, h); }
__device__ __forceinline__ float rbf(float f){ return bf2f(f2bf(f)); }
__device__ __forceinline__ uint32_t pk2(float a, float b){
  f32x2 v = {a,b}; bf16x2_t r = __builtin_convertvector(v, bf16x2_t); return __builtin_bit_cast(uint32_t, r); }
__device__ __forceinline__ float lo16f(uint32_t w){ return __builtin_bit_cast(float, w << 16); }
__device__ __forceinline__ float hi16f(uint32_t w){ return __builtin_bit_cast(float, w & 0xffff0000u); }

struct cpx { float re, im; };
__device__ __forceinline__ float amul(float a, float b){ float r; asm("v_mul_f32 %0, %1, %2" : "=v"(r) : "v"(a), "v"(b)); return r; }
__device__ __forceinline__ float aadd(float a, float b){ float r; asm("v_add_f32 %0, %1, %2" : "=v"(r) : "v"(a), "v"(b)); return r; }
__device__ __forceinline__ float afma(float a, float b, float c){ float r; asm("v_fma_f32 %0, %1, %2, %3" : "=v"(r) : "v"(a), "v"(b), "v"(c)); return r; }
__device__ __forceinline__ float afms(float a, float b, float c){ float r; asm("v_fma_f32 %0, %1, %2, -%3" : "=v"(r) : "v"(a), "v"(b), "v"(c)); return r; }
__device__ __forceinline__ cpx cmul(cpx a, cpx b){
  f32x2 A = {a.re, a.im}, B = {b.re, b.im}, T, R;
  asm("v_pk_mul_f32 %0, %1, %2 op_sel:[1,1] op_sel_hi:[1,0] neg_lo:[1,0]" : "=v"(T) : "v"(A), "v"(B));
  asm("v_pk_fma_f32 %0, %1, %2, %3 op_sel:[0,0,0] op_sel_hi:[0,1,1]" : "=v"(R) : "v"(A), "v"(B), "v"(T));
  cpx r; r.re = R.x; r.im = R.y; return r; }
__device__ __forceinline__ cpx cexp_(float re, float im){ float e = expf(re); float s,c; sincosf(im,&s,&c); cpx r; r.re=e*c; r.im=e*s; return r; }

#define MFMA16(a,b,c) __builtin_amdgcn_mfma_f32_16x16x32_bf16(a,b,c,0,0,0)
typedef uint32_t u32x2 __attribute__((ext_vector_type(2)));
typedef uint32_t u32x4 __attribute__((ext_vector_type(4)));
#define TR 544
#define TSZ (57*TR)
#define UST 4352
#define TOFF 17408
#define KST 4352
#define KS2 1536
#define XOFF (TOFF + 4*TSZ)
#define NOFF XOFF
#define FCS 320
#define FTS (12*FCS)
#define FTOFF (XOFF + 4*KS2)
#define K1OFF (FTOFF + 4*FTS)
#define YFOFF (TOFF + 45056 + 16)
#define YFST 6176
#define YAOFF (TOFF + 69888)
#define YOFF (TOFF + 36864)
#define YST 8448
#define YBOFF (YOFF + 4*YST)
#define YB2 4352
#define XCP 5504
#define XCH 11008
#define KBO 44032
#define KCP 4224
#define KCH 8464
#define YBO 77888
#define CNTOFF (K1OFF + 16)
#define LDS_TOTAL (CNTOFF + 80)
typedef uint32_t u32x6 __attribute__((ext_vector_type(6)));
typedef _Float16 f16x8 __attribute__((ext_vector_type(8)));
typedef _Float16 f16x2 __attribute__((ext_vector_type(2)));
#define MFMAH(a,b,c) __builtin_amdgcn_mfma_f32_16x16x32_f16(a,b,c,0,0,0)
__device__ __forceinline__ uint32_t pkh(float a, float b){
  f32x2 v = {a,b}; f16x2 r = __builtin_convertvector(v, f16x2); return __builtin_bit_cast(uint32_t, r); }
struct cp2 { cpx a, b; };
__device__ __forceinline__ cp2 cmul2(const cp2& x, const cp2& y){ cp2 r; r.a = cmul(x.a, y.a); r.b = cmul(x.b, y.b); return r; }
__device__ __forceinline__ void putg(char* p, const cp2& x){ *(u32x2*)p = u32x2{pkh(x.a.re, x.a.im), pkh(x.b.re, x.b.im)}; }
__device__ __forceinline__ void putx(char* p, const cp2& x){ *(u32x2*)p = u32x2{pkh(x.a.re, -x.a.im), pkh(x.b.re, -x.b.im)}; }

__device__ __forceinline__ uint32_t resid2(uint32_t h, float a, float b){
  return pk2(a - lo16f(h), b - hi16f(h));
}
__device__ __forceinline__ int f2i(float x){ return __builtin_bit_cast(int, x); }
__device__ __forceinline__ float i2f(int x){ return __builtin_bit_cast(float, x); }
__device__ __forceinline__ f32x4 shrN1(f32x4 x){
  f32x4 r;
  #pragma unroll
  for (int i=0;i<4;i++) r[i] = i2f(__builtin_amdgcn_update_dpp(0, f2i(x[i]), 0x111, 0xf, 0xf, true));
  return r;
}
__device__ __forceinline__ f32x4 shrN2(f32x4 x){
  f32x4 r;
  #pragma unroll
  for (int i=0;i<4;i++) r[i] = i2f(__builtin_amdgcn_update_dpp(0, f2i(x[i]), 0x112, 0xf, 0xf, true));
  return r;
}
__device__ __forceinline__ f32x4 shlN1(f32x4 x){
  f32x4 r;
  #pragma unroll
  for (int i=0;i<4;i++) r[i] = i2f(__builtin_amdgcn_update_dpp(0, f2i(x[i]), 0x101, 0xf, 0xf, true));
  return r;
}
__device__ __forceinline__ f32x4 shlN2(f32x4 x){
  f32x4 r;
  #pragma unroll
  for (int i=0;i<4;i++) r[i] = i2f(__builtin_amdgcn_update_dpp(0, f2i(x[i]), 0x102, 0xf, 0xf, true));
  return r;
}
__device__ __forceinline__ f32x4 shlN8(f32x4 x){
  f32x4 r;
  #pragma unroll
  for (int i=0;i<4;i++) r[i] = i2f(__builtin_amdgcn_update_dpp(0, f2i(x[i]), 0x108, 0xf, 0xf, true));
  return r;
}
__device__ __forceinline__ u32x2 wshr1(u32x2 old, u32x2 x){
  u32x2 r;
  r.x = (uint32_t)__builtin_amdgcn_update_dpp((int)old.x, (int)x.x, 0x138, 0xf, 0xf, false);
  r.y = (uint32_t)__builtin_amdgcn_update_dpp((int)old.y, (int)x.y, 0x138, 0xf, 0xf, false);
  return r;
}
struct W6 { uint32_t w[6]; };
__device__ __forceinline__ W6 uwin(const char* p){
  u32x2 a = *(const u32x2*)p, b = *(const u32x2*)(p+8), c = *(const u32x2*)(p+16);
  W6 r; r.w[0]=a.x; r.w[1]=a.y; r.w[2]=b.x; r.w[3]=b.y; r.w[4]=c.x; r.w[5]=c.y; return r;
}
__device__ __forceinline__ bf16x8 usel(const W6& W, int x, uint32_t sel){
  u32x4 r;
  r.x = __builtin_amdgcn_perm(W.w[x+1], W.w[x],   sel); r.y = __builtin_amdgcn_perm(W.w[x+2], W.w[x+1], sel);
  r.z = __builtin_amdgcn_perm(W.w[x+3], W.w[x+2], sel); r.w = __builtin_amdgcn_perm(W.w[x+4], W.w[x+3], sel);
  return __builtin_bit_cast(bf16x8, r);
}
__device__ __forceinline__ bf16x8 uodd(const W6& W, int x){
  u32x4 r;
  r.x = __builtin_amdgcn_alignbit(W.w[x+1], W.w[x],   16); r.y = __builtin_amdgcn_alignbit(W.w[x+2], W.w[x+1], 16);
  r.z = __builtin_amdgcn_alignbit(W.w[x+3], W.w[x+2], 16); r.w = __builtin_amdgcn_alignbit(W.w[x+4], W.w[x+3], 16);
  return __builtin_bit_cast(bf16x8, r);
}
__device__ __forceinline__ bf16x8 uevn(const W6& W, int x){
  u32x4 r = {W.w[x], W.w[x+1], W.w[x+2], W.w[x+3]};
  return __builtin_bit_cast(bf16x8, r);
}
__device__ __forceinline__ void put_u(char* smem, int tr, u32x2 r0, u32x2 r1){
  uint32_t d[4];
  d[0] = __builtin_amdgcn_perm(r1.x, r0.x, 0x05040100); d[1] = __builtin_amdgcn_perm(r1.x, r0.x, 0x07060302);
  d[2] = __builtin_amdgcn_perm(r1.y, r0.y, 0x05040100); d[3] = __builtin_amdgcn_perm(r1.y, r0.y, 0x07060302);
  const int a = tr >> 6, pos = tr & 63;
  char* p = smem + 272*a + 16 + 4*pos;
  #pragma unroll
  for (int c=0;c<4;c++) *(uint32_t*)(p + c*UST) = d[c];
  if (pos >= 60 && a < 15){
    #pragma unroll
    for (int c=0;c<4;c++) *(uint32_t*)(p + c*UST + 16) = d[c];
  }
  if (tr < 16) *(uint32_t*)(smem + (tr>>2)*UST + 4*(tr&3)) = 0u;
}

#define LDK(F, dl, c) asm volatile("ds_read_b128 %0, %1 offset:%2" : "=v"(F) : "v"(kbase), "i"(256*(dl) + 64*(3-(c))))
#define LDK4(S, dl) LDK(S[0], dl, 0); LDK(S[1], dl, 1); LDK(S[2], dl, 2); LDK(S[3], dl, 3)
#define WAITK(n, S) asm volatile("s_waitcnt lgkmcnt(" #n ")" : "+v"(S[0]), "+v"(S[1]), "+v"(S[2]), "+v"(S[3]))

template<int NT, int R0>
__device__ __forceinline__ void near_conv(char* smem, int ch, int m, int kg){
  const uint32_t kbase = (uint32_t)(uintptr_t)smem + NOFF + ch*KS2 + 16*(m - kg + 3);
#define LDN(F, hs, c) asm volatile("ds_read_b128 %0, %1 offset:%2" : "=v"(F) : "v"(kbase), "i"(256*(4-(hs)) + 64*(3-(c))))
#define LDN4(S, hs) LDN(S[0], hs, 0); LDN(S[1], hs, 1); LDN(S[2], hs, 2); LDN(S[3], hs, 3)
  bf16x8 KA[4], KB[4];
  LDN4(KA, 0);
  bf16x8 U[NT][4];
  {
    const char* pp = smem + ch*UST + 272*m + 16*kg + (R0 ? 8 : 0);
    #pragma unroll
    for (int c=0;c<4;c++){
      W6 W = uwin(pp + 64*c);
      #pragma unroll
      for (int r=0;r<NT;r++){ const int rel = R0 + r + 1 - (R0 ? 4 : 0); U[r][c] = (rel & 1) ? uodd(W, rel >> 1) : uevn(W, rel >> 1); }
    }
  }
  f32x4 acc[NT];
  #pragma unroll
  for (int r=0;r<NT;r++) acc[r] = f32x4{0,0,0,0};
#define NH(hs, CUR, NXT) \
  if ((hs) < 4){ LDN4(NXT, (hs)+1); WAITK(4, CUR); } else { WAITK(0, CUR); } \
  if ((hs) > 0){ _Pragma("unroll") for (int r=0;r<NT;r++) acc[r] = shrN1(acc[r]); } \
  _Pragma("unroll") for (int c=0;c<4;c++){ _Pragma("unroll") for (int r=0;r<NT;r++) acc[r] = MFMA16(CUR[c], U[r][c], acc[r]); }
  NH(0, KA, KB) NH(1, KB, KA) NH(2, KA, KB) NH(3, KB, KA) NH(4, KA, KB)
  float* yb = (float*)(smem + YAOFF + ch*YST) + (R0 + 32*kg + 132*m);
  #pragma unroll
  for (int i=0;i<4;i++){
    #pragma unroll
    for (int r=0;r<NT;r++) yb[8*i + r] = acc[r][i];
  }
}
__device__ __forceinline__ bf16x8 band(int sh, int w, int kg){
  const uint32_t by = (((1u << w) - 1u) << sh) >> (8*kg);
  u32x4 r;
  #pragma unroll
  for (int k2=0;k2<4;k2++){ uint32_t x = (by >> (2*k2)) & 3u; x = (x | (x << 15)) & 0x00010001u; r[k2] = x * 0x3f80u; }
  return __builtin_bit_cast(bf16x8, r);
}
__device__ __forceinline__ void split2(const f32x4& a, const f32x4& b, bf16x8& h, bf16x8& l){
  u32x4 hh, ll;
  #pragma unroll
  for (int i=0;i<4;i++){ uint32_t x = pk2(a[i], b[i]); hh[i] = x; ll[i] = resid2(x, a[i], b[i]); }
  h = __builtin_bit_cast(bf16x8, hh); l = __builtin_bit_cast(bf16x8, ll);
}
__device__ __forceinline__ bf16x8 pack2(const f32x4& a, const f32x4& b){
  u32x4 hh;
  #pragma unroll
  for (int i=0;i<4;i++) hh[i] = pk2(a[i], b[i]);
  return __builtin_bit_cast(bf16x8, hh);
}
template<int A0, int A1>
__device__ __forceinline__ void far_conv(char* smem, int ch, int m, int kg){
  const char* ub = smem + ch*UST;
  const f32x4 Z4 = {0,0,0,0};
  const int B0 = 2*m + kg - 2;
  const char* va0 = ub + (B0 < 0 ? 0 : 272*(B0 >> 4) + 16 + 16*(B0 & 15));
  const char* va1 = ub + 272*((B0+32) >> 4) + 16 + 16*((B0+32) & 15);
  const int cp = (3 - m) & 3;
  const char* ft = smem + FTOFF + ch*FTS + cp*FCS + 4*(47 - m + 4*kg - cp);
  char* yf = smem + YFOFF + ch*YFST + 64*m + 16*kg;
  if (A0 == 0 && kg == 0 && m < 4) *(float*)(smem + YFOFF + ch*YFST - 16 + 4*m) = 0.f;
  f32x4 acc[6];
  #pragma unroll
  for (int a=0;a<6;a++) acc[a] = Z4;
  {
    bf16x8 M[4];
    M[0] = band(14 + m, 3, kg); M[1] = band(9 + m, 5, kg); M[2] = band(6 + m, 3, kg); M[3] = band(1 + m, 5, kg);
    bf16x8 Ch[3][2];
    #pragma unroll
    for (int dl=0;dl<3;dl++){
      #pragma unroll
      for (int h=0;h<2;h++) Ch[dl][h] = *(const bf16x8*)(ft + h*(4*FCS) - 64*dl);
    }
    #pragma unroll
    for (int b=(A0 > 2 ? A0-2 : 0);b<A1;b++){
      const bf16x8 V = *(const bf16x8*)(b ? va1 + 544*(b-1) : va0);
      f32x4 F[4];
      #pragma unroll
      for (int p=0;p<4;p++) F[p] = MFMA16(V, M[p], Z4);
      #pragma unroll
      for (int h=0;h<2;h++){
        const bf16x8 Bh = pack2(F[2*h], F[2*h+1]);
        #pragma unroll
        for (int dl=0;dl<3;dl++) if (b + dl >= A0 && b + dl < A1) acc[b+dl] = MFMA16(Bh, Ch[dl][h], acc[b+dl]);
      }
      if (b < 2 && b >= A0) *(f32x4*)(yf + 1024*b) = acc[b];
    }
  }
  {
    const char* vb0 = ub + (B0 < 1 ? 0 : 272*((B0-1) >> 4) + 16 + 16*((B0-1) & 15));
    const char* vb1 = ub + 272*((B0+31) >> 4) + 16 + 16*((B0+31) & 15);
    const char* f8 = smem + FTOFF + ch*FTS + 8*FCS + cp*FCS + 4*(63 - m + 4*kg - cp);
    const bf16x8 Ma = band(8 + m, 7, kg), Mb = band(7 + m, 9, kg), Mc = band(15 + m, 2, kg);
    const float k1024 = ((const float*)(smem + K1OFF))[ch];
    bf16x8 Ch[4];
    #pragma unroll
    for (int dl=0;dl<A1-2;dl++) Ch[dl] = *(const bf16x8*)(f8 - 64*dl);
    #pragma unroll
    for (int b=0;b<A1-2;b++){
      const bf16x8 V = *(const bf16x8*)(b ? va1 + 544*(b-1) : va0);
      const bf16x8 V2 = *(const bf16x8*)(b ? vb1 + 544*(b-1) : vb0);
      const f32x4 Fa = MFMA16(V, Ma, Z4), Fb = MFMA16(V2, Mb, Z4);
      const bf16x8 Bh = pack2(Fa, Fb);
      if (b + 2 >= A0){
        const f32x4 Fc = MFMA16(Mc, V, Z4);
        #pragma unroll
        for (int i=0;i<4;i++) acc[b+2][i] = __builtin_fmaf(k1024, Fc[i], acc[b+2][i]);
      }
      #pragma unroll
      for (int dl=0;dl<4;dl++) if (b + 2 + dl >= A0 && b + 2 + dl < A1) acc[b+2+dl] = MFMA16(Bh, Ch[dl], acc[b+2+dl]);
    }
  }
  #pragma unroll
  for (int a=(A0 > 2 ? A0 : 2);a<A1;a++) *(f32x4*)(yf + 1024*a) = acc[a];
}

__global__ __launch_bounds__(1024) void mono_kernel(const u16* __restrict__ u, const u16* __restrict__ logA, const u16* __restrict__ Aim,
    const u16* __restrict__ Cre, const u16* __restrict__ Cim, const u16* __restrict__ logdt, const u16* __restrict__ Dskip,
    const u16* __restrict__ q, const u16* __restrict__ k, const u16* __restrict__ v, const u16* __restrict__ shift_k,
    const u16* __restrict__ ssmk, const u16* __restrict__ ssmD, u16* __restrict__ out
){
#define TSP(i)
  TSP(0); TSP(1); TSP(2); TSP(3); TSP(4); TSP(5);
  extern __shared__ __attribute__((aligned(16))) char smem[];
  const int bid = blockIdx.x, t = threadIdx.x;
  const int w5 = bid >> 3;
  const bool isA = w5 < 16;
  const int c0 = (bid & 7)*64 + (w5 & 15)*4;
  const int wave = t >> 6, lane = t & 63, m = lane & 15, kg = lane >> 4;
  asm volatile("" :: "s"(u), "s"(logA), "s"(Aim), "s"(Cre), "s"(Cim), "s"(logdt), "s"(Dskip), "s"(q), "s"(k), "s"(v), "s"(shift_k), "s"(ssmk), "s"(ssmD), "s"(out));
  u32x2 dsk;
  if (t < 20) ((unsigned*)(smem + CNTOFF))[t] = 0u;
  int vz; asm volatile("v_mov_b32 %0, 0" : "=v"(vz));
  asm volatile("global_load_dwordx2 %0, %1, %2" : "=v"(dsk) : "v"(2*c0 + vz), "s"(isA ? Dskip : ssmD));
  if (isA){
    const int ch = wave & 3, rj = wave >> 2;
    const int d = c0 + ch;
    char* tb = smem + TOFF + ch*TSZ;
    unsigned* cnt = (unsigned*)(smem + CNTOFF);
    uint32_t ldt, pcr, pci, lde, pla, pai;
    asm volatile("global_load_ushort %0, %1, %2" : "=v"(lde) : "v"(2*(c0 + ((t >> 7) & 3))), "s"(logdt));
    asm volatile("global_load_ushort %0, %1, %2" : "=v"(pla) : "v"(2*(c0*NS_ + (t & 511))), "s"(logA));
    asm volatile("global_load_ushort %0, %1, %2" : "=v"(pai) : "v"(2*(c0*NS_ + (t & 511))), "s"(Aim));
    asm volatile("global_load_ushort %0, %1, %2" : "=v"(ldt) : "v"(2*d), "s"(logdt));
    asm volatile("global_load_dword %0, %1, %2" : "=v"(pcr) : "v"(2*d*NS_ + 4*lane), "s"(Cre));
    asm volatile("global_load_dword %0, %1, %2" : "=v"(pci) : "v"(2*d*NS_ + 4*lane), "s"(Cim));
#define PWAIT(n) asm volatile("s_waitcnt vmcnt(" #n ")" : "+v"(lde), "+v"(pla), "+v"(pai), "+v"(ldt), "+v"(pcr), "+v"(pci), "+v"(dsk))
    __builtin_amdgcn_sched_barrier(0);
    __builtin_amdgcn_s_barrier();
    __builtin_amdgcn_sched_barrier(0);
    u32x2 uu[4];
    const int tl = (t + 64*(w5 & 15)) & 511;
    cp2 C; float ksc;
    auto prep = [&](){
      uint32_t e0 = pcr & 0x7f807f80u, e1 = pci & 0x7f807f80u;
      uint32_t x = max(max(e0 & 0xffffu, e0 >> 16), max(e1 & 0xffffu, e1 >> 16));
      x = max(x, (uint32_t)__builtin_amdgcn_update_dpp(0, (int)x, 0xb1, 0xf, 0xf, true));
      x = max(x, (uint32_t)__builtin_amdgcn_update_dpp(0, (int)x, 0x4e, 0xf, 0xf, true));
      x = max(x, (uint32_t)__builtin_amdgcn_update_dpp(0, (int)x, 0x141, 0xf, 0xf, true));
      x = max(x, (uint32_t)__builtin_amdgcn_update_dpp(0, (int)x, 0x140, 0xf, 0xf, true));
      const uint32_t y = max(max((uint32_t)__builtin_amdgcn_readlane((int)x, 0), (uint32_t)__builtin_amdgcn_readlane((int)x, 16)),
                             max((uint32_t)__builtin_amdgcn_readlane((int)x, 32), (uint32_t)__builtin_amdgcn_readlane((int)x, 48)));
      const int sh = 127 - (int)(y >> 7);
      C.a.re = ldexpf(lo16f(pcr), sh); C.a.im = ldexpf(lo16f(pci), sh);
      C.b.re = ldexpf(hi16f(pcr), sh); C.b.im = ldexpf(hi16f(pci), sh);
      ksc = ldexpf(2.f*rbf(expf(lo16f(ldt))), -sh);
      asm volatile("" : "+v"(C.a.re), "+v"(C.a.im), "+v"(C.b.re), "+v"(C.b.im), "+v"(ksc));
    };
    if (wave >= 8){
      PWAIT(0);
      prep();
    } else {
      PWAIT(3);
      const float dte = rbf(expf(lo16f(lde)));
      const float zre = -rbf(expf(lo16f(pla))) * dte;
      const float zim = lo16f(pai) * dte;
      const cpx e = cexp_(zre, zim), f = cexp_(zre*128.f, zim*128.f);
      char* ep = smem + XOFF + (t >> 7)*2048 + 8*(t & 127);
      *(float2*)(ep) = make_float2(e.re, e.im);
      *(float2*)(ep + 1024) = make_float2(f.re, f.im);
      PWAIT(0);
      prep();
    }
    asm volatile("s_waitcnt lgkmcnt(0)" ::: "memory"); __builtin_amdgcn_s_barrier();
    TSP(1);
    if (wave >= 8){
      __builtin_amdgcn_sched_barrier(0);
      const u16* up = u + (2*tl)*D_ + c0;
      uu[0] = *(const u32x2*)(up);           uu[1] = *(const u32x2*)(up + D_);
      uu[2] = *(const u32x2*)(up + 1024*D_); uu[3] = *(const u32x2*)(up + 1025*D_);
      __builtin_amdgcn_sched_barrier(0);
    }
    char* col = tb + 8*lane;
    char* nb = smem + NOFF + ch*KS2;
    char* ft = smem + FTOFF + ch*FTS;
    {
      const char* eb_ = smem + XOFF + ch*2048;
      cp2 e1, e2;
      { f32x4 a = *(const f32x4*)(eb_ + 16*lane); e2.a.re = a[0]; e2.a.im = a[1]; e2.b.re = a[2]; e2.b.im = a[3];
        f32x4 c = *(const f32x4*)(eb_ + 1024 + 16*lane); e1.a.re = c[0]; e1.a.im = c[1]; e1.b.re = c[2]; e1.b.im = c[3]; }
      __builtin_amdgcn_fence(__ATOMIC_RELEASE, "workgroup");
      if (lane == 0) __hip_atomic_fetch_add(cnt + 2, 1u, __ATOMIC_RELAXED, __HIP_MEMORY_SCOPE_WORKGROUP);
      if (rj == 2){
        __builtin_amdgcn_s_setprio(3);
        cp2 e8 = cmul2(e2, e2); e8 = cmul2(e8, e8); e8 = cmul2(e8, e8);
        *(u32x2*)(col + 41*TR) = u32x2{0x3c00u, 0x3c00u};
        cp2 e16 = cmul2(e8, e8);
        cp2 xa = e8, xb = e16;
        #pragma unroll
        for (int i=1;i<16;i+=2){
          putx(col + (41+i)*TR, xa); if (i < 15) putx(col + (42+i)*TR, xb);
          if (i < 15) xa = cmul2(xa, e16);
          if (i < 13) xb = cmul2(xb, e16);
        }
        __builtin_amdgcn_fence(__ATOMIC_RELEASE, "workgroup");
        if (lane == 0) __hip_atomic_fetch_add(cnt + 4 + ch, 1u, __ATOMIC_RELAXED, __HIP_MEMORY_SCOPE_WORKGROUP);
        __builtin_amdgcn_s_setprio(0);
      } else if (rj == 3){
        __builtin_amdgcn_s_setprio(2);
        cp2 eb = cmul2(e1, e1); eb = cmul2(eb, eb); eb = cmul2(eb, eb);
        cp2 cb = cmul2(C, eb);
        #pragma unroll
        for (int i=0;i<9;i++){ putg(col + (32+i)*TR, cb); if (i < 8) cb = cmul2(cb, e1); }
      } else if (rj == 0){
        cp2 cb = C, cd = cmul2(C, e1);
        #pragma unroll
        for (int p=0;p<8;p++){
          putg(col + p*TR, cb); putg(col + (8+p)*TR, cd);
          if (p < 7){ cb = cmul2(cb, e2); cd = cmul2(cd, e2); }
        }
      } else {
        __builtin_amdgcn_s_setprio(1);
        cp2 S2 = cmul2(e2, e2), S4 = cmul2(S2, S2), S6 = cmul2(S4, S2);
        cp2 cb = cmul2(C, cmul2(e1, e1));
        #pragma unroll
        for (int a=0;a<2;a++){
          putg(col + (16+4*a)*TR, cb);
          putg(col + (16+4*a+1)*TR, cmul2(cb, S2));
          putg(col + (16+4*a+2)*TR, cmul2(cb, S4));
          putg(col + (16+4*a+3)*TR, cmul2(cb, S6));
          cb = cmul2(cb, e1);
        }
        #pragma unroll
        for (int a=0;a<4;a++){
          putg(col + (24+2*a)*TR, cb);
          putg(col + (24+2*a+1)*TR, cmul2(cb, S4));
          if (a < 3) cb = cmul2(cb, e1);
        }
      }
    }
    TSP(2);
    __builtin_amdgcn_fence(__ATOMIC_RELEASE, "workgroup");
    while (__hip_atomic_load(cnt + 2, __ATOMIC_RELAXED, __HIP_MEMORY_SCOPE_WORKGROUP) < 16u ||
           __hip_atomic_load(cnt + 4 + ch, __ATOMIC_RELAXED, __HIP_MEMORY_SCOPE_WORKGROUP) < 1u) __builtin_amdgcn_s_sleep(1);
    __builtin_amdgcn_fence(__ATOMIC_ACQUIRE, "workgroup");
    if (rj != 2){
      const char* ga = tb + (16*(rj == 3 ? 2 : rj) + m)*TR + 16*kg;
      const char* xb = tb + (41 + m)*TR + 16*kg;
      f16x8 GA[8], XB[8];
      #pragma unroll
      for (int kc=0;kc<8;kc++){ GA[kc] = *(const f16x8*)(ga + 64*kc); XB[kc] = *(const f16x8*)(xb + 64*kc); }
      __builtin_amdgcn_sched_barrier(0);
      f32x4 a4 = {0,0,0,0}, b4 = {0,0,0,0};
      #pragma unroll
      for (int kc=0;kc<8;kc+=2){ a4 = MFMAH(GA[kc], XB[kc], a4); b4 = MFMAH(GA[kc+1], XB[kc+1], b4); }
      float kv[4];
      #pragma unroll
      for (int i=0;i<4;i++) kv[i] = (a4[i] + b4[i]) * ksc;
      if (rj == 0){
        *(u32x2*)(nb + 16*(15 + 16*(kg >> 1) + m) + 8*(1 - (kg & 1))) = u32x2{pk2(kv[3], kv[2]), pk2(kv[1], kv[0])};
      } else if (rj == 1){
        const float k8 = i2f(__builtin_amdgcn_update_dpp(0, f2i(kv[0]), 0x130, 0xf, 0xf, true));
        if (kg < 2){
          *(u32x4*)(nb + 16*(47 + 16*kg + m)) = u32x4{pk2(k8, kv[3]), pk2(kv[2], kv[2]), pk2(kv[2], kv[1]), pk2(kv[0], kv[0])};
        } else {
          if (lane == 32) *(u32x4*)(nb + 16*79) = u32x4{0u, 0u, pk2(0.f, kv[0]), pk2(kv[0], kv[0])};
          #pragma unroll
          for (int i=0;i<4;i++){
            const int x1 = 32*(2*(kg-2) + (i >> 1)) + (i & 1) + 2*m - 1;
            char* p = ft + ((x1 >> 1) & 1)*(4*FCS) + 4*(47 - (x1 >> 2)) + 2*(x1 & 1);
            const u16 hv = f2bf(x1 >= 0 ? kv[i] : 0.f);
            #pragma unroll
            for (int c=0;c<4;c++) *(u16*)(p + c*(FCS-4)) = hv;
          }
        }
      } else {
        if (lane <= 32){
          const int x8 = 64*kg + m - 1;
          const int z = x8 < 0 ? 16 : 63 - (x8 >> 1);
          char* p = ft + (x8 < 0 ? 4*FCS : 8*FCS) + 4*z + 2*(x8 & 1);
          const u16 hv = f2bf(kv[0]);
          #pragma unroll
          for (int c=0;c<4;c++) *(u16*)(p + c*(FCS-4)) = hv;
          if (lane == 0) ((float*)(smem + K1OFF))[ch] = kv[0];
        }
        if (kg < 2){
          #pragma unroll
          for (int i=1;i<4;i++){
            const int x8 = 16*(4*kg + i) + m - 1;
            char* p = ft + 8*FCS + 4*(63 - (x8 >> 1)) + 2*(x8 & 1);
            const u16 hv = f2bf(kv[i]);
            #pragma unroll
            for (int c=0;c<4;c++) *(u16*)(p + c*(FCS-4)) = hv;
          }
        }
      }
      __builtin_amdgcn_s_setprio(0);
    } else {
      if (lane < 30) *(u32x4*)(nb + 16*(lane < 15 ? lane : lane + 65)) = u32x4{0,0,0,0};
      const int s5 = lane & 31, z = s5 < 16 ? s5 : s5 + 32;
      #pragma unroll
      for (int c=0;c<4;c++) if (z >= c) *(uint32_t*)(ft + (lane >> 5)*(4*FCS) + c*FCS + 4*(z - c)) = 0u;
      if (m - kg < 12) *(uint32_t*)(ft + 8*FCS + kg*FCS + 4*(64 + m - kg)) = 0u;
    }
    if (wave >= 8){ put_u(smem, tl, uu[0], uu[1]); put_u(smem, tl + 512, uu[2], uu[3]); }
    __syncthreads();
    TSP(3);
  } else {
    __builtin_amdgcn_sched_barrier(0);
    const u32x4 sx = *(const u32x4*)(ssmk + (c0+(t >> 8))*L_ + 8*(t & 255));
    const u32x4 wa = *(const u32x4*)(shift_k + c0*4), wb = *(const u32x4*)(shift_k + c0*4 + 8);
    const int tr = (t & 512) | ((t + 64*(w5 & 15)) & 511);
    const int r0 = 2*(tr - lane);
    const u16* hp = k + (r0 ? r0 - 3 : 0)*D_ + c0 + vz;
    __builtin_amdgcn_sched_barrier(0);
    if (wave >= 8){ __builtin_amdgcn_s_barrier(); __builtin_amdgcn_s_setprio(HPRIO); }
    __builtin_amdgcn_sched_barrier(0);
    u32x2 h0 = *(const u32x2*)(hp), h1 = *(const u32x2*)(hp + D_), h2 = *(const u32x2*)(hp + 2*D_);
    __builtin_amdgcn_sched_barrier(0);
    const u32x2 k0 = *(const u32x2*)(k + (2*tr)*D_ + c0);
    const u32x2 k1 = *(const u32x2*)(k + (2*tr+1)*D_ + c0);
    const u32x2 v0 = *(const u32x2*)(v + (2*tr)*D_ + c0);
    const u32x2 v1 = *(const u32x2*)(v + (2*tr+1)*D_ + c0);
    __builtin_amdgcn_sched_barrier(0);
    if (wave < 8){
      if (t < 384){
        const int cc = (t >> 4) & 7;
        *(u32x4*)(smem + (t < 128 ? cc*XCP + 5072 : t < 256 ? KBO + (cc >> 1)*KCH + (cc & 1)*KCP : YBO + 32768 + (cc >> 1)*8704 + (cc & 1)*256) + 16*(t & 15)) = u32x4{0,0,0,0};
      }
      asm volatile("s_waitcnt lgkmcnt(0)" ::: "memory"); __builtin_amdgcn_s_barrier();
    }
    __builtin_amdgcn_sched_barrier(0);
    {
      char* kb = smem + KBO + (t >> 8)*KCH + 128 + 16*(t & 255);
      *(u32x4*)(kb) = sx;
      uint32_t* k2 = (uint32_t*)(kb + KCP + 4);
      k2[0] = sx.x; k2[1] = sx.y; k2[2] = sx.z; k2[3] = sx.w;
    }
    const uint32_t cad = (uint32_t)(uintptr_t)smem + CNTOFF, on1 = 1u;
    if (wave >= 8 && lane == 0) asm volatile("ds_add_u32 %0, %1 offset:72" :: "v"(cad), "v"(on1) : "memory");
    if (r0 == 0){ h0 = u32x2{0u,0u}; h1 = u32x2{0u,0u}; h2 = u32x2{0u,0u}; }
    TSP(1);
    u32x2 kk[5];
    kk[1] = wshr1(h1, k0); kk[2] = wshr1(h2, k1); kk[0] = wshr1(h0, kk[2]);
    kk[3] = k0; kk[4] = k1;
    const int dl0 = 1023 - tr, dl1 = 1022 - tr;
    char* x0 = smem + 4*dl0 + 32*(dl0 >> 5);
    char* x1 = smem + 4*dl1 + 32*(dl1 >> 5) + XCP + 2;
    #pragma unroll
    for (int p=0;p<2;p++){
      #pragma unroll
      for (int e=0;e<2;e++){
        float r2[2];
        #pragma unroll
        for (int j=0;j<2;j++){
          const u32x2 vv = j ? v1 : v0;
          const u32x4 ww = p ? wb : wa;
          const uint32_t wl = e ? ww.z : ww.x, wh = e ? ww.w : ww.y;
          const float w0 = lo16f(wl), w1 = hi16f(wl), w2 = lo16f(wh), w3 = hi16f(wh);
          auto g = [&](int idx){ uint32_t x = kk[idx][p]; return e? hi16f(x) : lo16f(x); };
          float acc = rbf(g(j)*w3);
          acc = rbf(acc + rbf(g(j+1)*w2));
          acc = rbf(acc + rbf(g(j+2)*w1));
          acc = rbf(acc + rbf(g(j+3)*w0));
          r2[j] = acc * (e? hi16f(vv[p]) : lo16f(vv[p]));
        }
        const uint32_t d = pk2(r2[1], r2[0]);
        const int c = 2*p + e;
        *(uint32_t*)(x0 + c*XCH) = d;
        *(u16*)(x0 + c*XCH + XCP) = (u16)(d >> 16);
        *(u16*)(x1 + c*XCH) = (u16)d;
      }
    }
    if (lane == 0) asm volatile("ds_add_u32 %0, %1 offset:72" :: "v"(cad + (wave >= 8 ? 4 : 0)), "v"(on1) : "memory");
    TSP(2);
  }
  int sig = -1, fi = -1;
  if (isA){
    const int ch = wave >> 2, role = (wave + ch) & 3;
    if (role == 0){ __builtin_amdgcn_s_setprio(2); near_conv<4,0>(smem, ch, m, kg); }
    else if (role == 1) near_conv<4,4>(smem, ch, m, kg);
    else if (role == 2){ __builtin_amdgcn_s_setprio(3); far_conv<0,4>(smem, ch, m, kg); }
    else { __builtin_amdgcn_s_setprio(3); far_conv<4,6>(smem, ch, m, kg); }
    TSP(5);
    sig = role == 1;
    fi = role >= 2 ? 2*ch + role - 2 : -1;
  } else if (wave < 8){
    const int ch = wave & 3, w = wave >> 2;
    const uint32_t sb = (uint32_t)(uintptr_t)smem;
    const int f4 = (m >> 2) & 1;
    uint32_t xa = sb + ch*XCH + ((m & 1) ? 0 : XCP) + 160*(31 - (m & 2) - (m >> 3)) + 8*(1 - f4) + 16*kg - 640*3;
    uint32_t xb = xa + ((kg == 3 && f4 == 0) ? 32 : 0);
    const uint32_t kb0 = sb + KBO + ch*KCH + ((m & 1) ? 0 : KCP) + 16*((m >> 1) + kg + 1) + 256*w;
    char* yp = smem + YBO + (w ? 32768 + 512 + ch*8704 : ch*8192) + 8*lane;
    uint32_t cw = sb + CNTOFF + 4*w + 4*lane;
    const unsigned* cnt = (const unsigned*)(smem + CNTOFF);
    const uint32_t one = 1u;
    const f32x4 Z4 = {0,0,0,0};
    f32x4 W0 = Z4, W1 = Z4, W2 = Z4, W3 = Z4;
    bf16x8 K0, K1, K2, K3;
    u32x2 xl[4][4], xh[4][4];
#define SB_ __builtin_amdgcn_sched_barrier(0)
#define LK(F, off) asm volatile("ds_read_b128 %0, %1 offset:%2" : "=v"(F) : "v"(kb), "i"(off))
#define LX(a, c) asm volatile("ds_read_b64 %0, %2 offset:%4\n\tds_read_b64 %1, %3 offset:%5" : "=&v"(xl[a][c]), "=&v"(xh[a][c]) \
      : "v"(xa), "v"(((c) & 1) ? xb : xa), "i"(640*(3-(a)) + 160*((c) >> 1) + 64*((c) & 1)), "i"(640*(3-(a)) + 160*((c) >> 1) + 64*((c) & 1) + 8))
#define LX4(c) LX(0, c); LX(1, c); LX(2, c); LX(3, c)
#define WK(n, F) asm volatile("s_waitcnt lgkmcnt(" #n ")" : "+v"(F))
#define WX(n, F, c) asm volatile("s_waitcnt lgkmcnt(" #n ")" : "+v"(F), "+v"(xl[0][c]), "+v"(xh[0][c]), "+v"(xl[1][c]), "+v"(xh[1][c]), \
      "+v"(xl[2][c]), "+v"(xh[2][c]), "+v"(xl[3][c]), "+v"(xh[3][c]))
#define XF(a, c) __builtin_bit_cast(bf16x8, __builtin_shufflevector(xl[a][c], xh[a][c], 0, 1, 2, 3))
#define MG(n, c, F) { W0 = MFMA16(XF(0, c), F, W0); SB_; if (n > 1) W1 = MFMA16(XF(1, c), F, W1); if (n > 2) W2 = MFMA16(XF(2, c), F, W2); \
      if (n > 3) W3 = MFMA16(XF(3, c), F, W3); SB_; }
#define MS(n, F) { W0 = MFMA16(XF(0, 0), F, W1); SB_; if (n > 1){ W1 = MFMA16(XF(1, 0), F, W2); SB_; } if (n > 2){ W2 = MFMA16(XF(2, 0), F, W3); SB_; } \
      if (n > 3){ W3 = MFMA16(XF(3, 0), F, Z4); SB_; } }
#define EMT(qo) { *(f32x2*)(yp + 1024*(qo)) = f32x2{W0[0], W0[1]}; *(f32x2*)(yp + 1024*(qo) + 512) = f32x2{W0[2], W0[3]}; \
      asm volatile("s_mov_b64 exec, 3\n\tds_add_u32 %0, %1 offset:%2\n\ts_mov_b64 exec, -1" :: "v"(cw), "v"(one), "i"(8*(qo)) : "memory"); SB_; }
    #pragma unroll 1
    for (int pass = 0; pass < 2; pass++){
      uint32_t kb = kb0;
      while (__hip_atomic_load(cnt + 18 + pass, __ATOMIC_RELAXED, __HIP_MEMORY_SCOPE_WORKGROUP) < (pass ? 8u : 16u)) __builtin_amdgcn_s_sleep(1);
      __builtin_amdgcn_fence(__ATOMIC_ACQUIRE, "workgroup");
      if (pass == 0){ TSP(3); }
      SB_;
      LK(K0, 0); LX4(0); LK(K1, 64); LX4(1);
      WX(9, K0, 0); MG(4, 0, K0)
      LK(K2, 128); LX4(2);
      WX(9, K1, 1); MG(4, 1, K1)
      LK(K3, 192); LX4(3); LK(K0, 512); LK(K1, 576);
      WX(11, K2, 2); MG(4, 2, K2)
      LK(K2, 640);
      WX(3, K3, 3); MG(4, 3, K3)
      LK(K3, 704);
      EMT(0)
      const int nl = pass ? 0 : 4;
      #pragma unroll 1
      for (int it = 0; it < nl; it++){
        kb += 512; yp += 1024; cw += 8;
        WK(5, K0); MS(4, K0) LK(K0, 512);
        WK(5, K1); MG(4, 1, K1) LK(K1, 576);
        WK(5, K2); MG(4, 2, K2) LK(K2, 640);
        WK(5, K3); MG(4, 3, K3) LK(K3, 704);
        EMT(0)
      }
      WK(5, K0); MS(3, K0) LK(K0, 1024);
      WK(5, K1); MG(3, 1, K1) LK(K1, 1088);
      WK(5, K2); MG(3, 2, K2) LK(K2, 1152);
      WK(5, K3); MG(3, 3, K3) LK(K3, 1216);
      EMT(1)
      WK(5, K0); MS(2, K0) LK(K0, 1536);
      WK(5, K1); MG(2, 1, K1) LK(K1, 1600);
      WK(5, K2); MG(2, 2, K2) LK(K2, 1664);
      WK(5, K3); MG(2, 3, K3) LK(K3, 1728);
      EMT(2)
      WK(5, K0); MS(1, K0)
      WK(4, K1); MG(1, 1, K1)
      WK(3, K2); MG(1, 2, K2)
      WK(2, K3); MG(1, 3, K3)
      EMT(3)
      if (pass == 0){
        TSP(4);
        W3 = W0;
        { f32x2 a = *(const f32x2*)(yp), b = *(const f32x2*)(yp + 512); W0 = f32x4{a.x, a.y, b.x, b.y}; }
        { f32x2 a = *(const f32x2*)(yp + 1024), b = *(const f32x2*)(yp + 1536); W1 = f32x4{a.x, a.y, b.x, b.y}; }
        { f32x2 a = *(const f32x2*)(yp + 2048), b = *(const f32x2*)(yp + 2560); W2 = f32x4{a.x, a.y, b.x, b.y}; }
        xa -= 2560; xb -= 2560;
      }
    }
    TSP(5);
  } else {
    __builtin_amdgcn_s_setprio(2);
    const int j0 = (wave + w5) & 7;
    const int lr = 2*(m & 1) + 4*(kg & 1) + 8*(m >> 1) + 64*(kg >> 1);
    u32x2 qr[4];
    {
      const u16* qp = q + (128*j0 + lr)*D_ + c0;
      qr[0] = *(const u32x2*)(qp); qr[1] = *(const u32x2*)(qp + D_);
      qr[2] = *(const u32x2*)(qp + 1024*D_); qr[3] = *(const u32x2*)(qp + 1025*D_);
    }
    __builtin_amdgcn_sched_barrier(0);
    #pragma unroll
    for (int st=0; st<2; st++){
      const int cj = j0 + 8*st;
      const char* yb = smem + YBO + 512*cj + 8*lane;
      const int dl = 1023 - 64*cj - (lr >> 1);
      const char* up = smem + 4*dl + 32*(dl >> 5);
      const unsigned tgt = st ? (j0 ? 16u : 12u) : (j0 ? 8u : 4u);
      while (__hip_atomic_load((const unsigned*)(smem + CNTOFF) + cj, __ATOMIC_RELAXED, __HIP_MEMORY_SCOPE_WORKGROUP) < tgt) __builtin_amdgcn_s_sleep(1);
      __builtin_amdgcn_fence(__ATOMIC_ACQUIRE, "workgroup");
      float s0[4], s1[4];
      #pragma unroll
      for (int c=0;c<4;c++){
        uint32_t uu = *(const uint32_t*)(up + c*XCH);
        float sd = (c & 1) ? hi16f(dsk[c>>1]) : lo16f(dsk[c>>1]);
        s0[c] = rbf(hi16f(uu)*sd); s1[c] = rbf(lo16f(uu)*sd);
      }
      float o0[4], o1[4];
      #pragma unroll
      for (int c=0;c<4;c++){
        f32x2 a = *(const f32x2*)(yb + c*8192);
        const f32x2 b = *(const f32x2*)(yb + 32768 + c*8704); a.x = aadd(a.x, b.x); a.y = aadd(a.y, b.y);
        float q0 = (c & 1) ? hi16f(qr[2*st][c>>1]) : lo16f(qr[2*st][c>>1]);
        float q1 = (c & 1) ? hi16f(qr[2*st+1][c>>1]) : lo16f(qr[2*st+1][c>>1]);
        o0[c] = q0 * rbf(rbf(a.x) + s0[c]);
        o1[c] = q1 * rbf(rbf(a.y) + s1[c]);
      }
      u16* op = out + D_ + c0 + (128*cj + lr)*(2*D_);
      *(u32x2*)(op)        = u32x2{pk2(o0[0],o0[1]), pk2(o0[2],o0[3])};
      *(u32x2*)(op + 2*D_) = u32x2{pk2(o1[0],o1[1]), pk2(o1[2],o1[3])};
    }
  }
  TSP(6);
  if (sig >= 0){
    __builtin_amdgcn_fence(__ATOMIC_RELEASE, "workgroup");
    if (lane == 0) __hip_atomic_fetch_add((unsigned*)(smem + CNTOFF) + sig, 1u, __ATOMIC_RELAXED, __HIP_MEMORY_SCOPE_WORKGROUP);
  }
  if (fi >= 0){
    const int p = (fi*64 + lane + 32*(w5 & 15)) & 511;
    u16* op = out + c0;
    #pragma unroll
    for (int st=0; st<2; st++){
      const int tq = 4*(p >> 1) + (p & 1) + 2*st;
      const char* yb = smem + YAOFF + 8*tq + 16*(tq >> 6);
      const char* up = smem + 272*(tq >> 6) + 16 + 4*(tq & 63);
      float s0[4], s1[4];
      #pragma unroll
      for (int c=0;c<4;c++){
        uint32_t uu = *(const uint32_t*)(up + c*UST);
        float sd = (c & 1) ? hi16f(dsk[c>>1]) : lo16f(dsk[c>>1]);
        s0[c] = rbf(lo16f(uu)*sd); s1[c] = rbf(hi16f(uu)*sd);
      }
      while (__hip_atomic_load((const unsigned*)(smem + CNTOFF) + st, __ATOMIC_RELAXED, __HIP_MEMORY_SCOPE_WORKGROUP) < (st ? 4u : 12u)) __builtin_amdgcn_s_sleep(1);
      __builtin_amdgcn_fence(__ATOMIC_ACQUIRE, "workgroup");
      float o0[4], o1[4];
      const char* yf = smem + YFOFF + (tq >= 257 ? 4*(2*tq - 515) : -16);
      #pragma unroll
      for (int c=0;c<4;c++){
        f32x2 a = *(const f32x2*)(yb + c*YST);
        float b0 = *(const float*)(yf + c*YFST), b1 = *(const float*)(yf + c*YFST + 4);
        o0[c] = rbf(a.x + b0) + s0[c];
        o1[c] = rbf(a.y + b1) + s1[c];
      }
      *(u32x2*)(op + (2*tq)*(2*D_))   = u32x2{pk2(o0[0],o0[1]), pk2(o0[2],o0[3])};
      *(u32x2*)(op + (2*tq+1)*(2*D_)) = u32x2{pk2(o1[0],o1[1]), pk2(o1[2],o1[3])};
    }
  }
}

extern "C" void kernel_launch(void* const* d_in, const int* in_sizes, int n_in,
                              void* d_out, int out_size, void* d_ws, size_t ws_size,
                              hipStream_t stream) {
  mono_kernel<<<256, 1024, LDS_TOTAL, stream>>>((const u16*)d_in[0], (const u16*)d_in[1], (const u16*)d_in[2], (const u16*)d_in[3],
     (const u16*)d_in[4], (const u16*)d_in[5], (const u16*)d_in[6], (const u16*)d_in[7], (const u16*)d_in[8], (const u16*)d_in[9],
     (const u16*)d_in[10], (const u16*)d_in[11], (const u16*)d_in[12], (u16*)d_out
);
}
```

```cpp
#define PRF0 2
#define CROT 1
#define PR0 2
#define PFA 2
#define PFB 0
#define HPF 1
#define PFD 0
#define QPF 2
#define KPF 4
#define SWP 8
#include <hip/hip_runtime.h>
#include <hip/hip_bf16.h>
#include <stdint.h>

typedef unsigned short u16;
typedef short bf16x8 __attribute__((ext_vector_type(8)));
typedef float f32x4 __attribute__((ext_vector_type(4)));
typedef float f32x2 __attribute__((ext_vector_type(2)));
typedef __bf16 bf16x2_t __attribute__((ext_vector_type(2)));

#define ULATE 2
#define HPRIO 2
#define PRN 0
#define PRF1 1
#define L_ 2048
#define D_ 512
#define NS_ 128

__device__ __forceinline__ float bf2f(u16 x){ return __builtin_bit_cast(float, (uint32_t)x << 16); }
__device__ __forceinline__ u16 f2bf(float f){ __hip_bfloat16 h = __float2bfloat16(f); return __builtin_bit_cast(u16, h); }
__device__ __forceinline__ float rbf(float f){ return bf2f(f2bf(f)); }
__device__ __forceinline__ uint32_t pk2(float a, float b){
  f32x2 v = {a,b}; bf16x2_t r = __builtin_convertvector(v, bf16x2_t); return __builtin_bit_cast(uint32_t, r); }
__device__ __forceinline__ float lo16f(uint32_t w){ return __builtin_bit_cast(float, w << 16); }
__device__ __forceinline__ float hi16f(uint32_t w){ return __builtin_bit_cast(float, w & 0xffff0000u); }

struct cpx { float re, im; };
__device__ __forceinline__ float amul(float a, float b){ float r; asm("v_mul_f32 %0, %1, %2" : "=v"(r) : "v"(a), "v"(b)); return r; }
__device__ __forceinline__ float aadd(float a, float b){ float r; asm("v_add_f32 %0, %1, %2" : "=v"(r) : "v"(a), "v"(b)); return r; }
__device__ __forceinline__ float afma(float a, float b, float c){ float r; asm("v_fma_f32 %0, %1, %2, %3" : "=v"(r) : "v"(a), "v"(b), "v"(c)); return r; }
__device__ __forceinline__ float afms(float a, float b, float c){ float r; asm("v_fma_f32 %0, %1, %2, -%3" : "=v"(r) : "v"(a), "v"(b), "v"(c)); return r; }
__device__ __forceinline__ cpx cmul(cpx a, cpx b){
  f32x2 A = {a.re, a.im}, B = {b.re, b.im}, T, R;
  asm("v_pk_mul_f32 %0, %1, %2 op_sel:[1,1] op_sel_hi:[1,0] neg_lo:[1,0]" : "=v"(T) : "v"(A), "v"(B));
  asm("v_pk_fma_f32 %0, %1, %2, %3 op_sel:[0,0,0] op_sel_hi:[0,1,1]" : "=v"(R) : "v"(A), "v"(B), "v"(T));
  cpx r; r.re = R.x; r.im = R.y; return r; }
__device__ __forceinline__ cpx cexp_(float re, float im){ float e = expf(re); float s,c; sincosf(im,&s,&c); cpx r; r.re=e*c; r.im=e*s; return r; }

#define MFMA16(a,b,c) __builtin_amdgcn_mfma_f32_16x16x32_bf16(a,b,c,0,0,0)
typedef uint32_t u32x2 __attribute__((ext_vector_type(2)));
typedef uint32_t u32x4 __attribute__((ext_vector_type(4)));
#define TR 544
#define TSZ (57*TR)
#define UST 4352
#define TOFF 17408
#define KST 4352
#define KS2 1536
#define XOFF (TOFF + 4*TSZ)
#define NOFF XOFF
#define FCS 320
#define FTS (12*FCS)
#define FTOFF (XOFF + 4*KS2)
#define K1OFF (FTOFF + 4*FTS)
#define YFOFF (TOFF + 45056 + 16)
#define YFST 6176
#define YAOFF (TOFF + 69888)
#define YOFF (TOFF + 36864)
#define YST 8448
#define YBOFF (YOFF + 4*YST)
#define YB2 4352
#define XCP 5504
#define XCH 11008
#define KBO 44032
#define KCP 4224
#define KCH 8464
#define YBO 77888
#define CNTOFF (K1OFF + 16)
#define PFOFF (CNTOFF + 112)
#define LDS_TOTAL (PFOFF + 256)
typedef uint32_t u32x6 __attribute__((ext_vector_type(6)));
typedef _Float16 f16x8 __attribute__((ext_vector_type(8)));
typedef _Float16 f16x2 __attribute__((ext_vector_type(2)));
#define MFMAH(a,b,c) __builtin_amdgcn_mfma_f32_16x16x32_f16(a,b,c,0,0,0)
__device__ __forceinline__ uint32_t pkh(float a, float b){
  f32x2 v = {a,b}; f16x2 r = __builtin_convertvector(v, f16x2); return __builtin_bit_cast(uint32_t, r); }
struct cp2 { cpx a, b; };
__device__ __forceinline__ cp2 cmul2(const cp2& x, const cp2& y){ cp2 r; r.a = cmul(x.a, y.a); r.b = cmul(x.b, y.b); return r; }
__device__ __forceinline__ void putg(char* p, const cp2& x){ *(u32x2*)p = u32x2{pkh(x.a.re, x.a.im), pkh(x.b.re, x.b.im)}; }
__device__ __forceinline__ void putx(char* p, const cp2& x){ *(u32x2*)p = u32x2{pkh(x.a.re, -x.a.im), pkh(x.b.re, -x.b.im)}; }

__device__ __forceinline__ uint32_t resid2(uint32_t h, float a, float b){
  return pk2(a - lo16f(h), b - hi16f(h));
}
__device__ __forceinline__ int f2i(float x){ return __builtin_bit_cast(int, x); }
__device__ __forceinline__ float i2f(int x){ return __builtin_bit_cast(float, x); }
__device__ __forceinline__ f32x4 shrN1(f32x4 x){
  f32x4 r;
  #pragma unroll
  for (int i=0;i<4;i++) r[i] = i2f(__builtin_amdgcn_update_dpp(0, f2i(x[i]), 0x111, 0xf, 0xf, true));
  return r;
}
__device__ __forceinline__ f32x4 shrN2(f32x4 x){
  f32x4 r;
  #pragma unroll
  for (int i=0;i<4;i++) r[i] = i2f(__builtin_amdgcn_update_dpp(0, f2i(x[i]), 0x112, 0xf, 0xf, true));
  return r;
}
__device__ __forceinline__ f32x4 shlN1(f32x4 x){
  f32x4 r;
  #pragma unroll
  for (int i=0;i<4;i++) r[i] = i2f(__builtin_amdgcn_update_dpp(0, f2i(x[i]), 0x101, 0xf, 0xf, true));
  return r;
}
__device__ __forceinline__ f32x4 shlN2(f32x4 x){
  f32x4 r;
  #pragma unroll
  for (int i=0;i<4;i++) r[i] = i2f(__builtin_amdgcn_update_dpp(0, f2i(x[i]), 0x102, 0xf, 0xf, true));
  return r;
}
__device__ __forceinline__ f32x4 shlN8(f32x4 x){
  f32x4 r;
  #pragma unroll
  for (int i=0;i<4;i++) r[i] = i2f(__builtin_amdgcn_update_dpp(0, f2i(x[i]), 0x108, 0xf, 0xf, true));
  return r;
}
__device__ __forceinline__ u32x2 wshr1(u32x2 old, u32x2 x){
  u32x2 r;
  r.x = (uint32_t)__builtin_amdgcn_update_dpp((int)old.x, (int)x.x, 0x138, 0xf, 0xf, false);
  r.y = (uint32_t)__builtin_amdgcn_update_dpp((int)old.y, (int)x.y, 0x138, 0xf, 0xf, false);
  return r;
}
struct W6 { uint32_t w[6]; };
__device__ __forceinline__ W6 uwin(const char* p){
  u32x2 a = *(const u32x2*)p, b = *(const u32x2*)(p+8), c = *(const u32x2*)(p+16);
  W6 r; r.w[0]=a.x; r.w[1]=a.y; r.w[2]=b.x; r.w[3]=b.y; r.w[4]=c.x; r.w[5]=c.y; return r;
}
__device__ __forceinline__ bf16x8 usel(const W6& W, int x, uint32_t sel){
  u32x4 r;
  r.x = __builtin_amdgcn_perm(W.w[x+1], W.w[x],   sel); r.y = __builtin_amdgcn_perm(W.w[x+2], W.w[x+1], sel);
  r.z = __builtin_amdgcn_perm(W.w[x+3], W.w[x+2], sel); r.w = __builtin_amdgcn_perm(W.w[x+4], W.w[x+3], sel);
  return __builtin_bit_cast(bf16x8, r);
}
__device__ __forceinline__ bf16x8 uodd(const W6& W, int x){
  u32x4 r;
  r.x = __builtin_amdgcn_alignbit(W.w[x+1], W.w[x],   16); r.y = __builtin_amdgcn_alignbit(W.w[x+2], W.w[x+1], 16);
  r.z = __builtin_amdgcn_alignbit(W.w[x+3], W.w[x+2], 16); r.w = __builtin_amdgcn_alignbit(W.w[x+4], W.w[x+3], 16);
  return __builtin_bit_cast(bf16x8, r);
}
__device__ __forceinline__ bf16x8 uevn(const W6& W, int x){
  u32x4 r = {W.w[x], W.w[x+1], W.w[x+2], W.w[x+3]};
  return __builtin_bit_cast(bf16x8, r);
}
__device__ __forceinline__ void put_u(char* smem, int tr, u32x2 r0, u32x2 r1){
  uint32_t d[4];
  d[0] = __builtin_amdgcn_perm(r1.x, r0.x, 0x05040100); d[1] = __builtin_amdgcn_perm(r1.x, r0.x, 0x07060302);
  d[2] = __builtin_amdgcn_perm(r1.y, r0.y, 0x05040100); d[3] = __builtin_amdgcn_perm(r1.y, r0.y, 0x07060302);
  const int a = tr >> 6, pos = tr & 63;
  char* p = smem + 272*a + 16 + 4*pos;
  #pragma unroll
  for (int c=0;c<4;c++) *(uint32_t*)(p + c*UST) = d[c];
  if (pos >= 60 && a < 15){
    #pragma unroll
    for (int c=0;c<4;c++) *(uint32_t*)(p + c*UST + 16) = d[c];
  }
  if (tr < 16) *(uint32_t*)(smem + (tr>>2)*UST + 4*(tr&3)) = 0u;
}

#define LDK(F, dl, c) asm volatile("ds_read_b128 %0, %1 offset:%2" : "=v"(F) : "v"(kbase), "i"(256*(dl) + 64*(3-(c))))
#define LDK4(S, dl) LDK(S[0], dl, 0); LDK(S[1], dl, 1); LDK(S[2], dl, 2); LDK(S[3], dl, 3)
#define WAITK(n, S) asm volatile("s_waitcnt lgkmcnt(" #n ")" : "+v"(S[0]), "+v"(S[1]), "+v"(S[2]), "+v"(S[3]))

template<int NT, int R0>
__device__ __forceinline__ void near_conv(char* smem, int ch, int m, int kg){
  const uint32_t kbase = (uint32_t)(uintptr_t)smem + NOFF + ch*KS2 + 16*(m - kg + 3);
#define LDN(F, hs, c) asm volatile("ds_read_b128 %0, %1 offset:%2" : "=v"(F) : "v"(kbase), "i"(256*(4-(hs)) + 64*(3-(c))))
#define LDN4(S, hs) LDN(S[0], hs, 0); LDN(S[1], hs, 1); LDN(S[2], hs, 2); LDN(S[3], hs, 3)
  bf16x8 KA[4], KB[4];
  LDN4(KA, 0);
  bf16x8 U[NT][4];
  {
    const char* pp = smem + ch*UST + 272*m + 16*kg + (R0 ? 8 : 0);
    #pragma unroll
    for (int c=0;c<4;c++){
      W6 W = uwin(pp + 64*c);
      #pragma unroll
      for (int r=0;r<NT;r++){ const int rel = R0 + r + 1 - (R0 ? 4 : 0); U[r][c] = (rel & 1) ? uodd(W, rel >> 1) : uevn(W, rel >> 1); }
    }
  }
  f32x4 acc[NT];
  #pragma unroll
  for (int r=0;r<NT;r++) acc[r] = f32x4{0,0,0,0};
#define NH(hs, CUR, NXT) \
  if ((hs) < 4){ LDN4(NXT, (hs)+1); WAITK(4, CUR); } else { WAITK(0, CUR); } \
  if ((hs) > 0){ _Pragma("unroll") for (int r=0;r<NT;r++) acc[r] = shrN1(acc[r]); } \
  _Pragma("unroll") for (int c=0;c<4;c++){ _Pragma("unroll") for (int r=0;r<NT;r++) acc[r] = MFMA16(CUR[c], U[r][c], acc[r]); }
  NH(0, KA, KB) NH(1, KB, KA) NH(2, KA, KB) NH(3, KB, KA) NH(4, KA, KB)
  float* yb = (float*)(smem + YAOFF + ch*YST) + (R0 + 32*kg + 132*m);
  #pragma unroll
  for (int i=0;i<4;i++){
    #pragma unroll
    for (int r=0;r<NT;r++) yb[8*i + r] = acc[r][i];
  }
}

#define SB_ __builtin_amdgcn_sched_barrier(0)
#define TSA
#define TSB
#define TS4
__device__ __forceinline__ void near_pre(char* smem, int ch, int lane, uint32_t* P){
  const int m = lane & 15, kg = lane >> 4;
  const uint32_t sb = (uint32_t)(uintptr_t)smem;
  #pragma unroll
  for (int c=0;c<4;c++){
    const int dl = m - 15 + 4*c + kg;
    P[c] = sb + ch*UST + 272 + 16*dl + (dl >= 0 ? 16 : 0);
    P[4+c] = sb + ch*UST + (dl >= 0 ? 16 + 16*dl : 0);
  }
  P[8] = sb + NOFF + ch*KS2 + 4*(211 - (m >> 2) - 64*((m >> 1) & 1) + 4*kg);
  P[14] = P[8] - 832;
  P[9] = (m & 1) ? 0u : 16u;
  P[10] = YAOFF + ch*512 + 8*lane;
  P[11] = (m & 2) ? 0u : ~0u;
  P[12] = sb + CNTOFF; P[13] = 1u;
  #pragma unroll
  for (int c=15;c<36;c++) asm volatile("" : "=v"(P[c]));
}
__device__ __forceinline__ void near2(char* smem, const uint32_t* P TSA){
  bf16x8 K0[4], K1[4], K2;
  bf16x8 XA[4], XB[4];
#define NRD "ds_read_b128 %0, %4 offset:%8\n\tds_read_b128 %1, %5 offset:%8\n\tds_read_b128 %2, %6 offset:%8\n\tds_read_b128 %3, %7 offset:%8"
#define NX(X, u) asm volatile(NRD : "=&v"(X[0]), "=&v"(X[1]), "=&v"(X[2]), "=&v"(X[3]) : "v"(P[0]), "v"(P[1]), "v"(P[2]), "v"(P[3]), "i"(272*((u)-1))); SB_;
#define NX0(X) asm volatile(NRD : "=&v"(X[0]), "=&v"(X[1]), "=&v"(X[2]), "=&v"(X[3]) : "v"(P[4]), "v"(P[5]), "v"(P[6]), "v"(P[7]), "i"(0)); SB_;
#define NR(X, c, u) asm volatile("ds_read_b128 %0, %1 offset:%2" : "=v"(X[c]) : "v"(P[c]), "i"(272*((u)-1))); SB_;
#define NW3(n, X) asm volatile("s_waitcnt lgkmcnt(" #n ")" : "+v"(X[0]), "+v"(X[1]), "+v"(X[2])); SB_;
#define NW1(n, X) asm volatile("s_waitcnt lgkmcnt(" #n ")" : "+v"(X[3])); SB_;
#define NM(T, K, c, X, S) T = MFMA16(K[c], X[c], S); SB_;
#define NSIG(sl) asm volatile("s_mov_b64 exec, 1\n\tds_add_u32 %0, %1 offset:%2\n\ts_mov_b64 exec, -1" :: "v"(P[12]), "v"(P[13]), "i"(32 + 4*(sl)) : "memory"); SB_;
#define NA(Tc, Tp) ys = f32x2{Tc[0], Tc[1]} + f32x2{Tp[2], Tp[3]}; asm volatile("" : "+v"(ys)); SB_;
#define NA0(Tc) ys = f32x2{Tc[0], Tc[1]}; SB_;
#define NS(sl) *(f32x2*)(yp + 2048*(sl)) = ys; SB_;
#define KR(R, pb, o) asm volatile("ds_read2_b32 %0, %3 offset0:%4 offset1:%5\n\tds_read2_b32 %1, %3 offset0:%6 offset1:%7\n\tds_read_b32 %2, %3 offset:%8" \
    : "=&v"(R.a), "=&v"(R.b), "=&v"(R.c) : "v"(P[pb]), "i"(o), "i"((o)+1), "i"((o)+2), "i"((o)+3), "i"(4*(o)+16)); SB_;
#define KT_(R) "+v"(R.a), "+v"(R.b), "+v"(R.c)
#define KA(R) u32x4{__builtin_amdgcn_alignbit(R.a.y, R.a.x, sh), __builtin_amdgcn_alignbit(R.b.x, R.a.y, sh), __builtin_amdgcn_alignbit(R.b.y, R.b.x, sh), __builtin_amdgcn_alignbit(R.c, R.b.y, sh)}
#define KF_(K, R) { u32x4 r_ = KA(R); asm volatile("" : "+v"(r_)); K = __builtin_bit_cast(bf16x8, r_); } SB_;
  struct KRaw { u32x2 a, b; uint32_t c; };
  KRaw A0, A1, A2, A3, B0, B1, B2, B3, C0;
  const uint32_t sh = P[9];
  char* yp = smem + P[10];
  const f32x4 Z4 = {0,0,0,0};
  f32x4 T0, T1, T2, T3, T4, T5;
  f32x2 ys;
  KR(A0, 8, 0) KR(A1, 8, 16) KR(A2, 8, 32) KR(A3, 8, 48)
  NX0(XA) NX(XB, 1)
  asm volatile("s_waitcnt lgkmcnt(8)" : KT_(A0), KT_(A1), KT_(A2), KT_(A3)); SB_;
  KF_(K0[0], A0) KR(B0, 14, 80) KF_(K0[1], A1) KR(B1, 14, 96) KF_(K0[2], A2) KR(B2, 14, 112) KF_(K0[3], A3) KR(B3, 14, 128) KR(C0, 14, 0)
  asm volatile("s_waitcnt lgkmcnt(15)" : "+v"(XA[0]), "+v"(XA[1]), "+v"(XA[2]), "+v"(XA[3]), "+v"(XB[0]), "+v"(XB[1]), "+v"(XB[2]), "+v"(XB[3])); SB_;
  NM(T0, K0, 0, XA, Z4) NM(T1, K0, 0, XB, Z4)
  NM(T0, K0, 1, XA, T0) NM(T1, K0, 1, XB, T1)
  NM(T0, K0, 2, XA, T0) NM(T1, K0, 2, XB, T1)
  NM(T0, K0, 3, XA, T0) NM(T1, K0, 3, XB, T1)
  asm volatile("s_waitcnt lgkmcnt(0)" : KT_(B0), KT_(B1), KT_(B2), KT_(B3), KT_(C0)); SB_;
  KF_(K1[0], B0)
  NA0(T0) NS(0) NSIG(0)
  NM(T2, K1, 0, XA, Z4)
  NA(T1, T0) NS(1) NSIG(1)

  NM(T3, K1, 0, XB, Z4)
  NR(XA, 0, 2) NR(XB, 0, 3) KF_(K1[1], B1)
  NM(T2, K1, 1, XA, T2) NM(T3, K1, 1, XB, T3)
  NR(XA, 1, 2) NR(XB, 1, 3) KF_(K1[2], B2)
  NM(T2, K1, 2, XA, T2) NM(T3, K1, 2, XB, T3)
  NR(XA, 2, 2) NR(XB, 2, 3) KF_(K1[3], B3)
  NM(T2, K1, 3, XA, T2) NM(T3, K1, 3, XB, T3)
  { u32x4 r_ = KA(C0); r_ = r_ & P[11]; asm volatile("" : "+v"(r_)); K2 = __builtin_bit_cast(bf16x8, r_); } SB_;
  T4 = MFMA16(K2, XA[3], Z4); SB_; T5 = MFMA16(K2, XB[3], Z4); SB_;
  NR(XA, 3, 2) NR(XB, 3, 3)
  NW3(3, XA)
  NM(T2, K0, 0, XA, T2) NM(T4, K1, 0, XA, T4)
  NR(XA, 0, 4)
  NM(T2, K0, 1, XA, T2)
  NM(T4, K1, 1, XA, T4)
  NR(XA, 1, 4)
  NM(T2, K0, 2, XA, T2)
  NM(T4, K1, 2, XA, T4)
  NR(XA, 2, 4)
  NW1(4, XA)
  NM(T2, K0, 3, XA, T2) NM(T4, K1, 3, XA, T4)
  T0 = MFMA16(K2, XA[3], Z4); SB_;
  NR(XA, 3, 4)
  NW3(6, XB)
  NM(T3, K0, 0, XB, T3) NM(T5, K1, 0, XB, T5)
  NR(XB, 0, 5)
  NA(T2, T1)
  NM(T3, K0, 1, XB, T3)
  NS(2)
  NM(T5, K1, 1, XB, T5)
  NR(XB, 1, 5)
  NM(T3, K0, 2, XB, T3)
  NSIG(2)
  NM(T5, K1, 2, XB, T5)
  NR(XB, 2, 5)
  NW1(9, XB)
  NM(T3, K0, 3, XB, T3) NM(T5, K1, 3, XB, T5)
  T1 = MFMA16(K2, XB[3], Z4); SB_;
  NR(XB, 3, 5)
  NW3(7, XA)
  NM(T4, K0, 0, XA, T4) NM(T0, K1, 0, XA, T0)
  NR(XA, 0, 6)
  NA(T3, T2)
  NM(T4, K0, 1, XA, T4)
  NS(3)
  NM(T0, K1, 1, XA, T0)
  NR(XA, 1, 6)
  NM(T4, K0, 2, XA, T4)
  NSIG(3)
  NM(T0, K1, 2, XA, T0)
  NR(XA, 2, 6)
  NW1(11, XA)
  NM(T4, K0, 3, XA, T4) NM(T0, K1, 3, XA, T0)
  T2 = MFMA16(K2, XA[3], Z4); SB_;
  NR(XA, 3, 6)
  NW3(7, XB)
  NM(T5, K0, 0, XB, T5) NM(T1, K1, 0, XB, T1)
  NR(XB, 0, 7)
  NA(T4, T3)
  NM(T5, K0, 1, XB, T5)
  NS(4)
  NM(T1, K1, 1, XB, T1)
  NR(XB, 1, 7)
  NM(T5, K0, 2, XB, T5)
  NSIG(4)
  NM(T1, K1, 2, XB, T1)
  NR(XB, 2, 7)
  NW1(11, XB)
  NM(T5, K0, 3, XB, T5) NM(T1, K1, 3, XB, T1)
  T3 = MFMA16(K2, XB[3], Z4); SB_;
  NR(XB, 3, 7)
  NW3(7, XA)
  NM(T0, K0, 0, XA, T0) NM(T2, K1, 0, XA, T2)
  NR(XA, 0, 8)
  NA(T5, T4)
  NM(T0, K0, 1, XA, T0)
  NS(5)
  NM(T2, K1, 1, XA, T2)
  NR(XA, 1, 8)
  NM(T0, K0, 2, XA, T0)
  NSIG(5)
  NM(T2, K1, 2, XA, T2)
  NR(XA, 2, 8)
  NW1(11, XA)
  NM(T0, K0, 3, XA, T0) NM(T2, K1, 3, XA, T2)
  T4 = MFMA16(K2, XA[3], Z4); SB_;
  NR(XA, 3, 8)
  NW3(7, XB)
  NM(T1, K0, 0, XB, T1) NM(T3, K1, 0, XB, T3)
  NR(XB, 0, 9)
  NA(T0, T5)
  NM(T1, K0, 1, XB, T1)
  NS(6)
  NM(T3, K1, 1, XB, T3)
  NR(XB, 1, 9)
  NM(T1, K0, 2, XB, T1)
  NSIG(6)
  NM(T3, K1, 2, XB, T3)
  NR(XB, 2, 9)
  NW1(11, XB)
  NM(T1, K0, 3, XB, T1) NM(T3, K1, 3, XB, T3)
  T5 = MFMA16(K2, XB[3], Z4); SB_;
  NR(XB, 3, 9)
  NW3(7, XA)
  NM(T2, K0, 0, XA, T2) NM(T4, K1, 0, XA, T4)
  NR(XA, 0, 10)
  NA(T1, T0)
  NM(T2, K0, 1, XA, T2)
  NS(7)
  NM(T4, K1, 1, XA, T4)
  NR(XA, 1, 10)
  NM(T2, K0, 2, XA, T2)
  NSIG(7)
  TS4 SB_;
  __builtin_amdgcn_s_setprio(PRN); SB_;
  NM(T4, K1, 2, XA, T4)
  NR(XA, 2, 10)
  NW1(11, XA)
  NM(T2, K0, 3, XA, T2) NM(T4, K1, 3, XA, T4)
  T0 = MFMA16(K2, XA[3], Z4); SB_;
  NR(XA, 3, 10)
  NW3(7, XB)
  NM(T3, K0, 0, XB, T3) NM(T5, K1, 0, XB, T5)
  NR(XB, 0, 11)
  NA(T2, T1)
  NM(T3, K0, 1, XB, T3)
  NS(8)
  NM(T5, K1, 1, XB, T5)
  NR(XB, 1, 11)
  NM(T3, K0, 2, XB, T3)
  NSIG(8)
  NM(T5, K1, 2, XB, T5)
  NR(XB, 2, 11)
  NW1(11, XB)
  NM(T3, K0, 3, XB, T3) NM(T5, K1, 3, XB, T5)
  T1 = MFMA16(K2, XB[3], Z4); SB_;
  NR(XB, 3, 11)
  NW3(7, XA)
  NM(T4, K0, 0, XA, T4) NM(T0, K1, 0, XA, T0)
  NR(XA, 0, 12)
  NA(T3, T2)
  NM(T4, K0, 1, XA, T4)
  NS(9)
  NM(T0, K1, 1, XA, T0)
  NR(XA, 1, 12)
  NM(T4, K0, 2, XA, T4)
  NSIG(9)
  NM(T0, K1, 2, XA, T0)
  NR(XA, 2, 12)
  NW1(11, XA)
  NM(T4, K0, 3, XA, T4) NM(T0, K1, 3, XA, T0)
  T2 = MFMA16(K2, XA[3], Z4); SB_;
  NR(XA, 3, 12)
  NW3(7, XB)
  NM(T5, K0, 0, XB, T5) NM(T1, K1, 0, XB, T1)
  NR(XB, 0, 13)
  NA(T4, T3)
  NM(T5, K0, 1, XB, T5)
  NS(10)
  NM(T1, K1, 1, XB, T1)
  NR(XB, 1, 13)
  NM(T5, K0, 2, XB, T5)
  NSIG(10)
  NM(T1, K1, 2, XB, T1)
  NR(XB, 2, 13)
  NW1(11, XB)
  NM(T5, K0, 3, XB, T5) NM(T1, K1, 3, XB, T1)
  T3 = MFMA16(K2, XB[3], Z4); SB_;
  NR(XB, 3, 13)
  NW3(7, XA)
  NM(T0, K0, 0, XA, T0) NM(T2, K1, 0, XA, T2)
  NR(XA, 0, 14)
  NA(T5, T4)
  NM(T0, K0, 1, XA, T0)
  NS(11)
  NM(T2, K1, 1, XA, T2)
  NR(XA, 1, 14)
  NM(T0, K0, 2, XA, T0)
  NSIG(11)
  NM(T2, K1, 2, XA, T2)
  NR(XA, 2, 14)
  NW1(11, XA)
  NM(T0, K0, 3, XA, T0) NM(T2, K1, 3, XA, T2)
  NR(XA, 3, 14)
  NW3(7, XB)
  NM(T1, K0, 0, XB, T1) NM(T3, K1, 0, XB, T3)
  NR(XB, 0, 15)
  NA(T0, T5)
  NM(T1, K0, 1, XB, T1)
  NS(12)
  NM(T3, K1, 1, XB, T3)
  NR(XB, 1, 15)
  NM(T1, K0, 2, XB, T1)
  NSIG(12)
  NM(T3, K1, 2, XB, T3)
  NR(XB, 2, 15)
  NW1(11, XB)
  NM(T1, K0, 3, XB, T1) NM(T3, K1, 3, XB, T3)
  NR(XB, 3, 15)
  NW3(7, XA)
  NM(T2, K0, 0, XA, T2)
  NW3(1, XB)
  NM(T3, K0, 0, XB, T3)
  NA(T1, T0)
  NM(T2, K0, 1, XA, T2)
  NS(13)
  NM(T3, K0, 1, XB, T3)
  NSIG(13)
  NM(T2, K0, 2, XA, T2) NM(T3, K0, 2, XB, T3)
  NW1(8, XA) NW1(2, XB)
  NM(T2, K0, 3, XA, T2) NM(T3, K0, 3, XB, T3)
  NA(T2, T1)
  NS(14)
  NSIG(14)
  NA(T3, T2)
  NS(15)
  NSIG(15)
  asm volatile(".Lcpf_a:");
}
__device__ __forceinline__ bf16x8 band(int sh, int w, int kg){
  const uint32_t by = (((1u << w) - 1u) << sh) >> (8*kg);
  u32x4 r;
  #pragma unroll
  for (int k2=0;k2<4;k2++){ uint32_t x = (by >> (2*k2)) & 3u; x = (x | (x << 15)) & 0x00010001u; r[k2] = x * 0x3f80u; }
  return __builtin_bit_cast(bf16x8, r);
}
__device__ __forceinline__ void split2(const f32x4& a, const f32x4& b, bf16x8& h, bf16x8& l){
  u32x4 hh, ll;
  #pragma unroll
  for (int i=0;i<4;i++){ uint32_t x = pk2(a[i], b[i]); hh[i] = x; ll[i] = resid2(x, a[i], b[i]); }
  h = __builtin_bit_cast(bf16x8, hh); l = __builtin_bit_cast(bf16x8, ll);
}
__device__ __forceinline__ bf16x8 pack2(const f32x4& a, const f32x4& b){
  u32x4 hh;
  #pragma unroll
  for (int i=0;i<4;i++) hh[i] = pk2(a[i], b[i]);
  return __builtin_bit_cast(bf16x8, hh);
}
__device__ __forceinline__ void far_pre(char* smem, int wv, int ch, int m, int kg, uint32_t* P){
  {
    const int i = wv & 7;
    const uint32_t sh = (0x0f78169eu >> (4*i)) & 15u, w = (0x02975353u >> (4*i)) & 15u;
    const uint32_t by = __builtin_amdgcn_ubfe((((1u << w) - 1u) << sh) << m, 8*kg, 8);
    const uint32_t z = __umul24(by, 0x8001u);
    const uint32_t mu = i == 6 ? 0xffffu : 0x3f80u;
    u32x4 r;
    #pragma unroll
    for (int k2=0;k2<4;k2++) r[k2] = __umul24((z >> (2*k2)) & 0x00010001u, mu);
    *(u32x4*)(smem + 1024*i + 16*(m + 16*kg)) = r;
  }
  const uint32_t sb = (uint32_t)(uintptr_t)smem, ub = sb + ch*UST;
  const int B0 = 2*m + kg - 2, cp = (3 - m) & 3;
  P[28] = ub + (B0 < 0 ? 0 : 272*(B0 >> 4) + 16 + 16*(B0 & 15));
  P[29] = ub + 272*((B0+32) >> 4) + 16 + 16*((B0+32) & 15);
  P[30] = ub + (B0 < 1 ? 0 : 272*((B0-1) >> 4) + 16 + 16*((B0-1) & 15));
  P[31] = ub + 272*((B0+31) >> 4) + 16 + 16*((B0+31) & 15);
  P[32] = sb + FTOFF + ch*FTS + cp*FCS + 4*(47 - m + 4*kg - cp) - 128;
  P[33] = YFOFF + ch*YFST + 64*m + 16*kg;
  P[34] = sb + CNTOFF; P[35] = sb + K1OFF + 4*ch;
}
#define PB(i) __builtin_bit_cast(bf16x8, u32x4{P[4*(i)], P[4*(i)+1], P[4*(i)+2], P[4*(i)+3]})
#define FL(X, a, o) asm volatile("ds_read_b128 %0, %1 offset:%2" : "=v"(X) : "v"(P[a]), "i"(o)); SB_;
__device__ __forceinline__ void far2(char* smem, int lane, const uint32_t* P TSA){
  const f32x4 Z4 = {0,0,0,0}; f32x4 a0, a1, a2, a3, a4, a5; char* yf = smem + P[33]; uint32_t one = 1u; asm volatile("" : "+v"(one));
  bf16x8 V0_1; FL(V0_1, 28, 0)
  bf16x8 C00; FL(C00, 32, 128)
  bf16x8 C10; FL(C10, 32, 64)
  bf16x8 C20; FL(C20, 32, 0)
  bf16x8 V1_2; FL(V1_2, 29, 0)
  bf16x8 C01; FL(C01, 32, 1408)
  bf16x8 C11; FL(C11, 32, 1344)
  bf16x8 C21; FL(C21, 32, 1280)
  asm volatile("s_waitcnt lgkmcnt(7)" : "+v"(V0_1)); SB_;
  const f32x4 Fa0 = MFMA16(V0_1, PB(0), Z4); SB_; const f32x4 Fb0 = MFMA16(V0_1, PB(1), Z4); SB_;
  const f32x4 Fa1 = MFMA16(V0_1, PB(2), Z4); SB_; const f32x4 Fb1 = MFMA16(V0_1, PB(3), Z4); SB_;
  bf16x8 B0 = pack2(Fa0, Fb0); asm volatile("" : "+v"(B0)); SB_;
  asm volatile("s_waitcnt lgkmcnt(3)" : "+v"(V1_2)); SB_;
  const f32x4 Fa2 = MFMA16(V1_2, PB(0), Z4); SB_; const f32x4 Fb2 = MFMA16(V1_2, PB(1), Z4); SB_;
  bf16x8 V2_3; FL(V2_3, 29, 544)
  asm volatile("s_waitcnt lgkmcnt(5)" : "+v"(C00), "+v"(C10), "+v"(C20)); SB_;
  a0 = MFMA16(B0, C00, Z4); SB_;
  a1 = MFMA16(B0, C10, Z4); SB_;
  a2 = MFMA16(B0, C20, Z4); SB_;
  bf16x8 B1 = pack2(Fa1, Fb1); asm volatile("" : "+v"(B1)); SB_;
  const f32x4 Fa3 = MFMA16(V1_2, PB(2), Z4); SB_; const f32x4 Fb3 = MFMA16(V1_2, PB(3), Z4); SB_;
  asm volatile("s_waitcnt lgkmcnt(1)" : "+v"(C01), "+v"(C11), "+v"(C21)); SB_;
  a0 = MFMA16(B1, C01, a0); SB_;
  a1 = MFMA16(B1, C11, a1); SB_;
  a2 = MFMA16(B1, C21, a2); SB_;
  bf16x8 B2 = pack2(Fa2, Fb2); asm volatile("" : "+v"(B2)); SB_;
  asm volatile("s_waitcnt lgkmcnt(0)" : "+v"(V2_3)); SB_;
  const f32x4 Fa4 = MFMA16(V2_3, PB(0), Z4); SB_; const f32x4 Fb4 = MFMA16(V2_3, PB(1), Z4); SB_;
  bf16x8 V0_4; FL(V0_4, 28, 0)
  bf16x8 W0; FL(W0, 30, 0)
  uint32_t k1; asm volatile("ds_read_b32 %0, %1" : "=v"(k1) : "v"(P[35])); SB_;
  a1 = MFMA16(B2, C00, a1); SB_;
  a2 = MFMA16(B2, C10, a2); SB_;
  a3 = MFMA16(B2, C20, Z4); SB_;
  bf16x8 B3 = pack2(Fa3, Fb3); asm volatile("" : "+v"(B3)); SB_;
  const f32x4 Fa5 = MFMA16(V2_3, PB(2), Z4); SB_; const f32x4 Fb5 = MFMA16(V2_3, PB(3), Z4); SB_;
  bf16x8 V3_5; FL(V3_5, 29, 1088)
  bf16x8 E0; FL(E0, 32, 2752)
  bf16x8 E1; FL(E1, 32, 2688)
  a1 = MFMA16(B3, C01, a1); SB_;
  a2 = MFMA16(B3, C11, a2); SB_;
  a3 = MFMA16(B3, C21, a3); SB_;
  bf16x8 B4 = pack2(Fa4, Fb4); asm volatile("" : "+v"(B4)); SB_;
  asm volatile("s_waitcnt lgkmcnt(3)" : "+v"(V0_4), "+v"(W0), "+v"(k1)); SB_;
  const uint32_t kk = pk2(i2f(k1), i2f(k1)); u32x4 mk_ = u32x4{P[24] & kk, P[25] & kk, P[26] & kk, P[27] & kk}; asm volatile("" : "+v"(mk_)); const bf16x8 Mk = __builtin_bit_cast(bf16x8, mk_); SB_;
  const f32x4 Fa6 = MFMA16(V0_4, PB(4), Z4); SB_; const f32x4 Fb6 = MFMA16(W0, PB(5), Z4); SB_;
  a2 = MFMA16(Mk, V0_4, a2); SB_;
  if (lane < 4) *(float*)(yf - 16 - 60*lane) = 0.f; SB_;
  *(f32x4*)(yf + 0) = a0; SB_;
  *(f32x4*)(yf + 1024) = a1; SB_;
  asm volatile("s_mov_b64 exec, 1\n\tds_add_u32 %0, %1 offset:96\n\ts_mov_b64 exec, -1" :: "v"(P[34]), "v"(one) : "memory"); SB_;
  TS4 SB_;
  a2 = MFMA16(B4, C00, a2); SB_;
  a3 = MFMA16(B4, C10, a3); SB_;
  bf16x8 B5 = pack2(Fa5, Fb5); asm volatile("" : "+v"(B5)); SB_;
  asm volatile("s_waitcnt lgkmcnt(6)" : "+v"(V3_5)); SB_;
  const f32x4 Fa7 = MFMA16(V3_5, PB(0), Z4); SB_; const f32x4 Fb7 = MFMA16(V3_5, PB(1), Z4); SB_;
  bf16x8 V1_6; FL(V1_6, 29, 0)
  bf16x8 W1; FL(W1, 31, 0)
  a2 = MFMA16(B5, C01, a2); SB_;
  a3 = MFMA16(B5, C11, a3); SB_;
  bf16x8 B6 = pack2(Fa6, Fb6); asm volatile("" : "+v"(B6)); SB_;
  const f32x4 Fa8 = MFMA16(V3_5, PB(2), Z4); SB_; const f32x4 Fb8 = MFMA16(V3_5, PB(3), Z4); SB_;
  asm volatile("s_waitcnt lgkmcnt(6)" : "+v"(E0), "+v"(E1)); SB_;
  a2 = MFMA16(B6, E0, a2); SB_;
  a3 = MFMA16(B6, E1, a3); SB_;
  bf16x8 B7 = pack2(Fa7, Fb7); asm volatile("" : "+v"(B7)); SB_;
  asm volatile("s_waitcnt lgkmcnt(0)" : "+v"(V1_6), "+v"(W1)); SB_;
  const f32x4 Fa9 = MFMA16(V1_6, PB(4), Z4); SB_; const f32x4 Fb9 = MFMA16(W1, PB(5), Z4); SB_;
  a3 = MFMA16(Mk, V1_6, a3); SB_;
  a3 = MFMA16(B7, C00, a3); SB_;
  bf16x8 B8 = pack2(Fa8, Fb8); asm volatile("" : "+v"(B8)); SB_;
  a3 = MFMA16(B8, C01, a3); SB_;
  bf16x8 B9 = pack2(Fa9, Fb9); asm volatile("" : "+v"(B9)); SB_;
  a3 = MFMA16(B9, E0, a3); SB_;
  *(f32x4*)(yf + 2048) = a2; SB_;
  *(f32x4*)(yf + 3072) = a3; SB_;
  asm volatile("s_mov_b64 exec, 1\n\tds_add_u32 %0, %1 offset:100\n\ts_mov_b64 exec, -1" :: "v"(P[34]), "v"(one) : "memory"); SB_;
}
__device__ __forceinline__ void far3(char* smem, int lane, const uint32_t* P TSA){
  const f32x4 Z4 = {0,0,0,0}; f32x4 a0, a1, a2, a3, a4, a5; char* yf = smem + P[33]; uint32_t one = 1u; asm volatile("" : "+v"(one));
  bf16x8 V2_1; FL(V2_1, 29, 544)
  bf16x8 C20; FL(C20, 32, 0)
  bf16x8 W2; FL(W2, 31, 544)
  uint32_t k1; asm volatile("ds_read_b32 %0, %1" : "=v"(k1) : "v"(P[35])); SB_;
  bf16x8 C21; FL(C21, 32, 1280)
  bf16x8 V3_2; FL(V3_2, 29, 1088)
  bf16x8 E0; FL(E0, 32, 2752)
  bf16x8 E1; FL(E1, 32, 2688)
  asm volatile("s_waitcnt lgkmcnt(7)" : "+v"(V2_1)); SB_;
  const f32x4 Fa0 = MFMA16(V2_1, PB(0), Z4); SB_; const f32x4 Fb0 = MFMA16(V2_1, PB(1), Z4); SB_;
  const f32x4 Fa1 = MFMA16(V2_1, PB(2), Z4); SB_; const f32x4 Fb1 = MFMA16(V2_1, PB(3), Z4); SB_;
  bf16x8 B0 = pack2(Fa0, Fb0); asm volatile("" : "+v"(B0)); SB_;
  asm volatile("s_waitcnt lgkmcnt(4)" : "+v"(W2), "+v"(k1)); SB_;
  const uint32_t kk = pk2(i2f(k1), i2f(k1)); u32x4 mk_ = u32x4{P[24] & kk, P[25] & kk, P[26] & kk, P[27] & kk}; asm volatile("" : "+v"(mk_)); const bf16x8 Mk = __builtin_bit_cast(bf16x8, mk_); SB_;
  const f32x4 Fa2 = MFMA16(V2_1, PB(4), Z4); SB_; const f32x4 Fb2 = MFMA16(W2, PB(5), Z4); SB_;
  a4 = MFMA16(Mk, V2_1, Z4); SB_;
  bf16x8 C10; FL(C10, 32, 64)
  asm volatile("s_waitcnt lgkmcnt(7)" : "+v"(C20)); SB_;
  a4 = MFMA16(B0, C20, a4); SB_;
  bf16x8 B1 = pack2(Fa1, Fb1); asm volatile("" : "+v"(B1)); SB_;
  asm volatile("s_waitcnt lgkmcnt(3)" : "+v"(V3_2)); SB_;
  const f32x4 Fa3 = MFMA16(V3_2, PB(0), Z4); SB_; const f32x4 Fb3 = MFMA16(V3_2, PB(1), Z4); SB_;
  bf16x8 W3; FL(W3, 31, 1088)
  bf16x8 C11; FL(C11, 32, 1344)
  asm volatile("s_waitcnt lgkmcnt(6)" : "+v"(C21)); SB_;
  a4 = MFMA16(B1, C21, a4); SB_;
  bf16x8 B2 = pack2(Fa2, Fb2); asm volatile("" : "+v"(B2)); SB_;
  const f32x4 Fa4 = MFMA16(V3_2, PB(2), Z4); SB_; const f32x4 Fb4 = MFMA16(V3_2, PB(3), Z4); SB_;
  bf16x8 V4_3; FL(V4_3, 29, 1632)
  asm volatile("s_waitcnt lgkmcnt(4)" : "+v"(E0), "+v"(E1)); SB_;
  a4 = MFMA16(B2, E0, a4); SB_;
  a5 = MFMA16(B2, E1, Z4); SB_;
  bf16x8 B3 = pack2(Fa3, Fb3); asm volatile("" : "+v"(B3)); SB_;
  asm volatile("s_waitcnt lgkmcnt(2)" : "+v"(W3)); SB_;
  const f32x4 Fa5 = MFMA16(V3_2, PB(4), Z4); SB_; const f32x4 Fb5 = MFMA16(W3, PB(5), Z4); SB_;
  a5 = MFMA16(Mk, V3_2, a5); SB_;
  bf16x8 C00; FL(C00, 32, 128)
  asm volatile("s_waitcnt lgkmcnt(4)" : "+v"(C10)); SB_;
  a4 = MFMA16(B3, C10, a4); SB_;
  a5 = MFMA16(B3, C20, a5); SB_;
  bf16x8 B4 = pack2(Fa4, Fb4); asm volatile("" : "+v"(B4)); SB_;
  asm volatile("s_waitcnt lgkmcnt(1)" : "+v"(V4_3)); SB_;
  const f32x4 Fa6 = MFMA16(V4_3, PB(0), Z4); SB_; const f32x4 Fb6 = MFMA16(V4_3, PB(1), Z4); SB_;
  bf16x8 V0_4; FL(V0_4, 28, 0)
  bf16x8 W0; FL(W0, 30, 0)
  bf16x8 C01; FL(C01, 32, 1408)
  asm volatile("s_waitcnt lgkmcnt(5)" : "+v"(C11)); SB_;
  a4 = MFMA16(B4, C11, a4); SB_;
  a5 = MFMA16(B4, C21, a5); SB_;
  bf16x8 B5 = pack2(Fa5, Fb5); asm volatile("" : "+v"(B5)); SB_;
  const f32x4 Fa7 = MFMA16(V4_3, PB(2), Z4); SB_; const f32x4 Fb7 = MFMA16(V4_3, PB(3), Z4); SB_;
  bf16x8 V5_5; FL(V5_5, 29, 2176)
  bf16x8 E2; FL(E2, 32, 2624)
  bf16x8 E3; FL(E3, 32, 2560)
  a5 = MFMA16(B5, E0, a5); SB_;
  bf16x8 B6 = pack2(Fa6, Fb6); asm volatile("" : "+v"(B6)); SB_;
  asm volatile("s_waitcnt lgkmcnt(4)" : "+v"(V0_4), "+v"(W0)); SB_;
  const f32x4 Fa8 = MFMA16(V0_4, PB(4), Z4); SB_; const f32x4 Fb8 = MFMA16(W0, PB(5), Z4); SB_;
  asm volatile("s_waitcnt lgkmcnt(6)" : "+v"(C00)); SB_;
  a4 = MFMA16(B6, C00, a4); SB_;
  a5 = MFMA16(B6, C10, a5); SB_;
  TS4 SB_;
  bf16x8 B7 = pack2(Fa7, Fb7); asm volatile("" : "+v"(B7)); SB_;
  asm volatile("s_waitcnt lgkmcnt(2)" : "+v"(V5_5)); SB_;
  const f32x4 Fa9 = MFMA16(V5_5, PB(0), Z4); SB_; const f32x4 Fb9 = MFMA16(V5_5, PB(1), Z4); SB_;
  bf16x8 V1_6; FL(V1_6, 29, 0)
  bf16x8 W1; FL(W1, 31, 0)
  asm volatile("s_waitcnt lgkmcnt(5)" : "+v"(C01)); SB_;
  a4 = MFMA16(B7, C01, a4); SB_;
  a5 = MFMA16(B7, C11, a5); SB_;
  bf16x8 B8 = pack2(Fa8, Fb8); asm volatile("" : "+v"(B8)); SB_;
  const f32x4 Fa10 = MFMA16(V5_5, PB(2), Z4); SB_; const f32x4 Fb10 = MFMA16(V5_5, PB(3), Z4); SB_;
  asm volatile("s_waitcnt lgkmcnt(2)" : "+v"(E2), "+v"(E3)); SB_;
  a4 = MFMA16(B8, E2, a4); SB_;
  a5 = MFMA16(B8, E3, a5); SB_;
  bf16x8 B9 = pack2(Fa9, Fb9); asm volatile("" : "+v"(B9)); SB_;
  asm volatile("s_waitcnt lgkmcnt(0)" : "+v"(V1_6), "+v"(W1)); SB_;
  const f32x4 Fa11 = MFMA16(V1_6, PB(4), Z4); SB_; const f32x4 Fb11 = MFMA16(W1, PB(5), Z4); SB_;
  a5 = MFMA16(B9, C00, a5); SB_;
  bf16x8 B10 = pack2(Fa10, Fb10); asm volatile("" : "+v"(B10)); SB_;
  a5 = MFMA16(B10, C01, a5); SB_;
  bf16x8 B11 = pack2(Fa11, Fb11); asm volatile("" : "+v"(B11)); SB_;
  a4 = MFMA16(B11, E1, a4); SB_;
  a5 = MFMA16(B11, E2, a5); SB_;
  *(f32x4*)(yf + 4096) = a4; SB_;
  *(f32x4*)(yf + 5120) = a5; SB_;
  asm volatile("s_mov_b64 exec, 1\n\tds_add_u32 %0, %1 offset:104\n\ts_mov_b64 exec, -1" :: "v"(P[34]), "v"(one) : "memory"); SB_;
}
__global__ __launch_bounds__(1024) void mono_kernel(const u16* __restrict__ u, const u16* __restrict__ logA, const u16* __restrict__ Aim,
    const u16* __restrict__ Cre, const u16* __restrict__ Cim, const u16* __restrict__ logdt, const u16* __restrict__ Dskip,
    const u16* __restrict__ q, const u16* __restrict__ k, const u16* __restrict__ v, const u16* __restrict__ shift_k,
    const u16* __restrict__ ssmk, const u16* __restrict__ ssmD, u16* __restrict__ out
){
#define TSP(i)
  TSP(0); TSP(1); TSP(2); TSP(3); TSP(4); TSP(5);
  extern __shared__ __attribute__((aligned(16))) char smem[];
  const int bid = blockIdx.x, t = threadIdx.x;
  const int w5 = bid >> 3;
  const bool isA = w5 < 16;
  const int c0 = (bid & 7)*64 + (w5 & 15)*4;
  const int wave = t >> 6, lane = t & 63, m = lane & 15, kg = lane >> 4;
  {
    unsigned long long pc; int la, lb, lc;
#define CFETCH asm volatile("s_mov_b32 m0, %2\n\ts_nop 0\n\tglobal_load_lds_dword %0, %1" :: "v"(co), "s"(pc), "s"((uint32_t)(uintptr_t)smem + PFOFF) : "m0")
    asm volatile("s_getpc_b64 %0\n.Lcpf_here:\n\ts_mov_b32 %1, .Lcpf_a-.Lcpf_here\n\ts_mov_b32 %2, .Lcpf_b-.Lcpf_here\n\ts_mov_b32 %3, .Lcpf_c-.Lcpf_here" : "=s"(pc), "=s"(la), "=s"(lb), "=s"(lc));
    const uint32_t co = 128u*(uint32_t)t + (isA ? 0u : (uint32_t)la & ~127u);
    if (co < (uint32_t)(isA ? la : max(lb, lc))) CFETCH;
  }
  asm volatile("" :: "s"(u), "s"(logA), "s"(Aim), "s"(Cre), "s"(Cim), "s"(logdt), "s"(Dskip), "s"(q), "s"(k), "s"(v), "s"(shift_k), "s"(ssmk), "s"(ssmD), "s"(out));
  u32x2 dsk = {0u, 0u};
  if (t < 28) ((unsigned*)(smem + CNTOFF))[t] = 0u;
  int vz; asm volatile("v_mov_b32 %0, 0" : "=v"(vz));
  const int wv = __builtin_amdgcn_readfirstlane(wave);
  const uint32_t pfl = (uint32_t)(uintptr_t)smem + PFOFF;
#define PFETCH(off, ptr) asm volatile("s_mov_b32 m0, %2\n\ts_nop 0\n\tglobal_load_lds_dword %0, %1" :: "v"(off), "s"(ptr), "s"(pfl) : "m0")
  if (!isA && wv < PFB) PFETCH((128*(w5 & 15) + 64*(wv & 1) + lane)*(2*D_) + 128*(bid & 7), wv < 2 ? k : wv < 4 ? v : q);
  if (isA){
   uint32_t P[36];
   const int role = wv >> 2, chc = (wv + CROT*role) & 3;
   {
    const int ch = wave & 3, rj = wave >> 2;
    const int d = c0 + ch;
    char* tb = smem + TOFF + ch*TSZ;
    unsigned* cnt = (unsigned*)(smem + CNTOFF);
    uint32_t ldt, pcr, pci, lde, pla, pai;
    asm volatile("s_load_dwordx2 %0, %1, 0x0" : "=s"(dsk) : "s"(Dskip + c0));
    asm volatile("global_load_ushort %0, %1, %2" : "=v"(lde) : "v"(2*(c0 + ((t >> 7) & 3))), "s"(logdt));
    asm volatile("global_load_ushort %0, %1, %2" : "=v"(pla) : "v"(2*(c0*NS_ + (t & 511))), "s"(logA));
    asm volatile("global_load_ushort %0, %1, %2" : "=v"(pai) : "v"(2*(c0*NS_ + (t & 511))), "s"(Aim));
    asm volatile("global_load_ushort %0, %1, %2" : "=v"(ldt) : "v"(2*d), "s"(logdt));
    asm volatile("global_load_dword %0, %1, %2" : "=v"(pcr) : "v"(2*d*NS_ + 4*lane), "s"(Cre));
    asm volatile("global_load_dword %0, %1, %2" : "=v"(pci) : "v"(2*d*NS_ + 4*lane), "s"(Cim));
#define PWAIT(n) asm volatile("s_waitcnt vmcnt(" #n ")" : "+v"(lde), "+v"(pla), "+v"(pai), "+v"(ldt), "+v"(pcr), "+v"(pci))
    if (wv >= 8) PFETCH((128*(w5 & 15) + 16*(wv & 7) + (lane >> 2))*(2*D_) + 128*(bid & 7), u);
    if (wv < 8) PFETCH((64*(w5 & 15) + 16*(wv & 3) + (lane >> 2))*(2*D_) + 128*(bid & 7), wv < 4 ? k : v);
    __builtin_amdgcn_sched_barrier(0);
    __builtin_amdgcn_s_barrier();
    __builtin_amdgcn_sched_barrier(0);
    u32x2 uu[4];
    const int tl = (t + 64*(w5 & 15)) & 511;
    cp2 C; float ksc;
    auto prep = [&](){
      uint32_t e0 = pcr & 0x7f807f80u, e1 = pci & 0x7f807f80u;
      uint32_t x = max(max(e0 & 0xffffu, e0 >> 16), max(e1 & 0xffffu, e1 >> 16));
      x = max(x, (uint32_t)__builtin_amdgcn_update_dpp(0, (int)x, 0xb1, 0xf, 0xf, true));
      x = max(x, (uint32_t)__builtin_amdgcn_update_dpp(0, (int)x, 0x4e, 0xf, 0xf, true));
      x = max(x, (uint32_t)__builtin_amdgcn_update_dpp(0, (int)x, 0x141, 0xf, 0xf, true));
      x = max(x, (uint32_t)__builtin_amdgcn_update_dpp(0, (int)x, 0x140, 0xf, 0xf, true));
      const uint32_t y = max(max((uint32_t)__builtin_amdgcn_readlane((int)x, 0), (uint32_t)__builtin_amdgcn_readlane((int)x, 16)),
                             max((uint32_t)__builtin_amdgcn_readlane((int)x, 32), (uint32_t)__builtin_amdgcn_readlane((int)x, 48)));
      const int sh = 127 - (int)(y >> 7);
      C.a.re = ldexpf(lo16f(pcr), sh); C.a.im = ldexpf(lo16f(pci), sh);
      C.b.re = ldexpf(hi16f(pcr), sh); C.b.im = ldexpf(hi16f(pci), sh);
      ksc = ldexpf(2.f*rbf(expf(lo16f(ldt))), -sh);
      asm volatile("" : "+v"(C.a.re), "+v"(C.a.im), "+v"(C.b.re), "+v"(C.b.im), "+v"(ksc));
    };
    if (wave >= 8){
      for (int i=0;i<28;i++) asm volatile("" : "=v"(P[i]));
      far_pre(smem, wv, chc, m, kg, P);
      for (int i=0;i<36;i+=4) asm volatile("" : "+v"(P[i]), "+v"(P[i+1]), "+v"(P[i+2]), "+v"(P[i+3]));
      SB_;
      PWAIT(1);
      prep();
    } else {
      for (int i=0;i<36;i++) asm volatile("" : "=v"(P[i]));
      __builtin_amdgcn_s_setprio(PR0);
      PWAIT(4);
      const float dte = rbf(expf(lo16f(lde)));
      const float zre = -rbf(expf(lo16f(pla))) * dte;
      const float zim = lo16f(pai) * dte;
      const cpx e = cexp_(zre, zim), f = cexp_(zre*128.f, zim*128.f);
      char* ep = smem + XOFF + (t >> 7)*2048 + 8*(t & 127);
      *(float2*)(ep) = make_float2(e.re, e.im);
      *(float2*)(ep + 1024) = make_float2(f.re, f.im);
      PWAIT(1);
      prep();
      __builtin_amdgcn_s_setprio(0);
    }
    asm volatile("s_waitcnt lgkmcnt(0)" : "+s"(dsk) :: "memory"); __builtin_amdgcn_s_barrier();
    TSP(1);
    if (wave >= 8){
      __builtin_amdgcn_sched_barrier(0);
      const u16* up = u + (2*tl)*D_ + c0;
      uu[0] = *(const u32x2*)(up);           uu[1] = *(const u32x2*)(up + D_);
      uu[2] = *(const u32x2*)(up + 1024*D_); uu[3] = *(const u32x2*)(up + 1025*D_);
      __builtin_amdgcn_sched_barrier(0);
    }
    char* col = tb + 8*lane;
    char* nb = smem + NOFF + ch*KS2;
    char* ft = smem + FTOFF + ch*FTS;
    {
      const char* eb_ = smem + XOFF + ch*2048;
      cp2 e1, e2;
      { f32x4 a = *(const f32x4*)(eb_ + 16*lane); e2.a.re = a[0]; e2.a.im = a[1]; e2.b.re = a[2]; e2.b.im = a[3];
        f32x4 c = *(const f32x4*)(eb_ + 1024 + 16*lane); e1.a.re = c[0]; e1.a.im = c[1]; e1.b.re = c[2]; e1.b.im = c[3]; }
      if (wv >= 8){
        #pragma unroll
        for (int i=0;i<7;i++){ const u32x4 r = *(const u32x4*)(smem + 1024*i + 16*lane); P[4*i] = r[0]; P[4*i+1] = r[1]; P[4*i+2] = r[2]; P[4*i+3] = r[3]; }
      }
      __builtin_amdgcn_fence(__ATOMIC_RELEASE, "workgroup");
      if (lane == 0) __hip_atomic_fetch_add(cnt + 2, 1u, __ATOMIC_RELAXED, __HIP_MEMORY_SCOPE_WORKGROUP);
      if (rj == 2){
        __builtin_amdgcn_s_setprio(3);
        cp2 e8 = cmul2(e2, e2); e8 = cmul2(e8, e8); e8 = cmul2(e8, e8);
        *(u32x2*)(col + 41*TR) = u32x2{0x3c00u, 0x3c00u};
        cp2 e16 = cmul2(e8, e8);
        cp2 xa = e8, xb = e16;
        #pragma unroll
        for (int i=1;i<16;i+=2){
          putx(col + (41+i)*TR, xa); if (i < 15) putx(col + (42+i)*TR, xb);
          if (i < 15) xa = cmul2(xa, e16);
          if (i < 13) xb = cmul2(xb, e16);
        }
        __builtin_amdgcn_fence(__ATOMIC_RELEASE, "workgroup");
        if (lane == 0) __hip_atomic_fetch_add(cnt + 4 + ch, 1u, __ATOMIC_RELAXED, __HIP_MEMORY_SCOPE_WORKGROUP);
        __builtin_amdgcn_s_setprio(0);
      } else if (rj == 3){
        __builtin_amdgcn_s_setprio(2);
        cp2 eb = cmul2(e1, e1); eb = cmul2(eb, eb); eb = cmul2(eb, eb);
        cp2 cb = cmul2(C, eb);
        #pragma unroll
        for (int i=0;i<9;i++){ putg(col + (32+i)*TR, cb); if (i < 8) cb = cmul2(cb, e1); }
      } else if (rj == 0){
        cp2 cb = C, cd = cmul2(C, e1);
        #pragma unroll
        for (int p=0;p<8;p++){
          putg(col + p*TR, cb); putg(col + (8+p)*TR, cd);
          if (p < 7){ cb = cmul2(cb, e2); cd = cmul2(cd, e2); }
        }
      } else {
        __builtin_amdgcn_s_setprio(1);
        cp2 S2 = cmul2(e2, e2), S4 = cmul2(S2, S2), S6 = cmul2(S4, S2);
        cp2 cb = cmul2(C, cmul2(e1, e1));
        #pragma unroll
        for (int a=0;a<2;a++){
          putg(col + (16+4*a)*TR, cb);
          putg(col + (16+4*a+1)*TR, cmul2(cb, S2));
          putg(col + (16+4*a+2)*TR, cmul2(cb, S4));
          putg(col + (16+4*a+3)*TR, cmul2(cb, S6));
          cb = cmul2(cb, e1);
        }
        #pragma unroll
        for (int a=0;a<4;a++){
          putg(col + (24+2*a)*TR, cb);
          putg(col + (24+2*a+1)*TR, cmul2(cb, S4));
          if (a < 3) cb = cmul2(cb, e1);
        }
      }
    }
    TSP(2);
    SB_; if ((wv >> 1) == 2) PFETCH((1024 + 64*(w5 & 15) + lane)*(2*D_) + 128*(bid & 7), (wv & 1) ? v : k); SB_;
    __builtin_amdgcn_fence(__ATOMIC_RELEASE, "workgroup");
    while (__hip_atomic_load(cnt + 2, __ATOMIC_RELAXED, __HIP_MEMORY_SCOPE_WORKGROUP) < 16u ||
           __hip_atomic_load(cnt + 4 + ch, __ATOMIC_RELAXED, __HIP_MEMORY_SCOPE_WORKGROUP) < 1u) __builtin_amdgcn_s_sleep(1);
    __builtin_amdgcn_fence(__ATOMIC_ACQUIRE, "workgroup");
    if (rj != 2){
      const char* ga = tb + (16*(rj == 3 ? 2 : rj) + m)*TR + 16*kg;
      const char* xb = tb + (41 + m)*TR + 16*kg;
      f16x8 GA[8], XB[8];
      #pragma unroll
      for (int kc=0;kc<8;kc++){ GA[kc] = *(const f16x8*)(ga + 64*kc); XB[kc] = *(const f16x8*)(xb + 64*kc); }
      __builtin_amdgcn_sched_barrier(0);
      if (role == 0){ near_pre(smem, chc, lane, P);
      for (int i=0;i<36;i+=4) asm volatile("" : "+v"(P[i]), "+v"(P[i+1]), "+v"(P[i+2]), "+v"(P[i+3]));
      SB_;
      }
      f32x4 a4 = {0,0,0,0}, b4 = {0,0,0,0};
      #pragma unroll
      for (int kc=0;kc<8;kc+=2){ a4 = MFMAH(GA[kc], XB[kc], a4); b4 = MFMAH(GA[kc+1], XB[kc+1], b4); }
      float kv[4];
      #pragma unroll
      for (int i=0;i<4;i++) kv[i] = (a4[i] + b4[i]) * ksc;
      if (rj == 0){
        if (lane == 0) kv[0] += (ch & 1) ? hi16f(dsk[ch >> 1]) : lo16f(dsk[ch >> 1]);
        *(u32x2*)(nb + 16*(67 - 16*(kg >> 1) - m) + 8*(1 - (kg & 1))) = u32x2{pk2(kv[3], kv[2]), pk2(kv[1], kv[0])};
      } else if (rj == 1){
        const float k8 = i2f(__builtin_amdgcn_update_dpp(0, f2i(kv[0]), 0x130, 0xf, 0xf, true));
        if (kg < 2){
          *(u32x4*)(nb + 16*(35 - 16*kg - m)) = u32x4{pk2(k8, kv[3]), pk2(kv[2], kv[2]), pk2(kv[2], kv[1]), pk2(kv[0], kv[0])};
        } else {
          if (lane == 32) *(u32x4*)(nb + 16*3) = u32x4{0u, 0u, pk2(0.f, kv[0]), pk2(kv[0], kv[0])};
          #pragma unroll
          for (int i=0;i<4;i++){
            const int x1 = 32*(2*(kg-2) + (i >> 1)) + (i & 1) + 2*m - 1;
            char* p = ft + ((x1 >> 1) & 1)*(4*FCS) + 4*(47 - (x1 >> 2)) + 2*(x1 & 1);
            const u16 hv = f2bf(x1 >= 0 ? kv[i] : 0.f);
            #pragma unroll
            for (int c=0;c<4;c++) *(u16*)(p + c*(FCS-4)) = hv;
          }
        }
      } else {
        if (lane <= 32){
          const int x8 = 64*kg + m - 1;
          const int z = x8 < 0 ? 16 : 63 - (x8 >> 1);
          char* p = ft + (x8 < 0 ? 4*FCS : 8*FCS) + 4*z + 2*(x8 & 1);
          const u16 hv = f2bf(kv[0]);
          #pragma unroll
          for (int c=0;c<4;c++) *(u16*)(p + c*(FCS-4)) = hv;
          if (lane == 0) ((float*)(smem + K1OFF))[ch] = kv[0];
        }
        if (kg < 2){
          #pragma unroll
          for (int i=1;i<4;i++){
            const int x8 = 16*(4*kg + i) + m - 1;
            char* p = ft + 8*FCS + 4*(63 - (x8 >> 1)) + 2*(x8 & 1);
            const u16 hv = f2bf(kv[i]);
            #pragma unroll
            for (int c=0;c<4;c++) *(u16*)(p + c*(FCS-4)) = hv;
          }
        }
      }
      __builtin_amdgcn_s_setprio(0);
    } else {
      if (lane < 4) *(u32x4*)(nb + 16*(lane < 3 ? lane : 68)) = u32x4{0,0,0,0};
      const int s5 = lane & 31, z = s5 < 16 ? s5 : s5 + 32;
      #pragma unroll
      for (int c=0;c<4;c++) if (z >= c) *(uint32_t*)(ft + (lane >> 5)*(4*FCS) + c*FCS + 4*(z - c)) = 0u;
      if (m - kg < 12) *(uint32_t*)(ft + 8*FCS + kg*FCS + 4*(64 + m - kg)) = 0u;
    }
    if (wave >= 8){ put_u(smem, tl, uu[0], uu[1]); put_u(smem, tl + 512, uu[2], uu[3]); }
    if (role == 3) __builtin_amdgcn_s_setprio(PRF1); else if (role == 2) __builtin_amdgcn_s_setprio(PRF0); else __builtin_amdgcn_s_setprio(3);
    asm volatile("s_waitcnt lgkmcnt(0)" ::: "memory"); __builtin_amdgcn_s_barrier(); SB_;
    TSP(3);
   }
   {
    const int ch = chc;
    unsigned* cnt = (unsigned*)(smem + CNTOFF);
    if (role == 0){ near2(smem, P TSB); TSP(5); }
    else {
      if (role == 2) far2(smem, lane, P TSB); else if (role == 3) far3(smem, lane, P TSB);
      __builtin_amdgcn_s_setprio(3);
      TSP(5);
      #pragma unroll 1
      for (int cj = role == 1 ? ch : 4*role + ch; cj < (role == 1 ? 8 : 16); cj += (role == 1 ? 4 : 16)){
        const int g = cj >> 2;
        while (__hip_atomic_load(cnt + 8 + cj, __ATOMIC_RELAXED, __HIP_MEMORY_SCOPE_WORKGROUP) < 4u ||
               (g >= 1 && __hip_atomic_load(cnt + 23 + g, __ATOMIC_RELAXED, __HIP_MEMORY_SCOPE_WORKGROUP) < 4u) ||
               (g == 3 && __hip_atomic_load(cnt + 25, __ATOMIC_RELAXED, __HIP_MEMORY_SCOPE_WORKGROUP) < 4u)) __builtin_amdgcn_s_sleep(1);
        __builtin_amdgcn_fence(__ATOMIC_ACQUIRE, "workgroup");
        const int tr = 128*cj + 8*m + 2*kg;
        const char* yb = smem + YAOFF + 2048*cj + 8*lane;
        const char* yf = smem + YFOFF + (tr >= 514 ? 4*(tr - 515) : -16);
        f32x2 o[4];
        #pragma unroll
        for (int c=0;c<4;c++){
          o[c] = *(const f32x2*)(yb + 512*c);
          if (g){ o[c].x += *(const float*)(yf + c*YFST); o[c].y += *(const float*)(yf + c*YFST + 4); }
        }
        u16* op = out + c0 + tr*(2*D_);
        *(u32x2*)(op)        = u32x2{pk2(o[0].x, o[1].x), pk2(o[2].x, o[3].x)};
        *(u32x2*)(op + 2*D_) = u32x2{pk2(o[0].y, o[1].y), pk2(o[2].y, o[3].y)};
      }
    }
   }
  TSP(6);
    __builtin_amdgcn_s_waitcnt(0x0F70);
  } else {
#define BXS 4352
#define BKO 34816
#define BKS 9344
#define BKC 4672
#define BYO 109568
   const bool Hi = w5 & 1;
   const int cb = (bid & 7)*64 + ((w5 & 15) >> 1)*8;
   const uint32_t sb = (uint32_t)(uintptr_t)smem;
   const uint32_t one = 1u;
   const int h = wv & 7;
   typedef uint32_t u32x16 __attribute__((ext_vector_type(16)));
   u32x16 W;
   asm volatile("s_load_dwordx16 %0, %1, 0x0" : "=s"(W) : "s"(shift_k + 4*cb));
   const uint32_t ro = 2048*lane;
#define LDU(U, hmsk, k0, k1, v0, v1, h1, h2, h3, ZH) { const u16* kq = k + (128*(U))*D_ + cb; const u16* vq = v + (128*(U))*D_ + cb; const u16* hq = kq - ((U) ? 3*D_ : 0); \
      asm volatile("global_load_dwordx4 %0, %2, %3\n\tglobal_load_dwordx4 %1, %2, %3 offset:1024" \
        : "=&v"(k0), "=&v"(k1) : "v"(ro), "s"(kq)); SB_; ZH SB_; \
      asm volatile("s_mov_b64 exec, %8\n\tglobal_load_dwordx4 %4, %5, %7\n\tglobal_load_dwordx4 %3, %5, %7 offset:1024\n\tglobal_load_dwordx4 %2, %5, %7 offset:2048\n\t" \
        "s_mov_b64 exec, -1\n\tglobal_load_dwordx4 %0, %5, %6\n\tglobal_load_dwordx4 %1, %5, %6 offset:1024" \
        : "=&v"(v0), "=&v"(v1), "+&v"(h1), "+&v"(h2), "+&v"(h3) : "v"(ro), "s"(vq), "s"(hq), "s"(hmsk)); SB_; }
   f32x4 sx[4];
   auto PS = [&](const u32x4& k0, const u32x4& k1, const u32x4& h1, const u32x4& h2, const u32x4& h3){
      #pragma unroll
      for (int p=0;p<4;p++){
        const uint32_t k1m = (uint32_t)__builtin_amdgcn_update_dpp((int)h1[p], (int)k1[p], 0x138, 0xf, 0xf, false);
        const uint32_t k0m = (uint32_t)__builtin_amdgcn_update_dpp((int)h2[p], (int)k0[p], 0x138, 0xf, 0xf, false);
        const uint32_t k1n = (uint32_t)__builtin_amdgcn_update_dpp((int)h3[p], (int)k1m, 0x138, 0xf, 0xf, false);
        #pragma unroll
        for (int e=0;e<2;e++){
          const uint32_t sel = e ? 0x07060302u : 0x05040100u;
          const uint32_t P0 = __builtin_amdgcn_perm(k0[p], k1[p], sel), Pm = __builtin_amdgcn_perm(k0m, k1m, sel);
          const uint32_t Q0 = __builtin_amdgcn_perm(k1m, k0[p], sel), Qm = __builtin_amdgcn_perm(k1n, k0m, sel);
          float s1, s0;
          asm("v_dot2_f32_bf16 %0, %2, %6, 0\n\tv_dot2_f32_bf16 %1, %3, %6, 0\n\tv_dot2_f32_bf16 %0, %4, %7, %0\n\tv_dot2_f32_bf16 %1, %5, %7, %1"
              : "=&v"(s1), "=&v"(s0) : "v"(P0), "v"(Q0), "v"(Pm), "v"(Qm), "s"(W[4*p + 2*e]), "s"(W[4*p + 2*e + 1]));
          sx[p][2*e] = s1; sx[p][2*e+1] = s0;
        }
      }
   };
   auto PV = [&](int U, const u32x4& v0, const u32x4& v1){
      char* xw = smem + 4*(1023 - 64*U - lane);
      #pragma unroll
      for (int p=0;p<4;p++){
        #pragma unroll
        for (int e=0;e<2;e++){
          const float x1 = amul(sx[p][2*e], e ? hi16f(v1[p]) : lo16f(v1[p])), x0 = amul(sx[p][2*e+1], e ? hi16f(v0[p]) : lo16f(v0[p]));
          *(uint32_t*)(xw + (2*p + e)*BXS) = pk2(x1, x0);
        }
      }
      asm volatile("s_mov_b64 exec, 1\n\tds_or_b32 %0, %1\n\ts_mov_b64 exec, -1" :: "v"(sb + CNTOFF), "v"(1u << U) : "memory");
   };
#define WKK(n, k0, k1, h1, h2, h3) asm volatile("s_waitcnt vmcnt(" #n ") lgkmcnt(0)" : "+v"(k0), "+v"(k1), "+v"(h1), "+v"(h2), "+v"(h3), "+s"(W))
#define WV(n, v0, v1) asm volatile("s_nop 2\n\ts_waitcnt vmcnt(" #n ")" : "+v"(v0), "+v"(v1), "+v"(sx[0]), "+v"(sx[1]), "+v"(sx[2]), "+v"(sx[3]))
   if ((wv ^ SWP) < 8){
    const int ch = h;
    {
      u32x4 a0, a1, a2, a3, a4, a5, a6;
      __builtin_amdgcn_s_barrier();
      SB_;
      LDU(h, h ? 1ull : 2ull, a0, a1, a2, a3, a4, a5, a6, a4 = (u32x4)(0u); a5 = a4; a6 = a4;)
      asm volatile("s_mov_b64 exec, 1\n\tds_add_u32 %0, %1 offset:48\n\ts_mov_b64 exec, -1" :: "v"(sb + CNTOFF), "v"(one) : "memory");
      if (lane < 16) *(u32x4*)(smem + ch*BXS + 4096 + 16*lane) = u32x4{0,0,0,0};
      SB_;
      WKK(2, a0, a1, a4, a5, a6);
      TSP(1);
      PS(a0, a1, a4, a5, a6);
      WV(0, a2, a3);
      PV(h, a2, a3);
      TSP(2);
    }
    const uint32_t xa = sb + ch*BXS + 16*(15 - m + kg);
    const uint32_t ka = sb + BKO + ch*BKS + ((m & 1) ? 0 : BKC) + 288*((m >> 1) & 1) + 16*kg + 4*(m >> 2) + 4;
    uint32_t fa = sb + CNTOFF;
    asm volatile("" : "+v"(fa));
    char* yp = smem + BYO + ch*512 + 8*lane;
    const f32x4 Z4 = {0,0,0,0};
    f32x4 T0 = Z4, T1 = Z4, T2 = Z4, T3 = Z4, T4 = Z4, T5 = Z4, T6 = Z4, T7 = Z4, T8 = Z4, T9 = Z4, T10 = Z4;
    bf16x8 XA[4], XB[4];
    u32x2 kl[3][4], kh[3][4];
    uint32_t fl, rdy = 0u;
#define XO(u, c) "i"(256*(15-(u)) + 64*(c))
#define RD4 "ds_read_b128 %0, %4 offset:%5\n\tds_read_b128 %1, %4 offset:%6\n\tds_read_b128 %2, %4 offset:%7\n\tds_read_b128 %3, %4 offset:%8\n\t"
#define LX4(X, u) asm volatile(RD4 : "=&v"(X[0]), "=&v"(X[1]), "=&v"(X[2]), "=&v"(X[3]) : "v"(xa), XO(u, 0), XO(u, 1), XO(u, 2), XO(u, 3)); SB_;
#define LE(X, u) asm volatile("s_bitcmp0_b32 %9, %10\n\ts_cbranch_scc1 1f\n\t" RD4 "1:" : "=&v"(X[0]), "=&v"(X[1]), "=&v"(X[2]), "=&v"(X[3]) \
      : "v"(xa), XO(u, 0), XO(u, 1), XO(u, 2), XO(u, 3), "s"(rdy), "i"(u) : "scc"); SB_;
#define RD4B "ds_read_b128 %0, %6 offset:%7\n\tds_read_b128 %1, %6 offset:%8\n\tds_read_b128 %2, %6 offset:%9\n\tds_read_b128 %3, %6 offset:%10\n\t"
#define LL(X, u) { uint32_t t_, v_; asm volatile("s_bitcmp1_b32 %12, %13\n\ts_cbranch_scc1 2f\n1:\n\tds_read_b32 %5, %11\n\ts_waitcnt lgkmcnt(0)\n\tv_readfirstlane_b32 %4, %5\n\t" \
      "s_nop 3\n\ts_bitcmp0_b32 %4, %13\n\ts_cbranch_scc1 1b\n\t" RD4B "2:" : "+v"(X[0]), "+v"(X[1]), "+v"(X[2]), "+v"(X[3]), "=&s"(t_), "=&v"(v_) \
      : "v"(xa), XO(u, 0), XO(u, 1), XO(u, 2), XO(u, 3), "v"(fa), "s"(rdy), "i"(u) : "scc", "memory"); SB_; }
#define LK1(s, j, c) asm volatile("ds_read2_b32 %0, %2 offset0:%3 offset1:%4\n\tds_read2_b32 %1, %2 offset0:%5 offset1:%6" : "=&v"(kl[s][c]), "=&v"(kh[s][c]) \
      : "v"(ka + 1152*((j) >> 1)), "i"(144*((j) & 1) + 16*(c)), "i"(144*((j) & 1) + 16*(c) + 1), "i"(144*((j) & 1) + 16*(c) + 2), "i"(144*((j) & 1) + 16*(c) + 3));
#define LKB(s, j) LK1(s, j, 0) LK1(s, j, 1) LK1(s, j, 2) LK1(s, j, 3) SB_;
#define FLG asm volatile("ds_read_b32 %0, %1" : "=v"(fl) : "v"(fa)); SB_;
#define WY(n, X) asm volatile("s_waitcnt lgkmcnt(" #n ")" : "+v"(X[0]), "+v"(X[1]), "+v"(X[2]), "+v"(X[3]), "+v"(fl)); SB_;
#define WX(n, X) WY(n, X) rdy = __builtin_amdgcn_readfirstlane(fl); SB_;
#define KT(s) "+v"(kl[s][0]), "+v"(kh[s][0]), "+v"(kl[s][1]), "+v"(kh[s][1]), "+v"(kl[s][2]), "+v"(kh[s][2]), "+v"(kl[s][3]), "+v"(kh[s][3])
#define TK(s) asm volatile("" : KT(s)); SB_;
#define WK(n, s) asm volatile("s_waitcnt lgkmcnt(" #n ")" : KT(s)); SB_;
#define POLL(n) while (!((rdy >> (n)) & 1u)){ rdy = __builtin_amdgcn_readfirstlane(__hip_atomic_load((const unsigned*)(smem + CNTOFF), __ATOMIC_RELAXED, __HIP_MEMORY_SCOPE_WORKGROUP)); } SB_;
#define KF(s, c) __builtin_bit_cast(bf16x8, __builtin_shufflevector(kl[s][c], kh[s][c], 0, 1, 2, 3))
#define MM(T, s, c, X) T = MFMA16(KF(s, c), X[c], T); SB_;
#define ESIG(sl) asm volatile("s_mov_b64 exec, 1\n\tds_add_u32 %0, %1 offset:%2\n\ts_mov_b64 exec, -1" :: "v"(fa), "v"(one), "i"(4 + 4*(sl)) : "memory"); SB_;
#define EMIT(sl, Tc, Tp) { *(f32x2*)(yp + 4096*(sl)) = f32x2{Tc[0], Tc[1]} + f32x2{Tp[2], Tp[3]}; ESIG(sl) }
#define EMIT0(sl, Tc) { *(f32x2*)(yp + 4096*(sl)) = f32x2{Tc[0], Tc[1]}; ESIG(sl) }
    POLL(16 + ch)
    if (Hi){
      LKB(2, 5) LKB(0, 6) LKB(1, 7)
      POLL(0) TSP(3); LX4(XA, 0) FLG
      WX(0, XA) TK(0) TK(1) TK(2) LE(XB, 1)
      MM(T4, 1, 0, XA) MM(T2, 0, 0, XA)
      MM(T4, 1, 1, XA) MM(T2, 0, 1, XA)
      MM(T4, 1, 2, XA) MM(T2, 0, 2, XA)
      MM(T4, 1, 3, XA) MM(T2, 0, 3, XA)
      LL(XB, 1) FLG
      MM(T0, 2, 0, XA) MM(T0, 2, 1, XA) MM(T0, 2, 2, XA) MM(T0, 2, 3, XA)
      WX(0, XB) LE(XA, 2)
      MM(T5, 1, 0, XB) MM(T3, 0, 0, XB)
      MM(T5, 1, 1, XB) MM(T3, 0, 1, XB)
      MM(T5, 1, 2, XB) MM(T3, 0, 2, XB)
      MM(T5, 1, 3, XB) MM(T3, 0, 3, XB)
      LL(XA, 2) FLG LKB(1, 4)
      MM(T1, 2, 0, XB) MM(T1, 2, 1, XB) MM(T1, 2, 2, XB) MM(T1, 2, 3, XB)
      WX(8, XA) LE(XB, 3)
      MM(T4, 0, 0, XA) MM(T2, 2, 0, XA)
      MM(T4, 0, 1, XA) MM(T2, 2, 1, XA)
      MM(T4, 0, 2, XA) MM(T2, 2, 2, XA)
      MM(T4, 0, 3, XA) MM(T2, 2, 3, XA)
      LL(XB, 3) FLG WK(4, 1)
      MM(T0, 1, 0, XA) MM(T0, 1, 1, XA) MM(T0, 1, 2, XA) MM(T0, 1, 3, XA)
      WX(0, XB) LE(XA, 4)
      MM(T5, 0, 0, XB) MM(T3, 2, 0, XB)
      MM(T5, 0, 1, XB) MM(T3, 2, 1, XB)
      MM(T5, 0, 2, XB) MM(T3, 2, 2, XB)
      MM(T5, 0, 3, XB) MM(T3, 2, 3, XB)
      LL(XA, 4) FLG LKB(0, 3)
      MM(T1, 1, 0, XB) MM(T1, 1, 1, XB) MM(T1, 1, 2, XB) MM(T1, 1, 3, XB)
      WX(8, XA) LE(XB, 5)
      MM(T4, 2, 0, XA) MM(T2, 1, 0, XA)
      MM(T4, 2, 1, XA) MM(T2, 1, 1, XA)
      MM(T4, 2, 2, XA) MM(T2, 1, 2, XA)
      MM(T4, 2, 3, XA) MM(T2, 1, 3, XA)
      LL(XB, 5) FLG WK(4, 0)
      MM(T0, 0, 0, XA) MM(T0, 0, 1, XA) MM(T0, 0, 2, XA) MM(T0, 0, 3, XA)
      WX(0, XB) LE(XA, 6)
      MM(T5, 2, 0, XB) MM(T3, 1, 0, XB)
      MM(T5, 2, 1, XB) MM(T3, 1, 1, XB)
      MM(T5, 2, 2, XB) MM(T3, 1, 2, XB)
      MM(T5, 2, 3, XB) MM(T3, 1, 3, XB)
      LL(XA, 6) FLG LKB(2, 2)
      MM(T1, 0, 0, XB) MM(T1, 0, 1, XB) MM(T1, 0, 2, XB) MM(T1, 0, 3, XB)
      WX(8, XA) LE(XB, 7)
      MM(T4, 1, 0, XA) MM(T2, 0, 0, XA)
      MM(T4, 1, 1, XA) MM(T2, 0, 1, XA)
      MM(T4, 1, 2, XA) MM(T2, 0, 2, XA)
      MM(T4, 1, 3, XA) MM(T2, 0, 3, XA)
      LL(XB, 7) FLG WK(4, 2)
      MM(T0, 2, 0, XA) MM(T0, 2, 1, XA) MM(T0, 2, 2, XA) MM(T0, 2, 3, XA)
      WX(0, XB) LE(XA, 8)
      MM(T5, 1, 0, XB) MM(T3, 0, 0, XB)
      MM(T5, 1, 1, XB) MM(T3, 0, 1, XB)
      MM(T5, 1, 2, XB) MM(T3, 0, 2, XB)
      MM(T5, 1, 3, XB) MM(T3, 0, 3, XB)
      LL(XA, 8) FLG LKB(1, 1)
      MM(T1, 2, 0, XB) MM(T1, 2, 1, XB) MM(T1, 2, 2, XB) MM(T1, 2, 3, XB)
      WX(8, XA) LE(XB, 9)
      TSP(4);
      MM(T4, 0, 0, XA) MM(T2, 2, 0, XA)
      MM(T4, 0, 1, XA) MM(T2, 2, 1, XA)
      MM(T4, 0, 2, XA) MM(T2, 2, 2, XA)
      MM(T4, 0, 3, XA) MM(T2, 2, 3, XA)
      LL(XB, 9) FLG WK(4, 1)
      MM(T0, 1, 0, XA) MM(T0, 1, 1, XA) MM(T0, 1, 2, XA) MM(T0, 1, 3, XA)
      WX(0, XB) LE(XA, 10)
      MM(T5, 0, 0, XB) MM(T3, 2, 0, XB)
      MM(T5, 0, 1, XB) MM(T3, 2, 1, XB)
      MM(T5, 0, 2, XB) MM(T3, 2, 2, XB)
      MM(T5, 0, 3, XB) MM(T3, 2, 3, XB)
      LL(XA, 10) FLG LKB(0, 0)
      MM(T1, 1, 0, XB) MM(T1, 1, 1, XB) MM(T1, 1, 2, XB) MM(T1, 1, 3, XB)
      WX(8, XA) LE(XB, 11)
      MM(T4, 2, 0, XA) MM(T2, 1, 0, XA)
      MM(T4, 2, 1, XA) MM(T2, 1, 1, XA)
      MM(T4, 2, 2, XA) MM(T2, 1, 2, XA)
      MM(T4, 2, 3, XA) MM(T2, 1, 3, XA)
      LL(XB, 11) FLG WK(4, 0)
      MM(T0, 0, 0, XA) MM(T0, 0, 1, XA) MM(T0, 0, 2, XA) MM(T0, 0, 3, XA)
      WX(0, XB) LE(XA, 12)
      MM(T1, 0, 0, XB) MM(T3, 1, 0, XB)
      MM(T1, 0, 1, XB) MM(T3, 1, 1, XB)
      MM(T1, 0, 2, XB) MM(T3, 1, 2, XB)
      MM(T1, 0, 3, XB) MM(T3, 1, 3, XB)
      MM(T5, 2, 0, XB) MM(T5, 2, 1, XB)
      EMIT(0, T1, T0)
      LL(XA, 12) FLG
      MM(T5, 2, 2, XB) MM(T5, 2, 3, XB)
      WX(0, XA) LE(XB, 13)
      MM(T2, 0, 0, XA) MM(T4, 1, 0, XA)
      MM(T2, 0, 1, XA) MM(T4, 1, 1, XA)
      MM(T2, 0, 2, XA) MM(T4, 1, 2, XA)
      MM(T2, 0, 3, XA) MM(T4, 1, 3, XA)
      EMIT(1, T2, T1)
      LL(XB, 13) FLG
      WX(0, XB) LE(XA, 14)
      MM(T3, 0, 0, XB) MM(T5, 1, 0, XB)
      MM(T3, 0, 1, XB) MM(T5, 1, 1, XB)
      MM(T3, 0, 2, XB) MM(T5, 1, 2, XB)
      MM(T3, 0, 3, XB) MM(T5, 1, 3, XB)
      EMIT(2, T3, T2)
      LL(XA, 14) FLG
      WX(0, XA) LE(XB, 15)
      MM(T4, 0, 0, XA) MM(T4, 0, 1, XA) MM(T4, 0, 2, XA) MM(T4, 0, 3, XA)
      EMIT(3, T4, T3)
      LL(XB, 15) FLG
      WX(0, XB)
      MM(T5, 0, 0, XB) MM(T5, 0, 1, XB) MM(T5, 0, 2, XB) MM(T5, 0, 3, XB)
      EMIT(4, T5, T4)
      asm volatile(".Lcpf_c:");
    } else {
      LKB(0, 0) LKB(1, 1) LKB(2, 2)
      POLL(0) TSP(3); LX4(XA, 0) FLG
      WX(0, XA) TK(0) TK(1) TK(2) LE(XB, 1)
      MM(T0, 0, 0, XA) MM(T2, 1, 0, XA)
      MM(T0, 0, 1, XA) MM(T2, 1, 1, XA)
      MM(T0, 0, 2, XA) MM(T2, 1, 2, XA)
      MM(T0, 0, 3, XA) MM(T2, 1, 3, XA)
      MM(T4, 2, 0, XA) MM(T4, 2, 1, XA)
      EMIT0(0, T0)
      LL(XB, 1) FLG
      MM(T4, 2, 2, XA) MM(T4, 2, 3, XA)
      WX(0, XB) LE(XA, 2)
      MM(T1, 0, 0, XB) MM(T3, 1, 0, XB)
      MM(T1, 0, 1, XB) MM(T3, 1, 1, XB)
      MM(T1, 0, 2, XB) MM(T3, 1, 2, XB)
      MM(T1, 0, 3, XB) MM(T3, 1, 3, XB)
      MM(T5, 2, 0, XB) MM(T5, 2, 1, XB)
      EMIT(1, T1, T0)
      LL(XA, 2) FLG
      MM(T5, 2, 2, XB) MM(T5, 2, 3, XB)
      WX(0, XA) LE(XB, 3)
      MM(T2, 0, 0, XA) MM(T4, 1, 0, XA)
      MM(T2, 0, 1, XA) MM(T4, 1, 1, XA)
      MM(T2, 0, 2, XA) MM(T4, 1, 2, XA)
      MM(T2, 0, 3, XA) MM(T4, 1, 3, XA)
      MM(T6, 2, 0, XA) MM(T6, 2, 1, XA)
      EMIT(2, T2, T1)
      LL(XB, 3) FLG
      MM(T6, 2, 2, XA) MM(T6, 2, 3, XA)
      WX(0, XB) LE(XA, 4)
      MM(T3, 0, 0, XB) MM(T5, 1, 0, XB)
      MM(T3, 0, 1, XB) MM(T5, 1, 1, XB)
      MM(T3, 0, 2, XB) MM(T5, 1, 2, XB)
      MM(T3, 0, 3, XB) MM(T5, 1, 3, XB)
      MM(T7, 2, 0, XB) MM(T7, 2, 1, XB)
      EMIT(3, T3, T2)
      LL(XA, 4) FLG
      MM(T7, 2, 2, XB) MM(T7, 2, 3, XB)
      WX(0, XA) LX4(XB, 0)
      MM(T4, 0, 0, XA) MM(T8, 2, 0, XA)
      MM(T4, 0, 1, XA) MM(T8, 2, 1, XA)
      MM(T4, 0, 2, XA) MM(T8, 2, 2, XA)
      MM(T4, 0, 3, XA) MM(T8, 2, 3, XA)
      LKB(2, 3) LKB(0, 4)
      MM(T6, 1, 0, XA) MM(T6, 1, 1, XA)
      EMIT(4, T4, T3) T5[0] += T4[2]; T5[1] += T4[3]; SB_;
      MM(T6, 1, 2, XA) MM(T6, 1, 3, XA)
      LKB(1, 5)
      WY(8, XB) TK(0) TK(2)
      MM(T10, 2, 0, XA) MM(T10, 2, 1, XA)
      MM(T6, 2, 0, XB) MM(T10, 2, 2, XA) MM(T8, 0, 0, XB) MM(T10, 2, 3, XA)
      LX4(XA, 1)
      MM(T6, 2, 1, XB) MM(T8, 0, 1, XB)
      MM(T6, 2, 2, XB) MM(T8, 0, 2, XB)
      MM(T6, 2, 3, XB) MM(T8, 0, 3, XB)
      WK(4, 1)
      MM(T10, 1, 0, XB) MM(T10, 1, 1, XB) MM(T10, 1, 2, XB) MM(T10, 1, 3, XB)
      LKB(1, 1)
      WY(8, XA) LX4(XB, 2)
      MM(T7, 2, 0, XA) MM(T9, 0, 0, XA)
      MM(T7, 2, 1, XA) MM(T9, 0, 1, XA)
      MM(T7, 2, 2, XA) MM(T9, 0, 2, XA)
      MM(T7, 2, 3, XA) MM(T9, 0, 3, XA)
      WY(0, XB) TK(1) LX4(XA, 3) FLG
      MM(T8, 2, 0, XB) MM(T10, 0, 0, XB)
      MM(T8, 2, 1, XB) MM(T10, 0, 1, XB)
      MM(T8, 2, 2, XB) MM(T10, 0, 2, XB)
      MM(T8, 2, 3, XB) MM(T10, 0, 3, XB)
      LKB(0, 0)
      WX(8, XA) LE(XB, 5)
      MM(T9, 2, 0, XA) MM(T9, 2, 1, XA) MM(T9, 2, 2, XA) MM(T9, 2, 3, XA)
      LL(XB, 5) FLG LKB(2, 2) TSP(4);
      WX(8, XB) TK(0) LE(XA, 6)
      MM(T5, 0, 0, XB) MM(T7, 1, 0, XB)
      MM(T5, 0, 1, XB) MM(T7, 1, 1, XB)
      MM(T5, 0, 2, XB) MM(T7, 1, 2, XB)
      MM(T5, 0, 3, XB) MM(T7, 1, 3, XB)
      EMIT0(5, T5)
      LL(XA, 6) FLG
      WK(4, 2)
      MM(T9, 2, 0, XB) MM(T9, 2, 1, XB) MM(T9, 2, 2, XB) MM(T9, 2, 3, XB)
      WX(0, XA) LE(XB, 7)
      MM(T6, 0, 0, XA) MM(T8, 1, 0, XA)
      MM(T6, 0, 1, XA) MM(T8, 1, 1, XA)
      MM(T6, 0, 2, XA) MM(T8, 1, 2, XA)
      MM(T6, 0, 3, XA) MM(T8, 1, 3, XA)
      MM(T10, 2, 0, XA) MM(T10, 2, 1, XA)
      EMIT(6, T6, T5)
      LL(XB, 7) FLG
      MM(T10, 2, 2, XA) MM(T10, 2, 3, XA)
      WX(0, XB) LE(XA, 8)
      MM(T7, 0, 0, XB) MM(T9, 1, 0, XB)
      MM(T7, 0, 1, XB) MM(T9, 1, 1, XB)
      MM(T7, 0, 2, XB) MM(T9, 1, 2, XB)
      MM(T7, 0, 3, XB) MM(T9, 1, 3, XB)
      EMIT(7, T7, T6)
      LL(XA, 8) FLG
      WX(0, XA) LE(XB, 9)
      MM(T8, 0, 0, XA) MM(T10, 1, 0, XA)
      MM(T8, 0, 1, XA) MM(T10, 1, 1, XA)
      MM(T8, 0, 2, XA) MM(T10, 1, 2, XA)
      MM(T8, 0, 3, XA) MM(T10, 1, 3, XA)
      EMIT(8, T8, T7)
      LL(XB, 9) FLG
      WX(0, XB) LE(XA, 10)
      MM(T9, 0, 0, XB) MM(T9, 0, 1, XB) MM(T9, 0, 2, XB) MM(T9, 0, 3, XB)
      EMIT(9, T9, T8)
      LL(XA, 10) FLG
      WX(0, XA)
      MM(T10, 0, 0, XA) MM(T10, 0, 1, XA) MM(T10, 0, 2, XA) MM(T10, 0, 3, XA)
      EMIT(10, T10, T9)
      asm volatile(".Lcpf_b:");
    }
    TSP(5);
   } else {
    {
      const int ch = h;
      const u16* kp = ssmk + (cb + ch)*L_ + 8*lane;
      u32x4 S[4];
      S[0] = *(const u32x4*)(kp); S[1] = *(const u32x4*)(kp + 512); S[2] = *(const u32x4*)(kp + 1024);
      S[3] = *(const u32x4*)(kp + (Hi ? 1536 : 1024));
      const uint32_t dd = ssmD[cb + ch + vz];
      SB_;
      if (lane < 2) *(u32x4*)(smem + BKO + ch*BKS + lane*BKC) = u32x4{0,0,0,0};
      asm volatile("s_waitcnt lgkmcnt(0)" ::: "memory"); __builtin_amdgcn_s_barrier();
      SB_;
      const uint32_t f = pk2(lo16f(S[0].x) + lo16f(dd), hi16f(S[0].x));
      S[0].x = lane == 0 ? f : S[0].x;
      char* kw = smem + BKO + ch*BKS + 288*kg + 16*m + 16;
      uint32_t car = 0u;
      #pragma unroll
      for (int n=0;n<4;n++){
        const uint32_t pv = (uint32_t)__builtin_amdgcn_update_dpp((int)car, (int)S[n].w, 0x138, 0xf, 0xf, false);
        if (n < 3) car = (uint32_t)__builtin_amdgcn_update_dpp(0, (int)S[n].w, 0x13c, 0xf, 0xf, false);
        u32x4 O;
        O.x = __builtin_amdgcn_alignbit(S[n].x, pv, 16);     O.y = __builtin_amdgcn_alignbit(S[n].y, S[n].x, 16);
        O.z = __builtin_amdgcn_alignbit(S[n].z, S[n].y, 16); O.w = __builtin_amdgcn_alignbit(S[n].w, S[n].z, 16);
        if (n < 3 || Hi){
          *(u32x4*)(kw + 1152*n) = S[n]; *(u32x4*)(kw + 1152*n + BKC) = O;
          if (m == 15){ *(u32x4*)(kw + 1152*n + 32) = S[n]; *(u32x4*)(kw + 1152*n + BKC + 32) = O; }
        }
      }
      asm volatile("s_mov_b64 exec, 1\n\tds_or_b32 %0, %1\n\ts_mov_b64 exec, -1" :: "v"(sb + CNTOFF), "v"(0x10000u << ch) : "memory");
      SB_;
    }
    while (__hip_atomic_load((const unsigned*)(smem + CNTOFF) + 12, __ATOMIC_RELAXED, __HIP_MEMORY_SCOPE_WORKGROUP) < 8u) __builtin_amdgcn_s_sleep(1);
    SB_;
    TSP(1);
    if (h >= 6) PFETCH((128*(w5 & 15) + 64*(h & 1) + lane)*(2*D_) + 128*(bid & 7), q);
    SB_;
    __builtin_amdgcn_s_setprio(HPRIO);
    auto OC = [&](int cj, int sl, const u32x4& qa, const u32x4& qb){
      while (__hip_atomic_load((const unsigned*)(smem + CNTOFF) + 1 + sl, __ATOMIC_RELAXED, __HIP_MEMORY_SCOPE_WORKGROUP) < 8u) __builtin_amdgcn_s_sleep(1);
      __builtin_amdgcn_fence(__ATOMIC_ACQUIRE, "workgroup");
      const char* yb = smem + BYO + 4096*sl + 8*lane;
      f32x2 o[8];
      #pragma unroll
      for (int c=0;c<8;c++){
        const f32x2 y = *(const f32x2*)(yb + 512*c);
        o[c].x = amul(y.x, (c & 1) ? hi16f(qa[c >> 1]) : lo16f(qa[c >> 1])); o[c].y = amul(y.y, (c & 1) ? hi16f(qb[c >> 1]) : lo16f(qb[c >> 1]));
      }
      u16* op = out + D_ + cb + (128*cj + 8*m + 2*kg)*(2*D_);
      *(u32x4*)(op)        = u32x4{pk2(o[0].x,o[1].x), pk2(o[2].x,o[3].x), pk2(o[4].x,o[5].x), pk2(o[6].x,o[7].x)};
      *(u32x4*)(op + 2*D_) = u32x4{pk2(o[0].y,o[1].y), pk2(o[2].y,o[3].y), pk2(o[4].y,o[5].y), pk2(o[6].y,o[7].y)};
    };
    if (Hi || h < 3){
      u32x4 b0, b1, b2, b3, b4, b5, b6;
      LDU(h + 8, 1ull, b0, b1, b2, b3, b4, b5, b6, asm volatile("" : "=v"(b4), "=v"(b5), "=v"(b6));)
      WKK(2, b0, b1, b4, b5, b6);
      TSP(2);
      PS(b0, b1, b4, b5, b6);
      WV(0, b2, b3);
      PV(h + 8, b2, b3);
      TSP(3);
    }
    const int c1 = Hi ? 11 + h : (h < 3 ? 5 + h : h - 3);
    const int nc = Hi ? (h < 5) : (h >= 3 && h < 6 ? 2 : 1);
    #pragma unroll 1
    for (int i=0;i<nc;i++){
      const int cj = c1 + 8*i;
      const u16* qp = q + (128*cj + 8*m + 2*kg)*D_ + cb;
      const u32x4 qa = *(const u32x4*)(qp), qb = *(const u32x4*)(qp + D_);
      SB_;
      OC(cj, Hi ? h : cj, qa, qb);
      if (i == 0){ TSP(5); }
    }
   }
  TSP(6);
  }
}

extern "C" void kernel_launch(void* const* d_in, const int* in_sizes, int n_in,
                              void* d_out, int out_size, void* d_ws, size_t ws_size,
                              hipStream_t stream) {
  mono_kernel<<<256, 1024, LDS_TOTAL, stream>>>((const u16*)d_in[0], (const u16*)d_in[1], (const u16*)d_in[2], (const u16*)d_in[3],
     (const u16*)d_in[4], (const u16*)d_in[5], (const u16*)d_in[6], (const u16*)d_in[7], (const u16*)d_in[8], (const u16*)d_in[9],
     (const u16*)d_in[10], (const u16*)d_in[11], (const u16*)d_in[12], (u16*)d_out
);
}
```
